```python
import torch

def _softmax(x, axis=-1):
    return torch.softmax(x, dim=axis)

def _gelu(x):
    return torch.nn.functional.gelu(x, approximate='tanh')

def _silu(x):
    return torch.nn.functional.silu(x)

def _sigmoid(x):
    return torch.sigmoid(x)

def _layernorm(x, axis=-1, eps=1e-05):
    m = x.mean(dim=axis, keepdim=True)
    v = x.var(dim=axis, keepdim=True, unbiased=False)
    return (x - m) / torch.sqrt(v + eps)

def _scatter_add(tgt, idx, src, axis=0):
    return tgt.clone().index_add_(axis, idx, src)

def _segment_sum(src, seg, n):
    out = torch.zeros((n,) + src.shape[1:], dtype=src.dtype)
    return out.index_add_(0, seg, src)

def _pad(x, pw):
    flat = tuple((v for pair in reversed(pw) for v in pair))
    return torch.nn.functional.pad(x, flat)
kernel_name = 'standalone__factorization_afm__recsys'

def setup_inputs():

    _w = lambda a: (lambda t: t.to(torch.bfloat16) if t.is_floating_point() else t)(torch.as_tensor(a))
    B, F, D, V = (1024, 26, 128, min(1000000, 100000))
    A = D
    ii, jj = tuple(torch.triu_indices(F, F, 1).unbind(0))
    return {'idx': _w((lambda lo, hi, *s: torch.randint(lo, hi, s))(0, V, B, F).to(torch.int64)), 'table': _w((lambda *s: torch.randn(*s, dtype=torch.bfloat16) * 0.8)(V, D)), 'ii': _w(ii.to(torch.int64)), 'jj': _w(jj.to(torch.int64)), 'W': _w((lambda *s: torch.randn(*s, dtype=torch.bfloat16) * 0.4)(D, A) * (1.0 / D ** 0.5)), 'b': _w((lambda *s: torch.randn(*s, dtype=torch.bfloat16) * 0.4)(A)), 'h': _w((lambda *s: torch.randn(*s, dtype=torch.bfloat16) * 0.4)(A)), 'p': _w((lambda *s: torch.randn(*s, dtype=torch.bfloat16) * 0.8)(D))}

def reference(idx, table, ii, jj, W, b, h, p):
    emb = table[idx]
    ei = emb[:, ii, :].clone()
    ej = emb[:, jj, :].clone()
    pair = ei * ej
    hid = pair @ W + b
    hid = 0.5 * (hid + torch.abs(hid))
    e = hid @ h
    alpha = _softmax(e, axis=-1)
    pooled = torch.einsum('bp,bpd->bd', alpha, pair)
    return pooled @ p
```

```cpp
#include <hip/hip_runtime.h>
#include <hip/hip_bf16.h>
#include <stdint.h>

#define AFM_HAS_STAMPS 0

typedef float  f32x2  __attribute__((ext_vector_type(2)));
typedef __bf16 bf16x2 __attribute__((ext_vector_type(2)));

__device__ __forceinline__ float bf2f(unsigned short u) { return __builtin_bit_cast(float, (unsigned)u << 16); }
__device__ __forceinline__ unsigned short f2bf(float x) {
  f32x2 v = {x, 0.f}; bf16x2 r = __builtin_convertvector(v, bf16x2);
  return (unsigned short)(__builtin_bit_cast(unsigned, r) & 0xffffu);
}
__device__ __forceinline__ float rbf(float x) { return bf2f(f2bf(x)); }

extern "C" __device__ __attribute__((used, aligned(512))) const unsigned afm_lane_tab[804] = {
  738206208u, 533735168u, 697050480u, 864u, 743449092u, 539240196u, 719070580u, 20972672u, 748691976u, 539764488u, 554968440u, 1440u, 753934860u, 550250252u, 717235580u, 20973248u,
  759177744u, 555755280u, 613426560u, 2016u, 764420628u, 561260308u, 540550532u, 20971520u, 769663512u, 561784600u, 549463432u, 288u, 774906396u, 530327324u, 722740620u, 20972096u,
  738468432u, 538978128u, 618931888u, 3168u, 743711316u, 534521684u, 727983796u, 20974976u, 748954200u, 549988184u, 708585144u, 3744u, 754197084u, 555493212u, 723003068u, 20975552u,
  759439968u, 560998240u, 728508096u, 4320u, 764682852u, 556541796u, 624436932u, 20973824u, 769925736u, 530065256u, 702555848u, 2592u, 775168620u, 535570284u, 697575116u, 20974400u,
  738730656u, 701507488u, 697050480u, 1049456u, 743973540u, 707012516u, 719070580u, 22021264u, 749216424u, 707536808u, 554968440u, 1050032u, 754459308u, 718022572u, 717235580u, 22021840u,
  759702192u, 723527600u, 613426560u, 1050608u, 764945076u, 729032628u, 540550532u, 22020112u, 770187960u, 729556920u, 549463432u, 1048880u, 775430844u, 698099644u, 722740620u, 22020688u,
  738992880u, 706750448u, 618931888u, 1051760u, 744235764u, 702294004u, 727983796u, 22023568u, 749478648u, 717760504u, 708585144u, 1052336u, 754721532u, 723265532u, 723003068u, 22024144u,
  759964416u, 728770560u, 728508096u, 1052912u, 765207300u, 724314116u, 624436932u, 22022416u, 770450184u, 697837576u, 702555848u, 1051184u, 775693068u, 703342604u, 697575116u, 22022992u,
  739255104u, 617621568u, 529279600u, 2098048u, 744497988u, 623126596u, 549726836u, 23069856u, 749740872u, 623650888u, 555231864u, 2098624u, 754983756u, 634136652u, 560736892u, 23070432u,
  760226640u, 639641680u, 551299712u, 2099200u, 765469524u, 645146708u, 529803908u, 23068704u, 770712408u, 645671000u, 535308936u, 2097472u, 775955292u, 614213724u, 540813964u, 23069280u,
  739517328u, 622864528u, 613166000u, 2100352u, 744760212u, 618408084u, 633613236u, 23072160u, 750003096u, 633874584u, 639118264u, 2100928u, 755245980u, 639379612u, 644623292u, 23072736u,
  760488864u, 644884640u, 635186112u, 2101504u, 765731748u, 640428196u, 613690308u, 23071008u, 770974632u, 613951656u, 619195336u, 2099776u, 776217516u, 619456684u, 624700364u, 23071584u,
  739779552u, 544221408u, 529279600u, 3146640u, 745022436u, 544483556u, 549726836u, 24118448u, 750265320u, 544745704u, 555231864u, 3147216u, 755508204u, 545007852u, 560736892u, 24119024u,
  760751088u, 545270000u, 551299712u, 3147792u, 765993972u, 545532148u, 529803908u, 24117296u, 771236856u, 545794296u, 535308936u, 3146064u, 776479740u, 546056444u, 540813964u, 24117872u,
  740041776u, 565193008u, 613166000u, 3148944u, 745284660u, 565455156u, 633613236u, 24120752u, 750527544u, 565717304u, 639118264u, 3149520u, 755770428u, 565979452u, 644623292u, 24121328u,
  761013312u, 566241600u, 635186112u, 3150096u, 766256196u, 566503748u, 613690308u, 24119600u, 771499080u, 566765896u, 619195336u, 3148368u, 776741964u, 567028044u, 624700364u, 24120176u,
  358617600u, 8064u, 301992192u, 339744960u, 434116224u, 18882512u, 320866848u, 339744960u, 94372128u, 37756960u, 339741504u, 339744960u, 377491968u, 452993136u, 452991744u, 132121440u,
  150998400u, 75505856u, 377490816u, 452992032u, 37750464u, 94380304u, 396365472u, 283118688u, 75497472u, 113254752u, 415240128u, 283118688u, 396366624u, 471867824u, 471866400u, 358619616u,
  169873056u, 151003648u, 4608u, 1270894656u, 415240992u, 169878096u, 18879264u, 1044396480u, 434115648u, 188752544u, 37753920u, 1044396480u, 396366912u, 207626992u, 56628576u, 1327516320u,
  415241568u, 226501440u, 75503232u, 1119897408u, 188747712u, 245375888u, 94377888u, 1365267072u, 320870016u, 264250336u, 113252544u, 1119897408u, 301995936u, 283124784u, 132127200u, 1365267072u,
  359666192u, 1056656u, 303040784u, 340793552u, 435164816u, 19931104u, 321915440u, 340793552u, 95420720u, 38805552u, 340790096u, 340793552u, 378540560u, 454041728u, 454040336u, 133170032u,
  152046992u, 76554448u, 378539408u, 454040624u, 38799056u, 95428896u, 397414064u, 284167280u, 76546064u, 114303344u, 416288720u, 284167280u, 397415216u, 472916416u, 472914992u, 359668208u,
  170921648u, 152052240u, 1053200u, 1271943248u, 416289584u, 170926688u, 19927856u, 1045445072u, 435164240u, 189801136u, 38802512u, 1045445072u, 397415504u, 208675584u, 57677168u, 1328564912u,
  416290160u, 227550032u, 76551824u, 1120946000u, 189796304u, 246424480u, 95426480u, 1366315664u, 321918608u, 265298928u, 114301136u, 1120946000u, 303044528u, 284173376u, 133175792u, 1366315664u,
  360714784u, 2105248u, 304089376u, 341842144u, 436213408u, 20979696u, 322964032u, 341842144u, 96469312u, 39854144u, 341838688u, 341842144u, 379589152u, 455090320u, 455088928u, 134218624u,
  153095584u, 77603040u, 379588000u, 455089216u, 39847648u, 96477488u, 398462656u, 285215872u, 77594656u, 115351936u, 417337312u, 285215872u, 398463808u, 473965008u, 473963584u, 360716800u,
  171970240u, 153100832u, 2101792u, 1272991840u, 417338176u, 171975280u, 20976448u, 1046493664u, 436212832u, 190849728u, 39851104u, 1046493664u, 398464096u, 209724176u, 58725760u, 1329613504u,
  417338752u, 228598624u, 77600416u, 1121994592u, 190844896u, 247473072u, 96475072u, 1367364256u, 322967200u, 266347520u, 115349728u, 1121994592u, 304093120u, 285221968u, 134224384u, 1367364256u,
  361763376u, 3153840u, 305137968u, 342890736u, 437262000u, 22028288u, 324012624u, 342890736u, 97517904u, 40902736u, 342887280u, 342890736u, 380637744u, 456138912u, 456137520u, 135267216u,
  154144176u, 78651632u, 380636592u, 456137808u, 40896240u, 97526080u, 399511248u, 286264464u, 78643248u, 116400528u, 418385904u, 286264464u, 399512400u, 475013600u, 475012176u, 361765392u,
  173018832u, 154149424u, 3150384u, 1274040432u, 418386768u, 173023872u, 22025040u, 1047542256u, 437261424u, 191898320u, 40899696u, 1047542256u, 399512688u, 210772768u, 59774352u, 1330662096u,
  418387344u, 229647216u, 78649008u, 1123043184u, 191893488u, 248521664u, 97523664u, 1368412848u, 324015792u, 267396112u, 116398320u, 1123043184u, 305141712u, 286270560u, 135272976u, 1368412848u,
  0u, 18874368u, 524304u, 37748740u, 75497504u, 56623112u, 76021808u, 75497484u, 304087104u, 94371856u, 304611408u, 113246228u, 379584608u, 132120600u, 380108912u, 28u,
  608174208u, 37748864u, 608698512u, 56623236u, 683671712u, 75497608u, 684196016u, 94371980u, 912261312u, 113246352u, 912785616u, 132120724u, 987758816u, 152u, 988283120u, 18874524u,
  1053216u, 19922944u, 1577520u, 38797316u, 76550720u, 57671688u, 77075024u, 76546060u, 305140320u, 95420432u, 305664624u, 114294804u, 380637824u, 133169176u, 381162128u, 1048604u,
  609227424u, 38797440u, 609751728u, 57671812u, 684724928u, 76546184u, 685249232u, 95420556u, 913314528u, 114294928u, 913838832u, 133169300u, 988812032u, 1048728u, 989336336u, 19923100u,
  2106432u, 20971520u, 2630736u, 39845892u, 77603936u, 58720264u, 78128240u, 77594636u, 306193536u, 96469008u, 306717840u, 115343380u, 381691040u, 134217752u, 382215344u, 2097180u,
  610280640u, 39846016u, 610804944u, 58720388u, 685778144u, 77594760u, 686302448u, 96469132u, 914367744u, 115343504u, 914892048u, 134217876u, 989865248u, 2097304u, 990389552u, 20971676u,
  3159648u, 22020096u, 3683952u, 40894468u, 78657152u, 59768840u, 79181456u, 78643212u, 307246752u, 97517584u, 307771056u, 116391956u, 382744256u, 135266328u, 383268560u, 3145756u,
  611333856u, 40894592u, 611858160u, 59768964u, 686831360u, 78643336u, 687355664u, 97517708u, 915420960u, 116392080u, 915945264u, 135266452u, 990918464u, 3145880u, 991442768u, 22020252u,
  0u, 0u, 0u, 0u, 0u, 0u, 16843008u, 16843009u, 16843009u, 16843009u, 16843009u, 16843009u, 33686017u, 33686018u, 33686018u, 33686018u,
  33686018u, 33686018u, 50529027u, 50529027u, 50529027u, 50529027u, 50529027u, 67371779u, 67372036u, 67372036u, 67372036u, 67372036u, 84149252u, 84215045u, 84215045u, 84215045u,
  84215045u, 100992261u, 101058054u, 101058054u, 101058054u, 101058054u, 117900806u, 117901063u, 117901063u, 117901063u, 117901063u, 134744072u, 134744072u, 134744072u, 134744072u, 151587080u,
  151587081u, 151587081u, 151587081u, 168430089u, 168430090u, 168430090u, 168430090u, 185273099u, 185273099u, 185273099u, 202115851u, 202116108u, 202116108u, 218893324u, 218959117u, 218959117u,
  235736333u, 235802126u, 235802126u, 252644878u, 252645135u, 252645135u, 269488144u, 269488144u, 286331152u, 286331153u, 303174161u, 303174162u, 320017171u, 336859923u, 353637396u, 370480405u,
  387388950u, 404166422u, 67305985u, 134678021u, 202050057u, 269422093u, 336794129u, 404166165u, 67306009u, 134678021u, 202050057u, 269422093u, 336794129u, 404166165u, 84149017u, 151521030u,
  218893066u, 286265102u, 353637138u, 421009174u, 117835012u, 185207048u, 252579084u, 319951120u, 387323156u, 100997400u, 168364039u, 235736075u, 303108111u, 370480147u, 102307863u, 168364039u,
  235736075u, 303108111u, 370480147u, 119085079u, 185207048u, 252579084u, 319951120u, 387323156u, 151525656u, 218893066u, 286265102u, 353637138u, 421009174u, 202050057u, 269422093u, 336794129u,
  404166165u, 202050073u, 269422093u, 336794129u, 404166165u, 218893081u, 286265102u, 353637138u, 421009174u, 252579084u, 319951120u, 387323156u, 235739416u, 303108111u, 370480147u, 236525591u,
  303108111u, 370480147u, 253302807u, 319951120u, 387323156u, 286267672u, 353637138u, 421009174u, 336794129u, 404166165u, 336794137u, 404166165u, 353637145u, 421009174u, 387323156u, 370481432u,
  370743319u, 387520535u, 421009688u, 421074969u
};

extern "C" __global__ __launch_bounds__(256, 1) __attribute__((aligned(4096)))
void afm_fast(const int* __restrict__ idx, const unsigned short* __restrict__ table,
                     const int* __restrict__ ii, const int* __restrict__ jj,
                     const unsigned short* __restrict__ W, const unsigned short* __restrict__ bvec,
                     const unsigned short* __restrict__ hvec, const unsigned short* __restrict__ pvec,
                     unsigned short* __restrict__ out, unsigned lim, unsigned kx
                     ) {
  __shared__ __attribute__((aligned(16))) char smem[107744];
  const unsigned lbase = (unsigned)(uintptr_t)(__attribute__((address_space(3))) char*)smem;
  unsigned flag;
  asm volatile(
      "s_load_dwordx16 s[8:23], %1, 0x0\n"
      "s_load_dwordx4 s[24:27], %1, 0x40\n"
      "s_mov_b32 s41, 0\n"
      "s_mov_b32 s42, 0\n"
      "s_mov_b32 s58, 0\n"
      "s_mov_b32 s35, 0\n"
      "0:\n"
      "s_getpc_b64 s[56:57]\n"
      "s_add_u32 s98, s56, afm_lane_tab@rel32@lo+4\n"
      "s_addc_u32 s99, s57, afm_lane_tab@rel32@hi+12\n"
      "v_and_b32 v255, 63, %3\n"
      "v_and_b32 v254, 127, %3\n"
      "v_lshrrev_b32 v253, 7, %3\n"
      "v_min_u32 v254, 81, v254\n"
      "v_mul_u32_u24 v253, 82, v253\n"
      "v_lshlrev_b32 v14, 4, v255\n"
      "v_lshlrev_b32 v252, 3, v255\n"
      "v_add_lshl_u32 v253, v253, v254, 2\n"
      "global_load_dwordx2 v[22:23], v252, s[98:99] offset:2048\n"
      "global_load_dword v20, v253, s[98:99] offset:2560\n"
      "global_load_dwordx4 v[16:19], v14, s[98:99]\n"
      "global_load_dwordx4 v[196:199], v14, s[98:99] offset:1024\n"
      "v_lshrrev_b32 v2, 6, %3\n"
      "s_bfe_u32 s44, %2, 0x30003\n"
      "s_mul_i32 s44, s44, 24\n"
      "v_mad_u32_u24 v255, v2, 6, v255\n"
      "s_mov_b64 exec, 0x3f\n"
      "v_add_lshl_u32 v255, v255, s44, 7\n"
      "global_load_dword v255, v255, s[56:57]\n"
      "s_mov_b64 exec, -1\n"
      "v_bfe_u32 v4, %3, 4, 2\n"
      "v_and_b32 v3, 15, %3\n"
      "v_readfirstlane_b32 s53, v2\n"
      "s_and_b32 s50, %2, 7\n"
      "s_lshr_b32 s51, %2, 3\n"
      "s_lshl2_add_u32 s51, s51, s53\n"
      "s_mul_i32 s51, s51, 104\n"
      "v_lshlrev_b32 v13, 4, v3\n"
      "v_lshlrev_b32 v225, 4, v254\n"
      "v_min_u32 v225, 1284, v225\n"
      "v_lshl_add_u32 v80, v2, 11, v14\n"
      "v_add_u32 v81, 8192, v80\n"
      "v_add_u32 v82, 16384, v80\n"
      "v_add_u32 v26, 24576, v80\n"
      "v_lshlrev_b32 v2, 4, v2\n"
      "s_mul_i32 s38, s53, 13920\n"
      "v_add_u32 v2, 37632, v2\n"
      "s_add_u32 s38, s38, 38144\n"
      "v_mul_u32_u24 v222, 288, v4\n"
      "v_add_u32 v213, s38, v13\n"
      "v_mov_b32 v5, 0\n"
      "v_add_u32 v1, v222, v213\n"
      "v_mov_b32 v222, 0x7fa5a5a5\n"
      "s_cmp_lg_u32 s35, 0\n"
      "s_cbranch_scc1 2f\n"
      "ds_write2_b32 v2, v5, v5 offset0:0 offset1:1\n"
      "ds_write_b32 v2, v5 offset:8\n"
      "ds_write_b32 v1, v222 offset:12736\n"
      "2:\n"
      "s_mov_b32 s43, 0xffff0000\n"
      "s_mov_b32 s56, 0\n"
      "s_mov_b32 s40, 0\n"
      "s_mov_b32 s55, 0\n"
      "s_and_b32 s46, s53, 2\n"
      "s_lshl_b32 s54, 3, s46\n"
      "s_bitcmp1_b32 s53, 0\n"
      "s_cselect_b64 s[100:101], -1, 0\n"
      "s_movk_i32 s98, 13920\n"
      "s_cselect_b32 s99, s98, 0\n"
      "s_cmovk_i32 s98, -13920\n"
      "s_cselect_b32 s46, 0, 18560\n"
      "s_cselect_b32 s47, 18560, 0\n"
      "v_add_u32 v212, s46, v14\n"
      "v_add_u32 v14, s47, v14\n"
      "s_sub_u32 s99, s38, s99\n"
      "v_cmp_eq_u32 vcc, 1, v4\n"
      "v_mov_b32 v222, 0x3300\n"
      "s_mov_b32 s46, 0\n"
      "v_cndmask_b32 v94, 0, v222, vcc\n"
      "s_mov_b32 s47, 0xc72c4bb9\n"
      "v_lshlrev_b32 v223, 4, v4\n"
      "v_lshrrev_b64 v[146:147], v223, s[46:47]\n"
      "v_lshlrev_b32 v138, 16, v146\n"
      "v_lshrrev_b32 v130, 16, v138\n"
      "v_mov_b32 v200, 0x4738aa3b\n"
      "v_lshlrev_b32 v224, 16, v94\n"
      "v_accvgpr_write_b32 a128, v130\n"
      "v_accvgpr_write_b32 a129, 0\n"
      "v_accvgpr_write_b32 a130, v138\n"
      "v_accvgpr_write_b32 a131, 0\n"
      "v_accvgpr_write_b32 a132, 0\n"
      "v_accvgpr_write_b32 a133, v130\n"
      "v_accvgpr_write_b32 a134, 0\n"
      "v_accvgpr_write_b32 a135, v138\n"
      "v_accvgpr_write_b32 a208, v94\n"
      "v_accvgpr_write_b32 a209, 0\n"
      "v_accvgpr_write_b32 a210, v224\n"
      "v_accvgpr_write_b32 a211, 0\n"
      "v_accvgpr_write_b32 a212, 0\n"
      "v_accvgpr_write_b32 a213, v94\n"
      "v_accvgpr_write_b32 a214, 0\n"
      "v_accvgpr_write_b32 a215, v224\n"
      "v_add_u32 v95, s38, v223\n"
      "v_and_b32 v227, 3, %3\n"
      "v_bfe_u32 v224, %3, 2, 2\n"
      "v_cmp_eq_u32 vcc, v227, v4\n"
      "v_mov_b32 v222, 0x3a00\n"
      "v_mov_b64 v[84:85], 0\n"
      "s_waitcnt lgkmcnt(0)\n"
      "s_mul_i32 s50, s50, s27\n"
      "s_add_u32 s50, s50, s51\n"
      "s_min_u32 s34, s50, s26\n"
      "s_load_dwordx16 s[60:75], s[8:9], s34\n"
      "s_load_dwordx8 s[76:83], s[8:9], s34 offset:0x40\n"
      "s_load_dwordx2 s[84:85], s[8:9], s34 offset:0x60\n"
      "global_load_dwordx4 v[96:99], v80, s[16:17] offset:0\n"
      "global_load_dwordx4 v[100:103], v80, s[16:17] offset:1024\n"
      "global_load_dwordx4 v[104:107], v81, s[16:17] offset:0\n"
      "global_load_dwordx4 v[108:111], v81, s[16:17] offset:1024\n"
      "v_mov_b64 v[24:25], s[20:21]\n"
      "s_mov_b32 exec_hi, 0\n"
      "v_mov_b64 v[24:25], s[18:19]\n"
      "s_mov_b32 exec_lo, 0xffff\n"
      "v_mov_b64 v[24:25], s[22:23]\n"
      "s_mov_b64 exec, -1\n"
      "v_add_co_u32 v24, s[28:29], v24, v13\n"
      "s_cmp_lt_u32 s53, 2\n"
      "s_cselect_b64 s[12:13], s[12:13], s[14:15]\n"
      "v_addc_co_u32 v25, s[28:29], 0, v25, s[28:29]\n"
      "global_load_dwordx4 v[192:195], v[24:25], off\n"
      "global_load_dwordx4 v[112:115], v82, s[16:17] offset:0\n"
      "global_load_dwordx4 v[116:119], v82, s[16:17] offset:1024\n"
      "global_load_dwordx4 v[120:123], v26, s[16:17] offset:0\n"
      "global_load_dwordx4 v[124:127], v26, s[16:17] offset:1024\n"
      "s_cmp_lg_u32 s35, 0\n"
      "s_cbranch_scc1 21f\n"
      "s_barrier\n"
      "21:\n"
      "v_mov_b64 v[88:89], 0\n"
      "v_cndmask_b32 v84, 0, v222, vcc\n"
      "v_lshlrev_b32 v222, 4, v224\n"
      "v_lshlrev_b64 v[86:87], v222, v[84:85]\n"
      "s_mov_b32 s46, 0x9905\n"
      "v_lshlrev_b32 v222, 2, v4\n"
      "v_mov_b32 v84, 0\n"
      "v_bfe_u32 v222, s46, v222, 4\n"
      "v_lshl_add_u32 v91, v222, 6, v213\n"
      "v_lshlrev_b32 v227, 4, v227\n"
      "s_mov_b64 s[46:47], 0x3c00\n"
      "v_lshlrev_b32 v5, 5, v3\n"
      "v_lshl_add_u32 v213, v4, 2, v213\n"
      "v_lshl_add_u32 v5, v4, 3, v5\n"
      "v_lshlrev_b64 v[222:223], v227, s[46:47]\n"
      "s_movk_i32 s46, 0x4400\n"
      "v_accvgpr_write_b32 a216, v222\n"
      "v_lshlrev_b64 v[172:173], v227, s[46:47]\n"
      "s_mov_b32 s49, 0x03000000\n"
      "v_accvgpr_write_b32 a217, v223\n"
      "s_movk_i32 s46, 288\n"
      "v_and_b32 v222, 7, %3\n"
      "v_bfe_u32 v223, %3, 3, 1\n"
      "v_mad_u32_u24 v201, v222, s46, v95\n"
      "v_mad_u32_u24 v203, v223, s46, v95\n"
      "s_movk_i32 s47, 320\n"
      "v_mad_u32_u24 v210, v3, s46, v95\n"
      "v_mad_u32_u24 v214, v223, s47, v95\n"
      "s_mov_b32 s59, 0\n"
      "s_mov_b32 s57, 0\n"
      "s_lshr_b32 s33, s34, 2\n"
      "s_mul_i32 s33, s33, 0xc4ec4ec5\n"
      "s_mov_b32 s36, 0\n"
      "s_mov_b32 s37, 0x7fa5a5a5\n"
      "global_load_dwordx4 v[80:83], v225, s[12:13]\n"
      "s_mov_b64 exec, 0\n"
      "s_cmp_eq_u32 s53, 0\n"
      "s_cbranch_scc1 40f\n"
      "s_cmp_eq_u32 s53, 1\n"
      "s_cbranch_scc1 41f\n"
      "s_cmp_eq_u32 s53, 2\n"
      "s_cbranch_scc1 42f\n"
      "s_branch 43f\n"
      "44:\n"
      "s_mov_b64 exec, -1\n"
      "s_waitcnt lgkmcnt(0)\n"
      "v_lshl_add_u32 v6, s60, 8, v13\n"
      "v_lshl_add_u32 v93, s64, 8, v13\n"
      "s_mov_b32 exec_lo, 0xffff0000\n"
      "v_lshl_add_u32 v6, s61, 8, v13\n"
      "v_lshl_add_u32 v93, s65, 8, v13\n"
      "s_mov_b32 exec_lo, 0\n"
      "v_lshl_add_u32 v6, s62, 8, v13\n"
      "v_lshl_add_u32 v93, s66, 8, v13\n"
      "s_mov_b32 exec_hi, 0xffff0000\n"
      "v_lshl_add_u32 v6, s63, 8, v13\n"
      "v_lshl_add_u32 v93, s67, 8, v13\n"
      "s_mov_b64 exec, -1\n"
      "global_load_dwordx4 v[228:231], v6, s[10:11] nt\n"
      "global_load_dwordx4 v[232:235], v93, s[10:11] nt\n"
      "v_lshl_add_u32 v8, s68, 8, v13\n"
      "v_lshl_add_u32 v9, s72, 8, v13\n"
      "v_lshl_add_u32 v10, s76, 8, v13\n"
      "v_lshl_add_u32 v11, s80, 8, v13\n"
      "v_lshl_add_u32 v12, s84, 8, v13\n"
      "s_mov_b32 exec_lo, 0xffff0000\n"
      "v_lshl_add_u32 v8, s69, 8, v13\n"
      "v_lshl_add_u32 v9, s73, 8, v13\n"
      "v_lshl_add_u32 v10, s77, 8, v13\n"
      "v_lshl_add_u32 v11, s81, 8, v13\n"
      "v_lshl_add_u32 v12, s85, 8, v13\n"
      "s_mov_b32 exec_lo, 0\n"
      "v_lshl_add_u32 v8, s70, 8, v13\n"
      "v_lshl_add_u32 v9, s74, 8, v13\n"
      "v_lshl_add_u32 v10, s78, 8, v13\n"
      "v_lshl_add_u32 v11, s82, 8, v13\n"
      "s_mov_b32 exec_hi, 0xffff0000\n"
      "v_lshl_add_u32 v8, s71, 8, v13\n"
      "v_lshl_add_u32 v9, s75, 8, v13\n"
      "v_lshl_add_u32 v10, s79, 8, v13\n"
      "v_lshl_add_u32 v11, s83, 8, v13\n"
      "s_mov_b64 exec, -1\n"
      "s_lshl_b32 s46, s53, 8\n"
      "s_lshl_b32 s47, s53, 5\n"
      "s_waitcnt vmcnt(16)\n"
      "v_add_u32_sdwa v15, s46, v22 dst_sel:DWORD dst_unused:UNUSED_PAD src0_sel:DWORD src1_sel:WORD_0\n"
      "v_add_u32_sdwa v92, s46, v22 dst_sel:DWORD dst_unused:UNUSED_PAD src0_sel:DWORD src1_sel:WORD_1\n"
      "v_add_u32_sdwa v7, s47, v23 dst_sel:DWORD dst_unused:UNUSED_PAD src0_sel:DWORD src1_sel:WORD_0\n"
      "v_add_u32_sdwa v202, s38, v23 dst_sel:DWORD dst_unused:UNUSED_PAD src0_sel:DWORD src1_sel:WORD_1\n"
      "s_waitcnt vmcnt(15)\n"
      "v_and_b32 v160, 0xff, v20\n"
      "v_bfe_u32 v161, v20, 8, 8\n"
      "v_bfe_u32 v162, v20, 16, 8\n"
      "v_lshrrev_b32 v163, 24, v20\n"
      "s_waitcnt vmcnt(14)\n"
      "v_add_u32_sdwa v204, s38, v16 dst_sel:DWORD dst_unused:UNUSED_PAD src0_sel:DWORD src1_sel:WORD_0\n"
      "v_add_u32_sdwa v205, s38, v16 dst_sel:DWORD dst_unused:UNUSED_PAD src0_sel:DWORD src1_sel:WORD_1\n"
      "v_add_u32_sdwa v206, s38, v17 dst_sel:DWORD dst_unused:UNUSED_PAD src0_sel:DWORD src1_sel:WORD_0\n"
      "v_add_u32_sdwa v207, s38, v17 dst_sel:DWORD dst_unused:UNUSED_PAD src0_sel:DWORD src1_sel:WORD_1\n"
      "v_add_u32_sdwa v208, s38, v18 dst_sel:DWORD dst_unused:UNUSED_PAD src0_sel:DWORD src1_sel:WORD_0\n"
      "v_add_u32_sdwa v209, s38, v18 dst_sel:DWORD dst_unused:UNUSED_PAD src0_sel:DWORD src1_sel:WORD_1\n"
      "v_add_u32_sdwa v211, s38, v19 dst_sel:DWORD dst_unused:UNUSED_PAD src0_sel:DWORD src1_sel:WORD_0\n"
      "v_add_u32_sdwa v215, s38, v19 dst_sel:DWORD dst_unused:UNUSED_PAD src0_sel:DWORD src1_sel:WORD_1\n"
      "s_waitcnt vmcnt(13)\n"
      "v_add_u32_sdwa v216, s38, v196 dst_sel:DWORD dst_unused:UNUSED_PAD src0_sel:DWORD src1_sel:WORD_0\n"
      "v_add_u32_sdwa v217, s38, v196 dst_sel:DWORD dst_unused:UNUSED_PAD src0_sel:DWORD src1_sel:WORD_1\n"
      "v_add_u32_sdwa v218, s38, v197 dst_sel:DWORD dst_unused:UNUSED_PAD src0_sel:DWORD src1_sel:WORD_0\n"
      "v_add_u32_sdwa v219, s38, v197 dst_sel:DWORD dst_unused:UNUSED_PAD src0_sel:DWORD src1_sel:WORD_1\n"
      "v_add_u32_sdwa v220, s38, v198 dst_sel:DWORD dst_unused:UNUSED_PAD src0_sel:DWORD src1_sel:WORD_0\n"
      "v_add_u32_sdwa v221, s38, v198 dst_sel:DWORD dst_unused:UNUSED_PAD src0_sel:DWORD src1_sel:WORD_1\n"
      "s_cmp_lg_u32 s58, 0\n"
      "s_cbranch_scc1 70f\n"
      "71:\n"
      "s_waitcnt vmcnt(11)\n"
      "v_mfma_f32_16x16x32_bf16 v[128:131], v[86:89], v[96:99], 0\n"
      "s_waitcnt vmcnt(10)\n"
      "v_mfma_f32_16x16x32_bf16 v[132:135], v[86:89], v[100:103], 0\n"
      "v_mfma_f32_16x16x32_bf16 v[136:139], v[84:87], v[96:99], 0\n"
      "v_mfma_f32_16x16x32_bf16 v[140:143], v[84:87], v[100:103], 0\n"
      "v_max3_f32 v224, |v97|, |v98|, |v103|\n"
      "s_waitcnt vmcnt(9)\n"
      "v_mfma_f32_16x16x32_bf16 v[144:147], v[86:89], v[104:107], 0\n"
      "s_nop 0\n"
      "v_cvt_pk_f16_f32 v164, v128, v129\n"
      "v_cvt_pk_f16_f32 v165, v130, v131\n"
      "s_waitcnt vmcnt(8)\n"
      "v_mfma_f32_16x16x32_bf16 v[148:151], v[86:89], v[108:111], 0\n"
      "v_cvt_pk_f16_f32 v166, v132, v133\n"
      "v_cvt_pk_f16_f32 v167, v134, v135\n"
      "v_mfma_f32_16x16x32_bf16 v[152:155], v[84:87], v[104:107], 0\n"
      "ds_write_b128 v15, v[164:167] offset:0\n"
      "v_cvt_pk_f16_f32 v168, v136, v137\n"
      "v_cvt_pk_f16_f32 v169, v138, v139\n"
      "v_mfma_f32_16x16x32_bf16 v[156:159], v[84:87], v[108:111], 0\n"
      "v_cvt_pk_f16_f32 v170, v140, v141\n"
      "v_cvt_pk_f16_f32 v171, v142, v143\n"
      "global_load_dwordx4 v[236:239], v8, s[10:11] nt\n"
      "s_waitcnt vmcnt(7)\n"
      "v_mfma_f32_16x16x32_bf16 v[128:131], v[86:89], v[112:115], 0\n"
      "ds_write_b128 v15, v[168:171] offset:18560\n"
      "v_cvt_pk_f16_f32 v164, v144, v145\n"
      "v_cvt_pk_f16_f32 v165, v146, v147\n"
      "s_waitcnt vmcnt(6)\n"
      "v_mfma_f32_16x16x32_bf16 v[132:135], v[86:89], v[116:119], 0\n"
      "v_cvt_pk_f16_f32 v166, v148, v149\n"
      "v_cvt_pk_f16_f32 v167, v150, v151\n"
      "v_mfma_f32_16x16x32_bf16 v[136:139], v[84:87], v[112:115], 0\n"
      "ds_write_b128 v15, v[164:167] offset:1152\n"
      "v_cvt_pk_f16_f32 v168, v152, v153\n"
      "v_cvt_pk_f16_f32 v169, v154, v155\n"
      "v_mfma_f32_16x16x32_bf16 v[140:143], v[84:87], v[116:119], 0\n"
      "v_cvt_pk_f16_f32 v170, v156, v157\n"
      "v_cvt_pk_f16_f32 v171, v158, v159\n"
      "v_max3_f32 v224, |v112|, |v117|, v224\n"
      "s_mov_b32 s48, 0x3d800000\n"
      "v_cmp_le_f32 s[28:29], s48, v224\n"
      "s_waitcnt vmcnt(5)\n"
      "v_mfma_f32_16x16x32_bf16 v[144:147], v[86:89], v[120:123], 0\n"
      "ds_write_b128 v15, v[168:171] offset:19712\n"
      "v_cvt_pk_f16_f32 v164, v128, v129\n"
      "v_cvt_pk_f16_f32 v165, v130, v131\n"
      "s_waitcnt vmcnt(4)\n"
      "v_mfma_f32_16x16x32_bf16 v[148:151], v[86:89], v[124:127], 0\n"
      "v_cvt_pk_f16_f32 v166, v132, v133\n"
      "v_cvt_pk_f16_f32 v167, v134, v135\n"
      "v_mfma_f32_16x16x32_bf16 v[152:155], v[84:87], v[120:123], 0\n"
      "ds_write_b128 v15, v[164:167] offset:2304\n"
      "v_cvt_pk_f16_f32 v168, v136, v137\n"
      "v_cvt_pk_f16_f32 v169, v138, v139\n"
      "v_mfma_f32_16x16x32_bf16 v[156:159], v[84:87], v[124:127], 0\n"
      "v_cvt_pk_f16_f32 v170, v140, v141\n"
      "v_cvt_pk_f16_f32 v171, v142, v143\n"
      "s_cmp_lg_u64 s[28:29], 0\n"
      "s_cselect_b32 s52, 13, 5\n"
      "ds_write_b128 v15, v[168:171] offset:20864\n"
      "v_mfma_f32_4x4x4_16b_bf16 v[176:179], v[172:173], v[192:193], 0\n"
      "v_cndmask_b32 v188, v192, v194, s[100:101]\n"
      "v_cvt_pk_f16_f32 v164, v144, v145\n"
      "v_cndmask_b32 v189, v193, v195, s[100:101]\n"
      "v_max3_f32 v222, |v192|, |v193|, |v195|\n"
      "v_cvt_pk_f16_f32 v165, v146, v147\n"
      "v_mfma_f32_4x4x4_16b_bf16 v[180:183], v[172:173], v[194:195], 0\n"
      "v_subrev_u32 v222, 0x3d800000, v222\n"
      "v_cvt_pk_f16_f32 v166, v148, v149\n"
      "v_mfma_f32_16x16x16_bf16 v[48:51], v[188:189], a[128:129], 0\n"
      "v_cmp_gt_u32 vcc, s49, v222\n"
      "v_cvt_pk_f16_f32 v167, v150, v151\n"
      "v_mfma_f32_16x16x16_bf16 v[52:55], v[188:189], a[130:131], 0\n"
      "v_cndmask_b32 v190, v194, v192, s[100:101]\n"
      "ds_write_b128 v15, v[164:167] offset:3456\n"
      "v_cndmask_b32 v191, v195, v193, s[100:101]\n"
      "v_mfma_f32_16x16x16_bf16 v[56:59], v[188:189], a[132:133], 0\n"
      "v_cvt_pk_f16_f32 v168, v152, v153\n"
      "s_and_b32 s44, vcc_lo, 0xffff\n"
      "s_cselect_b32 s44, vcc_hi, 0\n"
      "v_cvt_pk_f16_f32 v169, v154, v155\n"
      "v_mfma_f32_16x16x16_bf16 v[60:63], v[188:189], a[134:135], 0\n"
      "s_cmp_eq_u32 s44, 0\n"
      "v_cvt_pk_f16_f32 v170, v156, v157\n"
      "s_cbranch_scc1 22f\n"
      "23:\n"
      "v_mfma_f32_16x16x16_bf16 v[64:67], v[190:191], a[128:129], 0\n"
      "v_cvt_pk_f16_f32 v171, v158, v159\n"
      "v_cvt_pk_f16_f32 v184, v176, v177\n"
      "v_cvt_pk_f16_f32 v185, v178, v179\n"
      "ds_write_b128 v15, v[168:171] offset:22016\n"
      "v_mfma_f32_16x16x16_bf16 v[68:71], v[190:191], a[130:131], 0\n"
      "v_cvt_pk_f16_f32 v186, v180, v181\n"
      "ds_write_b32 v2, v2 offset:4\n"
      "v_cvt_pk_f16_f32 v187, v182, v183\n"
      "ds_write_b128 v91, v[184:187] offset:11904\n"
      "v_mfma_f32_16x16x16_bf16 v[72:75], v[190:191], a[132:133], 0\n"
      "v_mfma_f32_16x16x16_bf16 v[76:79], v[190:191], a[134:135], 0\n"
      "v_mov_b32 v3, s33\n"
      "global_load_ushort v254, v3, s[24:25]\n"
      "v_mfma_f32_16x16x16_bf16 v[16:19], v[188:189], a[208:209], 0\n"
      "ds_read_b128 a[184:187], v95 offset:12480\n"
      "ds_read_b128 a[188:191], v95 offset:12544\n"
      "v_mfma_f32_16x16x16_bf16 v[20:23], v[188:189], a[210:211], 0\n"
      "ds_read_b128 a[192:195], v95 offset:12608\n"
      "ds_read_b128 a[196:199], v95 offset:12672\n"
      "v_mfma_f32_16x16x16_bf16 v[24:27], v[188:189], a[212:213], 0\n"
      "ds_read_b64_tr_b16 a[152:153], v92 offset:0\n"
      "ds_read_b64_tr_b16 a[154:155], v92 offset:18560\n"
      "ds_read_b64_tr_b16 a[156:157], v92 offset:64\n"
      "v_mfma_f32_16x16x16_bf16 v[28:31], v[188:189], a[214:215], 0\n"
      "ds_read_b64_tr_b16 a[158:159], v92 offset:18624\n"
      "ds_read_b64_tr_b16 a[160:161], v92 offset:128\n"
      "ds_read_b64_tr_b16 a[162:163], v92 offset:18688\n"
      "v_mfma_f32_16x16x16_bf16 v[32:35], v[190:191], a[208:209], 0\n"
      "ds_read_b64_tr_b16 a[164:165], v92 offset:192\n"
      "ds_read_b64_tr_b16 a[166:167], v92 offset:18752\n"
      "ds_read_b64_tr_b16 a[168:169], v92 offset:2304\n"
      "v_mfma_f32_16x16x16_bf16 v[36:39], v[190:191], a[210:211], 0\n"
      "ds_read_b64_tr_b16 a[170:171], v92 offset:20864\n"
      "ds_read_b64_tr_b16 a[172:173], v92 offset:2368\n"
      "ds_read_b64_tr_b16 a[174:175], v92 offset:20928\n"
      "v_mfma_f32_16x16x16_bf16 v[40:43], v[190:191], a[212:213], 0\n"
      "ds_read_b64_tr_b16 a[176:177], v92 offset:2432\n"
      "ds_read_b64_tr_b16 a[178:179], v92 offset:20992\n"
      "ds_read_b64_tr_b16 a[180:181], v92 offset:2496\n"
      "v_mfma_f32_16x16x16_bf16 v[44:47], v[190:191], a[214:215], 0\n"
      "ds_read_b64_tr_b16 a[182:183], v92 offset:21056\n"
      "ds_read_b32 v224, v227 offset:37636\n"
      "s_waitcnt lgkmcnt(15)\n"
      "v_mfma_f32_16x16x32_f16 a[200:203], a[184:187], a[152:155], 0\n"
      "s_waitcnt vmcnt(4)\n"
      "v_cmp_eq_u64 vcc, v[80:81], v[160:161]\n"
      "s_waitcnt lgkmcnt(13)\n"
      "v_mfma_f32_16x16x32_f16 a[200:203], a[188:191], a[156:159], a[200:203]\n"
      "v_cmp_eq_u64 s[28:29], v[82:83], v[162:163]\n"
      "s_waitcnt lgkmcnt(11)\n"
      "v_mfma_f32_16x16x32_f16 a[200:203], a[192:195], a[160:163], a[200:203]\n"
      "s_and_b64 vcc, vcc, s[28:29]\n"
      "s_cmp_eq_u64 vcc, -1\n"
      "s_waitcnt lgkmcnt(9)\n"
      "v_mfma_f32_16x16x32_f16 a[200:203], a[196:199], a[164:167], a[200:203]\n"
      "s_cselect_b32 s52, s52, 1\n"
      "v_mov_b32 v223, s52\n"
      "s_waitcnt lgkmcnt(0)\n"
      "v_cmp_eq_u32 vcc, 0, v224\n"
      "s_cbranch_vccz 13f\n"
      "11:\n"
      "ds_read_b32 v224, v227 offset:37636\n"
      "s_waitcnt lgkmcnt(0)\n"
      "v_cmp_eq_u32 vcc, 0, v224\n"
      "s_cbranch_vccnz 11b\n"
      "13:\n"
      "v_mfma_f32_16x16x32_f16 a[204:207], a[184:187], a[168:171], 0\n"
      "ds_read_b128 a[0:3], v14 offset:0\n"
      "ds_read_b128 a[4:7], v14 offset:1152\n"
      "v_mfma_f32_16x16x32_f16 a[204:207], a[188:191], a[172:175], a[204:207]\n"
      "ds_read_b128 a[8:11], v14 offset:2304\n"
      "ds_read_b128 a[12:15], v14 offset:3456\n"
      "v_mfma_f32_16x16x32_f16 a[204:207], a[192:195], a[176:179], a[204:207]\n"
      "ds_read_b128 a[16:19], v14 offset:4640\n"
      "ds_read_b128 a[20:23], v14 offset:5792\n"
      "v_mfma_f32_16x16x32_f16 a[204:207], a[196:199], a[180:183], a[204:207]\n"
      "ds_read_b128 a[24:27], v14 offset:6944\n"
      "ds_read_b128 a[28:31], v14 offset:8096\n"
      "s_cmp_lg_u32 s58, 0\n"
      "s_cbranch_scc1 72f\n"
      "73:\n"
      "global_load_dwordx4 v[240:243], v9, s[10:11] nt\n"
      "s_waitcnt vmcnt(4)\n"
      "v_mfma_f32_4x4x4_16b_bf16 v[176:179], a[216:217], v[228:229], 0\n"
      "v_mfma_f32_4x4x4_16b_bf16 v[180:183], a[216:217], v[230:231], 0\n"
      "ds_read_b128 a[32:35], v14 offset:9280\n"
      "s_waitcnt vmcnt(3)\n"
      "v_mfma_f32_4x4x4_16b_bf16 v[144:147], a[216:217], v[232:233], 0\n"
      "ds_read_b128 a[36:39], v14 offset:10432\n"
      "v_mfma_f32_4x4x4_16b_bf16 v[148:151], a[216:217], v[234:235], 0\n"
      "ds_read_b128 a[40:43], v14 offset:11584\n"
      "ds_read_b128 a[44:47], v14 offset:12736\n"
      "v_cvt_pk_f16_f32 v184, v176, v177\n"
      "v_cvt_pk_f16_f32 v185, v178, v179\n"
      "v_cvt_pk_f16_f32 v186, v180, v181\n"
      "v_cvt_pk_f16_f32 v187, v182, v183\n"
      "ds_write_b128 v1, v[184:187] offset:0\n"
      "v_cvt_pk_f16_f32 v152, v144, v145\n"
      "v_cvt_pk_f16_f32 v153, v146, v147\n"
      "v_cvt_pk_f16_f32 v154, v148, v149\n"
      "v_cvt_pk_f16_f32 v155, v150, v151\n"
      "ds_write_b128 v1, v[152:155] offset:1152\n"
      "ds_read_b128 v[128:131], v201 offset:0\n"
      "ds_read_b128 v[160:163], v202 offset:0\n"
      "ds_read_b128 v[132:135], v201 offset:64\n"
      "ds_read_b128 v[164:167], v202 offset:64\n"
      "ds_read_b128 v[136:139], v201 offset:128\n"
      "ds_read_b128 v[168:171], v202 offset:128\n"
      "ds_read_b128 v[140:143], v201 offset:192\n"
      "ds_read_b128 v[172:175], v202 offset:192\n"
      "s_waitcnt vmcnt(2)\n"
      "v_mfma_f32_4x4x4_16b_bf16 v[112:115], a[216:217], v[236:237], 0\n"
      "v_mfma_f32_4x4x4_16b_bf16 v[116:119], a[216:217], v[238:239], 0\n"
      "s_nop 3\n"
      "v_cvt_pk_f16_f32 v120, v112, v113\n"
      "v_cvt_pk_f16_f32 v121, v114, v115\n"
      "v_cvt_pk_f16_f32 v122, v116, v117\n"
      "v_cvt_pk_f16_f32 v123, v118, v119\n"
      "ds_write_b128 v1, v[120:123] offset:2304\n"
      "s_waitcnt lgkmcnt(7)\n"
      "v_pk_mul_f16 v96, v128, v160\n"
      "v_pk_mul_f16 v97, v129, v161\n"
      "v_pk_mul_f16 v98, v130, v162\n"
      "v_pk_mul_f16 v99, v131, v163\n"
      "s_waitcnt lgkmcnt(5)\n"
      "v_pk_mul_f16 v100, v132, v164\n"
      "v_pk_mul_f16 v101, v133, v165\n"
      "v_pk_mul_f16 v102, v134, v166\n"
      "v_pk_mul_f16 v103, v135, v167\n"
      "s_waitcnt lgkmcnt(3)\n"
      "v_pk_mul_f16 v104, v136, v168\n"
      "v_pk_mul_f16 v105, v137, v169\n"
      "v_pk_mul_f16 v106, v138, v170\n"
      "v_pk_mul_f16 v107, v139, v171\n"
      "3:\n"
      "v_mfma_f32_16x16x32_f16 v[80:83], a[0:3], v[96:99], v[16:19]\n"
      "s_waitcnt lgkmcnt(1)\n"
      "v_pk_mul_f16 v108, v140, v172\n"
      "v_pk_mul_f16 v109, v141, v173\n"
      "v_mfma_f32_16x16x32_f16 v[80:83], a[4:7], v[100:103], v[80:83]\n"
      "v_pk_mul_f16 v110, v142, v174\n"
      "v_pk_mul_f16 v111, v143, v175\n"
      "v_mfma_f32_16x16x32_f16 v[80:83], a[8:11], v[104:107], v[80:83]\n"
      "ds_read_b128 a[60:63], v14 offset:17376\n"
      "ds_read_b128 a[56:59], v14 offset:16224\n"
      "v_mfma_f32_16x16x32_f16 v[80:83], a[12:15], v[108:111], v[80:83]\n"
      "ds_read_b128 a[52:55], v14 offset:15072\n"
      "ds_read_b128 a[48:51], v14 offset:13920\n"
      "v_mfma_f32_16x16x32_f16 v[84:87], a[28:31], v[108:111], v[20:23]\n"
      "ds_read_b128 a[64:67], v212 offset:0\n"
      "ds_read_b128 v[144:147], v203 offset:2304\n"
      "v_mfma_f32_16x16x32_f16 v[84:87], a[24:27], v[104:107], v[84:87]\n"
      "ds_read_b128 a[68:71], v212 offset:1152\n"
      "ds_read_b128 a[72:75], v212 offset:2304\n"
      "v_mfma_f32_16x16x32_f16 v[84:87], a[20:23], v[100:103], v[84:87]\n"
      "ds_read_b128 a[76:79], v212 offset:3456\n"
      "ds_read_b128 a[92:95], v212 offset:8096\n"
      "v_mfma_f32_16x16x32_f16 v[84:87], a[16:19], v[96:99], v[84:87]\n"
      "ds_read_b128 a[88:91], v212 offset:6944\n"
      "ds_read_b128 v[148:151], v203 offset:2368\n"
      "v_mfma_f32_16x16x32_f16 v[88:91], a[32:35], v[96:99], v[24:27]\n"
      "ds_read_b128 a[84:87], v212 offset:5792\n"
      "ds_read_b128 a[80:83], v212 offset:4640\n"
      "v_mfma_f32_16x16x32_f16 v[88:91], a[36:39], v[100:103], v[88:91]\n"
      "v_mul_f32 v188, |v80|, v48\n"
      "v_fma_f32 v188, |v81|, v49, v188\n"
      "v_mfma_f32_16x16x32_f16 v[88:91], a[40:43], v[104:107], v[88:91]\n"
      "ds_read_b128 a[96:99], v212 offset:9280\n"
      "v_fma_f32 v188, |v82|, v50, v188\n"
      "v_mfma_f32_16x16x32_f16 v[88:91], a[44:47], v[108:111], v[88:91]\n"
      "s_waitcnt lgkmcnt(13)\n"
      "ds_read_b128 a[100:103], v212 offset:10432\n"
      "v_fma_f32 v188, |v83|, v51, v188\n"
      "v_mfma_f32_16x16x32_f16 v[92:95], a[60:63], v[108:111], v[28:31]\n"
      "s_waitcnt lgkmcnt(10)\n"
      "v_pk_mul_f16 v112, v128, v144\n"
      "v_pk_mul_f16 v113, v129, v145\n"
      "v_mfma_f32_16x16x32_f16 v[92:95], a[56:59], v[104:107], v[92:95]\n"
      "ds_read_b128 a[104:107], v212 offset:11584\n"
      "v_pk_mul_f16 v114, v130, v146\n"
      "v_mfma_f32_16x16x32_f16 v[92:95], a[52:55], v[100:103], v[92:95]\n"
      "ds_read_b128 v[152:155], v203 offset:2432\n"
      "ds_read_b128 a[108:111], v212 offset:12736\n"
      "v_mfma_f32_16x16x32_f16 v[92:95], a[48:51], v[96:99], v[92:95]\n"
      "s_waitcnt lgkmcnt(12)\n"
      "v_fma_f32 v188, |v84|, v52, v188\n"
      "v_fma_f32 v188, |v85|, v53, v188\n"
      "v_mfma_f32_16x16x32_f16 v[80:83], a[64:67], v[96:99], v[32:35]\n"
      "ds_read_b128 a[124:127], v212 offset:17376\n"
      "v_pk_mul_f16 v115, v131, v147\n"
      "v_mfma_f32_16x16x32_f16 v[80:83], a[68:71], v[100:103], v[80:83]\n"
      "s_waitcnt lgkmcnt(11)\n"
      "v_fma_f32 v188, |v86|, v54, v188\n"
      "v_fma_f32 v188, |v87|, v55, v188\n"
      "v_mfma_f32_16x16x32_f16 v[80:83], a[72:75], v[104:107], v[80:83]\n"
      "s_waitcnt lgkmcnt(8)\n"
      "ds_read_b128 a[120:123], v212 offset:16224\n"
      "v_pk_mul_f16 v116, v132, v148\n"
      "v_mfma_f32_16x16x32_f16 v[80:83], a[76:79], v[108:111], v[80:83]\n"
      "ds_read_b128 a[116:119], v212 offset:15072\n"
      "v_pk_mul_f16 v117, v133, v149\n"
      "v_mfma_f32_16x16x32_f16 v[84:87], a[92:95], v[108:111], v[36:39]\n"
      "ds_read_b128 a[112:115], v212 offset:13920\n"
      "v_fma_f32 v188, |v88|, v56, v188\n"
      "v_mfma_f32_16x16x32_f16 v[84:87], a[88:91], v[104:107], v[84:87]\n"
      "s_waitcnt lgkmcnt(9)\n"
      "v_fma_f32 v188, |v89|, v57, v188\n"
      "v_pk_mul_f16 v118, v134, v150\n"
      "v_mfma_f32_16x16x32_f16 v[84:87], a[84:87], v[100:103], v[84:87]\n"
      "ds_read_b128 v[156:159], v203 offset:2496\n"
      "v_fma_f32 v188, |v90|, v58, v188\n"
      "v_mfma_f32_16x16x32_f16 v[84:87], a[80:83], v[96:99], v[84:87]\n"
      "s_waitcnt lgkmcnt(8)\n"
      "v_fma_f32 v188, |v91|, v59, v188\n"
      "v_pk_mul_f16 v119, v135, v151\n"
      "v_mfma_f32_16x16x32_f16 v[88:91], a[96:99], v[96:99], v[40:43]\n"
      "s_waitcnt lgkmcnt(6)\n"
      "v_pk_mul_f16 v120, v136, v152\n"
      "v_fma_f32 v188, |v92|, v60, v188\n"
      "v_mfma_f32_16x16x32_f16 v[88:91], a[100:103], v[100:103], v[88:91]\n"
      "s_waitcnt lgkmcnt(5)\n"
      "v_fma_f32 v188, |v93|, v61, v188\n"
      "v_pk_mul_f16 v121, v137, v153\n"
      "v_mfma_f32_16x16x32_f16 v[88:91], a[104:107], v[104:107], v[88:91]\n"
      "v_fma_f32 v188, |v94|, v62, v188\n"
      "v_fma_f32 v188, |v95|, v63, v188\n"
      "v_mfma_f32_16x16x32_f16 v[88:91], a[108:111], v[108:111], v[88:91]\n"
      "s_waitcnt lgkmcnt(3)\n"
      "v_pk_mul_f16 v122, v138, v154\n"
      "v_pk_mul_f16 v123, v139, v155\n"
      "v_mfma_f32_16x16x32_f16 v[92:95], a[124:127], v[108:111], v[44:47]\n"
      "global_load_dwordx4 v[244:247], v10, s[10:11] nt\n"
      "v_fma_f32 v188, |v80|, v64, v188\n"
      "v_mfma_f32_16x16x32_f16 v[92:95], a[120:123], v[104:107], v[92:95]\n"
      "s_waitcnt lgkmcnt(1)\n"
      "v_fma_f32 v188, |v81|, v65, v188\n"
      "v_fma_f32 v188, |v82|, v66, v188\n"
      "v_mfma_f32_16x16x32_f16 v[92:95], a[116:119], v[100:103], v[92:95]\n"
      "s_waitcnt lgkmcnt(0)\n"
      "v_pk_mul_f16 v124, v140, v156\n"
      "v_fma_f32 v188, |v83|, v67, v188\n"
      "v_mfma_f32_16x16x32_f16 v[92:95], a[112:115], v[96:99], v[92:95]\n"
      "v_pk_mul_f16 v125, v141, v157\n"
      "v_pk_mul_f16 v126, v142, v158\n"
      "v_mfma_f32_16x16x32_f16 v[80:83], a[0:3], v[112:115], v[16:19]\n"
      "ds_read_b128 v[160:163], v203 offset:2880\n"
      "v_pk_mul_f16 v127, v143, v159\n"
      "v_mfma_f32_16x16x32_f16 v[80:83], a[4:7], v[116:119], v[80:83]\n"
      "v_fma_f32 v188, |v84|, v68, v188\n"
      "v_fma_f32 v188, |v85|, v69, v188\n"
      "v_mfma_f32_16x16x32_f16 v[80:83], a[8:11], v[120:123], v[80:83]\n"
      "v_fma_f32 v188, |v86|, v70, v188\n"
      "v_fma_f32 v188, |v87|, v71, v188\n"
      "v_mfma_f32_16x16x32_f16 v[80:83], a[12:15], v[124:127], v[80:83]\n"
      "v_fma_f32 v188, |v88|, v72, v188\n"
      "v_fma_f32 v188, |v89|, v73, v188\n"
      "v_mfma_f32_16x16x32_f16 v[84:87], a[28:31], v[124:127], v[20:23]\n"
      "v_fma_f32 v188, |v90|, v74, v188\n"
      "v_fma_f32 v188, |v91|, v75, v188\n"
      "v_mfma_f32_16x16x32_f16 v[84:87], a[24:27], v[120:123], v[84:87]\n"
      "40:\n"
      "s_cbranch_execz 44b\n"
      "ds_read_b128 v[164:167], v203 offset:2944\n"
      "v_mfma_f32_16x16x32_f16 v[84:87], a[20:23], v[116:119], v[84:87]\n"
      "v_fma_f32 v188, |v92|, v76, v188\n"
      "v_fma_f32 v188, |v93|, v77, v188\n"
      "v_mfma_f32_16x16x32_f16 v[84:87], a[16:19], v[112:115], v[84:87]\n"
      "v_fma_f32 v188, |v94|, v78, v188\n"
      "v_fma_f32 v188, |v95|, v79, v188\n"
      "v_mfma_f32_16x16x32_f16 v[88:91], a[32:35], v[112:115], v[24:27]\n"
      "s_waitcnt lgkmcnt(1)\n"
      "v_pk_mul_f16 v96, v128, v160\n"
      "v_pk_mul_f16 v97, v129, v161\n"
      "v_mfma_f32_16x16x32_f16 v[88:91], a[36:39], v[116:119], v[88:91]\n"
      "v_pk_mul_f16 v98, v130, v162\n"
      "v_pk_mul_f16 v99, v131, v163\n"
      "v_mfma_f32_16x16x32_f16 v[88:91], a[40:43], v[120:123], v[88:91]\n"
      "ds_read_b128 v[168:171], v203 offset:3008\n"
      "v_mul_f32 v189, |v80|, v48\n"
      "v_mfma_f32_16x16x32_f16 v[88:91], a[44:47], v[124:127], v[88:91]\n"
      "s_waitcnt vmcnt(1)\n"
      "v_fma_f32 v189, |v81|, v49, v189\n"
      "v_fma_f32 v189, |v82|, v50, v189\n"
      "v_mfma_f32_4x4x4_16b_bf16 v[176:179], a[216:217], v[240:241], 0\n"
      "v_mfma_f32_16x16x32_f16 v[92:95], a[60:63], v[124:127], v[28:31]\n"
      "s_waitcnt lgkmcnt(1)\n"
      "v_pk_mul_f16 v100, v132, v164\n"
      "v_pk_mul_f16 v101, v133, v165\n"
      "v_mfma_f32_16x16x32_f16 v[92:95], a[56:59], v[120:123], v[92:95]\n"
      "v_fma_f32 v189, |v83|, v51, v189\n"
      "v_pk_mul_f16 v102, v134, v166\n"
      "v_mfma_f32_16x16x32_f16 v[92:95], a[52:55], v[116:119], v[92:95]\n"
      "v_pk_mul_f16 v103, v135, v167\n"
      "v_fma_f32 v189, |v84|, v52, v189\n"
      "v_mfma_f32_16x16x32_f16 v[92:95], a[48:51], v[112:115], v[92:95]\n"
      "v_fma_f32 v189, |v85|, v53, v189\n"
      "v_fma_f32 v189, |v86|, v54, v189\n"
      "v_mfma_f32_4x4x4_16b_bf16 v[180:183], a[216:217], v[242:243], 0\n"
      "v_mfma_f32_16x16x32_f16 v[80:83], a[64:67], v[112:115], v[32:35]\n"
      "s_waitcnt lgkmcnt(0)\n"
      "v_fma_f32 v189, |v87|, v55, v189\n"
      "v_pk_mul_f16 v104, v136, v168\n"
      "v_mfma_f32_16x16x32_f16 v[80:83], a[68:71], v[116:119], v[80:83]\n"
      "v_pk_mul_f16 v105, v137, v169\n"
      "v_pk_mul_f16 v106, v138, v170\n"
      "v_mfma_f32_16x16x32_f16 v[80:83], a[72:75], v[120:123], v[80:83]\n"
      "v_fma_f32 v189, |v88|, v56, v189\n"
      "v_fma_f32 v189, |v89|, v57, v189\n"
      "v_mfma_f32_16x16x32_f16 v[80:83], a[76:79], v[124:127], v[80:83]\n"
      "ds_read_b128 v[172:175], v203 offset:3072\n"
      "v_pk_mul_f16 v107, v139, v171\n"
      "v_mfma_f32_16x16x32_f16 v[84:87], a[92:95], v[124:127], v[36:39]\n"
      "v_fma_f32 v189, |v90|, v58, v189\n"
      "v_fma_f32 v189, |v91|, v59, v189\n"
      "v_mfma_f32_16x16x32_f16 v[84:87], a[88:91], v[120:123], v[84:87]\n"
      "v_cvt_pk_f16_f32 v184, v176, v177\n"
      "v_fma_f32 v189, |v92|, v60, v189\n"
      "v_mfma_f32_16x16x32_f16 v[84:87], a[84:87], v[116:119], v[84:87]\n"
      "v_fma_f32 v189, |v93|, v61, v189\n"
      "v_cvt_pk_f16_f32 v185, v178, v179\n"
      "v_mfma_f32_16x16x32_f16 v[84:87], a[80:83], v[112:115], v[84:87]\n"
      "v_fma_f32 v189, |v94|, v62, v189\n"
      "v_fma_f32 v189, |v95|, v63, v189\n"
      "v_mfma_f32_16x16x32_f16 v[88:91], a[96:99], v[112:115], v[40:43]\n"
      "v_cvt_pk_f16_f32 v186, v180, v181\n"
      "v_cvt_pk_f16_f32 v187, v182, v183\n"
      "v_mfma_f32_16x16x32_f16 v[88:91], a[100:103], v[116:119], v[88:91]\n"
      "ds_write_b128 v1, v[184:187] offset:3456\n"
      "v_mfma_f32_16x16x32_f16 v[88:91], a[104:107], v[120:123], v[88:91]\n"
      "v_fma_f32 v189, |v80|, v64, v189\n"
      "v_fma_f32 v189, |v81|, v65, v189\n"
      "v_mfma_f32_16x16x32_f16 v[88:91], a[108:111], v[124:127], v[88:91]\n"
      "s_waitcnt lgkmcnt(1)\n"
      "v_pk_mul_f16 v108, v140, v172\n"
      "v_fma_f32 v189, |v82|, v66, v189\n"
      "v_mfma_f32_16x16x32_f16 v[92:95], a[124:127], v[124:127], v[44:47]\n"
      "v_fma_f32 v189, |v83|, v67, v189\n"
      "v_pk_mul_f16 v109, v141, v173\n"
      "v_mfma_f32_16x16x32_f16 v[92:95], a[120:123], v[120:123], v[92:95]\n"
      "v_pk_mul_f16 v110, v142, v174\n"
      "v_pk_mul_f16 v111, v143, v175\n"
      "v_mfma_f32_16x16x32_f16 v[92:95], a[116:119], v[116:119], v[92:95]\n"
      "ds_read_b128 v[160:163], v203 offset:3456\n"
      "v_fma_f32 v189, |v84|, v68, v189\n"
      "v_mfma_f32_16x16x32_f16 v[92:95], a[112:115], v[112:115], v[92:95]\n"
      "v_fma_f32 v189, |v85|, v69, v189\n"
      "v_fma_f32 v189, |v86|, v70, v189\n"
      "v_mfma_f32_16x16x32_f16 v[80:83], a[0:3], v[96:99], v[16:19]\n"
      "v_fma_f32 v189, |v87|, v71, v189\n"
      "v_fma_f32 v189, |v88|, v72, v189\n"
      "v_mfma_f32_16x16x32_f16 v[80:83], a[4:7], v[100:103], v[80:83]\n"
      "v_fma_f32 v189, |v89|, v73, v189\n"
      "v_fma_f32 v189, |v90|, v74, v189\n"
      "v_mfma_f32_16x16x32_f16 v[80:83], a[8:11], v[104:107], v[80:83]\n"
      "ds_read_b128 v[164:167], v203 offset:3520\n"
      "v_fma_f32 v189, |v91|, v75, v189\n"
      "v_mfma_f32_16x16x32_f16 v[80:83], a[12:15], v[108:111], v[80:83]\n"
      "v_fma_f32 v189, |v92|, v76, v189\n"
      "v_fma_f32 v189, |v93|, v77, v189\n"
      "v_mfma_f32_16x16x32_f16 v[84:87], a[28:31], v[108:111], v[20:23]\n"
      "ds_write_b32 v7, a200 offset:37120\n"
      "v_mfma_f32_16x16x32_f16 v[84:87], a[24:27], v[104:107], v[84:87]\n"
      "ds_write_b32 v7, a204 offset:37376\n"
      "v_mfma_f32_16x16x32_f16 v[84:87], a[20:23], v[100:103], v[84:87]\n"
      "s_waitcnt lgkmcnt(3)\n"
      "v_pk_mul_f16 v112, v128, v160\n"
      "v_pk_mul_f16 v113, v129, v161\n"
      "v_mfma_f32_16x16x32_f16 v[84:87], a[16:19], v[96:99], v[84:87]\n"
      "v_fma_f32 v189, |v94|, v78, v189\n"
      "v_fma_f32 v189, |v95|, v79, v189\n"
      "v_mfma_f32_16x16x32_f16 v[88:91], a[32:35], v[96:99], v[24:27]\n"
      "v_pk_mul_f16 v114, v130, v162\n"
      "v_pk_mul_f16 v115, v131, v163\n"
      "v_mfma_f32_16x16x32_f16 v[88:91], a[36:39], v[100:103], v[88:91]\n"
      "ds_read_b128 v[168:171], v203 offset:3584\n"
      "v_mul_f32 v190, |v80|, v48\n"
      "v_mfma_f32_16x16x32_f16 v[88:91], a[40:43], v[104:107], v[88:91]\n"
      "v_fma_f32 v190, |v81|, v49, v190\n"
      "v_fma_f32 v190, |v82|, v50, v190\n"
      "v_mfma_f32_16x16x32_f16 v[88:91], a[44:47], v[108:111], v[88:91]\n"
      "s_waitcnt lgkmcnt(3)\n"
      "v_fma_f32 v190, |v83|, v51, v190\n"
      "v_pk_mul_f16 v116, v132, v164\n"
      "v_mfma_f32_16x16x32_f16 v[92:95], a[60:63], v[108:111], v[28:31]\n"
      "v_pk_mul_f16 v117, v133, v165\n"
      "v_pk_mul_f16 v118, v134, v166\n"
      "v_mfma_f32_16x16x32_f16 v[92:95], a[56:59], v[104:107], v[92:95]\n"
      "s_mov_b32 s35, 1\n"
      "ds_write_b32 v2, v223\n"
      "v_mfma_f32_16x16x32_f16 v[92:95], a[52:55], v[100:103], v[92:95]\n"
      "v_fma_f32 v190, |v84|, v52, v190\n"
      "v_fma_f32 v190, |v85|, v53, v190\n"
      "v_mfma_f32_16x16x32_f16 v[92:95], a[48:51], v[96:99], v[92:95]\n"
      "v_pk_mul_f16 v119, v135, v167\n"
      "v_fma_f32 v190, |v86|, v54, v190\n"
      "v_mfma_f32_16x16x32_f16 v[80:83], a[64:67], v[96:99], v[32:35]\n"
      "v_fma_f32 v190, |v87|, v55, v190\n"
      "v_fma_f32 v190, |v88|, v56, v190\n"
      "v_mfma_f32_16x16x32_f16 v[80:83], a[68:71], v[100:103], v[80:83]\n"
      "s_waitcnt lgkmcnt(1)\n"
      "v_pk_mul_f16 v120, v136, v168\n"
      "v_fma_f32 v190, |v89|, v57, v190\n"
      "v_mfma_f32_16x16x32_f16 v[80:83], a[72:75], v[104:107], v[80:83]\n"
      "ds_read_b128 v[172:175], v203 offset:3648\n"
      "v_pk_mul_f16 v121, v137, v169\n"
      "v_mfma_f32_16x16x32_f16 v[80:83], a[76:79], v[108:111], v[80:83]\n"
      "v_fma_f32 v190, |v90|, v58, v190\n"
      "v_fma_f32 v190, |v91|, v59, v190\n"
      "v_mfma_f32_16x16x32_f16 v[84:87], a[92:95], v[108:111], v[36:39]\n"
      "v_pk_mul_f16 v122, v138, v170\n"
      "v_pk_mul_f16 v123, v139, v171\n"
      "v_mfma_f32_16x16x32_f16 v[84:87], a[88:91], v[104:107], v[84:87]\n"
      "global_load_dwordx4 v[248:251], v11, s[10:11] nt\n"
      "v_fma_f32 v190, |v92|, v60, v190\n"
      "v_mfma_f32_16x16x32_f16 v[84:87], a[84:87], v[100:103], v[84:87]\n"
      "v_fma_f32 v190, |v93|, v61, v190\n"
      "v_fma_f32 v190, |v94|, v62, v190\n"
      "v_mfma_f32_16x16x32_f16 v[84:87], a[80:83], v[96:99], v[84:87]\n"
      "v_fma_f32 v190, |v95|, v63, v190\n"
      "v_fma_f32 v190, |v80|, v64, v190\n"
      "v_mfma_f32_16x16x32_f16 v[88:91], a[96:99], v[96:99], v[40:43]\n"
      "v_fma_f32 v190, |v81|, v65, v190\n"
      "v_fma_f32 v190, |v82|, v66, v190\n"
      "v_mfma_f32_16x16x32_f16 v[88:91], a[100:103], v[100:103], v[88:91]\n"
      "ds_read_b128 v[160:163], v203 offset:4032\n"
      "v_fma_f32 v190, |v83|, v67, v190\n"
      "v_mfma_f32_16x16x32_f16 v[88:91], a[104:107], v[104:107], v[88:91]\n"
      "s_waitcnt lgkmcnt(1)\n"
      "v_pk_mul_f16 v124, v140, v172\n"
      "v_pk_mul_f16 v125, v141, v173\n"
      "v_mfma_f32_16x16x32_f16 v[88:91], a[108:111], v[108:111], v[88:91]\n"
      "v_pk_mul_f16 v126, v142, v174\n"
      "v_pk_mul_f16 v127, v143, v175\n"
      "v_mfma_f32_16x16x32_f16 v[92:95], a[124:127], v[108:111], v[44:47]\n"
      "v_fma_f32 v190, |v84|, v68, v190\n"
      "v_fma_f32 v190, |v85|, v69, v190\n"
      "v_mfma_f32_16x16x32_f16 v[92:95], a[120:123], v[104:107], v[92:95]\n"
      "global_load_dwordx4 v[252:255], v12, s[10:11] nt\n"
      "v_fma_f32 v190, |v86|, v70, v190\n"
      "v_mfma_f32_16x16x32_f16 v[92:95], a[116:119], v[100:103], v[92:95]\n"
      "v_fma_f32 v190, |v87|, v71, v190\n"
      "v_fma_f32 v190, |v88|, v72, v190\n"
      "v_mfma_f32_16x16x32_f16 v[92:95], a[112:115], v[96:99], v[92:95]\n"
      "v_fma_f32 v190, |v89|, v73, v190\n"
      "v_fma_f32 v190, |v90|, v74, v190\n"
      "v_mfma_f32_16x16x32_f16 v[80:83], a[0:3], v[112:115], v[16:19]\n"
      "ds_read_b128 v[164:167], v203 offset:4096\n"
      "v_fma_f32 v190, |v91|, v75, v190\n"
      "v_mfma_f32_16x16x32_f16 v[80:83], a[4:7], v[116:119], v[80:83]\n"
      "s_waitcnt lgkmcnt(1)\n"
      "v_pk_mul_f16 v96, v128, v160\n"
      "v_pk_mul_f16 v97, v129, v161\n"
      "v_mfma_f32_16x16x32_f16 v[80:83], a[8:11], v[120:123], v[80:83]\n"
      "v_fma_f32 v190, |v92|, v76, v190\n"
      "v_fma_f32 v190, |v93|, v77, v190\n"
      "v_mfma_f32_16x16x32_f16 v[80:83], a[12:15], v[124:127], v[80:83]\n"
      "v_fma_f32 v190, |v94|, v78, v190\n"
      "v_fma_f32 v190, |v95|, v79, v190\n"
      "v_mfma_f32_16x16x32_f16 v[84:87], a[28:31], v[124:127], v[20:23]\n"
      "v_pk_mul_f16 v98, v130, v162\n"
      "v_pk_mul_f16 v99, v131, v163\n"
      "v_mfma_f32_16x16x32_f16 v[84:87], a[24:27], v[120:123], v[84:87]\n"
      "ds_read_b128 v[168:171], v203 offset:4160\n"
      "v_max3_f32 v226, |v228|, |v229|, |v230|\n"
      "v_mfma_f32_16x16x32_f16 v[84:87], a[20:23], v[116:119], v[84:87]\n"
      "v_mul_f32 v191, |v80|, v48\n"
      "v_fma_f32 v191, |v81|, v49, v191\n"
      "v_mfma_f32_16x16x32_f16 v[84:87], a[16:19], v[112:115], v[84:87]\n"
      "v_fma_f32 v191, |v82|, v50, v191\n"
      "v_fma_f32 v191, |v83|, v51, v191\n"
      "v_mfma_f32_16x16x32_f16 v[88:91], a[32:35], v[112:115], v[24:27]\n"
      "s_waitcnt lgkmcnt(1)\n"
      "v_pk_mul_f16 v100, v132, v164\n"
      "v_pk_mul_f16 v101, v133, v165\n"
      "v_mfma_f32_16x16x32_f16 v[88:91], a[36:39], v[116:119], v[88:91]\n"
      "v_pk_mul_f16 v102, v134, v166\n"
      "v_pk_mul_f16 v103, v135, v167\n"
      "v_mfma_f32_16x16x32_f16 v[88:91], a[40:43], v[120:123], v[88:91]\n"
      "v_fma_f32 v191, |v84|, v52, v191\n"
      "v_fma_f32 v191, |v85|, v53, v191\n"
      "v_mfma_f32_16x16x32_f16 v[88:91], a[44:47], v[124:127], v[88:91]\n"
      "v_fma_f32 v191, |v86|, v54, v191\n"
      "v_fma_f32 v191, |v87|, v55, v191\n"
      "v_mfma_f32_16x16x32_f16 v[92:95], a[60:63], v[124:127], v[28:31]\n"
      "ds_read_b128 v[172:175], v203 offset:4224\n"
      "ds_read_b128 v[144:147], v201 offset:2304\n"
      "v_mfma_f32_16x16x32_f16 v[92:95], a[56:59], v[120:123], v[92:95]\n"
      "s_waitcnt lgkmcnt(2)\n"
      "v_pk_mul_f16 v104, v136, v168\n"
      "v_pk_mul_f16 v105, v137, v169\n"
      "v_mfma_f32_16x16x32_f16 v[92:95], a[52:55], v[116:119], v[92:95]\n"
      "v_fma_f32 v191, |v88|, v56, v191\n"
      "v_fma_f32 v191, |v89|, v57, v191\n"
      "v_mfma_f32_16x16x32_f16 v[92:95], a[48:51], v[112:115], v[92:95]\n"
      "v_fma_f32 v191, |v90|, v58, v191\n"
      "v_fma_f32 v191, |v91|, v59, v191\n"
      "v_mfma_f32_16x16x32_f16 v[80:83], a[64:67], v[112:115], v[32:35]\n"
      "v_pk_mul_f16 v106, v138, v170\n"
      "v_pk_mul_f16 v107, v139, v171\n"
      "v_mfma_f32_16x16x32_f16 v[80:83], a[68:71], v[116:119], v[80:83]\n"
      "ds_read_b128 v[148:151], v201 offset:2368\n"
      "v_cmp_le_f32 vcc, 0x3f400000, v226\n"
      "v_mfma_f32_16x16x32_f16 v[80:83], a[72:75], v[120:123], v[80:83]\n"
      "v_fma_f32 v191, |v92|, v60, v191\n"
      "v_fma_f32 v191, |v93|, v61, v191\n"
      "v_mfma_f32_16x16x32_f16 v[80:83], a[76:79], v[124:127], v[80:83]\n"
      "s_quadmask_b64 s[44:45], vcc\n"
      "v_fma_f32 v191, |v94|, v62, v191\n"
      "v_fma_f32 v191, |v95|, v63, v191\n"
      "v_mfma_f32_16x16x32_f16 v[84:87], a[92:95], v[124:127], v[36:39]\n"
      "s_waitcnt lgkmcnt(2)\n"
      "v_pk_mul_f16 v108, v140, v172\n"
      "v_pk_mul_f16 v109, v141, v173\n"
      "v_mfma_f32_16x16x32_f16 v[84:87], a[88:91], v[120:123], v[84:87]\n"
      "s_quadmask_b32 s30, s44\n"
      "v_pk_mul_f16 v110, v142, v174\n"
      "v_pk_mul_f16 v111, v143, v175\n"
      "v_mfma_f32_16x16x32_f16 v[84:87], a[84:87], v[116:119], v[84:87]\n"
      "v_fma_f32 v191, |v80|, v64, v191\n"
      "v_fma_f32 v191, |v81|, v65, v191\n"
      "v_mfma_f32_16x16x32_f16 v[84:87], a[80:83], v[112:115], v[84:87]\n"
      "v_fma_f32 v191, |v82|, v66, v191\n"
      "v_fma_f32 v191, |v83|, v67, v191\n"
      "v_mfma_f32_16x16x32_f16 v[88:91], a[96:99], v[112:115], v[40:43]\n"
      "ds_read_b128 v[160:163], v202 offset:2304\n"
      "v_max3_f32 v226, |v232|, |v233|, |v234|\n"
      "v_mfma_f32_16x16x32_f16 v[88:91], a[100:103], v[116:119], v[88:91]\n"
      "ds_read_b128 v[152:155], v201 offset:2432\n"
      "v_cmp_le_f32 vcc, 0x3f400000, v226\n"
      "v_mfma_f32_16x16x32_f16 v[88:91], a[104:107], v[120:123], v[88:91]\n"
      "v_fma_f32 v191, |v84|, v68, v191\n"
      "v_fma_f32 v191, |v85|, v69, v191\n"
      "v_mfma_f32_16x16x32_f16 v[88:91], a[108:111], v[124:127], v[88:91]\n"
      "s_quadmask_b64 s[44:45], vcc\n"
      "v_fma_f32 v191, |v86|, v70, v191\n"
      "v_fma_f32 v191, |v87|, v71, v191\n"
      "v_mfma_f32_16x16x32_f16 v[92:95], a[124:127], v[124:127], v[44:47]\n"
      "s_quadmask_b32 s44, s44\n"
      "ds_read_b128 v[164:167], v202 offset:2368\n"
      "ds_read_b128 v[156:159], v201 offset:2496\n"
      "v_mfma_f32_16x16x32_f16 v[92:95], a[120:123], v[120:123], v[92:95]\n"
      "s_and_b32 s30, s30, s44\n"
      "v_max3_f32 v226, |v236|, |v237|, |v238|\n"
      "v_cmp_le_f32 vcc, 0x3f400000, v226\n"
      "v_mfma_f32_16x16x32_f16 v[92:95], a[116:119], v[116:119], v[92:95]\n"
      "v_fma_f32 v191, |v88|, v72, v191\n"
      "v_fma_f32 v191, |v89|, v73, v191\n"
      "v_mfma_f32_16x16x32_f16 v[92:95], a[112:115], v[112:115], v[92:95]\n"
      "s_quadmask_b64 s[44:45], vcc\n"
      "v_fma_f32 v191, |v90|, v74, v191\n"
      "v_fma_f32 v191, |v91|, v75, v191\n"
      "v_mfma_f32_16x16x32_f16 v[80:83], a[0:3], v[96:99], v[16:19]\n"
      "s_waitcnt lgkmcnt(3)\n"
      "v_pk_mul_f16 v112, v144, v160\n"
      "v_pk_mul_f16 v113, v145, v161\n"
      "v_mfma_f32_16x16x32_f16 v[80:83], a[4:7], v[100:103], v[80:83]\n"
      "s_quadmask_b32 s44, s44\n"
      "v_pk_mul_f16 v114, v146, v162\n"
      "v_pk_mul_f16 v115, v147, v163\n"
      "v_mfma_f32_16x16x32_f16 v[80:83], a[8:11], v[104:107], v[80:83]\n"
      "s_and_b32 s30, s30, s44\n"
      "v_fma_f32 v191, |v92|, v76, v191\n"
      "v_fma_f32 v191, |v93|, v77, v191\n"
      "v_mfma_f32_16x16x32_f16 v[80:83], a[12:15], v[108:111], v[80:83]\n"
      "s_waitcnt vmcnt(2)\n"
      "v_fma_f32 v191, |v94|, v78, v191\n"
      "v_fma_f32 v191, |v95|, v79, v191\n"
      "v_mfma_f32_4x4x4_16b_bf16 v[176:179], a[216:217], v[244:245], 0\n"
      "v_mfma_f32_16x16x32_f16 v[84:87], a[28:31], v[108:111], v[20:23]\n"
      "v_permlane32_swap_b32 v189, v191\n"
      "v_mfma_f32_16x16x32_f16 v[84:87], a[24:27], v[104:107], v[84:87]\n"
      "s_waitcnt lgkmcnt(1)\n"
      "ds_read_b128 v[168:171], v202 offset:2432\n"
      "v_pk_mul_f16 v116, v148, v164\n"
      "v_mfma_f32_16x16x32_f16 v[84:87], a[20:23], v[100:103], v[84:87]\n"
      "v_mul_f32 v192, |v80|, v48\n"
      "v_fma_f32 v192, |v81|, v49, v192\n"
      "v_mfma_f32_16x16x32_f16 v[84:87], a[16:19], v[96:99], v[84:87]\n"
      "v_pk_mul_f16 v117, v149, v165\n"
      "v_fma_f32 v192, |v82|, v50, v192\n"
      "v_mfma_f32_4x4x4_16b_bf16 v[180:183], a[216:217], v[246:247], 0\n"
      "v_mfma_f32_16x16x32_f16 v[88:91], a[32:35], v[96:99], v[24:27]\n"
      "v_fma_f32 v192, |v83|, v51, v192\n"
      "v_pk_mul_f16 v118, v150, v166\n"
      "v_mfma_f32_16x16x32_f16 v[88:91], a[36:39], v[100:103], v[88:91]\n"
      "v_pk_mul_f16 v119, v151, v167\n"
      "v_fma_f32 v192, |v84|, v52, v192\n"
      "v_mfma_f32_16x16x32_f16 v[88:91], a[40:43], v[104:107], v[88:91]\n"
      "v_fma_f32 v192, |v85|, v53, v192\n"
      "v_fma_f32 v192, |v86|, v54, v192\n"
      "v_mfma_f32_16x16x32_f16 v[88:91], a[44:47], v[108:111], v[88:91]\n"
      "v_fma_f32 v192, |v87|, v55, v192\n"
      "v_max3_f32 v226, |v240|, |v241|, |v242|\n"
      "v_mfma_f32_16x16x32_f16 v[92:95], a[60:63], v[108:111], v[28:31]\n"
      "s_waitcnt lgkmcnt(0)\n"
      "v_pk_mul_f16 v120, v152, v168\n"
      "v_pk_mul_f16 v121, v153, v169\n"
      "v_mfma_f32_16x16x32_f16 v[92:95], a[56:59], v[104:107], v[92:95]\n"
      "v_pk_mul_f16 v122, v154, v170\n"
      "v_cmp_le_f32 vcc, 0x3f400000, v226\n"
      "v_mfma_f32_16x16x32_f16 v[92:95], a[52:55], v[100:103], v[92:95]\n"
      "v_fma_f32 v192, |v88|, v56, v192\n"
      "v_fma_f32 v192, |v89|, v57, v192\n"
      "v_mfma_f32_16x16x32_f16 v[92:95], a[48:51], v[96:99], v[92:95]\n"
      "s_quadmask_b64 s[44:45], vcc\n"
      "ds_read_b128 v[172:175], v202 offset:2496\n"
      "v_pk_mul_f16 v123, v155, v171\n"
      "v_mfma_f32_16x16x32_f16 v[80:83], a[64:67], v[96:99], v[32:35]\n"
      "s_quadmask_b32 s44, s44\n"
      "v_fma_f32 v192, |v90|, v58, v192\n"
      "v_fma_f32 v192, |v91|, v59, v192\n"
      "v_mfma_f32_16x16x32_f16 v[80:83], a[68:71], v[100:103], v[80:83]\n"
      "s_and_b32 s30, s30, s44\n"
      "v_cvt_pk_f16_f32 v184, v176, v177\n"
      "v_cvt_pk_f16_f32 v185, v178, v179\n"
      "v_mfma_f32_16x16x32_f16 v[80:83], a[72:75], v[104:107], v[80:83]\n"
      "v_fma_f32 v192, |v92|, v60, v192\n"
      "v_fma_f32 v192, |v93|, v61, v192\n"
      "v_mfma_f32_16x16x32_f16 v[80:83], a[76:79], v[108:111], v[80:83]\n"
      "v_fma_f32 v192, |v94|, v62, v192\n"
      "v_fma_f32 v192, |v95|, v63, v192\n"
      "v_mfma_f32_16x16x32_f16 v[84:87], a[92:95], v[108:111], v[36:39]\n"
      "v_cvt_pk_f16_f32 v186, v180, v181\n"
      "v_max3_f32 v226, |v244|, |v245|, |v246|\n"
      "v_mfma_f32_16x16x32_f16 v[84:87], a[88:91], v[104:107], v[84:87]\n"
      "v_cvt_pk_f16_f32 v187, v182, v183\n"
      "v_cmp_le_f32 vcc, 0x3f400000, v226\n"
      "v_mfma_f32_16x16x32_f16 v[84:87], a[84:87], v[100:103], v[84:87]\n"
      "ds_write_b128 v1, v[184:187] offset:4608\n"
      "v_mfma_f32_16x16x32_f16 v[84:87], a[80:83], v[96:99], v[84:87]\n"
      "s_quadmask_b64 s[44:45], vcc\n"
      "v_fma_f32 v192, |v80|, v64, v192\n"
      "v_fma_f32 v192, |v81|, v65, v192\n"
      "v_mfma_f32_16x16x32_f16 v[88:91], a[96:99], v[96:99], v[40:43]\n"
      "s_waitcnt lgkmcnt(1)\n"
      "v_pk_mul_f16 v124, v156, v172\n"
      "v_fma_f32 v192, |v82|, v66, v192\n"
      "v_mfma_f32_16x16x32_f16 v[88:91], a[100:103], v[100:103], v[88:91]\n"
      "s_quadmask_b32 s44, s44\n"
      "v_fma_f32 v192, |v83|, v67, v192\n"
      "v_pk_mul_f16 v125, v157, v173\n"
      "v_mfma_f32_16x16x32_f16 v[88:91], a[104:107], v[104:107], v[88:91]\n"
      "s_and_b32 s30, s30, s44\n"
      "v_pk_mul_f16 v126, v158, v174\n"
      "v_pk_mul_f16 v127, v159, v175\n"
      "v_mfma_f32_16x16x32_f16 v[88:91], a[108:111], v[108:111], v[88:91]\n"
      "ds_read_b128 v[160:163], v203 offset:4608\n"
      "v_fma_f32 v192, |v84|, v68, v192\n"
      "v_mfma_f32_16x16x32_f16 v[92:95], a[124:127], v[108:111], v[44:47]\n"
      "s_waitcnt vmcnt(1)\n"
      "v_fma_f32 v192, |v85|, v69, v192\n"
      "v_max3_f32 v226, |v248|, |v249|, |v250|\n"
      "v_mfma_f32_16x16x32_f16 v[92:95], a[120:123], v[104:107], v[92:95]\n"
      "v_fma_f32 v192, |v86|, v70, v192\n"
      "v_fma_f32 v192, |v87|, v71, v192\n"
      "v_mfma_f32_16x16x32_f16 v[92:95], a[116:119], v[100:103], v[92:95]\n"
      "v_cmp_le_f32 vcc, 0x3f400000, v226\n"
      "v_fma_f32 v192, |v88|, v72, v192\n"
      "v_mfma_f32_16x16x32_f16 v[92:95], a[112:115], v[96:99], v[92:95]\n"
      "v_fma_f32 v192, |v89|, v73, v192\n"
      "v_fma_f32 v192, |v90|, v74, v192\n"
      "v_mfma_f32_16x16x32_f16 v[80:83], a[0:3], v[112:115], v[16:19]\n"
      "s_quadmask_b64 s[44:45], vcc\n"
      "ds_read_b128 v[164:167], v203 offset:4672\n"
      "v_fma_f32 v192, |v91|, v75, v192\n"
      "v_mfma_f32_16x16x32_f16 v[80:83], a[4:7], v[116:119], v[80:83]\n"
      "s_quadmask_b32 s44, s44\n"
      "ds_read_b128 v[168:171], v203 offset:4736\n"
      "ds_read_b32 v224, v227 offset:37632\n"
      "v_mfma_f32_16x16x32_f16 v[80:83], a[8:11], v[120:123], v[80:83]\n"
      "s_and_b32 s30, s30, s44\n"
      "v_fma_f32 v192, |v92|, v76, v192\n"
      "v_fma_f32 v192, |v93|, v77, v192\n"
      "v_mfma_f32_16x16x32_f16 v[80:83], a[12:15], v[124:127], v[80:83]\n"
      "s_waitcnt vmcnt(0) lgkmcnt(3)\n"
      "v_pk_mul_f16 v96, v128, v160\n"
      "v_max3_f32 v226, |v252|, |v253|, |v254|\n"
      "v_mfma_f32_16x16x32_f16 v[84:87], a[28:31], v[124:127], v[20:23]\n"
      "v_pk_mul_f16 v97, v129, v161\n"
      "v_cmp_le_f32 vcc, 0x3f400000, v226\n"
      "v_mfma_f32_16x16x32_f16 v[84:87], a[24:27], v[120:123], v[84:87]\n"
      "v_fma_f32 v192, |v94|, v78, v192\n"
      "v_fma_f32 v192, |v95|, v79, v192\n"
      "v_mfma_f32_16x16x32_f16 v[84:87], a[20:23], v[116:119], v[84:87]\n"
      "s_quadmask_b64 s[44:45], vcc\n"
      "v_pk_mul_f16 v98, v130, v162\n"
      "v_pk_mul_f16 v99, v131, v163\n"
      "v_mfma_f32_16x16x32_f16 v[84:87], a[16:19], v[112:115], v[84:87]\n"
      "s_quadmask_b32 s44, s44\n"
      "v_mul_f32 v193, |v80|, v48\n"
      "v_fma_f32 v193, |v81|, v49, v193\n"
      "v_mfma_f32_16x16x32_f16 v[88:91], a[32:35], v[112:115], v[24:27]\n"
      "s_and_b32 s30, s30, s44\n"
      "v_fma_f32 v193, |v82|, v50, v193\n"
      "v_fma_f32 v193, |v83|, v51, v193\n"
      "v_mfma_f32_16x16x32_f16 v[88:91], a[36:39], v[116:119], v[88:91]\n"
      "s_waitcnt lgkmcnt(2)\n"
      "v_pk_mul_f16 v100, v132, v164\n"
      "v_pk_mul_f16 v101, v133, v165\n"
      "v_mfma_f32_16x16x32_f16 v[88:91], a[40:43], v[120:123], v[88:91]\n"
      "s_xor_b32 s30, s30, 15\n"
      "v_pk_mul_f16 v102, v134, v166\n"
      "v_fma_f32 v193, |v84|, v52, v193\n"
      "v_mfma_f32_16x16x32_f16 v[88:91], a[44:47], v[124:127], v[88:91]\n"
      "s_or_b32 s30, s30, s41\n"
      "v_fma_f32 v193, |v85|, v53, v193\n"
      "v_pk_mul_f16 v103, v135, v167\n"
      "v_mfma_f32_16x16x32_f16 v[92:95], a[60:63], v[124:127], v[28:31]\n"
      "s_cmp_lg_u32 s55, 0\n"
      "s_cselect_b32 s30, 0, s30\n"
      "v_fma_f32 v193, |v86|, v54, v193\n"
      "v_mfma_f32_16x16x32_f16 v[92:95], a[56:59], v[120:123], v[92:95]\n"
      "s_waitcnt lgkmcnt(1)\n"
      "v_fma_f32 v193, |v87|, v55, v193\n"
      "v_pk_mul_f16 v104, v136, v168\n"
      "v_mfma_f32_16x16x32_f16 v[92:95], a[52:55], v[116:119], v[92:95]\n"
      "v_fma_f32 v193, |v88|, v56, v193\n"
      "v_fma_f32 v193, |v89|, v57, v193\n"
      "v_mfma_f32_16x16x32_f16 v[92:95], a[48:51], v[112:115], v[92:95]\n"
      "ds_read_b128 v[172:175], v203 offset:4800\n"
      "v_pk_mul_f16 v105, v137, v169\n"
      "v_mfma_f32_16x16x32_f16 v[80:83], a[64:67], v[112:115], v[32:35]\n"
      "ds_read_b64 v[176:177], v5 offset:37120\n"
      "v_fma_f32 v193, |v90|, v58, v193\n"
      "v_mfma_f32_16x16x32_f16 v[80:83], a[68:71], v[116:119], v[80:83]\n"
      "v_fma_f32 v193, |v91|, v59, v193\n"
      "v_pk_mul_f16 v106, v138, v170\n"
      "v_mfma_f32_16x16x32_f16 v[80:83], a[72:75], v[120:123], v[80:83]\n"
      "ds_read_b32 v226, v213 offset:12480\n"
      "v_pk_mul_f16 v107, v139, v171\n"
      "v_mfma_f32_16x16x32_f16 v[80:83], a[76:79], v[124:127], v[80:83]\n"
      "s_waitcnt lgkmcnt(3)\n"
      "v_cmp_ne_u32 vcc, 13, v224\n"
      "v_fma_f32 v193, |v92|, v60, v193\n"
      "v_mfma_f32_16x16x32_f16 v[84:87], a[92:95], v[124:127], v[36:39]\n"
      "v_fma_f32 v193, |v93|, v61, v193\n"
      "v_fma_f32 v193, |v94|, v62, v193\n"
      "v_mfma_f32_16x16x32_f16 v[84:87], a[88:91], v[120:123], v[84:87]\n"
      "s_or_b32 s45, vcc_lo, s30\n"
      "v_fma_f32 v193, |v95|, v63, v193\n"
      "v_add_f32 v189, v189, v191\n"
      "v_mfma_f32_16x16x32_f16 v[84:87], a[84:87], v[116:119], v[84:87]\n"
      "v_permlane32_swap_b32 v190, v192\n"
      "v_mfma_f32_16x16x32_f16 v[84:87], a[80:83], v[112:115], v[84:87]\n"
      "v_fma_f32 v193, |v80|, v64, v193\n"
      "v_fma_f32 v193, |v81|, v65, v193\n"
      "v_mfma_f32_16x16x32_f16 v[88:91], a[96:99], v[112:115], v[40:43]\n"
      "s_waitcnt lgkmcnt(2)\n"
      "v_pk_mul_f16 v108, v140, v172\n"
      "v_fma_f32 v193, |v82|, v66, v193\n"
      "v_mfma_f32_16x16x32_f16 v[88:91], a[100:103], v[116:119], v[88:91]\n"
      "v_fma_f32 v193, |v83|, v67, v193\n"
      "v_pk_mul_f16 v109, v141, v173\n"
      "v_mfma_f32_16x16x32_f16 v[88:91], a[104:107], v[120:123], v[88:91]\n"
      "s_waitcnt lgkmcnt(0)\n"
      "v_max_f16_sdwa v226, |v226|, |v226| dst_sel:DWORD dst_unused:UNUSED_PAD src0_sel:WORD_0 src1_sel:WORD_1\n"
      "v_cmp_le_f16 vcc, 0x6400, v226\n"
      "v_mfma_f32_16x16x32_f16 v[88:91], a[108:111], v[124:127], v[88:91]\n"
      "s_mov_b32 s44, 0x3d800000\n"
      "v_pk_mul_f16 v110, v142, v174\n"
      "v_pk_mul_f16 v111, v143, v175\n"
      "v_mfma_f32_16x16x32_f16 v[92:95], a[124:127], v[124:127], v[44:47]\n"
      "s_or_b32 s46, vcc_lo, s45\n"
      "v_fma_f32 v193, |v84|, v68, v193\n"
      "v_fma_f32 v193, |v85|, v69, v193\n"
      "v_mfma_f32_16x16x32_f16 v[92:95], a[120:123], v[120:123], v[92:95]\n"
      "s_mov_b32 s47, vcc_hi\n"
      "v_fma_f32 v193, |v86|, v70, v193\n"
      "v_fma_f32 v193, |v87|, v71, v193\n"
      "v_mfma_f32_16x16x32_f16 v[92:95], a[116:119], v[116:119], v[92:95]\n"
      "v_max_f32 v225, |v176|, |v177|\n"
      "v_cmp_le_f32 vcc, 0x43800000, v225\n"
      "v_mfma_f32_16x16x32_f16 v[92:95], a[112:115], v[112:115], v[92:95]\n"
      "v_cmp_le_f32 s[28:29], s44, v225\n"
      "v_fma_f32 v193, |v88|, v72, v193\n"
      "v_mfma_f32_16x16x32_f16 v[80:83], a[0:3], v[96:99], v[16:19]\n"
      "v_fma_f32 v193, |v89|, v73, v193\n"
      "v_fma_f32 v193, |v90|, v74, v193\n"
      "v_mfma_f32_16x16x32_f16 v[80:83], a[4:7], v[100:103], v[80:83]\n"
      "s_or_b64 vcc, vcc, s[46:47]\n"
      "v_fma_f32 v193, |v91|, v75, v193\n"
      "v_pk_mul_f16 v112, v144, v160\n"
      "v_mfma_f32_16x16x32_f16 v[80:83], a[8:11], v[104:107], v[80:83]\n"
      "s_cmp_eq_u64 s[28:29], 0\n"
      "s_cselect_b64 vcc, -1, vcc\n"
      "s_cmp_lg_u64 vcc, 0\n"
      "v_mfma_f32_16x16x32_f16 v[80:83], a[12:15], v[108:111], v[80:83]\n"
      "s_cbranch_scc1 5f\n"
      "4:\n"
      "v_mfma_f32_16x16x32_f16 v[84:87], a[28:31], v[108:111], v[20:23]\n"
      "v_cvt_pk_f16_f32 v176, v176, v177\n"
      "v_fma_f32 v193, |v92|, v76, v193\n"
      "v_mfma_f32_16x16x32_f16 v[84:87], a[24:27], v[104:107], v[84:87]\n"
      "s_lshl_b32 s49, s40, 1\n"
      "ds_write_b32 v213, v176 offset:11904\n"
      "v_mfma_f32_16x16x32_f16 v[84:87], a[20:23], v[100:103], v[84:87]\n"
      "s_add_u32 s51, s57, s49\n"
      "v_fma_f32 v193, |v93|, v77, v193\n"
      "v_pk_mul_f16 v113, v145, v161\n"
      "v_mfma_f32_16x16x32_f16 v[84:87], a[16:19], v[96:99], v[84:87]\n"
      "s_add_u32 s51, s51, s58\n"
      "v_fma_f32 v193, |v94|, v78, v193\n"
      "v_fma_f32 v193, |v95|, v79, v193\n"
      "v_mfma_f32_16x16x32_f16 v[88:91], a[32:35], v[96:99], v[24:27]\n"
      "s_add_u32 s51, s51, s56\n"
      "v_pk_mul_f16 v114, v146, v162\n"
      "v_pk_mul_f16 v115, v147, v163\n"
      "v_mfma_f32_16x16x32_f16 v[88:91], a[36:39], v[100:103], v[88:91]\n"
      "41:\n"
      "s_cbranch_execz 44b\n"
      "s_sub_u32 s51, 0, s51\n"
      "v_add_f32 v190, v190, v192\n"
      "v_mfma_f32_16x16x32_f16 v[88:91], a[40:43], v[104:107], v[88:91]\n"
      "s_add_u32 s49, s59, s49\n"
      "v_mul_f32 v194, |v80|, v48\n"
      "v_fma_f32 v194, |v81|, v49, v194\n"
      "v_mfma_f32_16x16x32_f16 v[88:91], a[44:47], v[108:111], v[88:91]\n"
      "s_sub_u32 s39, 5, s49\n"
      "v_permlane16_swap_b32 v189, v190\n"
      "v_mfma_f32_16x16x32_f16 v[92:95], a[60:63], v[108:111], v[28:31]\n"
      "v_fma_f32 v194, |v82|, v50, v194\n"
      "v_fma_f32 v194, |v83|, v51, v194\n"
      "v_mfma_f32_16x16x32_f16 v[92:95], a[56:59], v[104:107], v[92:95]\n"
      "v_pk_mul_f16 v116, v148, v164\n"
      "v_pk_mul_f16 v117, v149, v165\n"
      "v_mfma_f32_16x16x32_f16 v[92:95], a[52:55], v[100:103], v[92:95]\n"
      "v_add_f32 v189, v189, v190\n"
      "v_pk_mul_f16 v118, v150, v166\n"
      "v_mfma_f32_16x16x32_f16 v[92:95], a[48:51], v[96:99], v[92:95]\n"
      "v_fma_f32 v194, |v84|, v52, v194\n"
      "v_fma_f32 v194, |v85|, v53, v194\n"
      "v_mfma_f32_16x16x32_f16 v[80:83], a[64:67], v[96:99], v[32:35]\n"
      "v_pk_mul_f16 v119, v151, v167\n"
      "v_ldexp_f32 v200, v200, s51\n"
      "v_mfma_f32_16x16x32_f16 v[80:83], a[68:71], v[100:103], v[80:83]\n"
      "v_fma_f32 v194, |v86|, v54, v194\n"
      "v_fma_f32 v194, |v87|, v55, v194\n"
      "v_mfma_f32_16x16x32_f16 v[80:83], a[72:75], v[104:107], v[80:83]\n"
      "v_pk_mul_f16 v120, v152, v168\n"
      "v_pk_mul_f16 v121, v153, v169\n"
      "v_mfma_f32_16x16x32_f16 v[80:83], a[76:79], v[108:111], v[80:83]\n"
      "v_fma_f32 v194, |v88|, v56, v194\n"
      "v_fma_f32 v194, |v89|, v57, v194\n"
      "v_mfma_f32_16x16x32_f16 v[84:87], a[92:95], v[108:111], v[36:39]\n"
      "v_pk_mul_f16 v122, v154, v170\n"
      "v_fma_f32 v194, |v90|, v58, v194\n"
      "v_mfma_f32_16x16x32_f16 v[84:87], a[88:91], v[104:107], v[84:87]\n"
      "v_fma_f32 v194, |v91|, v59, v194\n"
      "v_pk_mul_f16 v123, v155, v171\n"
      "v_mfma_f32_16x16x32_f16 v[84:87], a[84:87], v[100:103], v[84:87]\n"
      "v_permlane32_swap_b32 v188, v193\n"
      "v_mfma_f32_16x16x32_f16 v[84:87], a[80:83], v[96:99], v[84:87]\n"
      "v_fma_f32 v194, |v92|, v60, v194\n"
      "v_fma_f32 v194, |v93|, v61, v194\n"
      "v_mfma_f32_4x4x4_16b_bf16 v[176:179], a[216:217], v[248:249], 0\n"
      "v_mfma_f32_16x16x32_f16 v[88:91], a[96:99], v[96:99], v[40:43]\n"
      "v_fma_f32 v194, |v94|, v62, v194\n"
      "v_fma_f32 v194, |v95|, v63, v194\n"
      "v_mfma_f32_16x16x32_f16 v[88:91], a[100:103], v[100:103], v[88:91]\n"
      "v_fma_f32 v194, |v80|, v64, v194\n"
      "v_fma_f32 v194, |v81|, v65, v194\n"
      "v_mfma_f32_16x16x32_f16 v[88:91], a[104:107], v[104:107], v[88:91]\n"
      "v_pk_mul_f16 v124, v156, v172\n"
      "v_fma_f32 v194, |v82|, v66, v194\n"
      "v_mfma_f32_16x16x32_f16 v[88:91], a[108:111], v[108:111], v[88:91]\n"
      "v_fma_f32 v194, |v83|, v67, v194\n"
      "v_pk_mul_f16 v125, v157, v173\n"
      "v_mfma_f32_4x4x4_16b_bf16 v[180:183], a[216:217], v[250:251], 0\n"
      "v_mfma_f32_16x16x32_f16 v[92:95], a[124:127], v[108:111], v[44:47]\n"
      "v_pk_mul_f16 v126, v158, v174\n"
      "v_pk_mul_f16 v127, v159, v175\n"
      "v_mfma_f32_16x16x32_f16 v[92:95], a[120:123], v[104:107], v[92:95]\n"
      "ds_read_b128 v[160:163], v203 offset:5184\n"
      "v_fma_f32 v194, |v84|, v68, v194\n"
      "v_mfma_f32_16x16x32_f16 v[92:95], a[116:119], v[100:103], v[92:95]\n"
      "v_fma_f32 v194, |v85|, v69, v194\n"
      "v_fma_f32 v194, |v86|, v70, v194\n"
      "v_mfma_f32_16x16x32_f16 v[92:95], a[112:115], v[96:99], v[92:95]\n"
      "v_fma_f32 v194, |v87|, v71, v194\n"
      "v_fma_f32 v194, |v88|, v72, v194\n"
      "v_mfma_f32_16x16x32_f16 v[80:83], a[0:3], v[112:115], v[16:19]\n"
      "v_fma_f32 v194, |v89|, v73, v194\n"
      "v_fma_f32 v194, |v90|, v74, v194\n"
      "v_mfma_f32_16x16x32_f16 v[80:83], a[4:7], v[116:119], v[80:83]\n"
      "ds_read_b128 v[164:167], v203 offset:5248\n"
      "v_fma_f32 v194, |v91|, v75, v194\n"
      "v_mfma_f32_16x16x32_f16 v[80:83], a[8:11], v[120:123], v[80:83]\n"
      "v_fma_f32 v194, |v92|, v76, v194\n"
      "v_fma_f32 v194, |v93|, v77, v194\n"
      "v_mfma_f32_16x16x32_f16 v[80:83], a[12:15], v[124:127], v[80:83]\n"
      "v_fma_f32 v194, |v94|, v78, v194\n"
      "v_fma_f32 v194, |v95|, v79, v194\n"
      "v_mfma_f32_16x16x32_f16 v[84:87], a[28:31], v[124:127], v[20:23]\n"
      "ds_read_b128 v[168:171], v203 offset:5312\n"
      "v_cvt_pk_f16_f32 v184, v176, v177\n"
      "v_mfma_f32_16x16x32_f16 v[84:87], a[24:27], v[120:123], v[84:87]\n"
      "s_waitcnt lgkmcnt(2)\n"
      "v_pk_mul_f16 v96, v128, v160\n"
      "v_pk_mul_f16 v97, v129, v161\n"
      "v_mfma_f32_16x16x32_f16 v[84:87], a[20:23], v[116:119], v[84:87]\n"
      "v_pk_mul_f16 v98, v130, v162\n"
      "v_pk_mul_f16 v99, v131, v163\n"
      "v_mfma_f32_16x16x32_f16 v[84:87], a[16:19], v[112:115], v[84:87]\n"
      "v_mul_f32 v195, |v80|, v48\n"
      "v_fma_f32 v195, |v81|, v49, v195\n"
      "v_mfma_f32_16x16x32_f16 v[88:91], a[32:35], v[112:115], v[24:27]\n"
      "v_fma_f32 v195, |v82|, v50, v195\n"
      "v_fma_f32 v195, |v83|, v51, v195\n"
      "v_mfma_f32_16x16x32_f16 v[88:91], a[36:39], v[116:119], v[88:91]\n"
      "s_waitcnt lgkmcnt(1)\n"
      "v_pk_mul_f16 v100, v132, v164\n"
      "v_pk_mul_f16 v101, v133, v165\n"
      "v_mfma_f32_16x16x32_f16 v[88:91], a[40:43], v[120:123], v[88:91]\n"
      "v_pk_mul_f16 v102, v134, v166\n"
      "v_fma_f32 v195, |v84|, v52, v195\n"
      "v_mfma_f32_16x16x32_f16 v[88:91], a[44:47], v[124:127], v[88:91]\n"
      "v_fma_f32 v195, |v85|, v53, v195\n"
      "v_pk_mul_f16 v103, v135, v167\n"
      "v_mfma_f32_16x16x32_f16 v[92:95], a[60:63], v[124:127], v[28:31]\n"
      "v_fma_f32 v195, |v86|, v54, v195\n"
      "v_fma_f32 v195, |v87|, v55, v195\n"
      "v_mfma_f32_16x16x32_f16 v[92:95], a[56:59], v[120:123], v[92:95]\n"
      "s_waitcnt lgkmcnt(0)\n"
      "v_pk_mul_f16 v104, v136, v168\n"
      "v_pk_mul_f16 v105, v137, v169\n"
      "v_mfma_f32_16x16x32_f16 v[92:95], a[52:55], v[116:119], v[92:95]\n"
      "v_fma_f32 v195, |v88|, v56, v195\n"
      "v_fma_f32 v195, |v89|, v57, v195\n"
      "v_mfma_f32_16x16x32_f16 v[92:95], a[48:51], v[112:115], v[92:95]\n"
      "v_fma_f32 v195, |v90|, v58, v195\n"
      "v_fma_f32 v195, |v91|, v59, v195\n"
      "v_mfma_f32_16x16x32_f16 v[80:83], a[64:67], v[112:115], v[32:35]\n"
      "v_pk_mul_f16 v106, v138, v170\n"
      "v_cvt_pk_f16_f32 v185, v178, v179\n"
      "v_mfma_f32_16x16x32_f16 v[80:83], a[68:71], v[116:119], v[80:83]\n"
      "ds_read_b128 v[172:175], v203 offset:5376\n"
      "v_pk_mul_f16 v107, v139, v171\n"
      "v_mfma_f32_16x16x32_f16 v[80:83], a[72:75], v[120:123], v[80:83]\n"
      "v_cvt_pk_f16_f32 v186, v180, v181\n"
      "v_cvt_pk_f16_f32 v187, v182, v183\n"
      "v_mfma_f32_16x16x32_f16 v[80:83], a[76:79], v[124:127], v[80:83]\n"
      "ds_write_b128 v1, v[184:187] offset:5760\n"
      "v_mfma_f32_16x16x32_f16 v[84:87], a[92:95], v[124:127], v[36:39]\n"
      "v_fma_f32 v195, |v92|, v60, v195\n"
      "v_fma_f32 v195, |v93|, v61, v195\n"
      "v_mfma_f32_16x16x32_f16 v[84:87], a[88:91], v[120:123], v[84:87]\n"
      "v_fma_f32 v195, |v94|, v62, v195\n"
      "v_fma_f32 v195, |v95|, v63, v195\n"
      "v_mfma_f32_16x16x32_f16 v[84:87], a[84:87], v[116:119], v[84:87]\n"
      "v_fma_f32 v195, |v80|, v64, v195\n"
      "v_fma_f32 v195, |v81|, v65, v195\n"
      "v_mfma_f32_16x16x32_f16 v[84:87], a[80:83], v[112:115], v[84:87]\n"
      "v_fma_f32 v195, |v82|, v66, v195\n"
      "v_fma_f32 v195, |v83|, v67, v195\n"
      "v_mfma_f32_16x16x32_f16 v[88:91], a[96:99], v[112:115], v[40:43]\n"
      "v_add_f32 v188, v188, v193\n"
      "v_add_u32_sdwa v10, s99, v199 dst_sel:DWORD dst_unused:UNUSED_PAD src0_sel:DWORD src1_sel:WORD_0\n"
      "v_mfma_f32_16x16x32_f16 v[88:91], a[100:103], v[116:119], v[88:91]\n"
      "s_waitcnt lgkmcnt(1)\n"
      "v_pk_mul_f16 v108, v140, v172\n"
      "v_pk_mul_f16 v109, v141, v173\n"
      "v_mfma_f32_16x16x32_f16 v[88:91], a[104:107], v[120:123], v[88:91]\n"
      "v_pk_mul_f16 v110, v142, v174\n"
      "v_pk_mul_f16 v111, v143, v175\n"
      "v_mfma_f32_16x16x32_f16 v[88:91], a[108:111], v[124:127], v[88:91]\n"
      "v_fma_f32 v195, |v84|, v68, v195\n"
      "v_fma_f32 v195, |v85|, v69, v195\n"
      "v_mfma_f32_4x4x4_16b_bf16 v[176:179], a[216:217], v[252:253], 0\n"
      "v_mfma_f32_16x16x32_f16 v[92:95], a[124:127], v[124:127], v[44:47]\n"
      "v_fma_f32 v195, |v86|, v70, v195\n"
      "v_fma_f32 v195, |v87|, v71, v195\n"
      "v_mfma_f32_16x16x32_f16 v[92:95], a[120:123], v[120:123], v[92:95]\n"
      "s_nop 0\n"
      "v_fma_f32 v195, |v88|, v72, v195\n"
      "v_fma_f32 v195, |v89|, v73, v195\n"
      "v_mfma_f32_16x16x32_f16 v[92:95], a[116:119], v[116:119], v[92:95]\n"
      "v_fma_f32 v195, |v90|, v74, v195\n"
      "v_fma_f32 v195, |v91|, v75, v195\n"
      "v_mfma_f32_16x16x32_f16 v[92:95], a[112:115], v[112:115], v[92:95]\n"
      "v_pk_mul_f16 v112, v144, v160\n"
      "v_pk_mul_f16 v113, v145, v161\n"
      "v_mfma_f32_4x4x4_16b_bf16 v[180:183], a[216:217], v[254:255], 0\n"
      "v_mfma_f32_16x16x32_f16 v[80:83], a[0:3], v[96:99], v[16:19]\n"
      "v_pk_mul_f16 v114, v146, v162\n"
      "v_pk_mul_f16 v115, v147, v163\n"
      "v_mfma_f32_16x16x32_f16 v[80:83], a[4:7], v[100:103], v[80:83]\n"
      "s_nop 0\n"
      "v_fma_f32 v195, |v92|, v76, v195\n"
      "v_fma_f32 v195, |v93|, v77, v195\n"
      "v_mfma_f32_16x16x32_f16 v[80:83], a[8:11], v[104:107], v[80:83]\n"
      "v_fma_f32 v195, |v94|, v78, v195\n"
      "v_fma_f32 v195, |v95|, v79, v195\n"
      "v_mfma_f32_16x16x32_f16 v[80:83], a[12:15], v[108:111], v[80:83]\n"
      "v_pk_mul_f16 v116, v148, v164\n"
      "v_pk_mul_f16 v117, v149, v165\n"
      "v_mfma_f32_16x16x32_f16 v[84:87], a[28:31], v[108:111], v[20:23]\n"
      "v_pk_mul_f16 v118, v150, v166\n"
      "v_pk_mul_f16 v119, v151, v167\n"
      "v_mfma_f32_16x16x32_f16 v[84:87], a[24:27], v[104:107], v[84:87]\n"
      "v_pk_mul_f16 v120, v152, v168\n"
      "v_pk_mul_f16 v121, v153, v169\n"
      "v_mfma_f32_16x16x32_f16 v[84:87], a[20:23], v[100:103], v[84:87]\n"
      "v_mul_f32 v190, |v80|, v48\n"
      "v_fma_f32 v190, |v81|, v49, v190\n"
      "v_mfma_f32_16x16x32_f16 v[84:87], a[16:19], v[96:99], v[84:87]\n"
      "v_fma_f32 v190, |v82|, v50, v190\n"
      "v_fma_f32 v190, |v83|, v51, v190\n"
      "v_mfma_f32_16x16x32_f16 v[88:91], a[32:35], v[96:99], v[24:27]\n"
      "v_pk_mul_f16 v122, v154, v170\n"
      "v_pk_mul_f16 v123, v155, v171\n"
      "v_mfma_f32_16x16x32_f16 v[88:91], a[36:39], v[100:103], v[88:91]\n"
      "v_pk_mul_f16 v124, v156, v172\n"
      "v_pk_mul_f16 v125, v157, v173\n"
      "v_mfma_f32_16x16x32_f16 v[88:91], a[40:43], v[104:107], v[88:91]\n"
      "v_fma_f32 v190, |v84|, v52, v190\n"
      "v_fma_f32 v190, |v85|, v53, v190\n"
      "v_mfma_f32_16x16x32_f16 v[88:91], a[44:47], v[108:111], v[88:91]\n"
      "v_fma_f32 v190, |v86|, v54, v190\n"
      "v_fma_f32 v190, |v87|, v55, v190\n"
      "v_mfma_f32_16x16x32_f16 v[92:95], a[60:63], v[108:111], v[28:31]\n"
      "v_pk_mul_f16 v126, v158, v174\n"
      "v_pk_mul_f16 v127, v159, v175\n"
      "v_mfma_f32_16x16x32_f16 v[92:95], a[56:59], v[104:107], v[92:95]\n"
      "v_cvt_pk_f16_f32 v184, v176, v177\n"
      "v_cvt_pk_f16_f32 v185, v178, v179\n"
      "v_mfma_f32_16x16x32_f16 v[92:95], a[52:55], v[100:103], v[92:95]\n"
      "v_fma_f32 v190, |v88|, v56, v190\n"
      "v_fma_f32 v190, |v89|, v57, v190\n"
      "v_mfma_f32_16x16x32_f16 v[92:95], a[48:51], v[96:99], v[92:95]\n"
      "v_fma_f32 v190, |v90|, v58, v190\n"
      "v_fma_f32 v190, |v91|, v59, v190\n"
      "v_mfma_f32_16x16x32_f16 v[80:83], a[64:67], v[96:99], v[32:35]\n"
      "v_cvt_pk_f16_f32 v186, v180, v181\n"
      "v_cvt_pk_f16_f32 v187, v182, v183\n"
      "v_mfma_f32_16x16x32_f16 v[80:83], a[68:71], v[100:103], v[80:83]\n"
      "ds_write_b128 v1, v[184:187] offset:6912\n"
      "v_mfma_f32_16x16x32_f16 v[80:83], a[72:75], v[104:107], v[80:83]\n"
      "v_fma_f32 v190, |v92|, v60, v190\n"
      "v_fma_f32 v190, |v93|, v61, v190\n"
      "v_mfma_f32_16x16x32_f16 v[80:83], a[76:79], v[108:111], v[80:83]\n"
      "v_fma_f32 v190, |v94|, v62, v190\n"
      "v_fma_f32 v190, |v95|, v63, v190\n"
      "v_mfma_f32_16x16x32_f16 v[84:87], a[92:95], v[108:111], v[36:39]\n"
      "ds_read_b128 v[160:163], v203 offset:5760\n"
      "ds_read_b128 v[176:179], v214 offset:11904\n"
      "v_mfma_f32_16x16x32_f16 v[84:87], a[88:91], v[104:107], v[84:87]\n"
      "ds_read_b128 a[152:155], v219 offset:0\n"
      "ds_read_b128 a[168:171], v220 offset:0\n"
      "v_mfma_f32_16x16x32_f16 v[84:87], a[84:87], v[100:103], v[84:87]\n"
      "v_fma_f32 v190, |v80|, v64, v190\n"
      "v_fma_f32 v190, |v81|, v65, v190\n"
      "v_mfma_f32_16x16x32_f16 v[84:87], a[80:83], v[96:99], v[84:87]\n"
      "v_fma_f32 v190, |v82|, v66, v190\n"
      "v_fma_f32 v190, |v83|, v67, v190\n"
      "v_mfma_f32_16x16x32_f16 v[88:91], a[96:99], v[96:99], v[40:43]\n"
      "ds_read_b128 a[184:187], v221 offset:0\n"
      "ds_read_b128 a[156:159], v219 offset:64\n"
      "v_mfma_f32_16x16x32_f16 v[88:91], a[100:103], v[100:103], v[88:91]\n"
      "ds_read_b128 a[172:175], v220 offset:64\n"
      "ds_read_b128 a[188:191], v221 offset:64\n"
      "v_mfma_f32_16x16x32_f16 v[88:91], a[104:107], v[104:107], v[88:91]\n"
      "v_fma_f32 v190, |v84|, v68, v190\n"
      "v_fma_f32 v190, |v85|, v69, v190\n"
      "v_mfma_f32_16x16x32_f16 v[88:91], a[108:111], v[108:111], v[88:91]\n"
      "v_fma_f32 v190, |v86|, v70, v190\n"
      "v_fma_f32 v190, |v87|, v71, v190\n"
      "v_mfma_f32_16x16x32_f16 v[92:95], a[124:127], v[108:111], v[44:47]\n"
      "s_waitcnt lgkmcnt(6)\n"
      "ds_read_b128 v[164:167], v203 offset:5824\n"
      "v_pk_mul_f16 v228, v128, v176\n"
      "v_mfma_f32_16x16x32_f16 v[92:95], a[120:123], v[104:107], v[92:95]\n"
      "v_pk_mul_f16 v229, v129, v177\n"
      "v_pk_mul_f16 v230, v130, v178\n"
      "v_mfma_f32_16x16x32_f16 v[92:95], a[116:119], v[100:103], v[92:95]\n"
      "v_fma_f32 v190, |v88|, v72, v190\n"
      "v_fma_f32 v190, |v89|, v73, v190\n"
      "v_mfma_f32_16x16x32_f16 v[92:95], a[112:115], v[96:99], v[92:95]\n"
      "v_fma_f32 v190, |v90|, v74, v190\n"
      "v_fma_f32 v190, |v91|, v75, v190\n"
      "v_mfma_f32_16x16x32_f16 v[80:83], a[0:3], v[112:115], v[16:19]\n"
      "v_pk_mul_f16 v96, v128, v160\n"
      "v_pk_mul_f16 v97, v129, v161\n"
      "v_mfma_f32_16x16x32_f16 v[80:83], a[4:7], v[116:119], v[80:83]\n"
      "v_pk_mul_f16 v98, v130, v162\n"
      "v_pk_mul_f16 v99, v131, v163\n"
      "v_mfma_f32_16x16x32_f16 v[80:83], a[8:11], v[120:123], v[80:83]\n"
      "v_fma_f32 v190, |v92|, v76, v190\n"
      "v_fma_f32 v190, |v93|, v77, v190\n"
      "v_mfma_f32_16x16x32_f16 v[80:83], a[12:15], v[124:127], v[80:83]\n"
      "v_fma_f32 v190, |v94|, v78, v190\n"
      "v_fma_f32 v190, |v95|, v79, v190\n"
      "v_mfma_f32_16x16x32_f16 v[84:87], a[28:31], v[124:127], v[20:23]\n"
      "s_waitcnt lgkmcnt(0)\n"
      "ds_read_b128 v[168:171], v203 offset:5888\n"
      "v_pk_mul_f16 v100, v132, v164\n"
      "v_mfma_f32_16x16x32_f16 v[84:87], a[24:27], v[120:123], v[84:87]\n"
      "v_pk_mul_f16 v101, v133, v165\n"
      "v_pk_mul_f16 v102, v134, v166\n"
      "v_mfma_f32_16x16x32_f16 v[84:87], a[20:23], v[116:119], v[84:87]\n"
      "v_mul_f32 v191, |v80|, v48\n"
      "v_fma_f32 v191, |v81|, v49, v191\n"
      "v_mfma_f32_16x16x32_f16 v[84:87], a[16:19], v[112:115], v[84:87]\n"
      "v_fma_f32 v191, |v82|, v50, v191\n"
      "v_fma_f32 v191, |v83|, v51, v191\n"
      "v_mfma_f32_16x16x32_f16 v[88:91], a[32:35], v[112:115], v[24:27]\n"
      "ds_read_b128 v[240:243], v201 offset:4608\n"
      "v_pk_mul_f16 v103, v135, v167\n"
      "v_mfma_f32_16x16x32_f16 v[88:91], a[36:39], v[116:119], v[88:91]\n"
      "ds_read_b128 v[172:175], v203 offset:5952\n"
      "ds_read_b128 v[244:247], v201 offset:4672\n"
      "v_mfma_f32_16x16x32_f16 v[88:91], a[40:43], v[120:123], v[88:91]\n"
      "v_fma_f32 v191, |v84|, v52, v191\n"
      "v_fma_f32 v191, |v85|, v53, v191\n"
      "v_mfma_f32_16x16x32_f16 v[88:91], a[44:47], v[124:127], v[88:91]\n"
      "v_fma_f32 v191, |v86|, v54, v191\n"
      "v_fma_f32 v191, |v87|, v55, v191\n"
      "v_mfma_f32_16x16x32_f16 v[92:95], a[60:63], v[124:127], v[28:31]\n"
      "s_waitcnt lgkmcnt(3)\n"
      "v_pk_mul_f16 v104, v136, v168\n"
      "v_pk_mul_f16 v105, v137, v169\n"
      "v_mfma_f32_16x16x32_f16 v[92:95], a[56:59], v[120:123], v[92:95]\n"
      "v_pk_mul_f16 v106, v138, v170\n"
      "v_pk_mul_f16 v107, v139, v171\n"
      "v_mfma_f32_16x16x32_f16 v[92:95], a[52:55], v[116:119], v[92:95]\n"
      "v_fma_f32 v191, |v88|, v56, v191\n"
      "v_fma_f32 v191, |v89|, v57, v191\n"
      "v_mfma_f32_16x16x32_f16 v[92:95], a[48:51], v[112:115], v[92:95]\n"
      "v_fma_f32 v191, |v90|, v58, v191\n"
      "v_fma_f32 v191, |v91|, v59, v191\n"
      "v_mfma_f32_16x16x32_f16 v[80:83], a[64:67], v[112:115], v[32:35]\n"
      "ds_read_b128 v[248:251], v201 offset:4736\n"
      "ds_read_b128 v[252:255], v201 offset:4800\n"
      "v_mfma_f32_16x16x32_f16 v[80:83], a[68:71], v[116:119], v[80:83]\n"
      "s_waitcnt lgkmcnt(3)\n"
      "v_pk_mul_f16 v108, v140, v172\n"
      "v_pk_mul_f16 v109, v141, v173\n"
      "v_mfma_f32_16x16x32_f16 v[80:83], a[72:75], v[120:123], v[80:83]\n"
      "v_fma_f32 v191, |v92|, v60, v191\n"
      "v_fma_f32 v191, |v93|, v61, v191\n"
      "v_mfma_f32_16x16x32_f16 v[80:83], a[76:79], v[124:127], v[80:83]\n"
      "v_fma_f32 v191, |v94|, v62, v191\n"
      "v_fma_f32 v191, |v95|, v63, v191\n"
      "v_mfma_f32_16x16x32_f16 v[84:87], a[92:95], v[124:127], v[36:39]\n"
      "v_pk_mul_f16 v110, v142, v174\n"
      "v_pk_mul_f16 v111, v143, v175\n"
      "v_mfma_f32_16x16x32_f16 v[84:87], a[88:91], v[120:123], v[84:87]\n"
      "v_pk_mul_f16 v231, v131, v179\n"
      "v_pk_mul_f16 v232, v144, v176\n"
      "v_mfma_f32_16x16x32_f16 v[84:87], a[84:87], v[116:119], v[84:87]\n"
      "v_fma_f32 v191, |v80|, v64, v191\n"
      "v_fma_f32 v191, |v81|, v65, v191\n"
      "v_mfma_f32_16x16x32_f16 v[84:87], a[80:83], v[112:115], v[84:87]\n"
      "v_fma_f32 v191, |v82|, v66, v191\n"
      "v_fma_f32 v191, |v83|, v67, v191\n"
      "v_mfma_f32_16x16x32_f16 v[88:91], a[96:99], v[112:115], v[40:43]\n"
      "v_pk_mul_f16 v233, v145, v177\n"
      "v_pk_mul_f16 v234, v146, v178\n"
      "v_mfma_f32_16x16x32_f16 v[88:91], a[100:103], v[116:119], v[88:91]\n"
      "v_pk_mul_f16 v235, v147, v179\n"
      "v_pk_mul_f16 v236, v240, v176\n"
      "v_mfma_f32_16x16x32_f16 v[88:91], a[104:107], v[120:123], v[88:91]\n"
      "v_fma_f32 v191, |v84|, v68, v191\n"
      "v_fma_f32 v191, |v85|, v69, v191\n"
      "v_mfma_f32_16x16x32_f16 v[88:91], a[108:111], v[124:127], v[88:91]\n"
      "v_fma_f32 v191, |v86|, v70, v191\n"
      "v_fma_f32 v191, |v87|, v71, v191\n"
      "v_mfma_f32_16x16x32_f16 v[92:95], a[124:127], v[124:127], v[44:47]\n"
      "v_pk_mul_f16 v237, v241, v177\n"
      "v_pk_mul_f16 v238, v242, v178\n"
      "v_mfma_f32_16x16x32_f16 v[92:95], a[120:123], v[120:123], v[92:95]\n"
      "v_pk_mul_f16 v120, v152, v168\n"
      "v_pk_mul_f16 v121, v153, v169\n"
      "v_mfma_f32_16x16x32_f16 v[92:95], a[116:119], v[116:119], v[92:95]\n"
      "v_fma_f32 v191, |v88|, v72, v191\n"
      "v_fma_f32 v191, |v89|, v73, v191\n"
      "v_mfma_f32_16x16x32_f16 v[92:95], a[112:115], v[112:115], v[92:95]\n"
      "v_fma_f32 v191, |v90|, v74, v191\n"
      "v_fma_f32 v191, |v91|, v75, v191\n"
      "v_mfma_f32_16x16x32_f16 v[80:83], a[0:3], v[96:99], v[16:19]\n"
      "v_pk_mul_f16 v112, v144, v160\n"
      "v_pk_mul_f16 v113, v145, v161\n"
      "v_mfma_f32_16x16x32_f16 v[80:83], a[4:7], v[100:103], v[80:83]\n"
      "v_pk_mul_f16 v114, v146, v162\n"
      "v_pk_mul_f16 v115, v147, v163\n"
      "v_mfma_f32_16x16x32_f16 v[80:83], a[8:11], v[104:107], v[80:83]\n"
      "v_fma_f32 v191, |v92|, v76, v191\n"
      "v_fma_f32 v191, |v93|, v77, v191\n"
      "v_mfma_f32_16x16x32_f16 v[80:83], a[12:15], v[108:111], v[80:83]\n"
      "v_fma_f32 v191, |v94|, v78, v191\n"
      "v_fma_f32 v191, |v95|, v79, v191\n"
      "v_mfma_f32_16x16x32_f16 v[84:87], a[28:31], v[108:111], v[20:23]\n"
      "v_pk_mul_f16 v116, v148, v164\n"
      "v_pk_mul_f16 v117, v149, v165\n"
      "v_mfma_f32_16x16x32_f16 v[84:87], a[24:27], v[104:107], v[84:87]\n"
      "v_pk_mul_f16 v118, v150, v166\n"
      "v_pk_mul_f16 v119, v151, v167\n"
      "v_mfma_f32_16x16x32_f16 v[84:87], a[20:23], v[100:103], v[84:87]\n"
      "v_mul_f32 v192, |v80|, v48\n"
      "v_fma_f32 v192, |v81|, v49, v192\n"
      "v_mfma_f32_16x16x32_f16 v[84:87], a[16:19], v[96:99], v[84:87]\n"
      "v_fma_f32 v192, |v82|, v50, v192\n"
      "v_fma_f32 v192, |v83|, v51, v192\n"
      "v_mfma_f32_16x16x32_f16 v[88:91], a[32:35], v[96:99], v[24:27]\n"
      "v_pk_mul_f16 v122, v154, v170\n"
      "v_pk_mul_f16 v123, v155, v171\n"
      "v_mfma_f32_16x16x32_f16 v[88:91], a[36:39], v[100:103], v[88:91]\n"
      "v_pk_mul_f16 v239, v243, v179\n"
      "v_pk_mul_f16 v124, v156, v172\n"
      "v_mfma_f32_16x16x32_f16 v[88:91], a[40:43], v[104:107], v[88:91]\n"
      "v_fma_f32 v192, |v84|, v52, v192\n"
      "v_fma_f32 v192, |v85|, v53, v192\n"
      "v_mfma_f32_16x16x32_f16 v[88:91], a[44:47], v[108:111], v[88:91]\n"
      "v_fma_f32 v192, |v86|, v54, v192\n"
      "v_fma_f32 v192, |v87|, v55, v192\n"
      "v_mfma_f32_16x16x32_f16 v[92:95], a[60:63], v[108:111], v[28:31]\n"
      "v_pk_mul_f16 v125, v157, v173\n"
      "v_pk_mul_f16 v126, v158, v174\n"
      "v_mfma_f32_16x16x32_f16 v[92:95], a[56:59], v[104:107], v[92:95]\n"
      "ds_read_b128 v[176:179], v214 offset:11968\n"
      "v_pk_mul_f16 v127, v159, v175\n"
      "v_mfma_f32_16x16x32_f16 v[92:95], a[52:55], v[100:103], v[92:95]\n"
      "v_fma_f32 v192, |v88|, v56, v192\n"
      "v_fma_f32 v192, |v89|, v57, v192\n"
      "v_mfma_f32_16x16x32_f16 v[92:95], a[48:51], v[96:99], v[92:95]\n"
      "v_fma_f32 v192, |v90|, v58, v192\n"
      "v_fma_f32 v192, |v91|, v59, v192\n"
      "v_mfma_f32_16x16x32_f16 a[128:131], v[228:231], a[152:155], 0\n"
      "v_add_u32_sdwa v11, s99, v199 dst_sel:DWORD dst_unused:UNUSED_PAD src0_sel:DWORD src1_sel:WORD_1\n"
      "v_mov_b32 v7, 0xf149f2ca\n"
      "v_mfma_f32_16x16x32_f16 v[80:83], a[64:67], v[96:99], v[32:35]\n"
      "v_add_u32 v12, s98, v1\n"
      "v_mov_b32 v8, 0xf149f2ca\n"
      "v_mfma_f32_16x16x32_f16 v[80:83], a[68:71], v[100:103], v[80:83]\n"
      "s_and_b32 s100, s100, 0x3bb93bb9\n"
      "v_fma_f32 v192, |v92|, v60, v192\n"
      "v_fma_f32 v192, |v93|, v61, v192\n"
      "v_mfma_f32_16x16x32_f16 v[80:83], a[72:75], v[104:107], v[80:83]\n"
      "s_xor_b32 s100, s100, 0xb900b9\n"
      "v_fma_f32 v192, |v94|, v62, v192\n"
      "v_fma_f32 v192, |v95|, v63, v192\n"
      "v_mfma_f32_16x16x32_f16 v[80:83], a[76:79], v[108:111], v[80:83]\n"
      "s_mov_b32 s101, s100\n"
      "ds_read_b128 a[160:163], v219 offset:128\n"
      "ds_read_b128 a[176:179], v220 offset:128\n"
      "v_mfma_f32_16x16x32_f16 v[84:87], a[92:95], v[108:111], v[36:39]\n"
      "s_waitcnt lgkmcnt(2)\n"
      "v_pk_mul_f16 v228, v132, v176\n"
      "v_pk_mul_f16 v229, v133, v177\n"
      "v_mfma_f32_16x16x32_f16 v[84:87], a[88:91], v[104:107], v[84:87]\n"
      "s_or_b32 s44, s55, s41\n"
      "v_pk_mul_f16 v230, v134, v178\n"
      "v_pk_mul_f16 v231, v135, v179\n"
      "v_mfma_f32_16x16x32_f16 v[84:87], a[84:87], v[100:103], v[84:87]\n"
      "s_cselect_b32 s44, 2, 1\n"
      "v_fma_f32 v192, |v80|, v64, v192\n"
      "v_fma_f32 v192, |v81|, v65, v192\n"
      "v_mfma_f32_16x16x32_f16 v[84:87], a[80:83], v[96:99], v[84:87]\n"
      "v_fma_f32 v192, |v82|, v66, v192\n"
      "v_fma_f32 v192, |v83|, v67, v192\n"
      "v_mfma_f32_16x16x32_f16 a[132:135], v[232:235], a[168:171], 0\n"
      "ds_read_b128 v[160:163], v203 offset:6336\n"
      "v_pk_mul_f16 v232, v148, v176\n"
      "v_mfma_f32_16x16x32_f16 v[88:91], a[96:99], v[96:99], v[40:43]\n"
      "v_pk_mul_f16 v233, v149, v177\n"
      "v_pk_mul_f16 v234, v150, v178\n"
      "v_mfma_f32_16x16x32_f16 v[88:91], a[100:103], v[100:103], v[88:91]\n"
      "v_fma_f32 v192, |v84|, v68, v192\n"
      "v_fma_f32 v192, |v85|, v69, v192\n"
      "v_mfma_f32_16x16x32_f16 v[88:91], a[104:107], v[104:107], v[88:91]\n"
      "v_fma_f32 v192, |v86|, v70, v192\n"
      "v_fma_f32 v192, |v87|, v71, v192\n"
      "v_mfma_f32_16x16x32_f16 v[88:91], a[108:111], v[108:111], v[88:91]\n"
      "ds_read_b128 v[164:167], v203 offset:6400\n"
      "v_pk_mul_f16 v235, v151, v179\n"
      "v_mfma_f32_16x16x32_f16 v[92:95], a[124:127], v[108:111], v[44:47]\n"
      "ds_read_b128 a[192:195], v221 offset:128\n"
      "v_mov_b32 v223, s44\n"
      "v_mfma_f32_16x16x32_f16 v[92:95], a[120:123], v[104:107], v[92:95]\n"
      "ds_write_b32 v2, v223 offset:8\n"
      "v_mfma_f32_16x16x32_f16 v[92:95], a[116:119], v[100:103], v[92:95]\n"
      "v_fma_f32 v192, |v88|, v72, v192\n"
      "v_fma_f32 v192, |v89|, v73, v192\n"
      "v_mfma_f32_16x16x32_f16 v[92:95], a[112:115], v[96:99], v[92:95]\n"
      "v_fma_f32 v192, |v90|, v74, v192\n"
      "v_fma_f32 v192, |v91|, v75, v192\n"
      "v_mfma_f32_16x16x32_f16 a[136:139], v[236:239], a[184:187], 0\n"
      "s_waitcnt lgkmcnt(3)\n"
      "v_pk_mul_f16 v96, v128, v160\n"
      "v_pk_mul_f16 v97, v129, v161\n"
      "v_mfma_f32_16x16x32_f16 v[80:83], a[0:3], v[112:115], v[16:19]\n"
      "v_pk_mul_f16 v98, v130, v162\n"
      "v_pk_mul_f16 v99, v131, v163\n"
      "v_mfma_f32_16x16x32_f16 v[80:83], a[4:7], v[116:119], v[80:83]\n"
      "v_fma_f32 v192, |v92|, v76, v192\n"
      "v_fma_f32 v192, |v93|, v77, v192\n"
      "v_mfma_f32_16x16x32_f16 v[80:83], a[8:11], v[120:123], v[80:83]\n"
      "v_fma_f32 v192, |v94|, v78, v192\n"
      "v_fma_f32 v192, |v95|, v79, v192\n"
      "v_mfma_f32_16x16x32_f16 v[80:83], a[12:15], v[124:127], v[80:83]\n"
      "s_waitcnt lgkmcnt(2)\n"
      "ds_read_b128 v[168:171], v203 offset:6464\n"
      "v_pk_mul_f16 v100, v132, v164\n"
      "v_mfma_f32_16x16x32_f16 v[84:87], a[28:31], v[124:127], v[20:23]\n"
      "v_permlane32_swap_b32 v194, v192\n"
      "v_mfma_f32_16x16x32_f16 v[84:87], a[24:27], v[120:123], v[84:87]\n"
      "v_pk_mul_f16 v101, v133, v165\n"
      "v_pk_mul_f16 v102, v134, v166\n"
      "v_mfma_f32_16x16x32_f16 v[84:87], a[20:23], v[116:119], v[84:87]\n"
      "v_mul_f32 v193, |v80|, v48\n"
      "v_fma_f32 v193, |v81|, v49, v193\n"
      "v_mfma_f32_16x16x32_f16 v[84:87], a[16:19], v[112:115], v[84:87]\n"
      "v_fma_f32 v193, |v82|, v50, v193\n"
      "v_fma_f32 v193, |v83|, v51, v193\n"
      "v_mfma_f32_16x16x32_f16 v[88:91], a[32:35], v[112:115], v[24:27]\n"
      "v_pk_mul_f16 v103, v135, v167\n"
      "v_pk_mul_f16 v236, v244, v176\n"
      "v_mfma_f32_16x16x32_f16 v[88:91], a[36:39], v[116:119], v[88:91]\n"
      "v_pk_mul_f16 v237, v245, v177\n"
      "v_pk_mul_f16 v238, v246, v178\n"
      "v_mfma_f32_16x16x32_f16 v[88:91], a[40:43], v[120:123], v[88:91]\n"
      "v_fma_f32 v193, |v84|, v52, v193\n"
      "v_fma_f32 v193, |v85|, v53, v193\n"
      "v_mfma_f32_16x16x32_f16 v[88:91], a[44:47], v[124:127], v[88:91]\n"
      "v_fma_f32 v193, |v86|, v54, v193\n"
      "v_fma_f32 v193, |v87|, v55, v193\n"
      "v_mfma_f32_16x16x32_f16 v[92:95], a[60:63], v[124:127], v[28:31]\n"
      "s_waitcnt lgkmcnt(0)\n"
      "v_pk_mul_f16 v104, v136, v168\n"
      "v_pk_mul_f16 v105, v137, v169\n"
      "v_mfma_f32_16x16x32_f16 v[92:95], a[56:59], v[120:123], v[92:95]\n"
      "v_pk_mul_f16 v106, v138, v170\n"
      "v_pk_mul_f16 v107, v139, v171\n"
      "v_mfma_f32_16x16x32_f16 v[92:95], a[52:55], v[116:119], v[92:95]\n"
      "v_fma_f32 v193, |v88|, v56, v193\n"
      "v_fma_f32 v193, |v89|, v57, v193\n"
      "v_mfma_f32_16x16x32_f16 v[92:95], a[48:51], v[112:115], v[92:95]\n"
      "42:\n"
      "s_cbranch_execz 44b\n"
      "v_fma_f32 v193, |v90|, v58, v193\n"
      "v_mfma_f32_16x16x32_f16 a[128:131], v[228:231], a[156:159], a[128:131]\n"
      "ds_read_b128 v[172:175], v203 offset:6528\n"
      "v_fma_f32 v193, |v91|, v59, v193\n"
      "v_mfma_f32_16x16x32_f16 v[80:83], a[64:67], v[112:115], v[32:35]\n"
      "v_pk_mul_f16 v239, v247, v179\n"
      "v_add_f32 v194, v194, v192\n"
      "v_mfma_f32_16x16x32_f16 v[80:83], a[68:71], v[116:119], v[80:83]\n"
      "v_fma_f32 v193, |v92|, v60, v193\n"
      "v_fma_f32 v193, |v93|, v61, v193\n"
      "v_mfma_f32_16x16x32_f16 v[80:83], a[72:75], v[120:123], v[80:83]\n"
      "v_fma_f32 v193, |v94|, v62, v193\n"
      "v_fma_f32 v193, |v95|, v63, v193\n"
      "v_mfma_f32_16x16x32_f16 v[80:83], a[76:79], v[124:127], v[80:83]\n"
      "ds_read_b128 v[176:179], v214 offset:12032\n"
      "ds_read_b128 a[164:167], v219 offset:192\n"
      "v_mfma_f32_16x16x32_f16 v[84:87], a[92:95], v[124:127], v[36:39]\n"
      "ds_read_b128 a[180:183], v220 offset:192\n"
      "ds_read_b128 a[196:199], v221 offset:192\n"
      "v_mfma_f32_16x16x32_f16 v[84:87], a[88:91], v[120:123], v[84:87]\n"
      "ds_read_b128 a[200:203], v215 offset:11904\n"
      "ds_read_b128 a[204:207], v215 offset:11968\n"
      "v_mfma_f32_16x16x32_f16 v[84:87], a[84:87], v[116:119], v[84:87]\n"
      "v_fma_f32 v193, |v80|, v64, v193\n"
      "v_fma_f32 v193, |v81|, v65, v193\n"
      "v_mfma_f32_16x16x32_f16 v[84:87], a[80:83], v[112:115], v[84:87]\n"
      "s_waitcnt lgkmcnt(6)\n"
      "v_pk_mul_f16 v108, v140, v172\n"
      "v_fma_f32 v193, |v82|, v66, v193\n"
      "v_mfma_f32_16x16x32_f16 a[132:135], v[232:235], a[172:175], a[132:135]\n"
      "v_fma_f32 v193, |v83|, v67, v193\n"
      "v_pk_mul_f16 v109, v141, v173\n"
      "v_mfma_f32_16x16x32_f16 v[88:91], a[96:99], v[112:115], v[40:43]\n"
      "v_pk_mul_f16 v110, v142, v174\n"
      "v_pk_mul_f16 v111, v143, v175\n"
      "v_mfma_f32_16x16x32_f16 v[88:91], a[100:103], v[116:119], v[88:91]\n"
      "v_fma_f32 v193, |v84|, v68, v193\n"
      "v_fma_f32 v193, |v85|, v69, v193\n"
      "v_mfma_f32_16x16x32_f16 v[88:91], a[104:107], v[120:123], v[88:91]\n"
      "v_fma_f32 v193, |v86|, v70, v193\n"
      "v_fma_f32 v193, |v87|, v71, v193\n"
      "v_mfma_f32_16x16x32_f16 v[88:91], a[108:111], v[124:127], v[88:91]\n"
      "s_waitcnt lgkmcnt(5)\n"
      "v_pk_mul_f16 v228, v136, v176\n"
      "v_pk_mul_f16 v229, v137, v177\n"
      "v_mfma_f32_16x16x32_f16 v[92:95], a[124:127], v[124:127], v[44:47]\n"
      "v_pk_mul_f16 v230, v138, v178\n"
      "v_pk_mul_f16 v231, v139, v179\n"
      "v_mfma_f32_16x16x32_f16 v[92:95], a[120:123], v[120:123], v[92:95]\n"
      "v_pk_mul_f16 v232, v152, v176\n"
      "v_pk_mul_f16 v120, v152, v168\n"
      "v_mfma_f32_16x16x32_f16 v[92:95], a[116:119], v[116:119], v[92:95]\n"
      "v_fma_f32 v193, |v88|, v72, v193\n"
      "v_fma_f32 v193, |v89|, v73, v193\n"
      "v_mfma_f32_16x16x32_f16 v[92:95], a[112:115], v[112:115], v[92:95]\n"
      "v_fma_f32 v193, |v90|, v74, v193\n"
      "v_fma_f32 v193, |v91|, v75, v193\n"
      "v_mfma_f32_16x16x32_f16 a[136:139], v[236:239], a[188:191], a[136:139]\n"
      "v_pk_mul_f16 v112, v144, v160\n"
      "v_pk_mul_f16 v113, v145, v161\n"
      "v_mfma_f32_16x16x32_f16 v[80:83], a[0:3], v[96:99], v[16:19]\n"
      "v_pk_mul_f16 v114, v146, v162\n"
      "v_pk_mul_f16 v115, v147, v163\n"
      "v_mfma_f32_16x16x32_f16 v[80:83], a[4:7], v[100:103], v[80:83]\n"
      "v_fma_f32 v193, |v92|, v76, v193\n"
      "v_fma_f32 v193, |v93|, v77, v193\n"
      "v_mfma_f32_16x16x32_f16 v[80:83], a[8:11], v[104:107], v[80:83]\n"
      "v_fma_f32 v193, |v94|, v78, v193\n"
      "v_fma_f32 v193, |v95|, v79, v193\n"
      "v_mfma_f32_16x16x32_f16 v[80:83], a[12:15], v[108:111], v[80:83]\n"
      "v_pk_mul_f16 v116, v148, v164\n"
      "v_pk_mul_f16 v117, v149, v165\n"
      "v_mfma_f32_16x16x32_f16 v[84:87], a[28:31], v[108:111], v[20:23]\n"
      "v_permlane32_swap_b32 v195, v193\n"
      "v_mfma_f32_16x16x32_f16 v[84:87], a[24:27], v[104:107], v[84:87]\n"
      "v_pk_mul_f16 v118, v150, v166\n"
      "v_pk_mul_f16 v119, v151, v167\n"
      "v_mfma_f32_16x16x32_f16 v[84:87], a[20:23], v[100:103], v[84:87]\n"
      "v_mul_f32 v196, |v80|, v48\n"
      "v_fma_f32 v196, |v81|, v49, v196\n"
      "v_mfma_f32_16x16x32_f16 v[84:87], a[16:19], v[96:99], v[84:87]\n"
      "v_fma_f32 v196, |v82|, v50, v196\n"
      "v_fma_f32 v196, |v83|, v51, v196\n"
      "v_mfma_f32_16x16x32_f16 v[88:91], a[32:35], v[96:99], v[24:27]\n"
      "v_pk_mul_f16 v233, v153, v177\n"
      "v_pk_mul_f16 v121, v153, v169\n"
      "v_mfma_f32_16x16x32_f16 v[88:91], a[36:39], v[100:103], v[88:91]\n"
      "v_pk_mul_f16 v234, v154, v178\n"
      "v_pk_mul_f16 v122, v154, v170\n"
      "v_mfma_f32_16x16x32_f16 v[88:91], a[40:43], v[104:107], v[88:91]\n"
      "v_fma_f32 v196, |v84|, v52, v196\n"
      "v_fma_f32 v196, |v85|, v53, v196\n"
      "v_mfma_f32_16x16x32_f16 v[88:91], a[44:47], v[108:111], v[88:91]\n"
      "v_fma_f32 v196, |v86|, v54, v196\n"
      "v_fma_f32 v196, |v87|, v55, v196\n"
      "v_mfma_f32_16x16x32_f16 v[92:95], a[60:63], v[108:111], v[28:31]\n"
      "v_pk_mul_f16 v235, v155, v179\n"
      "v_pk_mul_f16 v123, v155, v171\n"
      "v_mfma_f32_16x16x32_f16 v[92:95], a[56:59], v[104:107], v[92:95]\n"
      "v_pk_mul_f16 v236, v248, v176\n"
      "v_pk_mul_f16 v237, v249, v177\n"
      "v_mfma_f32_16x16x32_f16 v[92:95], a[52:55], v[100:103], v[92:95]\n"
      "v_fma_f32 v196, |v88|, v56, v196\n"
      "v_fma_f32 v196, |v89|, v57, v196\n"
      "v_mfma_f32_16x16x32_f16 v[92:95], a[48:51], v[96:99], v[92:95]\n"
      "v_fma_f32 v196, |v90|, v58, v196\n"
      "v_fma_f32 v196, |v91|, v59, v196\n"
      "v_mfma_f32_16x16x32_f16 a[128:131], v[228:231], a[160:163], a[128:131]\n"
      "v_pk_mul_f16 v238, v250, v178\n"
      "v_pk_mul_f16 v239, v251, v179\n"
      "v_mfma_f32_16x16x32_f16 v[80:83], a[64:67], v[96:99], v[32:35]\n"
      "v_pk_mul_f16 v124, v156, v172\n"
      "v_pk_mul_f16 v125, v157, v173\n"
      "v_mfma_f32_16x16x32_f16 v[80:83], a[68:71], v[100:103], v[80:83]\n"
      "v_fma_f32 v196, |v92|, v60, v196\n"
      "v_fma_f32 v196, |v93|, v61, v196\n"
      "v_mfma_f32_16x16x32_f16 v[80:83], a[72:75], v[104:107], v[80:83]\n"
      "v_fma_f32 v196, |v94|, v62, v196\n"
      "v_fma_f32 v196, |v95|, v63, v196\n"
      "v_mfma_f32_16x16x32_f16 v[80:83], a[76:79], v[108:111], v[80:83]\n"
      "ds_read_b128 v[176:179], v214 offset:12096\n"
      "v_pk_mul_f16 v126, v158, v174\n"
      "v_mfma_f32_16x16x32_f16 v[84:87], a[92:95], v[108:111], v[36:39]\n"
      "v_pk_mul_f16 v127, v159, v175\n"
      "v_add_f32 v195, v195, v193\n"
      "v_mfma_f32_16x16x32_f16 v[84:87], a[88:91], v[104:107], v[84:87]\n"
      "ds_read_b128 a[208:211], v215 offset:12032\n"
      "ds_read_b128 a[212:215], v215 offset:12096\n"
      "v_mfma_f32_16x16x32_f16 v[84:87], a[84:87], v[100:103], v[84:87]\n"
      "v_fma_f32 v196, |v80|, v64, v196\n"
      "v_fma_f32 v196, |v81|, v65, v196\n"
      "v_mfma_f32_16x16x32_f16 v[84:87], a[80:83], v[96:99], v[84:87]\n"
      "v_fma_f32 v196, |v82|, v66, v196\n"
      "v_fma_f32 v196, |v83|, v67, v196\n"
      "v_mfma_f32_16x16x32_f16 a[132:135], v[232:235], a[176:179], a[132:135]\n"
      "ds_read_b128 v[160:163], v202 offset:4608\n"
      "v_mfma_f32_16x16x32_f16 v[88:91], a[96:99], v[96:99], v[40:43]\n"
      "v_mfma_f32_16x16x32_f16 v[88:91], a[100:103], v[100:103], v[88:91]\n"
      "s_nop 1\n"
      "v_fma_f32 v196, |v84|, v68, v196\n"
      "v_fma_f32 v196, |v85|, v69, v196\n"
      "v_mfma_f32_16x16x32_f16 v[88:91], a[104:107], v[104:107], v[88:91]\n"
      "v_fma_f32 v196, |v86|, v70, v196\n"
      "v_fma_f32 v196, |v87|, v71, v196\n"
      "v_mfma_f32_16x16x32_f16 v[88:91], a[108:111], v[108:111], v[88:91]\n"
      "s_waitcnt lgkmcnt(3)\n"
      "ds_read_b128 v[164:167], v202 offset:4672\n"
      "v_pk_mul_f16 v228, v140, v176\n"
      "v_mfma_f32_16x16x32_f16 v[92:95], a[124:127], v[108:111], v[44:47]\n"
      "v_pk_mul_f16 v229, v141, v177\n"
      "v_pk_mul_f16 v230, v142, v178\n"
      "v_mfma_f32_16x16x32_f16 v[92:95], a[120:123], v[104:107], v[92:95]\n"
      "v_pk_mul_f16 v231, v143, v179\n"
      "v_pk_mul_f16 v232, v156, v176\n"
      "v_mfma_f32_16x16x32_f16 v[92:95], a[116:119], v[100:103], v[92:95]\n"
      "v_fma_f32 v196, |v88|, v72, v196\n"
      "v_fma_f32 v196, |v89|, v73, v196\n"
      "v_mfma_f32_16x16x32_f16 v[92:95], a[112:115], v[96:99], v[92:95]\n"
      "v_fma_f32 v196, |v90|, v74, v196\n"
      "v_fma_f32 v196, |v91|, v75, v196\n"
      "v_mfma_f32_16x16x32_f16 a[136:139], v[236:239], a[192:195], a[136:139]\n"
      "s_waitcnt lgkmcnt(1)\n"
      "v_pk_mul_f16 v96, v240, v160\n"
      "v_pk_mul_f16 v97, v241, v161\n"
      "v_mfma_f32_16x16x32_f16 v[80:83], a[0:3], v[112:115], v[16:19]\n"
      "v_pk_mul_f16 v98, v242, v162\n"
      "v_pk_mul_f16 v99, v243, v163\n"
      "v_mfma_f32_16x16x32_f16 v[80:83], a[4:7], v[116:119], v[80:83]\n"
      "v_fma_f32 v196, |v92|, v76, v196\n"
      "v_fma_f32 v196, |v93|, v77, v196\n"
      "v_mfma_f32_16x16x32_f16 v[80:83], a[8:11], v[120:123], v[80:83]\n"
      "v_fma_f32 v196, |v94|, v78, v196\n"
      "v_fma_f32 v196, |v95|, v79, v196\n"
      "v_mfma_f32_16x16x32_f16 v[80:83], a[12:15], v[124:127], v[80:83]\n"
      "s_waitcnt lgkmcnt(0)\n"
      "ds_read_b128 v[168:171], v202 offset:4736\n"
      "v_pk_mul_f16 v100, v244, v164\n"
      "v_mfma_f32_16x16x32_f16 v[84:87], a[28:31], v[124:127], v[20:23]\n"
      "v_permlane32_swap_b32 v190, v196\n"
      "v_mfma_f32_16x16x32_f16 v[84:87], a[24:27], v[120:123], v[84:87]\n"
      "v_pk_mul_f16 v101, v245, v165\n"
      "v_pk_mul_f16 v102, v246, v166\n"
      "v_mfma_f32_16x16x32_f16 v[84:87], a[20:23], v[116:119], v[84:87]\n"
      "v_mul_f32 v197, |v80|, v48\n"
      "v_fma_f32 v197, |v81|, v49, v197\n"
      "v_mfma_f32_16x16x32_f16 v[84:87], a[16:19], v[112:115], v[84:87]\n"
      "v_fma_f32 v197, |v82|, v50, v197\n"
      "v_fma_f32 v197, |v83|, v51, v197\n"
      "v_mfma_f32_16x16x32_f16 v[88:91], a[32:35], v[112:115], v[24:27]\n"
      "v_pk_mul_f16 v103, v247, v167\n"
      "v_pk_mul_f16 v233, v157, v177\n"
      "v_mfma_f32_16x16x32_f16 v[88:91], a[36:39], v[116:119], v[88:91]\n"
      "v_pk_mul_f16 v234, v158, v178\n"
      "v_pk_mul_f16 v235, v159, v179\n"
      "v_mfma_f32_16x16x32_f16 v[88:91], a[40:43], v[120:123], v[88:91]\n"
      "v_fma_f32 v197, |v84|, v52, v197\n"
      "v_fma_f32 v197, |v85|, v53, v197\n"
      "v_mfma_f32_16x16x32_f16 v[88:91], a[44:47], v[124:127], v[88:91]\n"
      "v_fma_f32 v197, |v86|, v54, v197\n"
      "v_fma_f32 v197, |v87|, v55, v197\n"
      "v_mfma_f32_16x16x32_f16 v[92:95], a[60:63], v[124:127], v[28:31]\n"
      "s_waitcnt lgkmcnt(0)\n"
      "v_pk_mul_f16 v104, v248, v168\n"
      "v_pk_mul_f16 v105, v249, v169\n"
      "v_mfma_f32_16x16x32_f16 v[92:95], a[56:59], v[120:123], v[92:95]\n"
      "v_pk_mul_f16 v106, v250, v170\n"
      "v_pk_mul_f16 v107, v251, v171\n"
      "v_mfma_f32_16x16x32_f16 v[92:95], a[52:55], v[116:119], v[92:95]\n"
      "v_fma_f32 v197, |v88|, v56, v197\n"
      "v_fma_f32 v197, |v89|, v57, v197\n"
      "v_mfma_f32_16x16x32_f16 v[92:95], a[48:51], v[112:115], v[92:95]\n"
      "v_fma_f32 v197, |v90|, v58, v197\n"
      "v_fma_f32 v197, |v91|, v59, v197\n"
      "v_mfma_f32_16x16x32_f16 a[128:131], v[228:231], a[164:167], a[128:131]\n"
      "ds_read_b128 v[172:175], v202 offset:4800\n"
      "v_pk_mul_f16 v236, v252, v176\n"
      "v_mfma_f32_16x16x32_f16 v[80:83], a[64:67], v[112:115], v[32:35]\n"
      "v_pk_mul_f16 v237, v253, v177\n"
      "v_pk_mul_f16 v238, v254, v178\n"
      "v_mfma_f32_16x16x32_f16 v[80:83], a[68:71], v[116:119], v[80:83]\n"
      "v_fma_f32 v197, |v92|, v60, v197\n"
      "v_fma_f32 v197, |v93|, v61, v197\n"
      "v_mfma_f32_16x16x32_f16 v[80:83], a[72:75], v[120:123], v[80:83]\n"
      "ds_write_b64 v218, a[128:129] offset:0\n"
      "v_mfma_f32_16x16x32_f16 v[80:83], a[76:79], v[124:127], v[80:83]\n"
      "v_fma_f32 v197, |v94|, v62, v197\n"
      "v_fma_f32 v197, |v95|, v63, v197\n"
      "v_mfma_f32_16x16x32_f16 v[84:87], a[92:95], v[124:127], v[36:39]\n"
      "ds_write_b64 v218, a[130:131] offset:8\n"
      "v_mfma_f32_16x16x32_f16 v[84:87], a[88:91], v[120:123], v[84:87]\n"
      "v_pk_mul_f16 v239, v255, v179\n"
      "v_add_f32 v190, v190, v196\n"
      "v_mfma_f32_16x16x32_f16 v[84:87], a[84:87], v[116:119], v[84:87]\n"
      "v_fma_f32 v197, |v80|, v64, v197\n"
      "v_fma_f32 v197, |v81|, v65, v197\n"
      "v_mfma_f32_16x16x32_f16 v[84:87], a[80:83], v[112:115], v[84:87]\n"
      "v_permlane16_swap_b32 v194, v190\n"
      "v_mfma_f32_16x16x32_f16 a[132:135], v[232:235], a[180:183], a[132:135]\n"
      "s_waitcnt lgkmcnt(2)\n"
      "v_pk_mul_f16 v108, v252, v172\n"
      "v_fma_f32 v197, |v82|, v66, v197\n"
      "v_mfma_f32_16x16x32_f16 v[88:91], a[96:99], v[112:115], v[40:43]\n"
      "v_fma_f32 v197, |v83|, v67, v197\n"
      "v_pk_mul_f16 v109, v253, v173\n"
      "v_mfma_f32_16x16x32_f16 v[88:91], a[100:103], v[116:119], v[88:91]\n"
      "v_pk_mul_f16 v110, v254, v174\n"
      "v_pk_mul_f16 v111, v255, v175\n"
      "v_mfma_f32_16x16x32_f16 v[88:91], a[104:107], v[120:123], v[88:91]\n"
      "ds_write_b64 v218, a[132:133] offset:1280\n"
      "v_mfma_f32_16x16x32_f16 v[88:91], a[108:111], v[124:127], v[88:91]\n"
      "v_fma_f32 v197, |v84|, v68, v197\n"
      "v_fma_f32 v197, |v85|, v69, v197\n"
      "v_mfma_f32_16x16x32_f16 v[92:95], a[124:127], v[124:127], v[44:47]\n"
      "ds_write_b64 v218, a[134:135] offset:1288\n"
      "v_mfma_f32_16x16x32_f16 v[92:95], a[120:123], v[120:123], v[92:95]\n"
      "v_fma_f32 v197, |v86|, v70, v197\n"
      "v_fma_f32 v197, |v87|, v71, v197\n"
      "v_mfma_f32_16x16x32_f16 v[92:95], a[116:119], v[116:119], v[92:95]\n"
      "ds_read_b128 v[160:163], v203 offset:6912\n"
      "v_fma_f32 v197, |v88|, v72, v197\n"
      "v_mfma_f32_16x16x32_f16 v[92:95], a[112:115], v[112:115], v[92:95]\n"
      "v_fma_f32 v197, |v89|, v73, v197\n"
      "v_fma_f32 v197, |v90|, v74, v197\n"
      "v_mfma_f32_16x16x32_f16 a[136:139], v[236:239], a[196:199], a[136:139]\n"
      "ds_read_b128 v[164:167], v203 offset:6976\n"
      "v_fma_f32 v197, |v91|, v75, v197\n"
      "v_mfma_f32_16x16x32_f16 v[80:83], a[0:3], v[96:99], v[16:19]\n"
      "ds_read_b128 v[168:171], v203 offset:7040\n"
      "v_add_f32 v194, v194, v190\n"
      "v_mfma_f32_16x16x32_f16 v[80:83], a[4:7], v[100:103], v[80:83]\n"
      "v_fma_f32 v197, |v92|, v76, v197\n"
      "v_fma_f32 v197, |v93|, v77, v197\n"
      "v_mfma_f32_16x16x32_f16 v[80:83], a[8:11], v[104:107], v[80:83]\n"
      "ds_write_b128 v218, a[136:139] offset:2560\n"
      "v_mfma_f32_16x16x32_f16 v[80:83], a[12:15], v[108:111], v[80:83]\n"
      "v_fma_f32 v197, |v94|, v78, v197\n"
      "v_fma_f32 v197, |v95|, v79, v197\n"
      "v_mfma_f32_16x16x32_f16 v[84:87], a[28:31], v[108:111], v[20:23]\n"
      "v_mfma_f32_16x16x32_f16 v[84:87], a[24:27], v[104:107], v[84:87]\n"
      "s_waitcnt lgkmcnt(3)\n"
      "v_pk_mul_f16 v112, v128, v160\n"
      "v_pk_mul_f16 v113, v129, v161\n"
      "v_mfma_f32_16x16x32_f16 v[84:87], a[20:23], v[100:103], v[84:87]\n"
      "v_permlane32_swap_b32 v191, v197\n"
      "v_mfma_f32_16x16x32_f16 v[84:87], a[16:19], v[96:99], v[84:87]\n"
      "v_pk_mul_f16 v114, v130, v162\n"
      "v_pk_mul_f16 v115, v131, v163\n"
      "v_mfma_f32_16x16x32_f16 v[88:91], a[32:35], v[96:99], v[24:27]\n"
      "v_mul_f32 v198, |v80|, v48\n"
      "v_fma_f32 v198, |v81|, v49, v198\n"
      "v_mfma_f32_16x16x32_f16 v[88:91], a[36:39], v[100:103], v[88:91]\n"
      "v_fma_f32 v198, |v82|, v50, v198\n"
      "v_fma_f32 v198, |v83|, v51, v198\n"
      "v_mfma_f32_16x16x32_f16 v[88:91], a[40:43], v[104:107], v[88:91]\n"
      "s_waitcnt lgkmcnt(2)\n"
      "v_pk_mul_f16 v116, v132, v164\n"
      "v_pk_mul_f16 v117, v133, v165\n"
      "v_mfma_f32_16x16x32_f16 v[88:91], a[44:47], v[108:111], v[88:91]\n"
      "v_pk_mul_f16 v118, v134, v166\n"
      "v_fma_f32 v198, |v84|, v52, v198\n"
      "v_mfma_f32_16x16x32_f16 v[92:95], a[60:63], v[108:111], v[28:31]\n"
      "v_fma_f32 v198, |v85|, v53, v198\n"
      "v_pk_mul_f16 v119, v135, v167\n"
      "v_mfma_f32_16x16x32_f16 v[92:95], a[56:59], v[104:107], v[92:95]\n"
      "v_fma_f32 v198, |v86|, v54, v198\n"
      "v_fma_f32 v198, |v87|, v55, v198\n"
      "v_mfma_f32_16x16x32_f16 v[92:95], a[52:55], v[100:103], v[92:95]\n"
      "s_waitcnt lgkmcnt(1)\n"
      "v_pk_mul_f16 v120, v136, v168\n"
      "v_fma_f32 v198, |v88|, v56, v198\n"
      "v_mfma_f32_16x16x32_f16 v[92:95], a[48:51], v[96:99], v[92:95]\n"
      "v_fma_f32 v198, |v89|, v57, v198\n"
      "v_pk_mul_f16 v121, v137, v169\n"
      "v_mfma_f32_16x16x32_f16 v[80:83], a[64:67], v[96:99], v[32:35]\n"
      "ds_read_b128 v[172:175], v203 offset:7104\n"
      "v_fma_f32 v198, |v90|, v58, v198\n"
      "v_mfma_f32_16x16x32_f16 v[80:83], a[68:71], v[100:103], v[80:83]\n"
      "v_fma_f32 v198, |v91|, v59, v198\n"
      "v_pk_mul_f16 v122, v138, v170\n"
      "v_mfma_f32_16x16x32_f16 v[80:83], a[72:75], v[104:107], v[80:83]\n"
      "v_pk_mul_f16 v123, v139, v171\n"
      "v_fma_f32 v198, |v92|, v60, v198\n"
      "v_mfma_f32_16x16x32_f16 v[80:83], a[76:79], v[108:111], v[80:83]\n"
      "v_fma_f32 v198, |v93|, v61, v198\n"
      "v_fma_f32 v198, |v94|, v62, v198\n"
      "v_mfma_f32_16x16x32_f16 v[84:87], a[92:95], v[108:111], v[36:39]\n"
      "v_fma_f32 v198, |v95|, v63, v198\n"
      "v_add_f32 v191, v191, v197\n"
      "v_mfma_f32_16x16x32_f16 v[84:87], a[88:91], v[104:107], v[84:87]\n"
      "ds_read2_b32 v[228:229], v204 offset0:0 offset1:8\n"
      "ds_read2_b32 v[230:231], v205 offset0:0 offset1:8\n"
      "v_mfma_f32_16x16x32_f16 v[84:87], a[84:87], v[100:103], v[84:87]\n"
      "v_permlane16_swap_b32 v195, v191\n"
      "v_mfma_f32_16x16x32_f16 v[84:87], a[80:83], v[96:99], v[84:87]\n"
      "v_fma_f32 v198, |v80|, v64, v198\n"
      "v_fma_f32 v198, |v81|, v65, v198\n"
      "v_mfma_f32_16x16x32_f16 v[88:91], a[96:99], v[96:99], v[40:43]\n"
      "s_waitcnt lgkmcnt(2)\n"
      "v_pk_mul_f16 v124, v140, v172\n"
      "v_fma_f32 v198, |v82|, v66, v198\n"
      "v_mfma_f32_16x16x32_f16 v[88:91], a[100:103], v[100:103], v[88:91]\n"
      "v_fma_f32 v198, |v83|, v67, v198\n"
      "v_pk_mul_f16 v125, v141, v173\n"
      "v_mfma_f32_16x16x32_f16 v[88:91], a[104:107], v[104:107], v[88:91]\n"
      "v_pk_mul_f16 v126, v142, v174\n"
      "v_pk_mul_f16 v127, v143, v175\n"
      "v_mfma_f32_16x16x32_f16 v[88:91], a[108:111], v[108:111], v[88:91]\n"
      "v_fma_f32 v198, |v84|, v68, v198\n"
      "v_fma_f32 v198, |v85|, v69, v198\n"
      "v_mfma_f32_16x16x32_f16 v[92:95], a[124:127], v[108:111], v[44:47]\n"
      "v_fma_f32 v198, |v86|, v70, v198\n"
      "v_fma_f32 v198, |v87|, v71, v198\n"
      "v_mfma_f32_16x16x32_f16 v[92:95], a[120:123], v[104:107], v[92:95]\n"
      "v_add_f32 v195, v195, v191\n"
      "v_pk_mul_f16 v104, v152, v168\n"
      "v_mfma_f32_16x16x32_f16 v[92:95], a[116:119], v[100:103], v[92:95]\n"
      "v_fma_f32 v198, |v88|, v72, v198\n"
      "v_fma_f32 v198, |v89|, v73, v198\n"
      "v_mfma_f32_16x16x32_f16 v[92:95], a[112:115], v[96:99], v[92:95]\n"
      "v_fma_f32 v198, |v90|, v74, v198\n"
      "v_fma_f32 v198, |v91|, v75, v198\n"
      "v_mfma_f32_16x16x32_f16 v[80:83], a[0:3], v[112:115], v[16:19]\n"
      "v_pk_mul_f16 v96, v144, v160\n"
      "v_pk_mul_f16 v97, v145, v161\n"
      "v_mfma_f32_16x16x32_f16 v[80:83], a[4:7], v[116:119], v[80:83]\n"
      "v_pk_mul_f16 v98, v146, v162\n"
      "v_pk_mul_f16 v99, v147, v163\n"
      "v_mfma_f32_16x16x32_f16 v[80:83], a[8:11], v[120:123], v[80:83]\n"
      "v_fma_f32 v198, |v92|, v76, v198\n"
      "v_fma_f32 v198, |v93|, v77, v198\n"
      "v_mfma_f32_16x16x32_f16 v[80:83], a[12:15], v[124:127], v[80:83]\n"
      "v_fma_f32 v198, |v94|, v78, v198\n"
      "v_fma_f32 v198, |v95|, v79, v198\n"
      "v_mfma_f32_16x16x32_f16 v[84:87], a[28:31], v[124:127], v[20:23]\n"
      "v_pk_mul_f16 v100, v148, v164\n"
      "v_pk_mul_f16 v101, v149, v165\n"
      "v_mfma_f32_16x16x32_f16 v[84:87], a[24:27], v[120:123], v[84:87]\n"
      "v_pk_mul_f16 v102, v150, v166\n"
      "v_pk_mul_f16 v103, v151, v167\n"
      "v_mfma_f32_16x16x32_f16 v[84:87], a[20:23], v[116:119], v[84:87]\n"
      "v_mul_f32 v192, |v80|, v48\n"
      "v_fma_f32 v192, |v81|, v49, v192\n"
      "v_mfma_f32_16x16x32_f16 v[84:87], a[16:19], v[112:115], v[84:87]\n"
      "v_fma_f32 v192, |v82|, v50, v192\n"
      "v_fma_f32 v192, |v83|, v51, v192\n"
      "v_mfma_f32_16x16x32_f16 v[88:91], a[32:35], v[112:115], v[24:27]\n"
      "v_pk_mul_f16 v105, v153, v169\n"
      "v_pk_mul_f16 v106, v154, v170\n"
      "v_mfma_f32_16x16x32_f16 v[88:91], a[36:39], v[116:119], v[88:91]\n"
      "ds_read_b32 v224, v227 offset:37640\n"
      "v_pk_mul_f16 v107, v155, v171\n"
      "v_mfma_f32_16x16x32_f16 v[88:91], a[40:43], v[120:123], v[88:91]\n"
      "v_fma_f32 v192, |v84|, v52, v192\n"
      "v_fma_f32 v192, |v85|, v53, v192\n"
      "v_mfma_f32_16x16x32_f16 v[88:91], a[44:47], v[124:127], v[88:91]\n"
      "v_fma_f32 v192, |v86|, v54, v192\n"
      "v_fma_f32 v192, |v87|, v55, v192\n"
      "v_mfma_f32_16x16x32_f16 v[92:95], a[60:63], v[124:127], v[28:31]\n"
      "ds_read2_b32 v[232:233], v206 offset0:0 offset1:8\n"
      "v_pk_mul_f16 v108, v156, v172\n"
      "v_mfma_f32_16x16x32_f16 v[92:95], a[56:59], v[120:123], v[92:95]\n"
      "ds_read2_b32 v[234:235], v207 offset0:0 offset1:8\n"
      "v_pk_mul_f16 v109, v157, v173\n"
      "v_mfma_f32_16x16x32_f16 v[92:95], a[52:55], v[116:119], v[92:95]\n"
      "v_fma_f32 v192, |v88|, v56, v192\n"
      "v_fma_f32 v192, |v89|, v57, v192\n"
      "v_mfma_f32_16x16x32_f16 v[92:95], a[48:51], v[112:115], v[92:95]\n"
      "v_fma_f32 v192, |v90|, v58, v192\n"
      "v_fma_f32 v192, |v91|, v59, v192\n"
      "v_mfma_f32_16x16x32_f16 v[80:83], a[64:67], v[112:115], v[32:35]\n"
      "ds_read2_b32 v[236:237], v208 offset0:0 offset1:8\n"
      "v_pk_mul_f16 v110, v158, v174\n"
      "v_mfma_f32_16x16x32_f16 v[80:83], a[68:71], v[116:119], v[80:83]\n"
      "ds_read2_b32 v[238:239], v209 offset0:0 offset1:8\n"
      "v_pk_mul_f16 v111, v159, v175\n"
      "v_mfma_f32_16x16x32_f16 v[80:83], a[72:75], v[120:123], v[80:83]\n"
      "v_fma_f32 v192, |v92|, v60, v192\n"
      "v_fma_f32 v192, |v93|, v61, v192\n"
      "v_mfma_f32_16x16x32_f16 v[80:83], a[76:79], v[124:127], v[80:83]\n"
      "v_fma_f32 v192, |v94|, v62, v192\n"
      "v_fma_f32 v192, |v95|, v63, v192\n"
      "v_mfma_f32_16x16x32_f16 v[84:87], a[92:95], v[124:127], v[36:39]\n"
      "s_waitcnt lgkmcnt(4)\n"
      "ds_read_b128 v[128:131], v210 offset:0\n"
      "v_cmp_eq_u32 vcc, 1, v224\n"
      "v_mfma_f32_16x16x32_f16 v[84:87], a[88:91], v[120:123], v[84:87]\n"
      "ds_read_b128 v[144:147], v211 offset:0\n"
      "ds_read_b128 v[132:135], v210 offset:64\n"
      "v_mfma_f32_16x16x32_f16 v[84:87], a[84:87], v[116:119], v[84:87]\n"
      "s_and_b32 s44, vcc_lo, s54\n"
      "v_fma_f32 v192, |v80|, v64, v192\n"
      "v_fma_f32 v192, |v81|, v65, v192\n"
      "v_mfma_f32_16x16x32_f16 v[84:87], a[80:83], v[112:115], v[84:87]\n"
      "s_cmp_lg_u32 s44, s54\n"
      "v_fma_f32 v192, |v82|, v66, v192\n"
      "v_fma_f32 v192, |v83|, v67, v192\n"
      "v_mfma_f32_16x16x32_f16 v[88:91], a[96:99], v[112:115], v[40:43]\n"
      "s_cbranch_scc1 80f\n"
      "81:\n"
      "v_mfma_f32_16x16x32_f16 v[88:91], a[100:103], v[116:119], v[88:91]\n"
      "ds_read_b128 v[148:151], v211 offset:64\n"
      "ds_read_b128 v[136:139], v210 offset:128\n"
      "v_mfma_f32_16x16x32_f16 v[88:91], a[104:107], v[120:123], v[88:91]\n"
      "v_fma_f32 v192, |v84|, v68, v192\n"
      "v_fma_f32 v192, |v85|, v69, v192\n"
      "v_mfma_f32_16x16x32_f16 v[88:91], a[108:111], v[124:127], v[88:91]\n"
      "v_fma_f32 v192, |v86|, v70, v192\n"
      "v_fma_f32 v192, |v87|, v71, v192\n"
      "v_mfma_f32_16x16x32_f16 v[92:95], a[124:127], v[124:127], v[44:47]\n"
      "v_pk_mul_f16 v124, v252, v172\n"
      "v_pk_mul_f16 v125, v253, v173\n"
      "v_mfma_f32_16x16x32_f16 v[92:95], a[120:123], v[120:123], v[92:95]\n"
      "v_pk_mul_f16 v120, v248, v168\n"
      "v_pk_mul_f16 v121, v249, v169\n"
      "v_mfma_f32_16x16x32_f16 v[92:95], a[116:119], v[116:119], v[92:95]\n"
      "v_fma_f32 v192, |v88|, v72, v192\n"
      "v_fma_f32 v192, |v89|, v73, v192\n"
      "v_mfma_f32_16x16x32_f16 v[92:95], a[112:115], v[112:115], v[92:95]\n"
      "v_fma_f32 v192, |v90|, v74, v192\n"
      "v_fma_f32 v192, |v91|, v75, v192\n"
      "v_mfma_f32_16x16x32_f16 v[80:83], a[0:3], v[96:99], v[16:19]\n"
      "v_pk_mul_f16 v112, v240, v160\n"
      "v_pk_mul_f16 v113, v241, v161\n"
      "v_mfma_f32_16x16x32_f16 v[80:83], a[4:7], v[100:103], v[80:83]\n"
      "v_pk_mul_f16 v114, v242, v162\n"
      "v_pk_mul_f16 v115, v243, v163\n"
      "v_mfma_f32_16x16x32_f16 v[80:83], a[8:11], v[104:107], v[80:83]\n"
      "v_fma_f32 v192, |v92|, v76, v192\n"
      "v_fma_f32 v192, |v93|, v77, v192\n"
      "v_mfma_f32_16x16x32_f16 v[80:83], a[12:15], v[108:111], v[80:83]\n"
      "v_fma_f32 v192, |v94|, v78, v192\n"
      "v_fma_f32 v192, |v95|, v79, v192\n"
      "v_mfma_f32_16x16x32_f16 v[84:87], a[28:31], v[108:111], v[20:23]\n"
      "v_pk_mul_f16 v116, v244, v164\n"
      "v_pk_mul_f16 v117, v245, v165\n"
      "v_mfma_f32_16x16x32_f16 v[84:87], a[24:27], v[104:107], v[84:87]\n"
      "v_pk_mul_f16 v118, v246, v166\n"
      "v_pk_mul_f16 v119, v247, v167\n"
      "v_mfma_f32_16x16x32_f16 v[84:87], a[20:23], v[100:103], v[84:87]\n"
      "v_mul_f32 v193, |v80|, v48\n"
      "v_fma_f32 v193, |v81|, v49, v193\n"
      "v_mfma_f32_16x16x32_f16 v[84:87], a[16:19], v[96:99], v[84:87]\n"
      "v_fma_f32 v193, |v82|, v50, v193\n"
      "v_fma_f32 v193, |v83|, v51, v193\n"
      "v_mfma_f32_16x16x32_f16 v[88:91], a[32:35], v[96:99], v[24:27]\n"
      "v_pk_mul_f16 v122, v250, v170\n"
      "v_pk_mul_f16 v123, v251, v171\n"
      "v_mfma_f32_16x16x32_f16 v[88:91], a[36:39], v[100:103], v[88:91]\n"
      "v_pk_mul_f16 v126, v254, v174\n"
      "v_pk_mul_f16 v127, v255, v175\n"
      "v_mfma_f32_16x16x32_f16 v[88:91], a[40:43], v[104:107], v[88:91]\n"
      "v_fma_f32 v193, |v84|, v52, v193\n"
      "v_fma_f32 v193, |v85|, v53, v193\n"
      "v_mfma_f32_16x16x32_f16 v[88:91], a[44:47], v[108:111], v[88:91]\n"
      "v_fma_f32 v193, |v86|, v54, v193\n"
      "v_fma_f32 v193, |v87|, v55, v193\n"
      "v_mfma_f32_16x16x32_f16 v[92:95], a[60:63], v[108:111], v[28:31]\n"
      "ds_read_b128 v[160:163], v10 offset:0\n"
      "ds_read_b128 v[240:243], v11 offset:0\n"
      "v_mfma_f32_16x16x32_f16 v[92:95], a[56:59], v[104:107], v[92:95]\n"
      "ds_read_b128 v[164:167], v10 offset:64\n"
      "ds_read_b128 v[244:247], v11 offset:64\n"
      "v_mfma_f32_16x16x32_f16 v[92:95], a[52:55], v[100:103], v[92:95]\n"
      "v_fma_f32 v193, |v88|, v56, v193\n"
      "v_fma_f32 v193, |v89|, v57, v193\n"
      "v_mfma_f32_16x16x32_f16 v[92:95], a[48:51], v[96:99], v[92:95]\n"
      "v_fma_f32 v193, |v90|, v58, v193\n"
      "v_fma_f32 v193, |v91|, v59, v193\n"
      "v_mfma_f32_16x16x32_f16 v[80:83], a[64:67], v[96:99], v[32:35]\n"
      "ds_read_b128 v[168:171], v10 offset:128\n"
      "ds_read_b128 v[248:251], v11 offset:128\n"
      "v_mfma_f32_16x16x32_f16 v[80:83], a[68:71], v[100:103], v[80:83]\n"
      "ds_read_b128 v[172:175], v10 offset:192\n"
      "ds_read_b128 v[252:255], v11 offset:192\n"
      "v_mfma_f32_16x16x32_f16 v[80:83], a[72:75], v[104:107], v[80:83]\n"
      "v_fma_f32 v193, |v92|, v60, v193\n"
      "v_fma_f32 v193, |v93|, v61, v193\n"
      "v_mfma_f32_16x16x32_f16 v[80:83], a[76:79], v[108:111], v[80:83]\n"
      "v_fma_f32 v193, |v94|, v62, v193\n"
      "v_fma_f32 v193, |v95|, v63, v193\n"
      "v_mfma_f32_16x16x32_f16 v[84:87], a[92:95], v[108:111], v[36:39]\n"
      "s_waitcnt lgkmcnt(6)\n"
      "v_pk_mul_f16 v160, v160, v240\n"
      "v_pk_mul_f16 v161, v161, v241\n"
      "v_mfma_f32_16x16x32_f16 v[84:87], a[88:91], v[104:107], v[84:87]\n"
      "v_pk_mul_f16 v162, v162, v242\n"
      "v_pk_mul_f16 v163, v163, v243\n"
      "v_mfma_f32_16x16x32_f16 v[84:87], a[84:87], v[100:103], v[84:87]\n"
      "43:\n"
      "s_cbranch_execz 44b\n"
      "v_fma_f32 v193, |v80|, v64, v193\n"
      "v_mfma_f32_16x16x32_f16 v[84:87], a[80:83], v[96:99], v[84:87]\n"
      "v_fma_f32 v193, |v81|, v65, v193\n"
      "v_fma_f32 v193, |v82|, v66, v193\n"
      "v_mfma_f32_16x16x32_f16 v[88:91], a[96:99], v[96:99], v[40:43]\n"
      "s_waitcnt lgkmcnt(4)\n"
      "v_fma_f32 v193, |v83|, v67, v193\n"
      "v_pk_mul_f16 v164, v164, v244\n"
      "v_mfma_f32_16x16x32_f16 v[88:91], a[100:103], v[100:103], v[88:91]\n"
      "v_pk_mul_f16 v165, v165, v245\n"
      "v_pk_mul_f16 v166, v166, v246\n"
      "v_mfma_f32_16x16x32_f16 v[88:91], a[104:107], v[104:107], v[88:91]\n"
      "v_fma_f32 v193, |v84|, v68, v193\n"
      "v_fma_f32 v193, |v85|, v69, v193\n"
      "v_mfma_f32_16x16x32_f16 v[88:91], a[108:111], v[108:111], v[88:91]\n"
      "v_fma_f32 v193, |v86|, v70, v193\n"
      "v_fma_f32 v193, |v87|, v71, v193\n"
      "v_mfma_f32_16x16x32_f16 v[92:95], a[124:127], v[108:111], v[44:47]\n"
      "s_waitcnt lgkmcnt(2)\n"
      "v_pk_mul_f16 v167, v167, v247\n"
      "v_pk_mul_f16 v168, v168, v248\n"
      "v_mfma_f32_16x16x32_f16 v[92:95], a[120:123], v[104:107], v[92:95]\n"
      "v_pk_mul_f16 v169, v169, v249\n"
      "v_pk_mul_f16 v170, v170, v250\n"
      "v_mfma_f32_16x16x32_f16 v[92:95], a[116:119], v[100:103], v[92:95]\n"
      "v_fma_f32 v193, |v88|, v72, v193\n"
      "v_fma_f32 v193, |v89|, v73, v193\n"
      "v_mfma_f32_16x16x32_f16 v[92:95], a[112:115], v[96:99], v[92:95]\n"
      "v_fma_f32 v193, |v90|, v74, v193\n"
      "v_fma_f32 v193, |v91|, v75, v193\n"
      "v_mfma_f32_16x16x32_f16 v[80:83], a[0:3], v[112:115], v[16:19]\n"
      "v_pk_mul_f16 v128, v128, v144\n"
      "v_pk_mul_f16 v171, v171, v251\n"
      "v_mfma_f32_16x16x32_f16 v[80:83], a[4:7], v[116:119], v[80:83]\n"
      "v_pk_mul_f16 v129, v129, v145\n"
      "v_pk_mul_f16 v130, v130, v146\n"
      "v_mfma_f32_16x16x32_f16 v[80:83], a[8:11], v[120:123], v[80:83]\n"
      "v_fma_f32 v193, |v92|, v76, v193\n"
      "v_fma_f32 v193, |v93|, v77, v193\n"
      "v_mfma_f32_16x16x32_f16 v[80:83], a[12:15], v[124:127], v[80:83]\n"
      "v_fma_f32 v193, |v94|, v78, v193\n"
      "v_fma_f32 v193, |v95|, v79, v193\n"
      "v_mfma_f32_16x16x32_f16 v[84:87], a[28:31], v[124:127], v[20:23]\n"
      "s_waitcnt lgkmcnt(0)\n"
      "v_pk_mul_f16 v131, v131, v147\n"
      "v_pk_mul_f16 v172, v172, v252\n"
      "v_mfma_f32_16x16x32_f16 v[84:87], a[24:27], v[120:123], v[84:87]\n"
      "ds_read_b128 v[152:155], v211 offset:128\n"
      "v_pk_mul_f16 v173, v173, v253\n"
      "v_mfma_f32_16x16x32_f16 v[84:87], a[20:23], v[116:119], v[84:87]\n"
      "v_mul_f32 v190, |v80|, v48\n"
      "v_fma_f32 v190, |v81|, v49, v190\n"
      "v_mfma_f32_16x16x32_f16 v[84:87], a[16:19], v[112:115], v[84:87]\n"
      "v_fma_f32 v190, |v82|, v50, v190\n"
      "v_fma_f32 v190, |v83|, v51, v190\n"
      "v_mfma_f32_16x16x32_f16 v[88:91], a[32:35], v[112:115], v[24:27]\n"
      "v_pk_mul_f16 v174, v174, v254\n"
      "v_pk_mul_f16 v132, v132, v148\n"
      "v_mfma_f32_16x16x32_f16 v[88:91], a[36:39], v[116:119], v[88:91]\n"
      "v_pk_mul_f16 v175, v175, v255\n"
      "v_pk_mul_f16 v133, v133, v149\n"
      "v_mfma_f32_16x16x32_f16 v[88:91], a[40:43], v[120:123], v[88:91]\n"
      "v_fma_f32 v190, |v84|, v52, v190\n"
      "v_fma_f32 v190, |v85|, v53, v190\n"
      "v_mfma_f32_16x16x32_f16 v[88:91], a[44:47], v[124:127], v[88:91]\n"
      "v_fma_f32 v190, |v86|, v54, v190\n"
      "v_fma_f32 v190, |v87|, v55, v190\n"
      "v_mfma_f32_16x16x32_f16 v[92:95], a[60:63], v[124:127], v[28:31]\n"
      "v_fmac_f32 v189, v200, v228\n"
      "v_pk_mul_f16 v134, v134, v150\n"
      "v_mfma_f32_16x16x32_f16 v[92:95], a[56:59], v[120:123], v[92:95]\n"
      "v_permlane32_swap_b32 v198, v192\n"
      "v_mfma_f32_16x16x32_f16 v[92:95], a[52:55], v[116:119], v[92:95]\n"
      "v_fma_f32 v190, |v88|, v56, v190\n"
      "v_fma_f32 v190, |v89|, v57, v190\n"
      "v_mfma_f32_16x16x32_f16 v[92:95], a[48:51], v[112:115], v[92:95]\n"
      "v_fma_f32 v190, |v90|, v58, v190\n"
      "v_fma_f32 v190, |v91|, v59, v190\n"
      "v_mfma_f32_16x16x32_f16 v[80:83], a[64:67], v[112:115], v[32:35]\n"
      "v_fmac_f32 v194, v200, v230\n"
      "v_pk_mul_f16 v135, v135, v151\n"
      "v_mfma_f32_16x16x32_f16 v[80:83], a[68:71], v[116:119], v[80:83]\n"
      "v_fmac_f32 v195, v200, v232\n"
      "v_max3_f32 v219, v189, v194, v195\n"
      "v_mfma_f32_16x16x32_f16 v[80:83], a[72:75], v[120:123], v[80:83]\n"
      "v_fma_f32 v190, |v92|, v60, v190\n"
      "v_fma_f32 v190, |v93|, v61, v190\n"
      "v_mfma_f32_16x16x32_f16 v[80:83], a[76:79], v[124:127], v[80:83]\n"
      "v_fma_f32 v190, |v94|, v62, v190\n"
      "v_fma_f32 v190, |v95|, v63, v190\n"
      "v_mfma_f32_16x16x32_f16 v[84:87], a[92:95], v[124:127], v[36:39]\n"
      "ds_read_b128 v[140:143], v210 offset:192\n"
      "v_min3_f32 v226, v189, v194, v195\n"
      "v_mfma_f32_16x16x32_f16 v[84:87], a[88:91], v[120:123], v[84:87]\n"
      "ds_read_b128 v[156:159], v211 offset:192\n"
      "v_sub_f32 v189, v189, v219\n"
      "v_mfma_f32_16x16x32_f16 v[84:87], a[84:87], v[116:119], v[84:87]\n"
      "v_fma_f32 v190, |v80|, v64, v190\n"
      "v_fma_f32 v190, |v81|, v65, v190\n"
      "v_mfma_f32_16x16x32_f16 v[84:87], a[80:83], v[112:115], v[84:87]\n"
      "v_fma_f32 v190, |v82|, v66, v190\n"
      "v_fma_f32 v190, |v83|, v67, v190\n"
      "v_mfma_f32_16x16x32_f16 v[88:91], a[96:99], v[112:115], v[40:43]\n"
      "s_waitcnt lgkmcnt(2)\n"
      "v_pk_mul_f16 v136, v136, v152\n"
      "v_sub_f32 v194, v194, v219\n"
      "v_mfma_f32_16x16x32_f16 v[88:91], a[100:103], v[116:119], v[88:91]\n"
      "v_pk_mul_f16 v137, v137, v153\n"
      "v_sub_f32 v195, v195, v219\n"
      "v_mfma_f32_16x16x32_f16 v[88:91], a[104:107], v[120:123], v[88:91]\n"
      "v_exp_f32 v189, v189\n"
      "v_mfma_f32_16x16x32_f16 v[88:91], a[108:111], v[124:127], v[88:91]\n"
      "v_exp_f32 v194, v194\n"
      "v_mfma_f32_16x16x32_f16 v[92:95], a[124:127], v[124:127], v[44:47]\n"
      "v_exp_f32 v195, v195\n"
      "v_mfma_f32_16x16x32_f16 v[92:95], a[120:123], v[120:123], v[92:95]\n"
      "v_fma_f32 v190, |v84|, v68, v190\n"
      "v_fma_f32 v190, |v85|, v69, v190\n"
      "v_mfma_f32_16x16x32_f16 v[92:95], a[116:119], v[116:119], v[92:95]\n"
      "v_fma_f32 v190, |v86|, v70, v190\n"
      "v_fma_f32 v190, |v87|, v71, v190\n"
      "v_mfma_f32_16x16x32_f16 v[92:95], a[112:115], v[112:115], v[92:95]\n"
      "v_fma_f32 v190, |v88|, v72, v190\n"
      "v_fma_f32 v190, |v89|, v73, v190\n"
      "v_mfma_f32_16x16x32_f16 v[80:83], a[0:3], v[160:163], v[16:19]\n"
      "v_fma_f32 v190, |v90|, v74, v190\n"
      "v_fma_f32 v190, |v91|, v75, v190\n"
      "v_mfma_f32_16x16x32_f16 v[80:83], a[4:7], v[164:167], v[80:83]\n"
      "v_pk_mul_f16 v138, v138, v154\n"
      "v_pk_mul_f16 v139, v139, v155\n"
      "v_mfma_f32_16x16x32_f16 v[80:83], a[8:11], v[168:171], v[80:83]\n"
      "v_fma_f32 v190, |v92|, v76, v190\n"
      "v_fma_f32 v190, |v93|, v77, v190\n"
      "v_mfma_f32_16x16x32_f16 v[80:83], a[12:15], v[172:175], v[80:83]\n"
      "v_fma_f32 v190, |v94|, v78, v190\n"
      "v_fma_f32 v190, |v95|, v79, v190\n"
      "v_mfma_f32_16x16x32_f16 v[84:87], a[28:31], v[172:175], v[20:23]\n"
      "v_mul_f32 v221, v189, v229\n"
      "v_add_f32 v198, v198, v192\n"
      "v_mfma_f32_16x16x32_f16 v[84:87], a[24:27], v[168:171], v[84:87]\n"
      "v_permlane32_swap_b32 v193, v190\n"
      "v_mfma_f32_16x16x32_f16 v[84:87], a[20:23], v[164:167], v[84:87]\n"
      "v_permlane16_swap_b32 v188, v198\n"
      "v_mfma_f32_16x16x32_f16 v[84:87], a[16:19], v[160:163], v[84:87]\n"
      "v_add_f32 v220, v189, v194\n"
      "v_mul_f32 v199, |v80|, v48\n"
      "v_mfma_f32_16x16x32_f16 v[88:91], a[32:35], v[160:163], v[24:27]\n"
      "s_waitcnt lgkmcnt(0)\n"
      "v_fma_f32 v199, |v81|, v49, v199\n"
      "v_pk_mul_f16 v140, v140, v156\n"
      "v_mfma_f32_16x16x32_f16 v[88:91], a[36:39], v[164:167], v[88:91]\n"
      "v_fmac_f32 v221, v194, v231\n"
      "v_fma_f32 v199, |v82|, v50, v199\n"
      "v_mfma_f32_16x16x32_f16 v[88:91], a[40:43], v[168:171], v[88:91]\n"
      "v_fma_f32 v199, |v83|, v51, v199\n"
      "v_pk_mul_f16 v141, v141, v157\n"
      "v_mfma_f32_16x16x32_f16 v[88:91], a[44:47], v[172:175], v[88:91]\n"
      "v_add_f32 v220, v220, v195\n"
      "v_add_f32 v188, v188, v198\n"
      "v_mfma_f32_16x16x32_f16 v[92:95], a[60:63], v[172:175], v[28:31]\n"
      "v_pk_mul_f16 v142, v142, v158\n"
      "v_fmac_f32 v221, v195, v233\n"
      "v_mfma_f32_16x16x32_f16 v[92:95], a[56:59], v[168:171], v[92:95]\n"
      "v_fmac_f32 v188, v200, v234\n"
      "v_pk_mul_f16 v143, v143, v159\n"
      "v_mfma_f32_16x16x32_f16 v[92:95], a[52:55], v[164:167], v[92:95]\n"
      "v_fma_f32 v199, |v84|, v52, v199\n"
      "v_fma_f32 v199, |v85|, v53, v199\n"
      "v_mfma_f32_16x16x32_f16 v[92:95], a[48:51], v[160:163], v[92:95]\n"
      "v_fma_f32 v199, |v86|, v54, v199\n"
      "v_fma_f32 v199, |v87|, v55, v199\n"
      "v_mfma_f32_16x16x32_f16 v[80:83], a[0:3], v[128:131], v[16:19]\n"
      "v_fma_f32 v199, |v88|, v56, v199\n"
      "v_fma_f32 v199, |v89|, v57, v199\n"
      "v_mfma_f32_16x16x32_f16 v[80:83], a[4:7], v[132:135], v[80:83]\n"
      "v_fma_f32 v199, |v90|, v58, v199\n"
      "v_fma_f32 v199, |v91|, v59, v199\n"
      "v_mfma_f32_16x16x32_f16 v[80:83], a[8:11], v[136:139], v[80:83]\n"
      "ds_read_b128 v[112:115], v216 offset:0\n"
      "ds_read_b128 v[144:147], v217 offset:0\n"
      "v_mfma_f32_16x16x32_f16 v[80:83], a[12:15], v[140:143], v[80:83]\n"
      "v_fma_f32 v199, |v92|, v60, v199\n"
      "v_fma_f32 v199, |v93|, v61, v199\n"
      "v_mfma_f32_16x16x32_f16 v[84:87], a[28:31], v[140:143], v[20:23]\n"
      "v_fma_f32 v199, |v94|, v62, v199\n"
      "v_fma_f32 v199, |v95|, v63, v199\n"
      "v_mfma_f32_16x16x32_f16 v[84:87], a[24:27], v[136:139], v[84:87]\n"
      "ds_write_b32 v1, v199 offset:12736\n"
      "v_mfma_f32_16x16x32_f16 v[84:87], a[20:23], v[132:135], v[84:87]\n"
      "ds_read_b128 v[116:119], v216 offset:64\n"
      "ds_read_b128 v[148:151], v217 offset:64\n"
      "v_mfma_f32_16x16x32_f16 v[84:87], a[16:19], v[128:131], v[84:87]\n"
      "v_mul_f32 v191, |v80|, v48\n"
      "v_fma_f32 v191, |v81|, v49, v191\n"
      "v_mfma_f32_16x16x32_f16 v[180:183], a[200:203], v[160:163], 0\n"
      "v_fma_f32 v191, |v82|, v50, v191\n"
      "v_fma_f32 v191, |v83|, v51, v191\n"
      "v_mfma_f32_16x16x32_f16 v[88:91], a[32:35], v[128:131], v[24:27]\n"
      "ds_read_b128 v[120:123], v216 offset:128\n"
      "ds_read_b128 v[152:155], v217 offset:128\n"
      "v_mfma_f32_16x16x32_f16 v[88:91], a[36:39], v[132:135], v[88:91]\n"
      "s_waitcnt lgkmcnt(5)\n"
      "v_pk_mul_f16 v112, v112, v144\n"
      "v_pk_mul_f16 v113, v113, v145\n"
      "v_mfma_f32_16x16x32_f16 v[88:91], a[40:43], v[136:139], v[88:91]\n"
      "v_pk_mul_f16 v114, v114, v146\n"
      "v_fma_f32 v191, |v84|, v52, v191\n"
      "v_mfma_f32_16x16x32_f16 v[88:91], a[44:47], v[140:143], v[88:91]\n"
      "v_fma_f32 v191, |v85|, v53, v191\n"
      "v_pk_mul_f16 v115, v115, v147\n"
      "v_mfma_f32_16x16x32_f16 v[92:95], a[60:63], v[140:143], v[28:31]\n"
      "v_fma_f32 v191, |v86|, v54, v191\n"
      "v_fma_f32 v191, |v87|, v55, v191\n"
      "v_mfma_f32_16x16x32_f16 v[92:95], a[56:59], v[136:139], v[92:95]\n"
      "s_waitcnt lgkmcnt(2)\n"
      "v_pk_mul_f16 v116, v116, v148\n"
      "v_pk_mul_f16 v117, v117, v149\n"
      "v_mfma_f32_16x16x32_f16 v[92:95], a[52:55], v[132:135], v[92:95]\n"
      "v_fma_f32 v191, |v88|, v56, v191\n"
      "v_fma_f32 v191, |v89|, v57, v191\n"
      "v_mfma_f32_16x16x32_f16 v[92:95], a[48:51], v[128:131], v[92:95]\n"
      "ds_read_b128 v[124:127], v216 offset:192\n"
      "v_pk_mul_f16 v118, v118, v150\n"
      "v_mfma_f32_16x16x32_f16 v[180:183], a[204:207], v[164:167], v[180:183]\n"
      "ds_read_b128 v[156:159], v217 offset:192\n"
      "v_fma_f32 v191, |v90|, v58, v191\n"
      "v_mfma_f32_16x16x32_f16 v[80:83], a[64:67], v[128:131], v[32:35]\n"
      "v_fma_f32 v191, |v91|, v59, v191\n"
      "v_pk_mul_f16 v119, v119, v151\n"
      "v_mfma_f32_16x16x32_f16 v[80:83], a[68:71], v[132:135], v[80:83]\n"
      "s_waitcnt lgkmcnt(2)\n"
      "v_pk_mul_f16 v120, v120, v152\n"
      "v_fma_f32 v191, |v92|, v60, v191\n"
      "v_mfma_f32_16x16x32_f16 v[80:83], a[72:75], v[136:139], v[80:83]\n"
      "v_fma_f32 v191, |v93|, v61, v191\n"
      "v_pk_mul_f16 v121, v121, v153\n"
      "v_mfma_f32_16x16x32_f16 v[80:83], a[76:79], v[140:143], v[80:83]\n"
      "v_fma_f32 v191, |v94|, v62, v191\n"
      "v_fma_f32 v191, |v95|, v63, v191\n"
      "v_mfma_f32_16x16x32_f16 v[84:87], a[92:95], v[140:143], v[36:39]\n"
      "v_pk_mul_f16 v122, v122, v154\n"
      "v_pk_mul_f16 v123, v123, v155\n"
      "v_mfma_f32_16x16x32_f16 v[84:87], a[88:91], v[136:139], v[84:87]\n"
      "ds_read_b32 v224, v12 offset:12736\n"
      "v_add_f32 v193, v193, v190\n"
      "v_mfma_f32_16x16x32_f16 v[84:87], a[84:87], v[132:135], v[84:87]\n"
      "v_fma_f32 v191, |v80|, v64, v191\n"
      "v_fma_f32 v191, |v81|, v65, v191\n"
      "v_mfma_f32_16x16x32_f16 v[84:87], a[80:83], v[128:131], v[84:87]\n"
      "v_permlane16_swap_b32 v193, v7\n"
      "v_mfma_f32_16x16x32_f16 v[88:91], a[96:99], v[128:131], v[40:43]\n"
      "s_waitcnt lgkmcnt(1)\n"
      "v_pk_mul_f16 v124, v124, v156\n"
      "v_fma_f32 v191, |v82|, v66, v191\n"
      "v_mfma_f32_16x16x32_f16 v[88:91], a[100:103], v[132:135], v[88:91]\n"
      "v_fma_f32 v191, |v83|, v67, v191\n"
      "v_pk_mul_f16 v125, v125, v157\n"
      "v_mfma_f32_16x16x32_f16 v[88:91], a[104:107], v[136:139], v[88:91]\n"
      "v_pk_mul_f16 v126, v126, v158\n"
      "v_pk_mul_f16 v127, v127, v159\n"
      "v_mfma_f32_16x16x32_f16 v[88:91], a[108:111], v[140:143], v[88:91]\n"
      "v_fma_f32 v191, |v84|, v68, v191\n"
      "v_fma_f32 v191, |v85|, v69, v191\n"
      "v_mfma_f32_16x16x32_f16 v[92:95], a[124:127], v[140:143], v[44:47]\n"
      "v_fma_f32 v191, |v86|, v70, v191\n"
      "v_fma_f32 v191, |v87|, v71, v191\n"
      "v_mfma_f32_16x16x32_f16 v[92:95], a[120:123], v[136:139], v[92:95]\n"
      "v_add_f32 v193, v193, v7\n"
      "v_fmac_f32 v193, v200, v236\n"
      "v_mfma_f32_16x16x32_f16 v[92:95], a[116:119], v[132:135], v[92:95]\n"
      "v_fma_f32 v191, |v88|, v72, v191\n"
      "v_fma_f32 v191, |v89|, v73, v191\n"
      "v_mfma_f32_16x16x32_f16 v[92:95], a[112:115], v[128:131], v[92:95]\n"
      "v_fma_f32 v191, |v90|, v74, v191\n"
      "v_fma_f32 v191, |v91|, v75, v191\n"
      "v_mfma_f32_16x16x32_f16 v[80:83], a[0:3], v[112:115], v[16:19]\n"
      "v_max3_f32 v222, v219, v188, v193\n"
      "v_min3_f32 v226, v226, v188, v193\n"
      "v_mfma_f32_16x16x32_f16 v[80:83], a[4:7], v[116:119], v[80:83]\n"
      "v_sub_f32 v219, v219, v222\n"
      "v_sub_f32 v188, v188, v222\n"
      "v_mfma_f32_16x16x32_f16 v[80:83], a[8:11], v[120:123], v[80:83]\n"
      "v_fma_f32 v191, |v92|, v76, v191\n"
      "v_fma_f32 v191, |v93|, v77, v191\n"
      "v_mfma_f32_16x16x32_f16 v[80:83], a[12:15], v[124:127], v[80:83]\n"
      "v_fma_f32 v191, |v94|, v78, v191\n"
      "v_fma_f32 v191, |v95|, v79, v191\n"
      "v_mfma_f32_16x16x32_f16 v[84:87], a[28:31], v[124:127], v[20:23]\n"
      "s_waitcnt lgkmcnt(0)\n"
      "v_sub_f32 v193, v193, v222\n"
      "v_cmp_eq_u32 vcc, s37, v224\n"
      "v_mfma_f32_16x16x32_f16 v[84:87], a[24:27], v[120:123], v[84:87]\n"
      "v_exp_f32 v219, v219\n"
      "v_mfma_f32_16x16x32_f16 v[84:87], a[20:23], v[116:119], v[84:87]\n"
      "s_or_b32 vcc_lo, vcc_lo, s36\n"
      "v_exp_f32 v188, v188\n"
      "v_mfma_f32_16x16x32_f16 v[84:87], a[16:19], v[112:115], v[84:87]\n"
      "s_cmp_lg_u64 vcc, 0\n"
      "v_exp_f32 v193, v193\n"
      "v_mfma_f32_16x16x32_f16 v[88:91], a[32:35], v[112:115], v[24:27]\n"
      "s_cbranch_scc1 82f\n"
      "83:\n"
      "v_mfma_f32_16x16x32_f16 v[88:91], a[36:39], v[116:119], v[88:91]\n"
      "v_mul_f32 v196, |v80|, v48\n"
      "v_fma_f32 v196, |v81|, v49, v196\n"
      "v_mfma_f32_16x16x32_f16 v[88:91], a[40:43], v[120:123], v[88:91]\n"
      "v_fma_f32 v196, |v82|, v50, v196\n"
      "v_fma_f32 v196, |v83|, v51, v196\n"
      "v_mfma_f32_16x16x32_f16 v[88:91], a[44:47], v[124:127], v[88:91]\n"
      "v_fma_f32 v196, |v84|, v52, v196\n"
      "v_fma_f32 v196, |v85|, v53, v196\n"
      "v_mfma_f32_16x16x32_f16 v[92:95], a[60:63], v[124:127], v[28:31]\n"
      "v_fma_f32 v196, |v86|, v54, v196\n"
      "v_fma_f32 v196, |v87|, v55, v196\n"
      "v_mfma_f32_16x16x32_f16 v[92:95], a[56:59], v[120:123], v[92:95]\n"
      "v_mul_f32 v221, v221, v219\n"
      "v_fma_f32 v220, v220, v219, v188\n"
      "v_mfma_f32_16x16x32_f16 v[92:95], a[52:55], v[116:119], v[92:95]\n"
      "v_fma_f32 v196, |v88|, v56, v196\n"
      "v_fma_f32 v196, |v89|, v57, v196\n"
      "v_mfma_f32_16x16x32_f16 v[92:95], a[48:51], v[112:115], v[92:95]\n"
      "v_fma_f32 v196, |v90|, v58, v196\n"
      "v_fma_f32 v196, |v91|, v59, v196\n"
      "v_mfma_f32_16x16x32_f16 v[80:83], a[64:67], v[112:115], v[32:35]\n"
      "v_fmac_f32 v221, v188, v235\n"
      "v_add_f32 v220, v220, v193\n"
      "v_mfma_f32_16x16x32_f16 v[80:83], a[68:71], v[116:119], v[80:83]\n"
      "v_fmac_f32 v221, v193, v237\n"
      "v_add_f32 v199, v199, v224\n"
      "v_mfma_f32_16x16x32_f16 v[80:83], a[72:75], v[120:123], v[80:83]\n"
      "v_fma_f32 v196, |v92|, v60, v196\n"
      "v_fma_f32 v196, |v93|, v61, v196\n"
      "v_mfma_f32_16x16x32_f16 v[80:83], a[76:79], v[124:127], v[80:83]\n"
      "v_fma_f32 v196, |v94|, v62, v196\n"
      "v_fma_f32 v196, |v95|, v63, v196\n"
      "v_mfma_f32_16x16x32_f16 v[84:87], a[92:95], v[124:127], v[36:39]\n"
      "v_fma_f32 v220, v226, 0, v220\n"
      "v_cndmask_b32 v199, v8, v199, s[100:101]\n"
      "v_mfma_f32_16x16x32_f16 v[84:87], a[88:91], v[120:123], v[84:87]\n"
      "v_mfma_f32_16x16x32_f16 v[84:87], a[84:87], v[116:119], v[84:87]\n"
      "s_nop 0\n"
      "v_fma_f32 v196, |v80|, v64, v196\n"
      "v_fma_f32 v196, |v81|, v65, v196\n"
      "v_mfma_f32_16x16x32_f16 v[84:87], a[80:83], v[112:115], v[84:87]\n"
      "v_fma_f32 v196, |v82|, v66, v196\n"
      "v_fma_f32 v196, |v83|, v67, v196\n"
      "v_mfma_f32_16x16x32_f16 v[88:91], a[96:99], v[112:115], v[40:43]\n"
      "v_permlane32_swap_b32 v199, v191\n"
      "v_mfma_f32_16x16x32_f16 v[88:91], a[100:103], v[116:119], v[88:91]\n"
      "v_add_f32 v199, v199, v191\n"
      "v_mfma_f32_16x16x32_f16 v[88:91], a[104:107], v[120:123], v[88:91]\n"
      "s_nop 0\n"
      "v_fma_f32 v196, |v84|, v68, v196\n"
      "v_fma_f32 v196, |v85|, v69, v196\n"
      "v_mfma_f32_16x16x32_f16 v[88:91], a[108:111], v[124:127], v[88:91]\n"
      "v_fma_f32 v196, |v86|, v70, v196\n"
      "v_fma_f32 v196, |v87|, v71, v196\n"
      "v_mfma_f32_16x16x32_f16 v[92:95], a[124:127], v[124:127], v[44:47]\n"
      "v_mfma_f32_16x16x32_f16 v[92:95], a[120:123], v[120:123], v[92:95]\n"
      "v_mfma_f32_16x16x32_f16 v[92:95], a[116:119], v[116:119], v[92:95]\n"
      "s_nop 2\n"
      "v_fma_f32 v196, |v88|, v72, v196\n"
      "v_fma_f32 v196, |v89|, v73, v196\n"
      "v_mfma_f32_16x16x32_f16 v[92:95], a[112:115], v[112:115], v[92:95]\n"
      "v_fma_f32 v196, |v90|, v74, v196\n"
      "v_fma_f32 v196, |v91|, v75, v196\n"
      "v_mfma_f32_16x16x32_f16 v[180:183], a[208:211], v[168:171], v[180:183]\n"
      "v_mfma_f32_16x16x32_f16 v[180:183], a[212:215], v[172:175], v[180:183]\n"
      "s_nop 3\n"
      "v_fma_f32 v196, |v92|, v76, v196\n"
      "v_fma_f32 v196, |v93|, v77, v196\n"
      "v_fma_f32 v196, |v94|, v78, v196\n"
      "v_fma_f32 v196, |v95|, v79, v196\n"
      "v_mov_b32_dpp v238, v180 quad_perm:[0,1,2,3] row_mask:0x1 bank_mask:0xf\n"
      "v_mov_b32_dpp v239, v181 quad_perm:[0,1,2,3] row_mask:0x1 bank_mask:0xf\n"
      "v_permlane32_swap_b32 v196, v8\n"
      "v_add_f32 v196, v196, v8\n"
      "s_movk_i32 s45, 0x207\n"
      "s_nop 0\n"
      "v_permlane16_swap_b32 v199, v196\n"
      "v_add_f32 v199, v199, v196\n"
      "v_fmac_f32 v199, v200, v238\n"
      "v_max_f32 v225, v222, v199\n"
      "v_fma_f32 v220, v199, 0, v220\n"
      "s_nop 0\n"
      "v_max_f32_dpp v223, v225, v225 row_ror:4 row_mask:0xf bank_mask:0xf\n"
      "v_max_f32_dpp v223, v225, v223 row_ror:8 row_mask:0xf bank_mask:0xf\n"
      "v_max_f32_dpp v223, v225, v223 row_ror:12 row_mask:0xf bank_mask:0xf\n"
      "s_nop 1\n"
      "v_max_f32_dpp v225, v223, v223 row_ror:1 row_mask:0xf bank_mask:0xf\n"
      "v_max_f32_dpp v225, v223, v225 row_ror:2 row_mask:0xf bank_mask:0xf\n"
      "v_max_f32_dpp v225, v223, v225 row_ror:3 row_mask:0xf bank_mask:0xf\n"
      "s_nop 1\n"
      "v_max_f32_dpp v225, v225, v225 row_bcast:15 row_mask:0xa bank_mask:0xf\n"
      "s_nop 1\n"
      "v_max_f32_dpp v225, v225, v225 row_bcast:31 row_mask:0xc bank_mask:0xf\n"
      "s_nop 0\n"
      "v_readlane_b32 s44, v225, 63\n"
      "s_nop 1\n"
      "v_subrev_f32 v223, s44, v222\n"
      "v_subrev_f32 v199, s44, v199\n"
      "v_exp_f32 v223, v223\n"
      "v_exp_f32 v199, v199\n"
      "v_mul_f32 v221, v221, v223\n"
      "v_fma_f32 v220, v220, v223, v199\n"
      "v_fmac_f32 v221, v199, v239\n"
      "s_nop 0\n"
      "v_add_f32_dpp v220, v220, v220 row_ror:8 row_mask:0xf bank_mask:0xf\n"
      "v_add_f32_dpp v221, v221, v221 row_ror:8 row_mask:0xf bank_mask:0xf\n"
      "s_nop 0\n"
      "v_add_f32_dpp v220, v220, v220 row_ror:4 row_mask:0xf bank_mask:0xf\n"
      "v_add_f32_dpp v221, v221, v221 row_ror:4 row_mask:0xf bank_mask:0xf\n"
      "s_nop 0\n"
      "v_add_f32_dpp v220, v220, v220 row_ror:2 row_mask:0xf bank_mask:0xf\n"
      "v_add_f32_dpp v221, v221, v221 row_ror:2 row_mask:0xf bank_mask:0xf\n"
      "s_nop 0\n"
      "v_add_f32_dpp v220, v220, v220 row_ror:1 row_mask:0xf bank_mask:0xf\n"
      "v_add_f32_dpp v221, v221, v221 row_ror:1 row_mask:0xf bank_mask:0xf\n"
      "s_nop 0\n"
      "v_add_f32_dpp v220, v220, v220 row_bcast:15 row_mask:0xa bank_mask:0xf\n"
      "v_add_f32_dpp v221, v221, v221 row_bcast:15 row_mask:0xa bank_mask:0xf\n"
      "s_nop 0\n"
      "v_add_f32_dpp v220, v220, v220 row_bcast:31 row_mask:0xc bank_mask:0xf\n"
      "v_add_f32_dpp v221, v221, v221 row_bcast:31 row_mask:0xc bank_mask:0xf\n"
      "s_mov_b32 exec_lo, 0\n"
      "v_rcp_f32 v220, v220\n"
      "v_ldexp_f32 v221, v221, s39\n"
      "s_mov_b32 exec_hi, 0x80000000\n"
      "v_mul_f32 v221, v221, v220\n"
      "v_cvt_pk_bf16_f32 v220, v221, v221\n"
      "global_store_short v3, v220, s[24:25]\n"
      "v_cmp_class_f32 vcc, v221, s45\n"
      "s_nop 0\n"
      "s_cmp_lt_i32 vcc_hi, 0\n"
      "s_cbranch_scc1 7f\n"
      "s_endpgm\n"
      "s_mov_b64 exec, -1\n"
      "s_branch 9f\n"
      "80:\n"
      "s_waitcnt vmcnt(0) lgkmcnt(0)\n"
      "ds_read_b32 v224, v227 offset:37640\n"
      "s_waitcnt lgkmcnt(0)\n"
      "v_cmp_eq_u32 vcc, 0, v224\n"
      "s_nop 4\n"
      "s_and_b32 s44, vcc_lo, s54\n"
      "s_cbranch_scc1 80b\n"
      "v_cmp_eq_u32 vcc, 1, v224\n"
      "s_nop 4\n"
      "s_and_b32 s44, vcc_lo, s54\n"
      "s_cmp_eq_u32 s44, s54\n"
      "s_cbranch_scc1 81b\n"
      "s_mov_b32 s36, 1\n"
      "s_add_u32 s44, s38, 1152\n"
      "v_cndmask_b32 v10, v201, v10, s[100:101]\n"
      "v_cndmask_b32 v11, v201, v11, s[100:101]\n"
      "v_mov_b32 v1, s44\n"
      "s_nop 1\n"
      "s_branch 81b\n"
      "82:\n"
      "s_cmp_lg_u32 s36, 0\n"
      "s_cbranch_scc1 84f\n"
      "85:\n"
      "ds_read_b32 v224, v12 offset:12736\n"
      "s_waitcnt lgkmcnt(0)\n"
      "v_cmp_eq_u32 vcc, s37, v224\n"
      "s_nop 4\n"
      "s_cmp_lg_u64 vcc, 0\n"
      "s_cbranch_scc1 85b\n"
      "s_branch 83b\n"
      "84:\n"
      "s_waitcnt lgkmcnt(0)\n"
      "v_mfma_f32_16x16x32_f16 v[240:243], a[64:67], v[160:163], v[32:35]\n"
      "v_mfma_f32_16x16x32_f16 v[240:243], a[68:71], v[164:167], v[240:243]\n"
      "v_mfma_f32_16x16x32_f16 v[240:243], a[72:75], v[168:171], v[240:243]\n"
      "v_mfma_f32_16x16x32_f16 v[240:243], a[76:79], v[172:175], v[240:243]\n"
      "v_mfma_f32_16x16x32_f16 v[244:247], a[80:83], v[160:163], v[36:39]\n"
      "v_mfma_f32_16x16x32_f16 v[244:247], a[84:87], v[164:167], v[244:247]\n"
      "v_mfma_f32_16x16x32_f16 v[244:247], a[88:91], v[168:171], v[244:247]\n"
      "v_mfma_f32_16x16x32_f16 v[244:247], a[92:95], v[172:175], v[244:247]\n"
      "v_mfma_f32_16x16x32_f16 v[248:251], a[96:99], v[160:163], v[40:43]\n"
      "v_mfma_f32_16x16x32_f16 v[248:251], a[100:103], v[164:167], v[248:251]\n"
      "v_mfma_f32_16x16x32_f16 v[248:251], a[104:107], v[168:171], v[248:251]\n"
      "v_mfma_f32_16x16x32_f16 v[248:251], a[108:111], v[172:175], v[248:251]\n"
      "v_mfma_f32_16x16x32_f16 v[252:255], a[112:115], v[160:163], v[44:47]\n"
      "v_mfma_f32_16x16x32_f16 v[252:255], a[116:119], v[164:167], v[252:255]\n"
      "v_mfma_f32_16x16x32_f16 v[252:255], a[120:123], v[168:171], v[252:255]\n"
      "v_mfma_f32_16x16x32_f16 v[252:255], a[124:127], v[172:175], v[252:255]\n"
      "s_nop 7\n"
      "v_mul_f32 v224, |v240|, v64\n"
      "v_fma_f32 v224, |v241|, v65, v224\n"
      "v_fma_f32 v224, |v242|, v66, v224\n"
      "v_fma_f32 v224, |v243|, v67, v224\n"
      "v_fma_f32 v224, |v244|, v68, v224\n"
      "v_fma_f32 v224, |v245|, v69, v224\n"
      "v_fma_f32 v224, |v246|, v70, v224\n"
      "v_fma_f32 v224, |v247|, v71, v224\n"
      "v_fma_f32 v224, |v248|, v72, v224\n"
      "v_fma_f32 v224, |v249|, v73, v224\n"
      "v_fma_f32 v224, |v250|, v74, v224\n"
      "v_fma_f32 v224, |v251|, v75, v224\n"
      "v_fma_f32 v224, |v252|, v76, v224\n"
      "v_fma_f32 v224, |v253|, v77, v224\n"
      "v_fma_f32 v224, |v254|, v78, v224\n"
      "v_fma_f32 v224, |v255|, v79, v224\n"
      "s_nop 1\n"
      "s_branch 83b\n"
      "5:\n"
      "s_waitcnt lgkmcnt(0)\n"
      "ds_read_b32 v224, v227 offset:37632\n"
      "s_waitcnt lgkmcnt(0)\n"
      "v_cmp_eq_u32 vcc, 0, v224\n"
      "s_nop 4\n"
      "s_cmp_lg_u32 vcc_lo, 0\n"
      "s_cbranch_scc1 5b\n"
      "v_cmp_gt_u32 vcc, 4, v224\n"
      "s_nop 4\n"
      "s_cmp_lg_u32 vcc_lo, 0\n"
      "s_cbranch_scc1 8f\n"
      "s_cmp_lg_u32 s58, 0\n"
      "s_cbranch_scc1 52f\n"
      "ds_read_b64 v[176:177], v5 offset:37120\n"
      "ds_read_b32 v226, v213 offset:12480\n"
      "s_waitcnt lgkmcnt(0)\n"
      "v_max_f16_sdwa v226, |v226|, |v226| dst_sel:DWORD dst_unused:UNUSED_PAD src0_sel:WORD_0 src1_sel:WORD_1\n"
      "v_max_f32 v225, |v176|, |v177|\n"
      "v_cvt_f32_f16 v226, v226\n"
      "s_nop 0\n"
      "v_max_f32_dpp v225, v225, v225 row_ror:8 row_mask:0xf bank_mask:0xf\n"
      "s_nop 1\n"
      "v_max_f32_dpp v225, v225, v225 row_ror:4 row_mask:0xf bank_mask:0xf\n"
      "s_nop 1\n"
      "v_max_f32_dpp v225, v225, v225 row_ror:2 row_mask:0xf bank_mask:0xf\n"
      "s_nop 1\n"
      "v_max_f32_dpp v225, v225, v225 row_ror:1 row_mask:0xf bank_mask:0xf\n"
      "s_nop 0\n"
      "v_readlane_b32 s48, v225, 0\n"
      "v_readlane_b32 s45, v225, 16\n"
      "v_readlane_b32 s46, v225, 32\n"
      "v_readlane_b32 s47, v225, 48\n"
      "s_max_u32 s48, s48, s45\n"
      "s_max_u32 s46, s46, s47\n"
      "s_max_u32 s48, s48, s46\n"
      "v_max_f32_dpp v226, v226, v226 row_ror:8 row_mask:0xf bank_mask:0xf\n"
      "s_nop 1\n"
      "v_max_f32_dpp v226, v226, v226 row_ror:4 row_mask:0xf bank_mask:0xf\n"
      "s_nop 1\n"
      "v_max_f32_dpp v226, v226, v226 row_ror:2 row_mask:0xf bank_mask:0xf\n"
      "s_nop 1\n"
      "v_max_f32_dpp v226, v226, v226 row_ror:1 row_mask:0xf bank_mask:0xf\n"
      "s_nop 0\n"
      "v_readlane_b32 s44, v226, 0\n"
      "v_readlane_b32 s45, v226, 16\n"
      "v_readlane_b32 s46, v226, 32\n"
      "v_readlane_b32 s47, v226, 48\n"
      "s_max_u32 s44, s44, s45\n"
      "s_max_u32 s46, s46, s47\n"
      "s_max_u32 s44, s44, s46\n"
      "s_add_u32 s48, s48, 0x7000000\n"
      "s_min_u32 s48, s48, 0x7f7fffff\n"
      "s_cmp_lt_u32 s48, s44\n"
      "s_cselect_b32 s48, -1, 0x3d800000\n"
      "s_cbranch_scc1 51f\n"
      "v_cmp_le_u32 vcc, 8, v224\n"
      "s_nop 4\n"
      "s_cmp_lg_u32 vcc_lo, 0\n"
      "s_cbranch_scc1 52f\n"
      "51:\n"
      "v_and_b32 v224, 63, %3\n"
      "v_lshlrev_b32 v224, 9, v224\n"
      "global_load_dwordx4 v[176:179], v224, s[16:17]\n"
      "global_load_dwordx4 v[180:183], v224, s[16:17] offset:336\n"
      "s_waitcnt vmcnt(0)\n"
      "v_max3_f32 v224, |v176|, |v177|, |v178|\n"
      "v_max3_f32 v224, |v179|, |v180|, v224\n"
      "v_max3_f32 v224, |v181|, |v182|, v224\n"
      "v_max_f32 v224, |v183|, v224\n"
      "s_nop 1\n"
      "v_max_f32_dpp v224, v224, v224 row_ror:8 row_mask:0xf bank_mask:0xf\n"
      "s_nop 1\n"
      "v_max_f32_dpp v224, v224, v224 row_ror:4 row_mask:0xf bank_mask:0xf\n"
      "s_nop 1\n"
      "v_max_f32_dpp v224, v224, v224 row_ror:2 row_mask:0xf bank_mask:0xf\n"
      "s_nop 1\n"
      "v_max_f32_dpp v224, v224, v224 row_ror:1 row_mask:0xf bank_mask:0xf\n"
      "s_nop 0\n"
      "v_readlane_b32 s44, v224, 0\n"
      "v_readlane_b32 s45, v224, 16\n"
      "v_readlane_b32 s46, v224, 32\n"
      "v_readlane_b32 s47, v224, 48\n"
      "s_max_u32 s44, s44, s45\n"
      "s_max_u32 s46, s46, s47\n"
      "s_max_u32 s44, s44, s46\n"
      "s_cmp_ge_u32 s44, s48\n"
      "s_cbranch_scc1 52f\n"
      "s_lshr_b32 s44, s44, 23\n"
      "s_sub_i32 s58, 135, s44\n"
      "s_cmp_eq_u32 s44, 0\n"
      "s_cselect_b32 s58, 0, s58\n"
      "s_max_i32 s58, s58, 11\n"
      "s_cmp_gt_u32 s58, 51\n"
      "s_cbranch_scc1 8f\n"
      "s_mov_b32 s41, 0\n"
      "s_mov_b32 s35, 0\n"
      "s_waitcnt vmcnt(0) lgkmcnt(0)\n"
      "s_barrier\n"
      "s_branch 0b\n"
      "52:\n"
      "s_cmp_lg_u32 s30, 0\n"
      "s_cbranch_scc1 6f\n"
      "ds_read_b64 v[176:177], v5 offset:37120\n"
      "s_waitcnt lgkmcnt(0)\n"
      "v_max_f32 v225, |v176|, |v177|\n"
      "s_nop 1\n"
      "v_max_f32_dpp v225, v225, v225 row_ror:8 row_mask:0xf bank_mask:0xf\n"
      "s_nop 1\n"
      "v_max_f32_dpp v225, v225, v225 row_ror:4 row_mask:0xf bank_mask:0xf\n"
      "s_nop 1\n"
      "v_max_f32_dpp v225, v225, v225 row_ror:2 row_mask:0xf bank_mask:0xf\n"
      "s_nop 1\n"
      "v_max_f32_dpp v225, v225, v225 row_ror:1 row_mask:0xf bank_mask:0xf\n"
      "s_nop 0\n"
      "v_readlane_b32 s44, v225, 0\n"
      "v_readlane_b32 s45, v225, 16\n"
      "v_readlane_b32 s46, v225, 32\n"
      "v_readlane_b32 s47, v225, 48\n"
      "s_max_u32 s44, s44, s45\n"
      "s_max_u32 s46, s46, s47\n"
      "s_max_u32 s44, s44, s46\n"
      "v_frexp_exp_i32_f32 v224, s44\n"
      "v_sub_u32 v224, 0, v224\n"
      "v_ldexp_f32 v176, v176, v224\n"
      "v_ldexp_f32 v177, v177, v224\n"
      "v_readfirstlane_b32 s56, v224\n"
      "s_nop 1\n"
      "s_branch 4b\n"
      "22:\n"
      "s_nop 7\n"
      "v_lshlrev_b32 v176, 16, v192\n"
      "v_and_b32 v177, s43, v192\n"
      "v_lshlrev_b32 v178, 16, v193\n"
      "v_and_b32 v179, s43, v193\n"
      "v_lshlrev_b32 v180, 16, v194\n"
      "v_and_b32 v181, s43, v194\n"
      "v_lshlrev_b32 v182, 16, v195\n"
      "v_and_b32 v183, s43, v195\n"
      "v_max3_f32 v222, |v176|, |v177|, |v178|\n"
      "v_max3_f32 v222, |v179|, |v180|, v222\n"
      "v_max3_f32 v222, |v181|, |v182|, v222\n"
      "v_max_f32 v222, |v183|, v222\n"
      "s_nop 1\n"
      "v_max_f32_dpp v222, v222, v222 row_ror:8 row_mask:0xf bank_mask:0xf\n"
      "s_nop 1\n"
      "v_max_f32_dpp v222, v222, v222 row_ror:4 row_mask:0xf bank_mask:0xf\n"
      "s_nop 1\n"
      "v_max_f32_dpp v222, v222, v222 row_ror:2 row_mask:0xf bank_mask:0xf\n"
      "s_nop 1\n"
      "v_max_f32_dpp v222, v222, v222 row_ror:1 row_mask:0xf bank_mask:0xf\n"
      "v_frexp_exp_i32_f32 v90, v222\n"
      "v_sub_u32 v90, 0, v90\n"
      "v_ldexp_f32 v176, v176, v90\n"
      "v_ldexp_f32 v177, v177, v90\n"
      "v_ldexp_f32 v178, v178, v90\n"
      "v_ldexp_f32 v179, v179, v90\n"
      "v_ldexp_f32 v180, v180, v90\n"
      "v_ldexp_f32 v181, v181, v90\n"
      "v_ldexp_f32 v182, v182, v90\n"
      "v_ldexp_f32 v183, v183, v90\n"
      "v_ldexp_f32 v176, v176, 9\n"
      "v_ldexp_f32 v177, v177, 9\n"
      "v_ldexp_f32 v178, v178, 9\n"
      "v_ldexp_f32 v179, v179, 9\n"
      "v_ldexp_f32 v180, v180, 9\n"
      "v_ldexp_f32 v181, v181, 9\n"
      "v_ldexp_f32 v182, v182, 9\n"
      "v_ldexp_f32 v183, v183, 9\n"
      "v_readlane_b32 s59, v90, 0\n"
      "v_readlane_b32 s57, v90, 32\n"
      "s_branch 23b\n"
      "70:\n"
      "s_mov_b32 s49, 0\n"
      "s_add_u32 s44, s58, 127\n"
      "s_lshl_b32 s44, s44, 23\n"
      "s_waitcnt vmcnt(0) lgkmcnt(0)\n"
      "s_nop 7\n"
      "v_lshlrev_b32 v128, 16, v96\n"
      "v_and_b32 v129, s43, v96\n"
      "v_mul_f32 v128, s44, v128\n"
      "v_mul_f32 v129, s44, v129\n"
      "v_cvt_pk_bf16_f32 v96, v128, v129\n"
      "v_lshlrev_b32 v128, 16, v97\n"
      "v_and_b32 v129, s43, v97\n"
      "v_mul_f32 v128, s44, v128\n"
      "v_mul_f32 v129, s44, v129\n"
      "v_cvt_pk_bf16_f32 v97, v128, v129\n"
      "v_lshlrev_b32 v128, 16, v98\n"
      "v_and_b32 v129, s43, v98\n"
      "v_mul_f32 v128, s44, v128\n"
      "v_mul_f32 v129, s44, v129\n"
      "v_cvt_pk_bf16_f32 v98, v128, v129\n"
      "v_lshlrev_b32 v128, 16, v99\n"
      "v_and_b32 v129, s43, v99\n"
      "v_mul_f32 v128, s44, v128\n"
      "v_mul_f32 v129, s44, v129\n"
      "v_cvt_pk_bf16_f32 v99, v128, v129\n"
      "v_lshlrev_b32 v128, 16, v100\n"
      "v_and_b32 v129, s43, v100\n"
      "v_mul_f32 v128, s44, v128\n"
      "v_mul_f32 v129, s44, v129\n"
      "v_cvt_pk_bf16_f32 v100, v128, v129\n"
      "v_lshlrev_b32 v128, 16, v101\n"
      "v_and_b32 v129, s43, v101\n"
      "v_mul_f32 v128, s44, v128\n"
      "v_mul_f32 v129, s44, v129\n"
      "v_cvt_pk_bf16_f32 v101, v128, v129\n"
      "v_lshlrev_b32 v128, 16, v102\n"
      "v_and_b32 v129, s43, v102\n"
      "v_mul_f32 v128, s44, v128\n"
      "v_mul_f32 v129, s44, v129\n"
      "v_cvt_pk_bf16_f32 v102, v128, v129\n"
      "v_lshlrev_b32 v128, 16, v103\n"
      "v_and_b32 v129, s43, v103\n"
      "v_mul_f32 v128, s44, v128\n"
      "v_mul_f32 v129, s44, v129\n"
      "v_cvt_pk_bf16_f32 v103, v128, v129\n"
      "v_lshlrev_b32 v128, 16, v104\n"
      "v_and_b32 v129, s43, v104\n"
      "v_mul_f32 v128, s44, v128\n"
      "v_mul_f32 v129, s44, v129\n"
      "v_cvt_pk_bf16_f32 v104, v128, v129\n"
      "v_lshlrev_b32 v128, 16, v105\n"
      "v_and_b32 v129, s43, v105\n"
      "v_mul_f32 v128, s44, v128\n"
      "v_mul_f32 v129, s44, v129\n"
      "v_cvt_pk_bf16_f32 v105, v128, v129\n"
      "v_lshlrev_b32 v128, 16, v106\n"
      "v_and_b32 v129, s43, v106\n"
      "v_mul_f32 v128, s44, v128\n"
      "v_mul_f32 v129, s44, v129\n"
      "v_cvt_pk_bf16_f32 v106, v128, v129\n"
      "v_lshlrev_b32 v128, 16, v107\n"
      "v_and_b32 v129, s43, v107\n"
      "v_mul_f32 v128, s44, v128\n"
      "v_mul_f32 v129, s44, v129\n"
      "v_cvt_pk_bf16_f32 v107, v128, v129\n"
      "v_lshlrev_b32 v128, 16, v108\n"
      "v_and_b32 v129, s43, v108\n"
      "v_mul_f32 v128, s44, v128\n"
      "v_mul_f32 v129, s44, v129\n"
      "v_cvt_pk_bf16_f32 v108, v128, v129\n"
      "v_lshlrev_b32 v128, 16, v109\n"
      "v_and_b32 v129, s43, v109\n"
      "v_mul_f32 v128, s44, v128\n"
      "v_mul_f32 v129, s44, v129\n"
      "v_cvt_pk_bf16_f32 v109, v128, v129\n"
      "v_lshlrev_b32 v128, 16, v110\n"
      "v_and_b32 v129, s43, v110\n"
      "v_mul_f32 v128, s44, v128\n"
      "v_mul_f32 v129, s44, v129\n"
      "v_cvt_pk_bf16_f32 v110, v128, v129\n"
      "v_lshlrev_b32 v128, 16, v111\n"
      "v_and_b32 v129, s43, v111\n"
      "v_mul_f32 v128, s44, v128\n"
      "v_mul_f32 v129, s44, v129\n"
      "v_cvt_pk_bf16_f32 v111, v128, v129\n"
      "v_lshlrev_b32 v128, 16, v112\n"
      "v_and_b32 v129, s43, v112\n"
      "v_mul_f32 v128, s44, v128\n"
      "v_mul_f32 v129, s44, v129\n"
      "v_cvt_pk_bf16_f32 v112, v128, v129\n"
      "v_lshlrev_b32 v128, 16, v113\n"
      "v_and_b32 v129, s43, v113\n"
      "v_mul_f32 v128, s44, v128\n"
      "v_mul_f32 v129, s44, v129\n"
      "v_cvt_pk_bf16_f32 v113, v128, v129\n"
      "v_lshlrev_b32 v128, 16, v114\n"
      "v_and_b32 v129, s43, v114\n"
      "v_mul_f32 v128, s44, v128\n"
      "v_mul_f32 v129, s44, v129\n"
      "v_cvt_pk_bf16_f32 v114, v128, v129\n"
      "v_lshlrev_b32 v128, 16, v115\n"
      "v_and_b32 v129, s43, v115\n"
      "v_mul_f32 v128, s44, v128\n"
      "v_mul_f32 v129, s44, v129\n"
      "v_cvt_pk_bf16_f32 v115, v128, v129\n"
      "v_lshlrev_b32 v128, 16, v116\n"
      "v_and_b32 v129, s43, v116\n"
      "v_mul_f32 v128, s44, v128\n"
      "v_mul_f32 v129, s44, v129\n"
      "v_cvt_pk_bf16_f32 v116, v128, v129\n"
      "v_lshlrev_b32 v128, 16, v117\n"
      "v_and_b32 v129, s43, v117\n"
      "v_mul_f32 v128, s44, v128\n"
      "v_mul_f32 v129, s44, v129\n"
      "v_cvt_pk_bf16_f32 v117, v128, v129\n"
      "v_lshlrev_b32 v128, 16, v118\n"
      "v_and_b32 v129, s43, v118\n"
      "v_mul_f32 v128, s44, v128\n"
      "v_mul_f32 v129, s44, v129\n"
      "v_cvt_pk_bf16_f32 v118, v128, v129\n"
      "v_lshlrev_b32 v128, 16, v119\n"
      "v_and_b32 v129, s43, v119\n"
      "v_mul_f32 v128, s44, v128\n"
      "v_mul_f32 v129, s44, v129\n"
      "v_cvt_pk_bf16_f32 v119, v128, v129\n"
      "v_lshlrev_b32 v128, 16, v120\n"
      "v_and_b32 v129, s43, v120\n"
      "v_mul_f32 v128, s44, v128\n"
      "v_mul_f32 v129, s44, v129\n"
      "v_cvt_pk_bf16_f32 v120, v128, v129\n"
      "v_lshlrev_b32 v128, 16, v121\n"
      "v_and_b32 v129, s43, v121\n"
      "v_mul_f32 v128, s44, v128\n"
      "v_mul_f32 v129, s44, v129\n"
      "v_cvt_pk_bf16_f32 v121, v128, v129\n"
      "v_lshlrev_b32 v128, 16, v122\n"
      "v_and_b32 v129, s43, v122\n"
      "v_mul_f32 v128, s44, v128\n"
      "v_mul_f32 v129, s44, v129\n"
      "v_cvt_pk_bf16_f32 v122, v128, v129\n"
      "v_lshlrev_b32 v128, 16, v123\n"
      "v_and_b32 v129, s43, v123\n"
      "v_mul_f32 v128, s44, v128\n"
      "v_mul_f32 v129, s44, v129\n"
      "v_cvt_pk_bf16_f32 v123, v128, v129\n"
      "v_lshlrev_b32 v128, 16, v124\n"
      "v_and_b32 v129, s43, v124\n"
      "v_mul_f32 v128, s44, v128\n"
      "v_mul_f32 v129, s44, v129\n"
      "v_cvt_pk_bf16_f32 v124, v128, v129\n"
      "v_lshlrev_b32 v128, 16, v125\n"
      "v_and_b32 v129, s43, v125\n"
      "v_mul_f32 v128, s44, v128\n"
      "v_mul_f32 v129, s44, v129\n"
      "v_cvt_pk_bf16_f32 v125, v128, v129\n"
      "v_lshlrev_b32 v128, 16, v126\n"
      "v_and_b32 v129, s43, v126\n"
      "v_mul_f32 v128, s44, v128\n"
      "v_mul_f32 v129, s44, v129\n"
      "v_cvt_pk_bf16_f32 v126, v128, v129\n"
      "v_lshlrev_b32 v128, 16, v127\n"
      "v_and_b32 v129, s43, v127\n"
      "v_mul_f32 v128, s44, v128\n"
      "v_mul_f32 v129, s44, v129\n"
      "v_cvt_pk_bf16_f32 v127, v128, v129\n"
      "s_nop 1\n"
      "s_branch 71b\n"
      "72:\n"
      "s_nop 7\n"
      "s_sub_u32 s44, 0, s58\n"
      "v_ldexp_f32 v16, v16, s58\n"
      "v_ldexp_f32 v48, v48, s44\n"
      "v_ldexp_f32 v17, v17, s58\n"
      "v_ldexp_f32 v49, v49, s44\n"
      "v_ldexp_f32 v18, v18, s58\n"
      "v_ldexp_f32 v50, v50, s44\n"
      "v_ldexp_f32 v19, v19, s58\n"
      "v_ldexp_f32 v51, v51, s44\n"
      "v_ldexp_f32 v20, v20, s58\n"
      "v_ldexp_f32 v52, v52, s44\n"
      "v_ldexp_f32 v21, v21, s58\n"
      "v_ldexp_f32 v53, v53, s44\n"
      "v_ldexp_f32 v22, v22, s58\n"
      "v_ldexp_f32 v54, v54, s44\n"
      "v_ldexp_f32 v23, v23, s58\n"
      "v_ldexp_f32 v55, v55, s44\n"
      "v_ldexp_f32 v24, v24, s58\n"
      "v_ldexp_f32 v56, v56, s44\n"
      "v_ldexp_f32 v25, v25, s58\n"
      "v_ldexp_f32 v57, v57, s44\n"
      "v_ldexp_f32 v26, v26, s58\n"
      "v_ldexp_f32 v58, v58, s44\n"
      "v_ldexp_f32 v27, v27, s58\n"
      "v_ldexp_f32 v59, v59, s44\n"
      "v_ldexp_f32 v28, v28, s58\n"
      "v_ldexp_f32 v60, v60, s44\n"
      "v_ldexp_f32 v29, v29, s58\n"
      "v_ldexp_f32 v61, v61, s44\n"
      "v_ldexp_f32 v30, v30, s58\n"
      "v_ldexp_f32 v62, v62, s44\n"
      "v_ldexp_f32 v31, v31, s58\n"
      "v_ldexp_f32 v63, v63, s44\n"
      "v_ldexp_f32 v32, v32, s58\n"
      "v_ldexp_f32 v64, v64, s44\n"
      "v_ldexp_f32 v33, v33, s58\n"
      "v_ldexp_f32 v65, v65, s44\n"
      "v_ldexp_f32 v34, v34, s58\n"
      "v_ldexp_f32 v66, v66, s44\n"
      "v_ldexp_f32 v35, v35, s58\n"
      "v_ldexp_f32 v67, v67, s44\n"
      "v_ldexp_f32 v36, v36, s58\n"
      "v_ldexp_f32 v68, v68, s44\n"
      "v_ldexp_f32 v37, v37, s58\n"
      "v_ldexp_f32 v69, v69, s44\n"
      "v_ldexp_f32 v38, v38, s58\n"
      "v_ldexp_f32 v70, v70, s44\n"
      "v_ldexp_f32 v39, v39, s58\n"
      "v_ldexp_f32 v71, v71, s44\n"
      "v_ldexp_f32 v40, v40, s58\n"
      "v_ldexp_f32 v72, v72, s44\n"
      "v_ldexp_f32 v41, v41, s58\n"
      "v_ldexp_f32 v73, v73, s44\n"
      "v_ldexp_f32 v42, v42, s58\n"
      "v_ldexp_f32 v74, v74, s44\n"
      "v_ldexp_f32 v43, v43, s58\n"
      "v_ldexp_f32 v75, v75, s44\n"
      "v_ldexp_f32 v44, v44, s58\n"
      "v_ldexp_f32 v76, v76, s44\n"
      "v_ldexp_f32 v45, v45, s58\n"
      "v_ldexp_f32 v77, v77, s44\n"
      "v_ldexp_f32 v46, v46, s58\n"
      "v_ldexp_f32 v78, v78, s44\n"
      "v_ldexp_f32 v47, v47, s58\n"
      "v_ldexp_f32 v79, v79, s44\n"
      "s_branch 73b\n"
      "6:\n"
      "s_waitcnt vmcnt(0) lgkmcnt(0)\n"
      "s_nop 7\n"
      "s_mov_b32 s55, 1\n"
      "s_mov_b32 s41, 1\n"
      "s_mov_b32 s45, 0x7fff7fff\n"
      "v_mov_b32 v226, 0\n"
      "v_and_b32 v176, s45, v228\n"
      "v_pk_max_u16 v226, v226, v176\n"
      "v_and_b32 v176, s45, v229\n"
      "v_pk_max_u16 v226, v226, v176\n"
      "v_and_b32 v176, s45, v230\n"
      "v_pk_max_u16 v226, v226, v176\n"
      "v_and_b32 v176, s45, v231\n"
      "v_pk_max_u16 v226, v226, v176\n"
      "v_and_b32 v176, s45, v232\n"
      "v_pk_max_u16 v226, v226, v176\n"
      "v_and_b32 v176, s45, v233\n"
      "v_pk_max_u16 v226, v226, v176\n"
      "v_and_b32 v176, s45, v234\n"
      "v_pk_max_u16 v226, v226, v176\n"
      "v_and_b32 v176, s45, v235\n"
      "v_pk_max_u16 v226, v226, v176\n"
      "v_and_b32 v176, s45, v236\n"
      "v_pk_max_u16 v226, v226, v176\n"
      "v_and_b32 v176, s45, v237\n"
      "v_pk_max_u16 v226, v226, v176\n"
      "v_and_b32 v176, s45, v238\n"
      "v_pk_max_u16 v226, v226, v176\n"
      "v_and_b32 v176, s45, v239\n"
      "v_pk_max_u16 v226, v226, v176\n"
      "v_and_b32 v176, s45, v240\n"
      "v_pk_max_u16 v226, v226, v176\n"
      "v_and_b32 v176, s45, v241\n"
      "v_pk_max_u16 v226, v226, v176\n"
      "v_and_b32 v176, s45, v242\n"
      "v_pk_max_u16 v226, v226, v176\n"
      "v_and_b32 v176, s45, v243\n"
      "v_pk_max_u16 v226, v226, v176\n"
      "v_and_b32 v176, s45, v244\n"
      "v_pk_max_u16 v226, v226, v176\n"
      "v_and_b32 v176, s45, v245\n"
      "v_pk_max_u16 v226, v226, v176\n"
      "v_and_b32 v176, s45, v246\n"
      "v_pk_max_u16 v226, v226, v176\n"
      "v_and_b32 v176, s45, v247\n"
      "v_pk_max_u16 v226, v226, v176\n"
      "v_and_b32 v176, s45, v248\n"
      "v_pk_max_u16 v226, v226, v176\n"
      "v_and_b32 v176, s45, v249\n"
      "v_pk_max_u16 v226, v226, v176\n"
      "v_and_b32 v176, s45, v250\n"
      "v_pk_max_u16 v226, v226, v176\n"
      "v_and_b32 v176, s45, v251\n"
      "v_pk_max_u16 v226, v226, v176\n"
      "v_and_b32 v176, s45, v252\n"
      "v_pk_max_u16 v226, v226, v176\n"
      "v_and_b32 v176, s45, v253\n"
      "v_pk_max_u16 v226, v226, v176\n"
      "v_and_b32 v176, s45, v254\n"
      "v_pk_max_u16 v226, v226, v176\n"
      "v_and_b32 v176, s45, v255\n"
      "v_pk_max_u16 v226, v226, v176\n"
      "v_lshlrev_b32 v176, 16, v226\n"
      "v_max_u32 v226, v226, v176\n"
      "s_nop 1\n"
      "v_max_u32_dpp v226, v226, v226 row_ror:8 row_mask:0xf bank_mask:0xf\n"
      "s_nop 1\n"
      "v_max_u32_dpp v226, v226, v226 row_ror:4 row_mask:0xf bank_mask:0xf\n"
      "s_nop 1\n"
      "v_max_u32_dpp v226, v226, v226 row_ror:2 row_mask:0xf bank_mask:0xf\n"
      "s_nop 1\n"
      "v_max_u32_dpp v226, v226, v226 row_ror:1 row_mask:0xf bank_mask:0xf\n"
      "s_nop 0\n"
      "v_readlane_b32 s44, v226, 0\n"
      "v_readlane_b32 s45, v226, 16\n"
      "v_readlane_b32 s46, v226, 32\n"
      "v_readlane_b32 s47, v226, 48\n"
      "s_max_u32 s44, s44, s45\n"
      "s_max_u32 s46, s46, s47\n"
      "s_max_u32 s44, s44, s46\n"
      "s_lshr_b32 s44, s44, 23\n"
      "s_sub_i32 s40, 137, s44\n"
      "s_cmp_eq_u32 s44, 0\n"
      "s_cselect_b32 s40, 0, s40\n"
      "s_add_i32 s45, s40, 33\n"
      "s_cmp_gt_u32 s45, 80\n"
      "s_cbranch_scc1 8f\n"
      "s_add_u32 s39, s40, 120\n"
      "s_lshl_b32 s39, s39, 23\n"
      "s_lshl_b32 s44, s40, 1\n"
      "s_sub_u32 s45, 127, s44\n"
      "s_add_u32 s44, s44, 127\n"
      "s_lshl_b32 s44, s44, 23\n"
      "s_lshl_b32 s45, s45, 23\n"
      "v_mul_f32 v16, s44, v16\n"
      "v_mul_f32 v48, s45, v48\n"
      "v_mul_f32 v17, s44, v17\n"
      "v_mul_f32 v49, s45, v49\n"
      "v_mul_f32 v18, s44, v18\n"
      "v_mul_f32 v50, s45, v50\n"
      "v_mul_f32 v19, s44, v19\n"
      "v_mul_f32 v51, s45, v51\n"
      "v_mul_f32 v20, s44, v20\n"
      "v_mul_f32 v52, s45, v52\n"
      "v_mul_f32 v21, s44, v21\n"
      "v_mul_f32 v53, s45, v53\n"
      "v_mul_f32 v22, s44, v22\n"
      "v_mul_f32 v54, s45, v54\n"
      "v_mul_f32 v23, s44, v23\n"
      "v_mul_f32 v55, s45, v55\n"
      "v_mul_f32 v24, s44, v24\n"
      "v_mul_f32 v56, s45, v56\n"
      "v_mul_f32 v25, s44, v25\n"
      "v_mul_f32 v57, s45, v57\n"
      "v_mul_f32 v26, s44, v26\n"
      "v_mul_f32 v58, s45, v58\n"
      "v_mul_f32 v27, s44, v27\n"
      "v_mul_f32 v59, s45, v59\n"
      "v_mul_f32 v28, s44, v28\n"
      "v_mul_f32 v60, s45, v60\n"
      "v_mul_f32 v29, s44, v29\n"
      "v_mul_f32 v61, s45, v61\n"
      "v_mul_f32 v30, s44, v30\n"
      "v_mul_f32 v62, s45, v62\n"
      "v_mul_f32 v31, s44, v31\n"
      "v_mul_f32 v63, s45, v63\n"
      "v_mul_f32 v32, s44, v32\n"
      "v_mul_f32 v64, s45, v64\n"
      "v_mul_f32 v33, s44, v33\n"
      "v_mul_f32 v65, s45, v65\n"
      "v_mul_f32 v34, s44, v34\n"
      "v_mul_f32 v66, s45, v66\n"
      "v_mul_f32 v35, s44, v35\n"
      "v_mul_f32 v67, s45, v67\n"
      "v_mul_f32 v36, s44, v36\n"
      "v_mul_f32 v68, s45, v68\n"
      "v_mul_f32 v37, s44, v37\n"
      "v_mul_f32 v69, s45, v69\n"
      "v_mul_f32 v38, s44, v38\n"
      "v_mul_f32 v70, s45, v70\n"
      "v_mul_f32 v39, s44, v39\n"
      "v_mul_f32 v71, s45, v71\n"
      "v_mul_f32 v40, s44, v40\n"
      "v_mul_f32 v72, s45, v72\n"
      "v_mul_f32 v41, s44, v41\n"
      "v_mul_f32 v73, s45, v73\n"
      "v_mul_f32 v42, s44, v42\n"
      "v_mul_f32 v74, s45, v74\n"
      "v_mul_f32 v43, s44, v43\n"
      "v_mul_f32 v75, s45, v75\n"
      "v_mul_f32 v44, s44, v44\n"
      "v_mul_f32 v76, s45, v76\n"
      "v_mul_f32 v45, s44, v45\n"
      "v_mul_f32 v77, s45, v77\n"
      "v_mul_f32 v46, s44, v46\n"
      "v_mul_f32 v78, s45, v78\n"
      "v_mul_f32 v47, s44, v47\n"
      "v_mul_f32 v79, s45, v79\n"
      "v_lshlrev_b32 v176, 16, v228\n"
      "v_and_b32 v177, s43, v228\n"
      "v_lshlrev_b32 v178, 16, v229\n"
      "v_and_b32 v179, s43, v229\n"
      "v_lshlrev_b32 v180, 16, v230\n"
      "v_and_b32 v181, s43, v230\n"
      "v_lshlrev_b32 v182, 16, v231\n"
      "v_and_b32 v183, s43, v231\n"
      "v_mul_f32 v176, s39, v176\n"
      "v_mul_f32 v177, s39, v177\n"
      "v_mul_f32 v178, s39, v178\n"
      "v_mul_f32 v179, s39, v179\n"
      "v_mul_f32 v180, s39, v180\n"
      "v_mul_f32 v181, s39, v181\n"
      "v_mul_f32 v182, s39, v182\n"
      "v_mul_f32 v183, s39, v183\n"
      "v_cvt_pk_f16_f32 v184, v176, v177\n"
      "v_cvt_pk_f16_f32 v185, v178, v179\n"
      "v_cvt_pk_f16_f32 v186, v180, v181\n"
      "v_cvt_pk_f16_f32 v187, v182, v183\n"
      "ds_write_b128 v1, v[184:187] offset:0\n"
      "v_lshlrev_b32 v176, 16, v232\n"
      "v_and_b32 v177, s43, v232\n"
      "v_lshlrev_b32 v178, 16, v233\n"
      "v_and_b32 v179, s43, v233\n"
      "v_lshlrev_b32 v180, 16, v234\n"
      "v_and_b32 v181, s43, v234\n"
      "v_lshlrev_b32 v182, 16, v235\n"
      "v_and_b32 v183, s43, v235\n"
      "v_mul_f32 v176, s39, v176\n"
      "v_mul_f32 v177, s39, v177\n"
      "v_mul_f32 v178, s39, v178\n"
      "v_mul_f32 v179, s39, v179\n"
      "v_mul_f32 v180, s39, v180\n"
      "v_mul_f32 v181, s39, v181\n"
      "v_mul_f32 v182, s39, v182\n"
      "v_mul_f32 v183, s39, v183\n"
      "v_cvt_pk_f16_f32 v184, v176, v177\n"
      "v_cvt_pk_f16_f32 v185, v178, v179\n"
      "v_cvt_pk_f16_f32 v186, v180, v181\n"
      "v_cvt_pk_f16_f32 v187, v182, v183\n"
      "ds_write_b128 v1, v[184:187] offset:1152\n"
      "v_lshlrev_b32 v176, 16, v236\n"
      "v_and_b32 v177, s43, v236\n"
      "v_lshlrev_b32 v178, 16, v237\n"
      "v_and_b32 v179, s43, v237\n"
      "v_lshlrev_b32 v180, 16, v238\n"
      "v_and_b32 v181, s43, v238\n"
      "v_lshlrev_b32 v182, 16, v239\n"
      "v_and_b32 v183, s43, v239\n"
      "v_mul_f32 v176, s39, v176\n"
      "v_mul_f32 v177, s39, v177\n"
      "v_mul_f32 v178, s39, v178\n"
      "v_mul_f32 v179, s39, v179\n"
      "v_mul_f32 v180, s39, v180\n"
      "v_mul_f32 v181, s39, v181\n"
      "v_mul_f32 v182, s39, v182\n"
      "v_mul_f32 v183, s39, v183\n"
      "v_cvt_pk_f16_f32 v184, v176, v177\n"
      "v_cvt_pk_f16_f32 v185, v178, v179\n"
      "v_cvt_pk_f16_f32 v186, v180, v181\n"
      "v_cvt_pk_f16_f32 v187, v182, v183\n"
      "ds_write_b128 v1, v[184:187] offset:2304\n"
      "v_lshlrev_b32 v176, 16, v240\n"
      "v_and_b32 v177, s43, v240\n"
      "v_lshlrev_b32 v178, 16, v241\n"
      "v_and_b32 v179, s43, v241\n"
      "v_lshlrev_b32 v180, 16, v242\n"
      "v_and_b32 v181, s43, v242\n"
      "v_lshlrev_b32 v182, 16, v243\n"
      "v_and_b32 v183, s43, v243\n"
      "v_mul_f32 v176, s39, v176\n"
      "v_mul_f32 v177, s39, v177\n"
      "v_mul_f32 v178, s39, v178\n"
      "v_mul_f32 v179, s39, v179\n"
      "v_mul_f32 v180, s39, v180\n"
      "v_mul_f32 v181, s39, v181\n"
      "v_mul_f32 v182, s39, v182\n"
      "v_mul_f32 v183, s39, v183\n"
      "v_cvt_pk_f16_f32 v184, v176, v177\n"
      "v_cvt_pk_f16_f32 v185, v178, v179\n"
      "v_cvt_pk_f16_f32 v186, v180, v181\n"
      "v_cvt_pk_f16_f32 v187, v182, v183\n"
      "ds_write_b128 v1, v[184:187] offset:3456\n"
      "v_lshlrev_b32 v176, 16, v244\n"
      "v_and_b32 v177, s43, v244\n"
      "v_lshlrev_b32 v178, 16, v245\n"
      "v_and_b32 v179, s43, v245\n"
      "v_lshlrev_b32 v180, 16, v246\n"
      "v_and_b32 v181, s43, v246\n"
      "v_lshlrev_b32 v182, 16, v247\n"
      "v_and_b32 v183, s43, v247\n"
      "v_mul_f32 v176, s39, v176\n"
      "v_mul_f32 v177, s39, v177\n"
      "v_mul_f32 v178, s39, v178\n"
      "v_mul_f32 v179, s39, v179\n"
      "v_mul_f32 v180, s39, v180\n"
      "v_mul_f32 v181, s39, v181\n"
      "v_mul_f32 v182, s39, v182\n"
      "v_mul_f32 v183, s39, v183\n"
      "v_cvt_pk_f16_f32 v184, v176, v177\n"
      "v_cvt_pk_f16_f32 v185, v178, v179\n"
      "v_cvt_pk_f16_f32 v186, v180, v181\n"
      "v_cvt_pk_f16_f32 v187, v182, v183\n"
      "ds_write_b128 v1, v[184:187] offset:4608\n"
      "v_lshlrev_b32 v176, 16, v248\n"
      "v_and_b32 v177, s43, v248\n"
      "v_lshlrev_b32 v178, 16, v249\n"
      "v_and_b32 v179, s43, v249\n"
      "v_lshlrev_b32 v180, 16, v250\n"
      "v_and_b32 v181, s43, v250\n"
      "v_lshlrev_b32 v182, 16, v251\n"
      "v_and_b32 v183, s43, v251\n"
      "v_mul_f32 v176, s39, v176\n"
      "v_mul_f32 v177, s39, v177\n"
      "v_mul_f32 v178, s39, v178\n"
      "v_mul_f32 v179, s39, v179\n"
      "v_mul_f32 v180, s39, v180\n"
      "v_mul_f32 v181, s39, v181\n"
      "v_mul_f32 v182, s39, v182\n"
      "v_mul_f32 v183, s39, v183\n"
      "v_cvt_pk_f16_f32 v184, v176, v177\n"
      "v_cvt_pk_f16_f32 v185, v178, v179\n"
      "v_cvt_pk_f16_f32 v186, v180, v181\n"
      "v_cvt_pk_f16_f32 v187, v182, v183\n"
      "ds_write_b128 v1, v[184:187] offset:5760\n"
      "v_lshlrev_b32 v176, 16, v252\n"
      "v_and_b32 v177, s43, v252\n"
      "v_lshlrev_b32 v178, 16, v253\n"
      "v_and_b32 v179, s43, v253\n"
      "v_lshlrev_b32 v180, 16, v254\n"
      "v_and_b32 v181, s43, v254\n"
      "v_lshlrev_b32 v182, 16, v255\n"
      "v_and_b32 v183, s43, v255\n"
      "v_mul_f32 v176, s39, v176\n"
      "v_mul_f32 v177, s39, v177\n"
      "v_mul_f32 v178, s39, v178\n"
      "v_mul_f32 v179, s39, v179\n"
      "v_mul_f32 v180, s39, v180\n"
      "v_mul_f32 v181, s39, v181\n"
      "v_mul_f32 v182, s39, v182\n"
      "v_mul_f32 v183, s39, v183\n"
      "v_cvt_pk_f16_f32 v184, v176, v177\n"
      "v_cvt_pk_f16_f32 v185, v178, v179\n"
      "v_cvt_pk_f16_f32 v186, v180, v181\n"
      "v_cvt_pk_f16_f32 v187, v182, v183\n"
      "ds_write_b128 v1, v[184:187] offset:6912\n"
      "s_sub_u32 s44, 93824, s38\n"
      "s_mov_b32 s98, 0\n"
      "v_add_u32 v1, s44, v1\n"
      "s_waitcnt lgkmcnt(0)\n"
      "ds_read_b128 v[128:131], v201 offset:0\n"
      "ds_read_b128 v[160:163], v202 offset:0\n"
      "ds_read_b128 v[132:135], v201 offset:64\n"
      "ds_read_b128 v[164:167], v202 offset:64\n"
      "ds_read_b128 v[136:139], v201 offset:128\n"
      "ds_read_b128 v[168:171], v202 offset:128\n"
      "ds_read_b128 v[140:143], v201 offset:192\n"
      "ds_read_b128 v[172:175], v202 offset:192\n"
      "s_waitcnt lgkmcnt(0)\n"
      "v_pk_mul_f16 v96, v128, v160\n"
      "v_pk_mul_f16 v97, v129, v161\n"
      "v_pk_mul_f16 v98, v130, v162\n"
      "v_pk_mul_f16 v99, v131, v163\n"
      "v_pk_mul_f16 v100, v132, v164\n"
      "v_pk_mul_f16 v101, v133, v165\n"
      "v_pk_mul_f16 v102, v134, v166\n"
      "v_pk_mul_f16 v103, v135, v167\n"
      "v_pk_mul_f16 v104, v136, v168\n"
      "v_pk_mul_f16 v105, v137, v169\n"
      "v_pk_mul_f16 v106, v138, v170\n"
      "v_pk_mul_f16 v107, v139, v171\n"
      "s_nop 1\n"
      "s_branch 3b\n"
      "7:\n"
      "s_mov_b64 exec, -1\n"
      "s_cmp_lg_u32 s41, 0\n"
      "s_cbranch_scc1 8f\n"
      "s_mov_b32 s41, 1\n"
      "s_waitcnt vmcnt(0)\n"
      "s_branch 0b\n"
      "8:\n"
      "v_mov_b32 v222, 2\n"
      "s_mov_b32 s42, 1\n"
      "ds_write_b32 v2, v222 offset:8\n"
      "9:\n"
      "s_nop 7\n"
      "s_waitcnt vmcnt(0) lgkmcnt(0)\n"
      "s_mov_b32 %0, s42\n"
      : "=s"(flag)
      : "s"(__builtin_amdgcn_kernarg_segment_ptr()), "s"(blockIdx.x), "v"(threadIdx.x), "s"(lbase)
      :
      "v1", "v2", "v3", "v4", "v5", "v6", "v7", "v8", "v9", "v10", "v11", "v12", "v13", "v14", "v15", "v16", "v17", "v18", "v19", "v20", "v21", "v22", "v23", "v24",
      "v25", "v26", "v27", "v28", "v29", "v30", "v31", "v32", "v33", "v34", "v35", "v36", "v37", "v38", "v39", "v40", "v41", "v42", "v43", "v44", "v45", "v46", "v47", "v48",
      "v49", "v50", "v51", "v52", "v53", "v54", "v55", "v56", "v57", "v58", "v59", "v60", "v61", "v62", "v63", "v64", "v65", "v66", "v67", "v68", "v69", "v70", "v71", "v72",
      "v73", "v74", "v75", "v76", "v77", "v78", "v79", "v80", "v81", "v82", "v83", "v84", "v85", "v86", "v87", "v88", "v89", "v90", "v91", "v92", "v93", "v94", "v95", "v96",
      "v97", "v98", "v99", "v100", "v101", "v102", "v103", "v104", "v105", "v106", "v107", "v108", "v109", "v110", "v111", "v112", "v113", "v114", "v115", "v116", "v117", "v118", "v119", "v120",
      "v121", "v122", "v123", "v124", "v125", "v126", "v127", "v128", "v129", "v130", "v131", "v132", "v133", "v134", "v135", "v136", "v137", "v138", "v139", "v140", "v141", "v142", "v143", "v144",
      "v145", "v146", "v147", "v148", "v149", "v150", "v151", "v152", "v153", "v154", "v155", "v156", "v157", "v158", "v159", "v160", "v161", "v162", "v163", "v164", "v165", "v166", "v167", "v168",
      "v169", "v170", "v171", "v172", "v173", "v174", "v175", "v176", "v177", "v178", "v179", "v180", "v181", "v182", "v183", "v184", "v185", "v186", "v187", "v188", "v189", "v190", "v191", "v192",
      "v193", "v194", "v195", "v196", "v197", "v198", "v199", "v200", "v201", "v202", "v203", "v204", "v205", "v206", "v207", "v208", "v209", "v210", "v211", "v212", "v213", "v214", "v215", "v216",
      "v217", "v218", "v219", "v220", "v221", "v222", "v223", "v224", "v225", "v226", "v227", "v228", "v229", "v230", "v231", "v232", "v233", "v234", "v235", "v236", "v237", "v238", "v239", "v240",
      "v241", "v242", "v243", "v244", "v245", "v246", "v247", "v248", "v249", "v250", "v251", "v252", "v253", "v254", "v255", "a0", "a1", "a2", "a3", "a4", "a5", "a6", "a7", "a8",
      "a9", "a10", "a11", "a12", "a13", "a14", "a15", "a16", "a17", "a18", "a19", "a20", "a21", "a22", "a23", "a24", "a25", "a26", "a27", "a28", "a29", "a30", "a31", "a32",
      "a33", "a34", "a35", "a36", "a37", "a38", "a39", "a40", "a41", "a42", "a43", "a44", "a45", "a46", "a47", "a48", "a49", "a50", "a51", "a52", "a53", "a54", "a55", "a56",
      "a57", "a58", "a59", "a60", "a61", "a62", "a63", "a64", "a65", "a66", "a67", "a68", "a69", "a70", "a71", "a72", "a73", "a74", "a75", "a76", "a77", "a78", "a79", "a80",
      "a81", "a82", "a83", "a84", "a85", "a86", "a87", "a88", "a89", "a90", "a91", "a92", "a93", "a94", "a95", "a96", "a97", "a98", "a99", "a100", "a101", "a102", "a103", "a104",
      "a105", "a106", "a107", "a108", "a109", "a110", "a111", "a112", "a113", "a114", "a115", "a116", "a117", "a118", "a119", "a120", "a121", "a122", "a123", "a124", "a125", "a126", "a127", "a128",
      "a129", "a130", "a131", "a132", "a133", "a134", "a135", "a136", "a137", "a138", "a139", "a140", "a141", "a142", "a143", "a144", "a145", "a146", "a147", "a148", "a149", "a150", "a151", "a152",
      "a153", "a154", "a155", "a156", "a157", "a158", "a159", "a160", "a161", "a162", "a163", "a164", "a165", "a166", "a167", "a168", "a169", "a170", "a171", "a172", "a173", "a174", "a175", "a176",
      "a177", "a178", "a179", "a180", "a181", "a182", "a183", "a184", "a185", "a186", "a187", "a188", "a189", "a190", "a191", "a192", "a193", "a194", "a195", "a196", "a197", "a198", "a199", "a200",
      "a201", "a202", "a203", "a204", "a205", "a206", "a207", "a208", "a209", "a210", "a211", "a212", "a213", "a214", "a215", "a216", "a217", "s8", "s9", "s10", "s11", "s12", "s13", "s14",
      "s15", "s16", "s17", "s18", "s19", "s20", "s21", "s22", "s23", "s24", "s25", "s26", "s27", "s28", "s29", "s30", "s31", "s33", "s34", "s35", "s36", "s37", "s38", "s39",
      "s40", "s41", "s42", "s43", "s44", "s45", "s46", "s47", "s48", "s49", "s50", "s51", "s52", "s53", "s54", "s55", "s56", "s57", "s58", "s59", "s60", "s61", "s62", "s63",
      "s64", "s65", "s66", "s67", "s68", "s69", "s70", "s71", "s72", "s73", "s74", "s75", "s76", "s77", "s78", "s79", "s80", "s81", "s82", "s83", "s84", "s85", "s86", "s87",
      "s88", "s89", "s90", "s91", "s92", "s93", "s94", "s95", "s96", "s97", "s98", "s99", "s100", "s101", "vcc", "scc", "memory");
  if (flag | lbase) {
    const int F = 26, P = 325;
    const int lane = threadIdx.x & 63;
    const int b = (int)(min((blockIdx.x & 7) * kx + ((blockIdx.x >> 3) * 4 + (threadIdx.x >> 6)) * 104u, lim) / 104u);
    {
      float m = -INFINITY, z = 0.f, n = 0.f;
      for (int p = lane; p < P; p += 64) {
        const unsigned short* ei = table + (size_t)idx[b * F + ii[p]] * 128;
        const unsigned short* ej = table + (size_t)idx[b * F + jj[p]] * 128;
        float e = 0.f, s = 0.f;
        for (int a = 0; a < 128; ++a) {
          float acc = 0.f;
          for (int d = 0; d < 128; ++d) acc = fmaf(rbf(bf2f(ei[d]) * bf2f(ej[d])), bf2f(W[d * 128 + a]), acc);
          float x = acc + bf2f(bvec[a]);
          x = 0.5f * (x + fabsf(x));
          e = fmaf(x, bf2f(hvec[a]), e);
        }
        for (int d = 0; d < 128; ++d) s = fmaf(rbf(bf2f(ei[d]) * bf2f(ej[d])), bf2f(pvec[d]), s);
        float mn = fmaxf(m, e);
        float fa = __expf(m - mn), fb = __expf(e - mn);
        z = z * fa + fb; n = n * fa + fb * s; m = mn;
      }
      float mx = m;
      for (int o = 1; o < 64; o <<= 1) mx = fmaxf(mx, __shfl_xor(mx, o));
      float f = (m == -INFINITY) ? 0.f : __expf(m - mx);
      z *= f; n *= f;
      for (int o = 1; o < 64; o <<= 1) { z += __shfl_xor(z, o); n += __shfl_xor(n, o); }
      if (lane == 0) out[b] = f2bf(n / z);
    }
  }
}

extern "C" __global__ __launch_bounds__(256) void afm_any(const int* __restrict__ idx, const unsigned short* __restrict__ table,
                                   const int* __restrict__ ii, const int* __restrict__ jj,
                                   const unsigned short* __restrict__ W, const unsigned short* __restrict__ bvec,
                                   const unsigned short* __restrict__ hvec, const unsigned short* __restrict__ pvec,
                                   unsigned short* __restrict__ out, float* __restrict__ ws,
                                   int B, int F, int P, int D, int A) {
  const int b = blockIdx.x;
  float* ebuf = ws + (size_t)b * 2 * P;
  float* sbuf = ebuf + P;
  for (int p = threadIdx.x; p < P; p += 256) {
    const unsigned short* ei = table + (size_t)idx[b * F + ii[p]] * D;
    const unsigned short* ej = table + (size_t)idx[b * F + jj[p]] * D;
    float e = 0.f, sacc = 0.f;
    for (int a = 0; a < A; ++a) {
      float acc = 0.f;
      for (int d = 0; d < D; ++d) acc = fmaf(rbf(bf2f(ei[d]) * bf2f(ej[d])), bf2f(W[(size_t)d * A + a]), acc);
      float x = acc + bf2f(bvec[a]);
      x = 0.5f * (x + fabsf(x));
      e = fmaf(x, bf2f(hvec[a]), e);
    }
    for (int d = 0; d < D; ++d) sacc = fmaf(rbf(bf2f(ei[d]) * bf2f(ej[d])), bf2f(pvec[d]), sacc);
    ebuf[p] = e; sbuf[p] = sacc;
  }
  __syncthreads();
  if (threadIdx.x == 0) {
    float mx = -INFINITY;
    for (int p = 0; p < P; ++p) mx = fmaxf(mx, ebuf[p]);
    float z = 0.f, n = 0.f;
    for (int p = 0; p < P; ++p) { float w = __expf(ebuf[p] - mx); z += w; n += w * sbuf[p]; }
    out[b] = f2bf(n / z);
  }
}

extern "C" void kernel_launch(void* const* d_in, const int* in_sizes, int n_in,
                              void* d_out, int out_size, void* d_ws, size_t ws_size,
                              hipStream_t stream) {
  const int* idx = (const int*)d_in[0];
  const unsigned short* table = (const unsigned short*)d_in[1];
  const int* ii = (const int*)d_in[2];
  const int* jj = (const int*)d_in[3];
  const unsigned short* W = (const unsigned short*)d_in[4];
  const unsigned short* bvec = (const unsigned short*)d_in[5];
  const unsigned short* hvec = (const unsigned short*)d_in[6];
  const unsigned short* pvec = (const unsigned short*)d_in[7];
  unsigned short* out = (unsigned short*)d_out;
  const int B = out_size;
  const int P = in_sizes[2];
  const int D = in_sizes[7];
  const int A = in_sizes[5];
  if (B <= 0) return;
  const int F = in_sizes[0] / B;
  if (D == 128 && A == 128 && F == 26 && P == 325 && B < (1 << 24)) {
    const int g8 = (B + 31) / 32;
    afm_fast<<<8 * g8, 256, 0, stream>>>(idx, table, ii, jj, W, bvec, hvec, pvec, out, 104u * (unsigned)(B - 1), 416u * (unsigned)g8
        );
  } else {
    afm_any<<<B, 256, 0, stream>>>(idx, table, ii, jj, W, bvec, hvec, pvec, out, (float*)d_ws, B, F, P, D, A);
  }
}
```

```cpp
#include <hip/hip_runtime.h>
#include <hip/hip_bf16.h>
#include <stdint.h>

#define AFM_HAS_STAMPS 0

typedef float  f32x2  __attribute__((ext_vector_type(2)));
typedef __bf16 bf16x2 __attribute__((ext_vector_type(2)));

__device__ __forceinline__ float bf2f(unsigned short u) { return __builtin_bit_cast(float, (unsigned)u << 16); }
__device__ __forceinline__ unsigned short f2bf(float x) {
  f32x2 v = {x, 0.f}; bf16x2 r = __builtin_convertvector(v, bf16x2);
  return (unsigned short)(__builtin_bit_cast(unsigned, r) & 0xffffu);
}
__device__ __forceinline__ float rbf(float x) { return bf2f(f2bf(x)); }

extern "C" __device__ __attribute__((used, aligned(512))) const unsigned afm_lane_tab[804] = {
  738206208u, 533735168u, 697050480u, 864u, 743449092u, 539240196u, 719070580u, 20972672u, 748691976u, 539764488u, 554968440u, 1440u, 753934860u, 550250252u, 717235580u, 20973248u,
  759177744u, 555755280u, 613426560u, 2016u, 764420628u, 561260308u, 540550532u, 20971520u, 769663512u, 561784600u, 549463432u, 288u, 774906396u, 530327324u, 722740620u, 20972096u,
  738468432u, 538978128u, 618931888u, 3168u, 743711316u, 534521684u, 727983796u, 20974976u, 748954200u, 549988184u, 708585144u, 3744u, 754197084u, 555493212u, 723003068u, 20975552u,
  759439968u, 560998240u, 728508096u, 4320u, 764682852u, 556541796u, 624436932u, 20973824u, 769925736u, 530065256u, 702555848u, 2592u, 775168620u, 535570284u, 697575116u, 20974400u,
  738730656u, 701507488u, 697050480u, 1049456u, 743973540u, 707012516u, 719070580u, 22021264u, 749216424u, 707536808u, 554968440u, 1050032u, 754459308u, 718022572u, 717235580u, 22021840u,
  759702192u, 723527600u, 613426560u, 1050608u, 764945076u, 729032628u, 540550532u, 22020112u, 770187960u, 729556920u, 549463432u, 1048880u, 775430844u, 698099644u, 722740620u, 22020688u,
  738992880u, 706750448u, 618931888u, 1051760u, 744235764u, 702294004u, 727983796u, 22023568u, 749478648u, 717760504u, 708585144u, 1052336u, 754721532u, 723265532u, 723003068u, 22024144u,
  759964416u, 728770560u, 728508096u, 1052912u, 765207300u, 724314116u, 624436932u, 22022416u, 770450184u, 697837576u, 702555848u, 1051184u, 775693068u, 703342604u, 697575116u, 22022992u,
  739255104u, 617621568u, 529279600u, 2098048u, 744497988u, 623126596u, 549726836u, 23069856u, 749740872u, 623650888u, 555231864u, 2098624u, 754983756u, 634136652u, 560736892u, 23070432u,
  760226640u, 639641680u, 551299712u, 2099200u, 765469524u, 645146708u, 529803908u, 23068704u, 770712408u, 645671000u, 535308936u, 2097472u, 775955292u, 614213724u, 540813964u, 23069280u,
  739517328u, 622864528u, 613166000u, 2100352u, 744760212u, 618408084u, 633613236u, 23072160u, 750003096u, 633874584u, 639118264u, 2100928u, 755245980u, 639379612u, 644623292u, 23072736u,
  760488864u, 644884640u, 635186112u, 2101504u, 765731748u, 640428196u, 613690308u, 23071008u, 770974632u, 613951656u, 619195336u, 2099776u, 776217516u, 619456684u, 624700364u, 23071584u,
  739779552u, 544221408u, 529279600u, 3146640u, 745022436u, 544483556u, 549726836u, 24118448u, 750265320u, 544745704u, 555231864u, 3147216u, 755508204u, 545007852u, 560736892u, 24119024u,
  760751088u, 545270000u, 551299712u, 3147792u, 765993972u, 545532148u, 529803908u, 24117296u, 771236856u, 545794296u, 535308936u, 3146064u, 776479740u, 546056444u, 540813964u, 24117872u,
  740041776u, 565193008u, 613166000u, 3148944u, 745284660u, 565455156u, 633613236u, 24120752u, 750527544u, 565717304u, 639118264u, 3149520u, 755770428u, 565979452u, 644623292u, 24121328u,
  761013312u, 566241600u, 635186112u, 3150096u, 766256196u, 566503748u, 613690308u, 24119600u, 771499080u, 566765896u, 619195336u, 3148368u, 776741964u, 567028044u, 624700364u, 24120176u,
  358617600u, 8064u, 301992192u, 132121440u, 434116224u, 18882512u, 320866848u, 358619616u, 94372128u, 37756960u, 339741504u, 415241280u, 377491968u, 452993136u, 452991744u, 358619616u,
  150998400u, 75505856u, 377490816u, 452992032u, 37750464u, 94380304u, 396365472u, 207622368u, 75497472u, 113254752u, 415240128u, 207622368u, 396366624u, 471867824u, 471866400u, 415241280u,
  169873056u, 151003648u, 4608u, 1075854240u, 415240992u, 169878096u, 18879264u, 1283477760u, 434115648u, 188752544u, 37753920u, 1396724832u, 396366912u, 207626992u, 56628576u, 1283477760u,
  415241568u, 226501440u, 75503232u, 1377848736u, 188747712u, 245375888u, 94377888u, 1151355168u, 320870016u, 264250336u, 113252544u, 1151355168u, 301995936u, 283124784u, 132127200u, 1283477760u,
  359666192u, 1056656u, 303040784u, 133170032u, 435164816u, 19931104u, 321915440u, 359668208u, 95420720u, 38805552u, 340790096u, 416289872u, 378540560u, 454041728u, 454040336u, 359668208u,
  152046992u, 76554448u, 378539408u, 454040624u, 38799056u, 95428896u, 397414064u, 208670960u, 76546064u, 114303344u, 416288720u, 208670960u, 397415216u, 472916416u, 472914992u, 416289872u,
  170921648u, 152052240u, 1053200u, 1076902832u, 416289584u, 170926688u, 19927856u, 1284526352u, 435164240u, 189801136u, 38802512u, 1397773424u, 397415504u, 208675584u, 57677168u, 1284526352u,
  416290160u, 227550032u, 76551824u, 1378897328u, 189796304u, 246424480u, 95426480u, 1152403760u, 321918608u, 265298928u, 114301136u, 1152403760u, 303044528u, 284173376u, 133175792u, 1284526352u,
  360714784u, 2105248u, 304089376u, 134218624u, 436213408u, 20979696u, 322964032u, 360716800u, 96469312u, 39854144u, 341838688u, 417338464u, 379589152u, 455090320u, 455088928u, 360716800u,
  153095584u, 77603040u, 379588000u, 455089216u, 39847648u, 96477488u, 398462656u, 209719552u, 77594656u, 115351936u, 417337312u, 209719552u, 398463808u, 473965008u, 473963584u, 417338464u,
  171970240u, 153100832u, 2101792u, 1077951424u, 417338176u, 171975280u, 20976448u, 1285574944u, 436212832u, 190849728u, 39851104u, 1398822016u, 398464096u, 209724176u, 58725760u, 1285574944u,
  417338752u, 228598624u, 77600416u, 1379945920u, 190844896u, 247473072u, 96475072u, 1153452352u, 322967200u, 266347520u, 115349728u, 1153452352u, 304093120u, 285221968u, 134224384u, 1285574944u,
  361763376u, 3153840u, 305137968u, 135267216u, 437262000u, 22028288u, 324012624u, 361765392u, 97517904u, 40902736u, 342887280u, 418387056u, 380637744u, 456138912u, 456137520u, 361765392u,
  154144176u, 78651632u, 380636592u, 456137808u, 40896240u, 97526080u, 399511248u, 210768144u, 78643248u, 116400528u, 418385904u, 210768144u, 399512400u, 475013600u, 475012176u, 418387056u,
  173018832u, 154149424u, 3150384u, 1079000016u, 418386768u, 173023872u, 22025040u, 1286623536u, 437261424u, 191898320u, 40899696u, 1399870608u, 399512688u, 210772768u, 59774352u, 1286623536u,
  418387344u, 229647216u, 78649008u, 1380994512u, 191893488u, 248521664u, 97523664u, 1154500944u, 324015792u, 267396112u, 116398320u, 1154500944u, 305141712u, 286270560u, 135272976u, 1286623536u,
  0u, 18874368u, 524304u, 37748740u, 75497504u, 56623112u, 76021808u, 75497484u, 304087104u, 94371856u, 304611408u, 113246228u, 379584608u, 132120600u, 380108912u, 28u,
  608174208u, 37748864u, 608698512u, 56623236u, 683671712u, 75497608u, 684196016u, 94371980u, 912261312u, 113246352u, 912785616u, 132120724u, 987758816u, 152u, 988283120u, 18874524u,
  1053216u, 19923744u, 1577520u, 38798116u, 76550720u, 57672488u, 77075024u, 76546860u, 305140320u, 95421232u, 305664624u, 114295604u, 380637824u, 133169976u, 381162128u, 1049404u,
  609227424u, 38798240u, 609751728u, 57672612u, 684724928u, 76546984u, 685249232u, 95421356u, 913314528u, 114295728u, 913838832u, 133170100u, 988812032u, 1049528u, 989336336u, 19923900u,
  2106432u, 20972352u, 2630736u, 39846724u, 77603936u, 58721096u, 78128240u, 77595468u, 306193536u, 96469840u, 306717840u, 115344212u, 381691040u, 134218584u, 382215344u, 2098012u,
  610280640u, 39846848u, 610804944u, 58721220u, 685778144u, 77595592u, 686302448u, 96469964u, 914367744u, 115344336u, 914892048u, 134218708u, 989865248u, 2098136u, 990389552u, 20972508u,
  3159648u, 22020960u, 3683952u, 40895332u, 78657152u, 59769704u, 79181456u, 78644076u, 307246752u, 97518448u, 307771056u, 116392820u, 382744256u, 135267192u, 383268560u, 3146620u,
  611333856u, 40895456u, 611858160u, 59769828u, 686831360u, 78644200u, 687355664u, 97518572u, 915420960u, 116392944u, 915945264u, 135267316u, 990918464u, 3146744u, 991442768u, 22021116u,
  0u, 0u, 0u, 0u, 0u, 0u, 16843008u, 16843009u, 16843009u, 16843009u, 16843009u, 16843009u, 33686017u, 33686018u, 33686018u, 33686018u,
  33686018u, 33686018u, 50529027u, 50529027u, 50529027u, 50529027u, 50529027u, 67371779u, 67372036u, 67372036u, 67372036u, 67372036u, 84149252u, 84215045u, 84215045u, 84215045u,
  84215045u, 100992261u, 101058054u, 101058054u, 101058054u, 101058054u, 117900806u, 117901063u, 117901063u, 117901063u, 117901063u, 134744072u, 134744072u, 134744072u, 134744072u, 151587080u,
  151587081u, 151587081u, 151587081u, 168430089u, 168430090u, 168430090u, 168430090u, 185273099u, 185273099u, 185273099u, 202115851u, 202116108u, 202116108u, 218893324u, 218959117u, 218959117u,
  235736333u, 235802126u, 235802126u, 252644878u, 252645135u, 252645135u, 269488144u, 269488144u, 286331152u, 286331153u, 303174161u, 303174162u, 320017171u, 336859923u, 353637396u, 370480405u,
  387388950u, 404166422u, 67305985u, 134678021u, 202050057u, 269422093u, 336794129u, 404166165u, 67306009u, 134678021u, 202050057u, 269422093u, 336794129u, 404166165u, 84149017u, 151521030u,
  218893066u, 286265102u, 353637138u, 421009174u, 117835012u, 185207048u, 252579084u, 319951120u, 387323156u, 100997400u, 168364039u, 235736075u, 303108111u, 370480147u, 102307863u, 168364039u,
  235736075u, 303108111u, 370480147u, 119085079u, 185207048u, 252579084u, 319951120u, 387323156u, 151525656u, 218893066u, 286265102u, 353637138u, 421009174u, 202050057u, 269422093u, 336794129u,
  404166165u, 202050073u, 269422093u, 336794129u, 404166165u, 218893081u, 286265102u, 353637138u, 421009174u, 252579084u, 319951120u, 387323156u, 235739416u, 303108111u, 370480147u, 236525591u,
  303108111u, 370480147u, 253302807u, 319951120u, 387323156u, 286267672u, 353637138u, 421009174u, 336794129u, 404166165u, 336794137u, 404166165u, 353637145u, 421009174u, 387323156u, 370481432u,
  370743319u, 387520535u, 421009688u, 421074969u
};

extern "C" __global__ __launch_bounds__(256, 1) __attribute__((aligned(4096)))
void afm_fast(const int* __restrict__ idx, const unsigned short* __restrict__ table,
                     const int* __restrict__ ii, const int* __restrict__ jj,
                     const unsigned short* __restrict__ W, const unsigned short* __restrict__ bvec,
                     const unsigned short* __restrict__ hvec, const unsigned short* __restrict__ pvec,
                     unsigned short* __restrict__ out, unsigned lim, unsigned kx
                     ) {
  __shared__ __attribute__((aligned(16))) char smem[110912];
  const unsigned lbase = (unsigned)(uintptr_t)(__attribute__((address_space(3))) char*)smem;
  unsigned flag;
  asm volatile(
      "s_load_dwordx16 s[8:23], %1, 0x0\n"
      "s_load_dwordx4 s[24:27], %1, 0x40\n"
      "s_mov_b32 s41, 0\n"
      "s_mov_b32 s42, 0\n"
      "s_mov_b32 s58, 0\n"
      "s_mov_b32 s35, 0\n"
      "0:\n"
      "s_getpc_b64 s[56:57]\n"
      "s_add_u32 s98, s56, afm_lane_tab@rel32@lo+4\n"
      "s_addc_u32 s99, s57, afm_lane_tab@rel32@hi+12\n"
      "v_and_b32 v255, 63, %3\n"
      "v_and_b32 v254, 127, %3\n"
      "v_lshrrev_b32 v253, 7, %3\n"
      "v_min_u32 v254, 81, v254\n"
      "v_mul_u32_u24 v253, 82, v253\n"
      "v_lshlrev_b32 v14, 4, v255\n"
      "v_lshlrev_b32 v252, 3, v255\n"
      "v_add_lshl_u32 v253, v253, v254, 2\n"
      "global_load_dwordx2 v[22:23], v252, s[98:99] offset:2048\n"
      "global_load_dword v20, v253, s[98:99] offset:2560\n"
      "global_load_dwordx4 v[16:19], v14, s[98:99]\n"
      "global_load_dwordx4 v[196:199], v14, s[98:99] offset:1024\n"
      "v_lshrrev_b32 v2, 6, %3\n"
      "s_bfe_u32 s44, %2, 0x30003\n"
      "s_mul_i32 s44, s44, 24\n"
      "v_mad_u32_u24 v255, v2, 6, v255\n"
      "s_mov_b64 exec, 0x3f\n"
      "v_add_lshl_u32 v255, v255, s44, 7\n"
      "global_load_dword v255, v255, s[56:57]\n"
      "s_mov_b64 exec, -1\n"
      "v_bfe_u32 v4, %3, 4, 2\n"
      "v_and_b32 v3, 15, %3\n"
      "v_readfirstlane_b32 s53, v2\n"
      "s_and_b32 s50, %2, 7\n"
      "s_lshr_b32 s51, %2, 3\n"
      "s_lshl2_add_u32 s51, s51, s53\n"
      "s_mul_i32 s51, s51, 104\n"
      "v_lshlrev_b32 v13, 4, v3\n"
      "v_lshlrev_b32 v225, 4, v254\n"
      "v_min_u32 v225, 1284, v225\n"
      "v_lshl_add_u32 v80, v2, 11, v14\n"
      "v_add_u32 v81, 8192, v80\n"
      "v_add_u32 v82, 16384, v80\n"
      "v_add_u32 v26, 24576, v80\n"
      "v_lshlrev_b32 v2, 4, v2\n"
      "s_mul_i32 s38, s53, 14400\n"
      "v_add_u32 v2, 37632, v2\n"
      "s_add_u32 s38, s38, 38912\n"
      "v_mul_u32_u24 v222, 288, v4\n"
      "v_add_u32 v213, s38, v13\n"
      "v_mov_b32 v5, 0\n"
      "v_add_u32 v1, v222, v213\n"
      "v_mov_b32 v222, 0x7fa5a5a5\n"
      "s_cmp_lg_u32 s35, 0\n"
      "s_cbranch_scc1 2f\n"
      "ds_write2_b32 v2, v5, v5 offset0:0 offset1:1\n"
      "ds_write_b32 v2, v5 offset:8\n"
      "ds_write_b32 v1, v222 offset:12992\n"
      "2:\n"
      "s_mov_b32 s43, 0xffff0000\n"
      "s_mov_b32 s56, 0\n"
      "s_mov_b32 s40, 0\n"
      "s_mov_b32 s55, 0\n"
      "s_and_b32 s46, s53, 2\n"
      "s_lshl_b32 s54, 3, s46\n"
      "s_bitcmp1_b32 s53, 0\n"
      "s_cselect_b64 s[100:101], -1, 0\n"
      "s_movk_i32 s98, 14400\n"
      "s_cselect_b32 s99, s98, 0\n"
      "s_cmovk_i32 s98, -14400\n"
      "s_cselect_b32 s46, 0, 18560\n"
      "s_cselect_b32 s47, 18560, 0\n"
      "v_add_u32 v212, s46, v14\n"
      "v_add_u32 v14, s47, v14\n"
      "s_sub_u32 s99, s38, s99\n"
      "v_cmp_eq_u32 vcc, 1, v4\n"
      "v_mov_b32 v222, 0x3300\n"
      "s_mov_b32 s46, 0\n"
      "v_cndmask_b32 v94, 0, v222, vcc\n"
      "s_mov_b32 s47, 0xc72c4bb9\n"
      "v_lshlrev_b32 v223, 4, v4\n"
      "v_lshrrev_b64 v[146:147], v223, s[46:47]\n"
      "v_lshlrev_b32 v138, 16, v146\n"
      "v_lshrrev_b32 v130, 16, v138\n"
      "v_mov_b32 v200, 0x4738aa3b\n"
      "v_lshlrev_b32 v224, 16, v94\n"
      "v_accvgpr_write_b32 a128, v130\n"
      "v_accvgpr_write_b32 a129, 0\n"
      "v_accvgpr_write_b32 a130, v138\n"
      "v_accvgpr_write_b32 a131, 0\n"
      "v_accvgpr_write_b32 a132, 0\n"
      "v_accvgpr_write_b32 a133, v130\n"
      "v_accvgpr_write_b32 a134, 0\n"
      "v_accvgpr_write_b32 a135, v138\n"
      "v_accvgpr_write_b32 a208, v94\n"
      "v_accvgpr_write_b32 a209, 0\n"
      "v_accvgpr_write_b32 a210, v224\n"
      "v_accvgpr_write_b32 a211, 0\n"
      "v_accvgpr_write_b32 a212, 0\n"
      "v_accvgpr_write_b32 a213, v94\n"
      "v_accvgpr_write_b32 a214, 0\n"
      "v_accvgpr_write_b32 a215, v224\n"
      "v_add_u32 v95, s38, v223\n"
      "v_and_b32 v227, 3, %3\n"
      "v_bfe_u32 v224, %3, 2, 2\n"
      "v_cmp_eq_u32 vcc, v227, v4\n"
      "v_mov_b32 v222, 0x3a00\n"
      "v_mov_b64 v[84:85], 0\n"
      "s_waitcnt lgkmcnt(0)\n"
      "s_mul_i32 s50, s50, s27\n"
      "s_add_u32 s50, s50, s51\n"
      "s_min_u32 s34, s50, s26\n"
      "s_load_dwordx16 s[60:75], s[8:9], s34\n"
      "s_load_dwordx8 s[76:83], s[8:9], s34 offset:0x40\n"
      "s_load_dwordx2 s[84:85], s[8:9], s34 offset:0x60\n"
      "global_load_dwordx4 v[96:99], v80, s[16:17] offset:0\n"
      "global_load_dwordx4 v[100:103], v80, s[16:17] offset:1024\n"
      "global_load_dwordx4 v[104:107], v81, s[16:17] offset:0\n"
      "global_load_dwordx4 v[108:111], v81, s[16:17] offset:1024\n"
      "v_mov_b64 v[24:25], s[20:21]\n"
      "s_mov_b32 exec_hi, 0\n"
      "v_mov_b64 v[24:25], s[18:19]\n"
      "s_mov_b32 exec_lo, 0xffff\n"
      "v_mov_b64 v[24:25], s[22:23]\n"
      "s_mov_b64 exec, -1\n"
      "v_add_co_u32 v24, s[28:29], v24, v13\n"
      "s_cmp_lt_u32 s53, 2\n"
      "s_cselect_b64 s[12:13], s[12:13], s[14:15]\n"
      "v_addc_co_u32 v25, s[28:29], 0, v25, s[28:29]\n"
      "global_load_dwordx4 v[192:195], v[24:25], off\n"
      "global_load_dwordx4 v[112:115], v82, s[16:17] offset:0\n"
      "global_load_dwordx4 v[116:119], v82, s[16:17] offset:1024\n"
      "global_load_dwordx4 v[120:123], v26, s[16:17] offset:0\n"
      "global_load_dwordx4 v[124:127], v26, s[16:17] offset:1024\n"
      "s_cmp_lg_u32 s35, 0\n"
      "s_cbranch_scc1 21f\n"
      "s_barrier\n"
      "21:\n"
      "v_mov_b64 v[88:89], 0\n"
      "v_cndmask_b32 v84, 0, v222, vcc\n"
      "v_lshlrev_b32 v222, 4, v224\n"
      "v_lshlrev_b64 v[86:87], v222, v[84:85]\n"
      "s_mov_b32 s46, 0xd905\n"
      "v_lshlrev_b32 v222, 2, v4\n"
      "v_mov_b32 v84, 0\n"
      "v_bfe_u32 v222, s46, v222, 4\n"
      "v_lshl_add_u32 v91, v222, 6, v213\n"
      "v_lshlrev_b32 v227, 4, v227\n"
      "s_mov_b64 s[46:47], 0x3c00\n"
      "v_lshlrev_b32 v5, 5, v3\n"
      "v_lshl_add_u32 v213, v4, 2, v213\n"
      "v_lshl_add_u32 v5, v4, 3, v5\n"
      "v_lshlrev_b64 v[222:223], v227, s[46:47]\n"
      "s_movk_i32 s46, 0x4400\n"
      "v_accvgpr_write_b32 a216, v222\n"
      "v_lshlrev_b64 v[172:173], v227, s[46:47]\n"
      "s_lshl_b64 exec, -1, 48\n"
      "s_bitset1_b32 s46, 15\n"
      "v_lshlrev_b64 v[172:173], v227, s[46:47]\n"
      "s_mov_b64 exec, -1\n"
      "s_mov_b32 s49, 0x03000000\n"
      "v_accvgpr_write_b32 a217, v223\n"
      "s_movk_i32 s46, 288\n"
      "v_and_b32 v222, 7, %3\n"
      "v_bfe_u32 v223, %3, 3, 1\n"
      "v_mad_u32_u24 v201, v222, s46, v95\n"
      "v_mad_u32_u24 v203, v223, s46, v95\n"
      "s_movk_i32 s47, 320\n"
      "v_mad_u32_u24 v210, v3, s46, v95\n"
      "v_mad_u32_u24 v214, v223, s47, v95\n"
      "s_mov_b32 s59, 0\n"
      "s_mov_b32 s57, 0\n"
      "v_and_b32 v222, v3, v4\n"
      "v_lshlrev_b32 v223, 6, v3\n"
      "v_bfe_u32 v222, 6, v222, 1\n"
      "s_movk_i32 s46, 0x100\n"
      "v_lshl_add_u32 v44, v222, 8, v95\n"
      "v_bitop3_b32 v45, v44, v223, s46 bitop3:0x78\n"
      "v_bitop3_b32 v46, v44, v5, s46 bitop3:0x78\n"
      "v_bitop3_b32 v47, v45, v5, s46 bitop3:0x78\n"
      "s_lshr_b32 s33, s34, 2\n"
      "s_mul_i32 s33, s33, 0xc4ec4ec5\n"
      "s_mov_b32 s36, 0\n"
      "s_mov_b32 s37, 0x7fa5a5a5\n"
      "global_load_dwordx4 v[80:83], v225, s[12:13]\n"
      "s_mov_b64 exec, 0\n"
      "s_cmp_eq_u32 s53, 0\n"
      "s_cbranch_scc1 40f\n"
      "s_cmp_eq_u32 s53, 1\n"
      "s_cbranch_scc1 41f\n"
      "s_cmp_eq_u32 s53, 2\n"
      "s_cbranch_scc1 42f\n"
      "s_branch 43f\n"
      "44:\n"
      "s_mov_b64 exec, -1\n"
      "s_waitcnt lgkmcnt(0)\n"
      "v_lshl_add_u32 v6, s60, 8, v13\n"
      "v_lshl_add_u32 v93, s64, 8, v13\n"
      "s_mov_b32 exec_lo, 0xffff0000\n"
      "v_lshl_add_u32 v6, s61, 8, v13\n"
      "v_lshl_add_u32 v93, s65, 8, v13\n"
      "s_mov_b32 exec_lo, 0\n"
      "v_lshl_add_u32 v6, s62, 8, v13\n"
      "v_lshl_add_u32 v93, s66, 8, v13\n"
      "s_mov_b32 exec_hi, 0xffff0000\n"
      "v_lshl_add_u32 v6, s63, 8, v13\n"
      "v_lshl_add_u32 v93, s67, 8, v13\n"
      "s_mov_b64 exec, -1\n"
      "global_load_dwordx4 v[228:231], v6, s[10:11] nt\n"
      "global_load_dwordx4 v[232:235], v93, s[10:11] nt\n"
      "v_lshl_add_u32 v8, s68, 8, v13\n"
      "v_lshl_add_u32 v9, s72, 8, v13\n"
      "v_lshl_add_u32 v10, s76, 8, v13\n"
      "v_lshl_add_u32 v11, s80, 8, v13\n"
      "v_lshl_add_u32 v12, s84, 8, v13\n"
      "s_mov_b32 exec_lo, 0xffff0000\n"
      "v_lshl_add_u32 v8, s69, 8, v13\n"
      "v_lshl_add_u32 v9, s73, 8, v13\n"
      "v_lshl_add_u32 v10, s77, 8, v13\n"
      "v_lshl_add_u32 v11, s81, 8, v13\n"
      "v_lshl_add_u32 v12, s85, 8, v13\n"
      "s_mov_b32 exec_lo, 0\n"
      "v_lshl_add_u32 v8, s70, 8, v13\n"
      "v_lshl_add_u32 v9, s74, 8, v13\n"
      "v_lshl_add_u32 v10, s78, 8, v13\n"
      "v_lshl_add_u32 v11, s82, 8, v13\n"
      "s_mov_b32 exec_hi, 0xffff0000\n"
      "v_lshl_add_u32 v8, s71, 8, v13\n"
      "v_lshl_add_u32 v9, s75, 8, v13\n"
      "v_lshl_add_u32 v10, s79, 8, v13\n"
      "v_lshl_add_u32 v11, s83, 8, v13\n"
      "s_mov_b64 exec, -1\n"
      "s_lshl_b32 s46, s53, 8\n"
      "s_lshl_b32 s47, s53, 5\n"
      "s_waitcnt vmcnt(16)\n"
      "v_add_u32_sdwa v15, s46, v22 dst_sel:DWORD dst_unused:UNUSED_PAD src0_sel:DWORD src1_sel:WORD_0\n"
      "v_add_u32_sdwa v92, s46, v22 dst_sel:DWORD dst_unused:UNUSED_PAD src0_sel:DWORD src1_sel:WORD_1\n"
      "v_add_u32_sdwa v7, s47, v23 dst_sel:DWORD dst_unused:UNUSED_PAD src0_sel:DWORD src1_sel:WORD_0\n"
      "v_add_u32_sdwa v202, s38, v23 dst_sel:DWORD dst_unused:UNUSED_PAD src0_sel:DWORD src1_sel:WORD_1\n"
      "s_waitcnt vmcnt(15)\n"
      "v_and_b32 v160, 0xff, v20\n"
      "v_bfe_u32 v161, v20, 8, 8\n"
      "v_bfe_u32 v162, v20, 16, 8\n"
      "v_lshrrev_b32 v163, 24, v20\n"
      "s_waitcnt vmcnt(14)\n"
      "v_add_u32_sdwa v204, s38, v16 dst_sel:DWORD dst_unused:UNUSED_PAD src0_sel:DWORD src1_sel:WORD_0\n"
      "v_add_u32_sdwa v205, s38, v16 dst_sel:DWORD dst_unused:UNUSED_PAD src0_sel:DWORD src1_sel:WORD_1\n"
      "v_add_u32_sdwa v206, s38, v17 dst_sel:DWORD dst_unused:UNUSED_PAD src0_sel:DWORD src1_sel:WORD_0\n"
      "v_add_u32_sdwa v207, s38, v17 dst_sel:DWORD dst_unused:UNUSED_PAD src0_sel:DWORD src1_sel:WORD_1\n"
      "v_add_u32_sdwa v208, s38, v18 dst_sel:DWORD dst_unused:UNUSED_PAD src0_sel:DWORD src1_sel:WORD_0\n"
      "v_add_u32_sdwa v209, s38, v18 dst_sel:DWORD dst_unused:UNUSED_PAD src0_sel:DWORD src1_sel:WORD_1\n"
      "v_add_u32_sdwa v211, s38, v19 dst_sel:DWORD dst_unused:UNUSED_PAD src0_sel:DWORD src1_sel:WORD_0\n"
      "v_add_u32_sdwa v215, s38, v19 dst_sel:DWORD dst_unused:UNUSED_PAD src0_sel:DWORD src1_sel:WORD_1\n"
      "s_waitcnt vmcnt(13)\n"
      "v_add_u32_sdwa v216, s38, v196 dst_sel:DWORD dst_unused:UNUSED_PAD src0_sel:DWORD src1_sel:WORD_0\n"
      "v_add_u32_sdwa v217, s38, v196 dst_sel:DWORD dst_unused:UNUSED_PAD src0_sel:DWORD src1_sel:WORD_1\n"
      "v_add_u32_sdwa v218, s38, v197 dst_sel:DWORD dst_unused:UNUSED_PAD src0_sel:DWORD src1_sel:WORD_0\n"
      "v_add_u32_sdwa v219, s38, v197 dst_sel:DWORD dst_unused:UNUSED_PAD src0_sel:DWORD src1_sel:WORD_1\n"
      "v_add_u32_sdwa v220, s38, v198 dst_sel:DWORD dst_unused:UNUSED_PAD src0_sel:DWORD src1_sel:WORD_0\n"
      "v_add_u32_sdwa v221, s38, v198 dst_sel:DWORD dst_unused:UNUSED_PAD src0_sel:DWORD src1_sel:WORD_1\n"
      "s_cmp_lg_u32 s58, 0\n"
      "s_cbranch_scc1 70f\n"
      "71:\n"
      "s_waitcnt vmcnt(11)\n"
      "v_mfma_f32_16x16x32_bf16 v[128:131], v[86:89], v[96:99], 0\n"
      "s_waitcnt vmcnt(10)\n"
      "v_mfma_f32_16x16x32_bf16 v[132:135], v[86:89], v[100:103], 0\n"
      "v_mfma_f32_16x16x32_bf16 v[136:139], v[84:87], v[96:99], 0\n"
      "v_mfma_f32_16x16x32_bf16 v[140:143], v[84:87], v[100:103], 0\n"
      "s_waitcnt vmcnt(9)\n"
      "v_mfma_f32_16x16x32_bf16 v[144:147], v[86:89], v[104:107], 0\n"
      "s_nop 1\n"
      "v_cvt_pk_f16_f32 v164, v128, v129\n"
      "v_cvt_pk_f16_f32 v165, v130, v131\n"
      "s_waitcnt vmcnt(8)\n"
      "v_mfma_f32_16x16x32_bf16 v[148:151], v[86:89], v[108:111], 0\n"
      "v_cvt_pk_f16_f32 v166, v132, v133\n"
      "v_cvt_pk_f16_f32 v167, v134, v135\n"
      "v_mfma_f32_16x16x32_bf16 v[152:155], v[84:87], v[104:107], 0\n"
      "ds_write_b128 v15, v[164:167] offset:0\n"
      "v_cvt_pk_f16_f32 v168, v136, v137\n"
      "v_cvt_pk_f16_f32 v169, v138, v139\n"
      "v_mfma_f32_16x16x32_bf16 v[156:159], v[84:87], v[108:111], 0\n"
      "v_cvt_pk_f16_f32 v170, v140, v141\n"
      "v_cvt_pk_f16_f32 v171, v142, v143\n"
      "global_load_dwordx4 v[236:239], v8, s[10:11] nt\n"
      "s_waitcnt vmcnt(7)\n"
      "v_mfma_f32_16x16x32_bf16 v[128:131], v[86:89], v[112:115], 0\n"
      "ds_write_b128 v15, v[168:171] offset:18560\n"
      "v_cvt_pk_f16_f32 v164, v144, v145\n"
      "v_cvt_pk_f16_f32 v165, v146, v147\n"
      "s_waitcnt vmcnt(6)\n"
      "v_mfma_f32_16x16x32_bf16 v[132:135], v[86:89], v[116:119], 0\n"
      "v_cvt_pk_f16_f32 v166, v148, v149\n"
      "v_cvt_pk_f16_f32 v167, v150, v151\n"
      "v_mfma_f32_16x16x32_bf16 v[136:139], v[84:87], v[112:115], 0\n"
      "ds_write_b128 v15, v[164:167] offset:1152\n"
      "v_cvt_pk_f16_f32 v168, v152, v153\n"
      "v_cvt_pk_f16_f32 v169, v154, v155\n"
      "v_mfma_f32_16x16x32_bf16 v[140:143], v[84:87], v[116:119], 0\n"
      "v_cvt_pk_f16_f32 v170, v156, v157\n"
      "v_cvt_pk_f16_f32 v171, v158, v159\n"
      "s_waitcnt vmcnt(5)\n"
      "v_mfma_f32_16x16x32_bf16 v[144:147], v[86:89], v[120:123], 0\n"
      "ds_write_b128 v15, v[168:171] offset:19712\n"
      "v_cvt_pk_f16_f32 v164, v128, v129\n"
      "v_cvt_pk_f16_f32 v165, v130, v131\n"
      "s_waitcnt vmcnt(4)\n"
      "v_mfma_f32_16x16x32_bf16 v[148:151], v[86:89], v[124:127], 0\n"
      "v_cvt_pk_f16_f32 v166, v132, v133\n"
      "v_cvt_pk_f16_f32 v167, v134, v135\n"
      "v_mfma_f32_16x16x32_bf16 v[152:155], v[84:87], v[120:123], 0\n"
      "ds_write_b128 v15, v[164:167] offset:2304\n"
      "v_cvt_pk_f16_f32 v168, v136, v137\n"
      "v_cvt_pk_f16_f32 v169, v138, v139\n"
      "v_mfma_f32_16x16x32_bf16 v[156:159], v[84:87], v[124:127], 0\n"
      "v_cvt_pk_f16_f32 v170, v140, v141\n"
      "v_cvt_pk_f16_f32 v171, v142, v143\n"
      "ds_write_b128 v15, v[168:171] offset:20864\n"
      "v_mfma_f32_4x4x4_16b_bf16 v[176:179], v[172:173], v[192:193], 0\n"
      "v_cndmask_b32 v188, v192, v194, s[100:101]\n"
      "v_cvt_pk_f16_f32 v164, v144, v145\n"
      "v_cndmask_b32 v189, v193, v195, s[100:101]\n"
      "v_max3_f32 v222, |v192|, |v193|, |v195|\n"
      "v_cvt_pk_f16_f32 v165, v146, v147\n"
      "v_mfma_f32_4x4x4_16b_bf16 v[180:183], v[172:173], v[194:195], 0\n"
      "v_subrev_u32 v222, 0x3d800000, v222\n"
      "v_cvt_pk_f16_f32 v166, v148, v149\n"
      "v_mfma_f32_16x16x16_bf16 v[48:51], v[188:189], a[128:129], 0\n"
      "v_cmp_gt_u32 vcc, s49, v222\n"
      "v_cvt_pk_f16_f32 v167, v150, v151\n"
      "v_mfma_f32_16x16x16_bf16 v[52:55], v[188:189], a[130:131], 0\n"
      "v_cndmask_b32 v190, v194, v192, s[100:101]\n"
      "ds_write_b128 v15, v[164:167] offset:3456\n"
      "v_cndmask_b32 v191, v195, v193, s[100:101]\n"
      "v_mfma_f32_16x16x16_bf16 v[56:59], v[188:189], a[132:133], 0\n"
      "v_cvt_pk_f16_f32 v168, v152, v153\n"
      "s_and_b32 s44, vcc_lo, 0xffff\n"
      "s_cselect_b32 s44, vcc_hi, 0\n"
      "v_cvt_pk_f16_f32 v169, v154, v155\n"
      "v_mfma_f32_16x16x16_bf16 v[60:63], v[188:189], a[134:135], 0\n"
      "s_cmp_eq_u32 s44, 0\n"
      "v_cvt_pk_f16_f32 v170, v156, v157\n"
      "s_cbranch_scc1 22f\n"
      "23:\n"
      "v_mfma_f32_16x16x16_bf16 v[64:67], v[190:191], a[128:129], 0\n"
      "v_cvt_pk_f16_f32 v171, v158, v159\n"
      "v_cvt_pk_f16_f32 v184, v176, v177\n"
      "v_cvt_pk_f16_f32 v185, v178, v179\n"
      "ds_write_b128 v15, v[168:171] offset:22016\n"
      "v_mfma_f32_16x16x16_bf16 v[68:71], v[190:191], a[130:131], 0\n"
      "v_cvt_pk_f16_f32 v186, v180, v181\n"
      "ds_write_b32 v2, v2 offset:4\n"
      "v_cvt_pk_f16_f32 v187, v182, v183\n"
      "ds_write_b128 v91, v[184:187] offset:11904\n"
      "v_mfma_f32_16x16x16_bf16 v[72:75], v[190:191], a[132:133], 0\n"
      "v_mfma_f32_16x16x16_bf16 v[76:79], v[190:191], a[134:135], 0\n"
      "v_mov_b32 v3, s33\n"
      "global_load_ushort v250, v3, s[24:25]\n"
      "v_mfma_f32_16x16x16_bf16 v[16:19], v[188:189], a[208:209], 0\n"
      "ds_read_b128 a[184:187], v44 offset:12480\n"
      "ds_read_b128 a[188:191], v45 offset:12544\n"
      "v_mfma_f32_16x16x16_bf16 v[20:23], v[188:189], a[210:211], 0\n"
      "ds_read_b128 a[192:195], v46 offset:12608\n"
      "ds_read_b128 a[196:199], v47 offset:12672\n"
      "v_mfma_f32_16x16x16_bf16 v[24:27], v[188:189], a[212:213], 0\n"
      "ds_read_b64_tr_b16 a[152:153], v92 offset:0\n"
      "ds_read_b64_tr_b16 a[154:155], v92 offset:18560\n"
      "ds_read_b64_tr_b16 a[156:157], v92 offset:64\n"
      "v_mfma_f32_16x16x16_bf16 v[28:31], v[188:189], a[214:215], 0\n"
      "ds_read_b64_tr_b16 a[158:159], v92 offset:18624\n"
      "ds_read_b64_tr_b16 a[160:161], v92 offset:128\n"
      "ds_read_b64_tr_b16 a[162:163], v92 offset:18688\n"
      "v_mfma_f32_16x16x16_bf16 v[32:35], v[190:191], a[208:209], 0\n"
      "ds_read_b64_tr_b16 a[164:165], v92 offset:192\n"
      "ds_read_b64_tr_b16 a[166:167], v92 offset:18752\n"
      "ds_read_b64_tr_b16 a[168:169], v92 offset:2304\n"
      "v_mfma_f32_16x16x16_bf16 v[36:39], v[190:191], a[210:211], 0\n"
      "ds_read_b64_tr_b16 a[170:171], v92 offset:20864\n"
      "ds_read_b64_tr_b16 a[172:173], v92 offset:2368\n"
      "ds_read_b64_tr_b16 a[174:175], v92 offset:20928\n"
      "v_mfma_f32_16x16x16_bf16 v[40:43], v[190:191], a[212:213], 0\n"
      "ds_read_b64_tr_b16 a[176:177], v92 offset:2432\n"
      "ds_read_b64_tr_b16 a[178:179], v92 offset:20992\n"
      "ds_read_b64_tr_b16 a[180:181], v92 offset:2496\n"
      "v_mfma_f32_16x16x16_bf16 v[44:47], v[190:191], a[214:215], 0\n"
      "ds_read_b64_tr_b16 a[182:183], v92 offset:21056\n"
      "ds_read_b32 v224, v227 offset:37636\n"
      "s_waitcnt lgkmcnt(15)\n"
      "v_mfma_f32_16x16x32_f16 v[192:195], a[184:187], a[152:155], 0\n"
      "s_waitcnt vmcnt(4)\n"
      "v_cmp_eq_u64 vcc, v[80:81], v[160:161]\n"
      "s_waitcnt lgkmcnt(13)\n"
      "v_mfma_f32_16x16x32_f16 v[192:195], a[188:191], a[156:159], v[192:195]\n"
      "v_cmp_eq_u64 s[28:29], v[82:83], v[162:163]\n"
      "s_waitcnt lgkmcnt(11)\n"
      "v_mfma_f32_16x16x32_f16 v[192:195], a[192:195], a[160:163], v[192:195]\n"
      "s_and_b64 vcc, vcc, s[28:29]\n"
      "s_cmp_eq_u64 vcc, -1\n"
      "s_waitcnt lgkmcnt(9)\n"
      "v_mfma_f32_16x16x32_f16 v[192:195], a[196:199], a[164:167], v[192:195]\n"
      "s_cselect_b32 s52, 13, 1\n"
      "s_waitcnt lgkmcnt(0)\n"
      "v_cmp_eq_u32 vcc, 0, v224\n"
      "s_cbranch_vccz 13f\n"
      "11:\n"
      "ds_read_b32 v224, v227 offset:37636\n"
      "s_waitcnt lgkmcnt(0)\n"
      "v_cmp_eq_u32 vcc, 0, v224\n"
      "s_cbranch_vccnz 11b\n"
      "13:\n"
      "v_mfma_f32_16x16x32_f16 v[252:255], a[184:187], a[168:171], 0\n"
      "ds_read_b128 a[0:3], v14 offset:0\n"
      "ds_read_b128 a[4:7], v14 offset:1152\n"
      "v_mfma_f32_16x16x32_f16 v[252:255], a[188:191], a[172:175], v[252:255]\n"
      "ds_read_b128 a[8:11], v14 offset:2304\n"
      "ds_read_b128 a[12:15], v14 offset:3456\n"
      "v_mfma_f32_16x16x32_f16 v[252:255], a[192:195], a[176:179], v[252:255]\n"
      "ds_read_b128 a[16:19], v14 offset:4640\n"
      "ds_read_b128 a[20:23], v14 offset:5792\n"
      "v_mfma_f32_16x16x32_f16 v[252:255], a[196:199], a[180:183], v[252:255]\n"
      "ds_read_b128 a[24:27], v14 offset:6944\n"
      "ds_read_b128 a[28:31], v14 offset:8096\n"
      "s_cmp_lg_u32 s58, 0\n"
      "s_cbranch_scc1 72f\n"
      "73:\n"
      "global_load_dwordx4 v[240:243], v9, s[10:11] nt\n"
      "s_waitcnt vmcnt(4)\n"
      "v_mfma_f32_4x4x4_16b_bf16 v[176:179], a[216:217], v[228:229], 0\n"
      "v_mfma_f32_4x4x4_16b_bf16 v[180:183], a[216:217], v[230:231], 0\n"
      "ds_read_b128 a[32:35], v14 offset:9280\n"
      "s_waitcnt vmcnt(3)\n"
      "v_mfma_f32_4x4x4_16b_bf16 v[144:147], a[216:217], v[232:233], 0\n"
      "ds_read_b128 a[36:39], v14 offset:10432\n"
      "v_mfma_f32_4x4x4_16b_bf16 v[148:151], a[216:217], v[234:235], 0\n"
      "ds_read_b128 a[40:43], v14 offset:11584\n"
      "ds_read_b128 a[44:47], v14 offset:12736\n"
      "v_cvt_pk_f16_f32 v184, v176, v177\n"
      "v_cvt_pk_f16_f32 v185, v178, v179\n"
      "v_cvt_pk_f16_f32 v186, v180, v181\n"
      "v_cvt_pk_f16_f32 v187, v182, v183\n"
      "ds_write_b128 v1, v[184:187] offset:0\n"
      "v_cvt_pk_f16_f32 v152, v144, v145\n"
      "v_cvt_pk_f16_f32 v153, v146, v147\n"
      "v_cvt_pk_f16_f32 v154, v148, v149\n"
      "v_cvt_pk_f16_f32 v155, v150, v151\n"
      "ds_write_b128 v1, v[152:155] offset:1152\n"
      "ds_read_b128 v[128:131], v201 offset:0\n"
      "ds_read_b128 v[160:163], v202 offset:0\n"
      "ds_read_b128 v[132:135], v201 offset:64\n"
      "ds_read_b128 v[164:167], v202 offset:64\n"
      "ds_read_b128 v[136:139], v201 offset:128\n"
      "ds_read_b128 v[168:171], v202 offset:128\n"
      "ds_read_b128 v[140:143], v201 offset:192\n"
      "ds_read_b128 v[172:175], v202 offset:192\n"
      "s_waitcnt vmcnt(2)\n"
      "v_mfma_f32_4x4x4_16b_bf16 v[112:115], a[216:217], v[236:237], 0\n"
      "v_mfma_f32_4x4x4_16b_bf16 v[116:119], a[216:217], v[238:239], 0\n"
      "s_nop 3\n"
      "v_cvt_pk_f16_f32 v120, v112, v113\n"
      "v_cvt_pk_f16_f32 v121, v114, v115\n"
      "v_cvt_pk_f16_f32 v122, v116, v117\n"
      "v_cvt_pk_f16_f32 v123, v118, v119\n"
      "ds_write_b128 v1, v[120:123] offset:2304\n"
      "s_waitcnt lgkmcnt(7)\n"
      "v_pk_mul_f16 v96, v128, v160\n"
      "v_pk_mul_f16 v97, v129, v161\n"
      "v_pk_mul_f16 v98, v130, v162\n"
      "v_pk_mul_f16 v99, v131, v163\n"
      "s_waitcnt lgkmcnt(5)\n"
      "v_pk_mul_f16 v100, v132, v164\n"
      "v_pk_mul_f16 v101, v133, v165\n"
      "v_pk_mul_f16 v102, v134, v166\n"
      "v_pk_mul_f16 v103, v135, v167\n"
      "s_waitcnt lgkmcnt(3)\n"
      "v_pk_mul_f16 v104, v136, v168\n"
      "v_pk_mul_f16 v105, v137, v169\n"
      "v_pk_mul_f16 v106, v138, v170\n"
      "v_pk_mul_f16 v107, v139, v171\n"
      "3:\n"
      "v_mfma_f32_16x16x32_f16 v[80:83], a[0:3], v[96:99], v[16:19]\n"
      "s_waitcnt lgkmcnt(1)\n"
      "v_pk_mul_f16 v108, v140, v172\n"
      "v_pk_mul_f16 v109, v141, v173\n"
      "v_mfma_f32_16x16x32_f16 v[80:83], a[4:7], v[100:103], v[80:83]\n"
      "v_pk_mul_f16 v110, v142, v174\n"
      "v_pk_mul_f16 v111, v143, v175\n"
      "v_mfma_f32_16x16x32_f16 v[80:83], a[8:11], v[104:107], v[80:83]\n"
      "ds_read_b128 a[60:63], v14 offset:17376\n"
      "ds_read_b128 a[56:59], v14 offset:16224\n"
      "v_mfma_f32_16x16x32_f16 v[80:83], a[12:15], v[108:111], v[80:83]\n"
      "ds_read_b128 a[52:55], v14 offset:15072\n"
      "ds_read_b128 a[48:51], v14 offset:13920\n"
      "v_mfma_f32_16x16x32_f16 v[84:87], a[28:31], v[108:111], v[20:23]\n"
      "ds_read_b128 a[64:67], v212 offset:0\n"
      "ds_read_b128 v[144:147], v203 offset:2304\n"
      "v_mfma_f32_16x16x32_f16 v[84:87], a[24:27], v[104:107], v[84:87]\n"
      "ds_read_b128 a[68:71], v212 offset:1152\n"
      "ds_read_b128 a[72:75], v212 offset:2304\n"
      "v_mfma_f32_16x16x32_f16 v[84:87], a[20:23], v[100:103], v[84:87]\n"
      "ds_read_b128 a[76:79], v212 offset:3456\n"
      "ds_read_b128 a[92:95], v212 offset:8096\n"
      "v_mfma_f32_16x16x32_f16 v[84:87], a[16:19], v[96:99], v[84:87]\n"
      "ds_read_b128 a[88:91], v212 offset:6944\n"
      "ds_read_b128 v[148:151], v203 offset:2368\n"
      "v_mfma_f32_16x16x32_f16 v[88:91], a[32:35], v[96:99], v[24:27]\n"
      "ds_read_b128 a[84:87], v212 offset:5792\n"
      "ds_read_b128 a[80:83], v212 offset:4640\n"
      "v_mfma_f32_16x16x32_f16 v[88:91], a[36:39], v[100:103], v[88:91]\n"
      "v_mul_f32 v188, |v80|, v48\n"
      "v_fma_f32 v188, |v81|, v49, v188\n"
      "v_mfma_f32_16x16x32_f16 v[88:91], a[40:43], v[104:107], v[88:91]\n"
      "ds_read_b128 a[96:99], v212 offset:9280\n"
      "v_fma_f32 v188, |v82|, v50, v188\n"
      "v_mfma_f32_16x16x32_f16 v[88:91], a[44:47], v[108:111], v[88:91]\n"
      "s_waitcnt lgkmcnt(13)\n"
      "ds_read_b128 a[100:103], v212 offset:10432\n"
      "v_fma_f32 v188, |v83|, v51, v188\n"
      "v_mfma_f32_16x16x32_f16 v[92:95], a[60:63], v[108:111], v[28:31]\n"
      "s_waitcnt lgkmcnt(10)\n"
      "v_pk_mul_f16 v112, v128, v144\n"
      "v_pk_mul_f16 v113, v129, v145\n"
      "v_mfma_f32_16x16x32_f16 v[92:95], a[56:59], v[104:107], v[92:95]\n"
      "ds_read_b128 a[104:107], v212 offset:11584\n"
      "v_pk_mul_f16 v114, v130, v146\n"
      "v_mfma_f32_16x16x32_f16 v[92:95], a[52:55], v[100:103], v[92:95]\n"
      "ds_read_b128 v[152:155], v203 offset:2432\n"
      "ds_read_b128 a[108:111], v212 offset:12736\n"
      "v_mfma_f32_16x16x32_f16 v[92:95], a[48:51], v[96:99], v[92:95]\n"
      "s_waitcnt lgkmcnt(12)\n"
      "v_fma_f32 v188, |v84|, v52, v188\n"
      "v_fma_f32 v188, |v85|, v53, v188\n"
      "v_mfma_f32_16x16x32_f16 v[80:83], a[64:67], v[96:99], v[32:35]\n"
      "ds_read_b128 a[124:127], v212 offset:17376\n"
      "v_pk_mul_f16 v115, v131, v147\n"
      "v_mfma_f32_16x16x32_f16 v[80:83], a[68:71], v[100:103], v[80:83]\n"
      "s_waitcnt lgkmcnt(11)\n"
      "v_fma_f32 v188, |v86|, v54, v188\n"
      "v_fma_f32 v188, |v87|, v55, v188\n"
      "v_mfma_f32_16x16x32_f16 v[80:83], a[72:75], v[104:107], v[80:83]\n"
      "s_waitcnt lgkmcnt(8)\n"
      "ds_read_b128 a[120:123], v212 offset:16224\n"
      "v_pk_mul_f16 v116, v132, v148\n"
      "v_mfma_f32_16x16x32_f16 v[80:83], a[76:79], v[108:111], v[80:83]\n"
      "ds_read_b128 a[116:119], v212 offset:15072\n"
      "v_pk_mul_f16 v117, v133, v149\n"
      "v_mfma_f32_16x16x32_f16 v[84:87], a[92:95], v[108:111], v[36:39]\n"
      "ds_read_b128 a[112:115], v212 offset:13920\n"
      "v_fma_f32 v188, |v88|, v56, v188\n"
      "v_mfma_f32_16x16x32_f16 v[84:87], a[88:91], v[104:107], v[84:87]\n"
      "s_waitcnt lgkmcnt(9)\n"
      "v_fma_f32 v188, |v89|, v57, v188\n"
      "v_pk_mul_f16 v118, v134, v150\n"
      "v_mfma_f32_16x16x32_f16 v[84:87], a[84:87], v[100:103], v[84:87]\n"
      "ds_read_b128 v[156:159], v203 offset:2496\n"
      "v_fma_f32 v188, |v90|, v58, v188\n"
      "v_mfma_f32_16x16x32_f16 v[84:87], a[80:83], v[96:99], v[84:87]\n"
      "s_waitcnt lgkmcnt(8)\n"
      "v_fma_f32 v188, |v91|, v59, v188\n"
      "v_pk_mul_f16 v119, v135, v151\n"
      "v_mfma_f32_16x16x32_f16 v[88:91], a[96:99], v[96:99], v[40:43]\n"
      "s_waitcnt lgkmcnt(6)\n"
      "v_pk_mul_f16 v120, v136, v152\n"
      "v_fma_f32 v188, |v92|, v60, v188\n"
      "v_mfma_f32_16x16x32_f16 v[88:91], a[100:103], v[100:103], v[88:91]\n"
      "s_waitcnt lgkmcnt(5)\n"
      "v_fma_f32 v188, |v93|, v61, v188\n"
      "v_pk_mul_f16 v121, v137, v153\n"
      "v_mfma_f32_16x16x32_f16 v[88:91], a[104:107], v[104:107], v[88:91]\n"
      "v_fma_f32 v188, |v94|, v62, v188\n"
      "v_fma_f32 v188, |v95|, v63, v188\n"
      "v_mfma_f32_16x16x32_f16 v[88:91], a[108:111], v[108:111], v[88:91]\n"
      "s_waitcnt lgkmcnt(3)\n"
      "v_pk_mul_f16 v122, v138, v154\n"
      "v_pk_mul_f16 v123, v139, v155\n"
      "v_mfma_f32_16x16x32_f16 v[92:95], a[124:127], v[108:111], v[44:47]\n"
      "global_load_dwordx4 v[244:247], v10, s[10:11] nt\n"
      "v_fma_f32 v188, |v80|, v64, v188\n"
      "v_mfma_f32_16x16x32_f16 v[92:95], a[120:123], v[104:107], v[92:95]\n"
      "s_waitcnt lgkmcnt(1)\n"
      "v_fma_f32 v188, |v81|, v65, v188\n"
      "v_fma_f32 v188, |v82|, v66, v188\n"
      "v_mfma_f32_16x16x32_f16 v[92:95], a[116:119], v[100:103], v[92:95]\n"
      "s_waitcnt lgkmcnt(0)\n"
      "v_pk_mul_f16 v124, v140, v156\n"
      "v_fma_f32 v188, |v83|, v67, v188\n"
      "v_mfma_f32_16x16x32_f16 v[92:95], a[112:115], v[96:99], v[92:95]\n"
      "v_pk_mul_f16 v125, v141, v157\n"
      "v_pk_mul_f16 v126, v142, v158\n"
      "v_mfma_f32_16x16x32_f16 v[80:83], a[0:3], v[112:115], v[16:19]\n"
      "ds_read_b128 v[160:163], v203 offset:2880\n"
      "v_pk_mul_f16 v127, v143, v159\n"
      "v_mfma_f32_16x16x32_f16 v[80:83], a[4:7], v[116:119], v[80:83]\n"
      "v_fma_f32 v188, |v84|, v68, v188\n"
      "v_fma_f32 v188, |v85|, v69, v188\n"
      "v_mfma_f32_16x16x32_f16 v[80:83], a[8:11], v[120:123], v[80:83]\n"
      "v_fma_f32 v188, |v86|, v70, v188\n"
      "v_fma_f32 v188, |v87|, v71, v188\n"
      "v_mfma_f32_16x16x32_f16 v[80:83], a[12:15], v[124:127], v[80:83]\n"
      "v_fma_f32 v188, |v88|, v72, v188\n"
      "v_fma_f32 v188, |v89|, v73, v188\n"
      "v_mfma_f32_16x16x32_f16 v[84:87], a[28:31], v[124:127], v[20:23]\n"
      "40:\n"
      "s_cbranch_execz 44b\n"
      "v_fma_f32 v188, |v90|, v74, v188\n"
      "v_mfma_f32_16x16x32_f16 v[84:87], a[24:27], v[120:123], v[84:87]\n"
      "ds_read_b128 v[164:167], v203 offset:2944\n"
      "v_fma_f32 v188, |v91|, v75, v188\n"
      "v_mfma_f32_16x16x32_f16 v[84:87], a[20:23], v[116:119], v[84:87]\n"
      "v_fma_f32 v188, |v92|, v76, v188\n"
      "v_fma_f32 v188, |v93|, v77, v188\n"
      "v_mfma_f32_16x16x32_f16 v[84:87], a[16:19], v[112:115], v[84:87]\n"
      "v_fma_f32 v188, |v94|, v78, v188\n"
      "v_fma_f32 v188, |v95|, v79, v188\n"
      "v_mfma_f32_16x16x32_f16 v[88:91], a[32:35], v[112:115], v[24:27]\n"
      "s_waitcnt lgkmcnt(1)\n"
      "v_pk_mul_f16 v96, v128, v160\n"
      "v_pk_mul_f16 v97, v129, v161\n"
      "v_mfma_f32_16x16x32_f16 v[88:91], a[36:39], v[116:119], v[88:91]\n"
      "v_pk_mul_f16 v98, v130, v162\n"
      "v_pk_mul_f16 v99, v131, v163\n"
      "v_mfma_f32_16x16x32_f16 v[88:91], a[40:43], v[120:123], v[88:91]\n"
      "ds_read_b128 v[168:171], v203 offset:3008\n"
      "v_mul_f32 v189, |v80|, v48\n"
      "v_mfma_f32_16x16x32_f16 v[88:91], a[44:47], v[124:127], v[88:91]\n"
      "s_waitcnt vmcnt(1)\n"
      "v_fma_f32 v189, |v81|, v49, v189\n"
      "v_fma_f32 v189, |v82|, v50, v189\n"
      "v_mfma_f32_4x4x4_16b_bf16 v[176:179], a[216:217], v[240:241], 0\n"
      "v_fma_f32 v189, |v83|, v51, v189\n"
      "v_mfma_f32_16x16x32_f16 v[92:95], a[60:63], v[124:127], v[28:31]\n"
      "s_waitcnt lgkmcnt(1)\n"
      "v_pk_mul_f16 v100, v132, v164\n"
      "v_pk_mul_f16 v101, v133, v165\n"
      "v_mfma_f32_16x16x32_f16 v[92:95], a[56:59], v[120:123], v[92:95]\n"
      "v_pk_mul_f16 v102, v134, v166\n"
      "v_pk_mul_f16 v103, v135, v167\n"
      "v_mfma_f32_16x16x32_f16 v[92:95], a[52:55], v[116:119], v[92:95]\n"
      "v_fma_f32 v189, |v84|, v52, v189\n"
      "v_fma_f32 v189, |v85|, v53, v189\n"
      "v_mfma_f32_16x16x32_f16 v[92:95], a[48:51], v[112:115], v[92:95]\n"
      "v_fma_f32 v189, |v86|, v54, v189\n"
      "v_fma_f32 v189, |v87|, v55, v189\n"
      "v_mfma_f32_4x4x4_16b_bf16 v[180:183], a[216:217], v[242:243], 0\n"
      "v_fma_f32 v189, |v88|, v56, v189\n"
      "v_mfma_f32_16x16x32_f16 v[80:83], a[64:67], v[112:115], v[32:35]\n"
      "s_waitcnt lgkmcnt(0)\n"
      "v_pk_mul_f16 v104, v136, v168\n"
      "v_pk_mul_f16 v105, v137, v169\n"
      "v_mfma_f32_16x16x32_f16 v[80:83], a[68:71], v[116:119], v[80:83]\n"
      "v_pk_mul_f16 v106, v138, v170\n"
      "v_fma_f32 v189, |v89|, v57, v189\n"
      "v_mfma_f32_16x16x32_f16 v[80:83], a[72:75], v[120:123], v[80:83]\n"
      "ds_read_b128 v[172:175], v203 offset:3072\n"
      "v_pk_mul_f16 v107, v139, v171\n"
      "v_mfma_f32_16x16x32_f16 v[80:83], a[76:79], v[124:127], v[80:83]\n"
      "v_fma_f32 v189, |v90|, v58, v189\n"
      "v_fma_f32 v189, |v91|, v59, v189\n"
      "v_mfma_f32_16x16x32_f16 v[84:87], a[92:95], v[124:127], v[36:39]\n"
      "v_cvt_pk_f16_f32 v184, v176, v177\n"
      "v_fma_f32 v189, |v92|, v60, v189\n"
      "v_mfma_f32_16x16x32_f16 v[84:87], a[88:91], v[120:123], v[84:87]\n"
      "v_fma_f32 v189, |v93|, v61, v189\n"
      "v_cvt_pk_f16_f32 v185, v178, v179\n"
      "v_mfma_f32_16x16x32_f16 v[84:87], a[84:87], v[116:119], v[84:87]\n"
      "v_fma_f32 v189, |v94|, v62, v189\n"
      "v_fma_f32 v189, |v95|, v63, v189\n"
      "v_mfma_f32_16x16x32_f16 v[84:87], a[80:83], v[112:115], v[84:87]\n"
      "v_cvt_pk_f16_f32 v186, v180, v181\n"
      "v_cvt_pk_f16_f32 v187, v182, v183\n"
      "v_mfma_f32_16x16x32_f16 v[88:91], a[96:99], v[112:115], v[40:43]\n"
      "ds_write_b128 v1, v[184:187] offset:3456\n"
      "v_mfma_f32_16x16x32_f16 v[88:91], a[100:103], v[116:119], v[88:91]\n"
      "v_fma_f32 v189, |v80|, v64, v189\n"
      "v_fma_f32 v189, |v81|, v65, v189\n"
      "v_mfma_f32_16x16x32_f16 v[88:91], a[104:107], v[120:123], v[88:91]\n"
      "s_waitcnt lgkmcnt(1)\n"
      "v_pk_mul_f16 v108, v140, v172\n"
      "v_fma_f32 v189, |v82|, v66, v189\n"
      "v_mfma_f32_16x16x32_f16 v[88:91], a[108:111], v[124:127], v[88:91]\n"
      "v_fma_f32 v189, |v83|, v67, v189\n"
      "v_pk_mul_f16 v109, v141, v173\n"
      "v_mfma_f32_16x16x32_f16 v[92:95], a[124:127], v[124:127], v[44:47]\n"
      "v_pk_mul_f16 v110, v142, v174\n"
      "v_pk_mul_f16 v111, v143, v175\n"
      "v_mfma_f32_16x16x32_f16 v[92:95], a[120:123], v[120:123], v[92:95]\n"
      "ds_read_b128 v[160:163], v203 offset:3456\n"
      "v_fma_f32 v189, |v84|, v68, v189\n"
      "v_mfma_f32_16x16x32_f16 v[92:95], a[116:119], v[116:119], v[92:95]\n"
      "v_fma_f32 v189, |v85|, v69, v189\n"
      "v_fma_f32 v189, |v86|, v70, v189\n"
      "v_mfma_f32_16x16x32_f16 v[92:95], a[112:115], v[112:115], v[92:95]\n"
      "v_fma_f32 v189, |v87|, v71, v189\n"
      "v_fma_f32 v189, |v88|, v72, v189\n"
      "v_mfma_f32_16x16x32_f16 v[80:83], a[0:3], v[96:99], v[16:19]\n"
      "v_fma_f32 v189, |v89|, v73, v189\n"
      "v_fma_f32 v189, |v90|, v74, v189\n"
      "v_mfma_f32_16x16x32_f16 v[80:83], a[4:7], v[100:103], v[80:83]\n"
      "ds_read_b128 v[164:167], v203 offset:3520\n"
      "v_fma_f32 v189, |v91|, v75, v189\n"
      "v_mfma_f32_16x16x32_f16 v[80:83], a[8:11], v[104:107], v[80:83]\n"
      "v_fma_f32 v189, |v92|, v76, v189\n"
      "v_fma_f32 v189, |v93|, v77, v189\n"
      "v_mfma_f32_16x16x32_f16 v[80:83], a[12:15], v[108:111], v[80:83]\n"
      "v_fma_f32 v189, |v94|, v78, v189\n"
      "v_fma_f32 v189, |v95|, v79, v189\n"
      "v_mfma_f32_16x16x32_f16 v[84:87], a[28:31], v[108:111], v[20:23]\n"
      "ds_write_b32 v7, v252 offset:37376\n"
      "v_mfma_f32_16x16x32_f16 v[84:87], a[24:27], v[104:107], v[84:87]\n"
      "s_waitcnt lgkmcnt(2)\n"
      "v_pk_mul_f16 v112, v128, v160\n"
      "v_pk_mul_f16 v113, v129, v161\n"
      "v_mfma_f32_16x16x32_f16 v[84:87], a[20:23], v[100:103], v[84:87]\n"
      "ds_write_b32 v7, v192 offset:37120\n"
      "v_mfma_f32_16x16x32_f16 v[84:87], a[16:19], v[96:99], v[84:87]\n"
      "v_pk_mul_f16 v114, v130, v162\n"
      "v_pk_mul_f16 v115, v131, v163\n"
      "v_mfma_f32_16x16x32_f16 v[88:91], a[32:35], v[96:99], v[24:27]\n"
      "ds_read_b128 v[168:171], v203 offset:3584\n"
      "v_mul_f32 v190, |v80|, v48\n"
      "v_mfma_f32_16x16x32_f16 v[88:91], a[36:39], v[100:103], v[88:91]\n"
      "v_fma_f32 v190, |v81|, v49, v190\n"
      "v_fma_f32 v190, |v82|, v50, v190\n"
      "v_mfma_f32_16x16x32_f16 v[88:91], a[40:43], v[104:107], v[88:91]\n"
      "s_waitcnt lgkmcnt(3)\n"
      "v_fma_f32 v190, |v83|, v51, v190\n"
      "v_pk_mul_f16 v116, v132, v164\n"
      "v_mfma_f32_16x16x32_f16 v[88:91], a[44:47], v[108:111], v[88:91]\n"
      "v_pk_mul_f16 v117, v133, v165\n"
      "v_pk_mul_f16 v118, v134, v166\n"
      "v_mfma_f32_16x16x32_f16 v[92:95], a[60:63], v[108:111], v[28:31]\n"
      "v_fma_f32 v190, |v84|, v52, v190\n"
      "v_fma_f32 v190, |v85|, v53, v190\n"
      "v_mfma_f32_16x16x32_f16 v[92:95], a[56:59], v[104:107], v[92:95]\n"
      "v_pk_mul_f16 v119, v135, v167\n"
      "v_fma_f32 v190, |v86|, v54, v190\n"
      "v_mfma_f32_16x16x32_f16 v[92:95], a[52:55], v[100:103], v[92:95]\n"
      "v_fma_f32 v190, |v87|, v55, v190\n"
      "v_fma_f32 v190, |v88|, v56, v190\n"
      "v_mfma_f32_16x16x32_f16 v[92:95], a[48:51], v[96:99], v[92:95]\n"
      "ds_read_b128 v[172:175], v203 offset:3648\n"
      "v_fma_f32 v190, |v89|, v57, v190\n"
      "v_mfma_f32_16x16x32_f16 v[80:83], a[64:67], v[96:99], v[32:35]\n"
      "s_waitcnt lgkmcnt(1)\n"
      "v_pk_mul_f16 v120, v136, v168\n"
      "v_pk_mul_f16 v121, v137, v169\n"
      "v_mfma_f32_16x16x32_f16 v[80:83], a[68:71], v[100:103], v[80:83]\n"
      "v_fma_f32 v190, |v90|, v58, v190\n"
      "v_fma_f32 v190, |v91|, v59, v190\n"
      "v_mfma_f32_16x16x32_f16 v[80:83], a[72:75], v[104:107], v[80:83]\n"
      "v_pk_mul_f16 v122, v138, v170\n"
      "v_pk_mul_f16 v123, v139, v171\n"
      "v_mfma_f32_16x16x32_f16 v[80:83], a[76:79], v[108:111], v[80:83]\n"
      "global_load_dwordx4 v[248:251], v11, s[10:11] nt\n"
      "v_fma_f32 v190, |v92|, v60, v190\n"
      "v_mfma_f32_16x16x32_f16 v[84:87], a[92:95], v[108:111], v[36:39]\n"
      "v_fma_f32 v190, |v93|, v61, v190\n"
      "v_fma_f32 v190, |v94|, v62, v190\n"
      "v_mfma_f32_16x16x32_f16 v[84:87], a[88:91], v[104:107], v[84:87]\n"
      "v_fma_f32 v190, |v95|, v63, v190\n"
      "v_max3_f32 v195, |v193|, |v194|, |v195|\n"
      "v_mfma_f32_16x16x32_f16 v[84:87], a[84:87], v[100:103], v[84:87]\n"
      "v_fma_f32 v190, |v80|, v64, v190\n"
      "v_fma_f32 v190, |v81|, v65, v190\n"
      "v_mfma_f32_16x16x32_f16 v[84:87], a[80:83], v[96:99], v[84:87]\n"
      "s_waitcnt lgkmcnt(0)\n"
      "v_pk_mul_f16 v124, v140, v172\n"
      "v_fma_f32 v190, |v82|, v66, v190\n"
      "v_mfma_f32_16x16x32_f16 v[88:91], a[96:99], v[96:99], v[40:43]\n"
      "v_fma_f32 v190, |v83|, v67, v190\n"
      "v_max3_f32 v194, |v253|, |v254|, |v255|\n"
      "v_mfma_f32_16x16x32_f16 v[88:91], a[100:103], v[100:103], v[88:91]\n"
      "v_pk_mul_f16 v125, v141, v173\n"
      "v_max_f32 v194, |v252|, v194\n"
      "v_mfma_f32_16x16x32_f16 v[88:91], a[104:107], v[104:107], v[88:91]\n"
      "v_pk_mul_f16 v126, v142, v174\n"
      "v_max_f32 v195, |v192|, v195\n"
      "v_mfma_f32_16x16x32_f16 v[88:91], a[108:111], v[108:111], v[88:91]\n"
      "ds_read_b128 v[160:163], v203 offset:4032\n"
      "v_pk_mul_f16 v127, v143, v175\n"
      "v_mfma_f32_16x16x32_f16 v[92:95], a[124:127], v[108:111], v[44:47]\n"
      "v_fma_f32 v190, |v84|, v68, v190\n"
      "v_fma_f32 v190, |v85|, v69, v190\n"
      "v_mfma_f32_16x16x32_f16 v[92:95], a[120:123], v[104:107], v[92:95]\n"
      "global_load_dwordx4 v[252:255], v12, s[10:11] nt\n"
      "v_cmp_le_f32 vcc, 0x3ca3d70a, v194\n"
      "v_mfma_f32_16x16x32_f16 v[92:95], a[116:119], v[100:103], v[92:95]\n"
      "v_fma_f32 v190, |v86|, v70, v190\n"
      "v_fma_f32 v190, |v87|, v71, v190\n"
      "v_mfma_f32_16x16x32_f16 v[92:95], a[112:115], v[96:99], v[92:95]\n"
      "s_or_b32 s44, vcc_lo, vcc_hi\n"
      "v_fma_f32 v190, |v88|, v72, v190\n"
      "v_fma_f32 v190, |v89|, v73, v190\n"
      "v_mfma_f32_16x16x32_f16 v[80:83], a[0:3], v[112:115], v[16:19]\n"
      "v_cmp_le_f32 vcc, 0x3ca3d70a, v195\n"
      "v_max_f32 v195, v195, v194\n"
      "v_mfma_f32_16x16x32_f16 v[80:83], a[4:7], v[116:119], v[80:83]\n"
      "v_fma_f32 v190, |v90|, v74, v190\n"
      "v_fma_f32 v190, |v91|, v75, v190\n"
      "v_mfma_f32_16x16x32_f16 v[80:83], a[8:11], v[120:123], v[80:83]\n"
      "s_or_b32 s45, vcc_lo, vcc_hi\n"
      "ds_read_b128 v[164:167], v203 offset:4096\n"
      "v_fma_f32 v190, |v92|, v76, v190\n"
      "v_mfma_f32_16x16x32_f16 v[80:83], a[12:15], v[124:127], v[80:83]\n"
      "s_waitcnt lgkmcnt(1)\n"
      "v_cmp_le_f32 vcc, 0x3f000000, v195\n"
      "v_pk_mul_f16 v96, v128, v160\n"
      "v_mfma_f32_16x16x32_f16 v[84:87], a[28:31], v[124:127], v[20:23]\n"
      "s_pack_ll_b32_b16 s46, s44, s45\n"
      "v_fma_f32 v190, |v93|, v77, v190\n"
      "v_pk_mul_f16 v97, v129, v161\n"
      "v_mfma_f32_16x16x32_f16 v[84:87], a[24:27], v[120:123], v[84:87]\n"
      "s_pack_hh_b32_b16 s44, s44, s45\n"
      "v_fma_f32 v190, |v94|, v78, v190\n"
      "v_fma_f32 v190, |v95|, v79, v190\n"
      "v_mfma_f32_16x16x32_f16 v[84:87], a[20:23], v[116:119], v[84:87]\n"
      "s_or_b32 s44, s44, s46\n"
      "v_pk_mul_f16 v98, v130, v162\n"
      "v_pk_mul_f16 v99, v131, v163\n"
      "v_mfma_f32_16x16x32_f16 v[84:87], a[16:19], v[112:115], v[84:87]\n"
      "s_cmp_eq_u64 vcc, 0\n"
      "ds_read_b128 v[168:171], v203 offset:4160\n"
      "v_mul_f32 v191, |v80|, v48\n"
      "v_mfma_f32_16x16x32_f16 v[88:91], a[32:35], v[112:115], v[24:27]\n"
      "s_cselect_b32 s45, 13, 9\n"
      "v_fma_f32 v191, |v81|, v49, v191\n"
      "v_fma_f32 v191, |v82|, v50, v191\n"
      "v_mfma_f32_16x16x32_f16 v[88:91], a[36:39], v[116:119], v[88:91]\n"
      "s_cmp_eq_u32 s44, -1\n"
      "ds_read_b128 v[172:175], v203 offset:4224\n"
      "v_fma_f32 v191, |v83|, v51, v191\n"
      "v_mfma_f32_16x16x32_f16 v[88:91], a[40:43], v[120:123], v[88:91]\n"
      "s_waitcnt lgkmcnt(2)\n"
      "v_pk_mul_f16 v100, v132, v164\n"
      "v_pk_mul_f16 v101, v133, v165\n"
      "v_mfma_f32_16x16x32_f16 v[88:91], a[44:47], v[124:127], v[88:91]\n"
      "s_cselect_b32 s44, s45, 5\n"
      "v_pk_mul_f16 v102, v134, v166\n"
      "v_fma_f32 v191, |v84|, v52, v191\n"
      "v_mfma_f32_16x16x32_f16 v[92:95], a[60:63], v[124:127], v[28:31]\n"
      "s_min_u32 s44, s44, s52\n"
      "v_fma_f32 v191, |v85|, v53, v191\n"
      "v_pk_mul_f16 v103, v135, v167\n"
      "v_mfma_f32_16x16x32_f16 v[92:95], a[56:59], v[120:123], v[92:95]\n"
      "v_fma_f32 v191, |v86|, v54, v191\n"
      "v_fma_f32 v191, |v87|, v55, v191\n"
      "v_mfma_f32_16x16x32_f16 v[92:95], a[52:55], v[116:119], v[92:95]\n"
      "v_mov_b32 v223, s44\n"
      "v_fma_f32 v191, |v88|, v56, v191\n"
      "v_mfma_f32_16x16x32_f16 v[92:95], a[48:51], v[112:115], v[92:95]\n"
      "s_mov_b32 s35, 1\n"
      "ds_write_b32 v2, v223\n"
      "v_mfma_f32_16x16x32_f16 v[80:83], a[64:67], v[112:115], v[32:35]\n"
      "s_waitcnt lgkmcnt(2)\n"
      "v_pk_mul_f16 v104, v136, v168\n"
      "v_fma_f32 v191, |v89|, v57, v191\n"
      "v_mfma_f32_16x16x32_f16 v[80:83], a[68:71], v[116:119], v[80:83]\n"
      "v_pk_mul_f16 v105, v137, v169\n"
      "v_fma_f32 v191, |v90|, v58, v191\n"
      "v_mfma_f32_16x16x32_f16 v[80:83], a[72:75], v[120:123], v[80:83]\n"
      "v_fma_f32 v191, |v91|, v59, v191\n"
      "v_pk_mul_f16 v106, v138, v170\n"
      "v_mfma_f32_16x16x32_f16 v[80:83], a[76:79], v[124:127], v[80:83]\n"
      "v_pk_mul_f16 v107, v139, v171\n"
      "v_fma_f32 v191, |v92|, v60, v191\n"
      "v_mfma_f32_16x16x32_f16 v[84:87], a[92:95], v[124:127], v[36:39]\n"
      "v_fma_f32 v191, |v93|, v61, v191\n"
      "v_fma_f32 v191, |v94|, v62, v191\n"
      "v_mfma_f32_16x16x32_f16 v[84:87], a[88:91], v[120:123], v[84:87]\n"
      "s_waitcnt lgkmcnt(1)\n"
      "v_fma_f32 v191, |v95|, v63, v191\n"
      "v_pk_mul_f16 v108, v140, v172\n"
      "v_mfma_f32_16x16x32_f16 v[84:87], a[84:87], v[116:119], v[84:87]\n"
      "v_fma_f32 v191, |v80|, v64, v191\n"
      "v_fma_f32 v191, |v81|, v65, v191\n"
      "v_mfma_f32_16x16x32_f16 v[84:87], a[80:83], v[112:115], v[84:87]\n"
      "v_fma_f32 v191, |v82|, v66, v191\n"
      "v_fma_f32 v191, |v83|, v67, v191\n"
      "v_mfma_f32_16x16x32_f16 v[88:91], a[96:99], v[112:115], v[40:43]\n"
      "ds_read_b128 v[144:147], v201 offset:2304\n"
      "v_pk_mul_f16 v109, v141, v173\n"
      "v_mfma_f32_16x16x32_f16 v[88:91], a[100:103], v[116:119], v[88:91]\n"
      "v_pk_mul_f16 v110, v142, v174\n"
      "v_pk_mul_f16 v111, v143, v175\n"
      "v_mfma_f32_16x16x32_f16 v[88:91], a[104:107], v[120:123], v[88:91]\n"
      "ds_read_b128 v[160:163], v202 offset:2304\n"
      "v_fma_f32 v191, |v84|, v68, v191\n"
      "v_mfma_f32_16x16x32_f16 v[88:91], a[108:111], v[124:127], v[88:91]\n"
      "v_fma_f32 v191, |v85|, v69, v191\n"
      "v_fma_f32 v191, |v86|, v70, v191\n"
      "v_mfma_f32_16x16x32_f16 v[92:95], a[124:127], v[124:127], v[44:47]\n"
      "ds_read_b128 v[148:151], v201 offset:2368\n"
      "v_fma_f32 v191, |v87|, v71, v191\n"
      "v_mfma_f32_16x16x32_f16 v[92:95], a[120:123], v[120:123], v[92:95]\n"
      "ds_read_b128 v[164:167], v202 offset:2368\n"
      "ds_read_b128 v[152:155], v201 offset:2432\n"
      "v_mfma_f32_16x16x32_f16 v[92:95], a[116:119], v[116:119], v[92:95]\n"
      "v_fma_f32 v191, |v88|, v72, v191\n"
      "v_fma_f32 v191, |v89|, v73, v191\n"
      "v_mfma_f32_16x16x32_f16 v[92:95], a[112:115], v[112:115], v[92:95]\n"
      "v_fma_f32 v191, |v90|, v74, v191\n"
      "v_fma_f32 v191, |v91|, v75, v191\n"
      "v_mfma_f32_16x16x32_f16 v[80:83], a[0:3], v[96:99], v[16:19]\n"
      "ds_read_b128 v[168:171], v202 offset:2432\n"
      "ds_read_b128 v[156:159], v201 offset:2496\n"
      "v_mfma_f32_16x16x32_f16 v[80:83], a[4:7], v[100:103], v[80:83]\n"
      "s_waitcnt vmcnt(2)\n"
      "v_min3_f32 v226, |v228|, |v233|, |v238|\n"
      "v_min3_f32 v226, |v243|, |v244|, v226\n"
      "v_mfma_f32_16x16x32_f16 v[80:83], a[8:11], v[104:107], v[80:83]\n"
      "s_waitcnt lgkmcnt(5)\n"
      "v_pk_mul_f16 v112, v144, v160\n"
      "v_pk_mul_f16 v113, v145, v161\n"
      "v_mfma_f32_16x16x32_f16 v[80:83], a[12:15], v[108:111], v[80:83]\n"
      "v_pk_mul_f16 v114, v146, v162\n"
      "v_fma_f32 v191, |v92|, v76, v191\n"
      "v_mfma_f32_4x4x4_16b_bf16 v[176:179], a[216:217], v[244:245], 0\n"
      "v_fma_f32 v191, |v93|, v77, v191\n"
      "v_mfma_f32_16x16x32_f16 v[84:87], a[28:31], v[108:111], v[20:23]\n"
      "v_pk_mul_f16 v115, v147, v163\n"
      "v_fma_f32 v191, |v94|, v78, v191\n"
      "v_mfma_f32_16x16x32_f16 v[84:87], a[24:27], v[104:107], v[84:87]\n"
      "s_waitcnt lgkmcnt(3)\n"
      "v_fma_f32 v191, |v95|, v79, v191\n"
      "v_pk_mul_f16 v116, v148, v164\n"
      "v_mfma_f32_16x16x32_f16 v[84:87], a[20:23], v[100:103], v[84:87]\n"
      "v_mul_f32 v192, |v80|, v48\n"
      "v_fma_f32 v192, |v81|, v49, v192\n"
      "v_mfma_f32_16x16x32_f16 v[84:87], a[16:19], v[96:99], v[84:87]\n"
      "v_permlane32_swap_b32 v189, v191\n"
      "v_mfma_f32_4x4x4_16b_bf16 v[180:183], a[216:217], v[246:247], 0\n"
      "v_pk_mul_f16 v117, v149, v165\n"
      "v_mfma_f32_16x16x32_f16 v[88:91], a[32:35], v[96:99], v[24:27]\n"
      "v_fma_f32 v192, |v82|, v50, v192\n"
      "v_fma_f32 v192, |v83|, v51, v192\n"
      "v_mfma_f32_16x16x32_f16 v[88:91], a[36:39], v[100:103], v[88:91]\n"
      "v_pk_mul_f16 v118, v150, v166\n"
      "v_pk_mul_f16 v119, v151, v167\n"
      "v_mfma_f32_16x16x32_f16 v[88:91], a[40:43], v[104:107], v[88:91]\n"
      "v_fma_f32 v192, |v84|, v52, v192\n"
      "v_fma_f32 v192, |v85|, v53, v192\n"
      "v_mfma_f32_16x16x32_f16 v[88:91], a[44:47], v[108:111], v[88:91]\n"
      "v_fma_f32 v192, |v86|, v54, v192\n"
      "v_fma_f32 v192, |v87|, v55, v192\n"
      "v_mfma_f32_16x16x32_f16 v[92:95], a[60:63], v[108:111], v[28:31]\n"
      "s_waitcnt lgkmcnt(1)\n"
      "v_pk_mul_f16 v120, v152, v168\n"
      "v_pk_mul_f16 v121, v153, v169\n"
      "v_mfma_f32_16x16x32_f16 v[92:95], a[56:59], v[104:107], v[92:95]\n"
      "v_pk_mul_f16 v122, v154, v170\n"
      "v_pk_mul_f16 v123, v155, v171\n"
      "v_mfma_f32_16x16x32_f16 v[92:95], a[52:55], v[100:103], v[92:95]\n"
      "v_fma_f32 v192, |v88|, v56, v192\n"
      "v_fma_f32 v192, |v89|, v57, v192\n"
      "v_mfma_f32_16x16x32_f16 v[92:95], a[48:51], v[96:99], v[92:95]\n"
      "ds_read_b128 v[172:175], v202 offset:2496\n"
      "v_fma_f32 v192, |v90|, v58, v192\n"
      "v_mfma_f32_16x16x32_f16 v[80:83], a[64:67], v[96:99], v[32:35]\n"
      "v_fma_f32 v192, |v91|, v59, v192\n"
      "v_cvt_pk_f16_f32 v184, v176, v177\n"
      "v_mfma_f32_16x16x32_f16 v[80:83], a[68:71], v[100:103], v[80:83]\n"
      "v_cvt_pk_f16_f32 v185, v178, v179\n"
      "v_cvt_pk_f16_f32 v186, v180, v181\n"
      "v_mfma_f32_16x16x32_f16 v[80:83], a[72:75], v[104:107], v[80:83]\n"
      "v_fma_f32 v192, |v92|, v60, v192\n"
      "v_fma_f32 v192, |v93|, v61, v192\n"
      "v_mfma_f32_16x16x32_f16 v[80:83], a[76:79], v[108:111], v[80:83]\n"
      "v_fma_f32 v192, |v94|, v62, v192\n"
      "v_fma_f32 v192, |v95|, v63, v192\n"
      "v_mfma_f32_16x16x32_f16 v[84:87], a[92:95], v[108:111], v[36:39]\n"
      "v_cvt_pk_f16_f32 v187, v182, v183\n"
      "v_add_f32 v189, v189, v191\n"
      "v_mfma_f32_16x16x32_f16 v[84:87], a[88:91], v[104:107], v[84:87]\n"
      "ds_write_b128 v1, v[184:187] offset:4608\n"
      "v_mfma_f32_16x16x32_f16 v[84:87], a[84:87], v[100:103], v[84:87]\n"
      "v_fma_f32 v192, |v80|, v64, v192\n"
      "v_fma_f32 v192, |v81|, v65, v192\n"
      "v_mfma_f32_16x16x32_f16 v[84:87], a[80:83], v[96:99], v[84:87]\n"
      "s_waitcnt lgkmcnt(1)\n"
      "v_pk_mul_f16 v124, v156, v172\n"
      "v_fma_f32 v192, |v82|, v66, v192\n"
      "v_mfma_f32_16x16x32_f16 v[88:91], a[96:99], v[96:99], v[40:43]\n"
      "v_fma_f32 v192, |v83|, v67, v192\n"
      "v_pk_mul_f16 v125, v157, v173\n"
      "v_mfma_f32_16x16x32_f16 v[88:91], a[100:103], v[100:103], v[88:91]\n"
      "v_pk_mul_f16 v126, v158, v174\n"
      "v_pk_mul_f16 v127, v159, v175\n"
      "v_mfma_f32_16x16x32_f16 v[88:91], a[104:107], v[104:107], v[88:91]\n"
      "ds_read_b128 v[160:163], v203 offset:4608\n"
      "v_fma_f32 v192, |v84|, v68, v192\n"
      "v_mfma_f32_16x16x32_f16 v[88:91], a[108:111], v[108:111], v[88:91]\n"
      "v_fma_f32 v192, |v85|, v69, v192\n"
      "v_fma_f32 v192, |v86|, v70, v192\n"
      "v_mfma_f32_16x16x32_f16 v[92:95], a[124:127], v[108:111], v[44:47]\n"
      "ds_read_b128 v[164:167], v203 offset:4672\n"
      "v_fma_f32 v192, |v87|, v71, v192\n"
      "v_mfma_f32_16x16x32_f16 v[92:95], a[120:123], v[104:107], v[92:95]\n"
      "s_waitcnt vmcnt(0)\n"
      "v_min3_f32 v226, |v249|, |v254|, v226\n"
      "v_cmp_le_f32 vcc, 0x3d800000, v226\n"
      "v_mfma_f32_16x16x32_f16 v[92:95], a[116:119], v[100:103], v[92:95]\n"
      "v_fma_f32 v192, |v88|, v72, v192\n"
      "v_fma_f32 v192, |v89|, v73, v192\n"
      "v_mfma_f32_16x16x32_f16 v[92:95], a[112:115], v[96:99], v[92:95]\n"
      "s_quadmask_b64 s[44:45], vcc\n"
      "v_fma_f32 v192, |v90|, v74, v192\n"
      "v_fma_f32 v192, |v91|, v75, v192\n"
      "v_mfma_f32_16x16x32_f16 v[80:83], a[0:3], v[112:115], v[16:19]\n"
      "s_quadmask_b32 s30, s44\n"
      "ds_read_b128 v[168:171], v203 offset:4736\n"
      "ds_read_b32 v224, v227 offset:37632\n"
      "v_mfma_f32_16x16x32_f16 v[80:83], a[4:7], v[116:119], v[80:83]\n"
      "s_xor_b32 s30, s30, 15\n"
      "ds_read_b64 v[176:177], v5 offset:37120\n"
      "ds_read_b32 v226, v213 offset:12480\n"
      "v_mfma_f32_16x16x32_f16 v[80:83], a[8:11], v[120:123], v[80:83]\n"
      "s_waitcnt lgkmcnt(5)\n"
      "v_pk_mul_f16 v96, v128, v160\n"
      "v_fma_f32 v192, |v92|, v76, v192\n"
      "v_mfma_f32_16x16x32_f16 v[80:83], a[12:15], v[124:127], v[80:83]\n"
      "s_or_b32 s30, s30, s41\n"
      "v_fma_f32 v192, |v93|, v77, v192\n"
      "v_pk_mul_f16 v97, v129, v161\n"
      "v_mfma_f32_16x16x32_f16 v[84:87], a[28:31], v[124:127], v[20:23]\n"
      "s_cmp_lg_u32 s55, 0\n"
      "v_fma_f32 v192, |v94|, v78, v192\n"
      "v_fma_f32 v192, |v95|, v79, v192\n"
      "v_mfma_f32_16x16x32_f16 v[84:87], a[24:27], v[120:123], v[84:87]\n"
      "s_cselect_b32 s30, 0, s30\n"
      "v_pk_mul_f16 v98, v130, v162\n"
      "v_pk_mul_f16 v99, v131, v163\n"
      "v_mfma_f32_16x16x32_f16 v[84:87], a[20:23], v[116:119], v[84:87]\n"
      "v_permlane32_swap_b32 v190, v192\n"
      "v_mfma_f32_16x16x32_f16 v[84:87], a[16:19], v[112:115], v[84:87]\n"
      "v_mul_f32 v193, |v80|, v48\n"
      "v_fma_f32 v193, |v81|, v49, v193\n"
      "v_mfma_f32_16x16x32_f16 v[88:91], a[32:35], v[112:115], v[24:27]\n"
      "v_fma_f32 v193, |v82|, v50, v193\n"
      "v_fma_f32 v193, |v83|, v51, v193\n"
      "v_mfma_f32_16x16x32_f16 v[88:91], a[36:39], v[116:119], v[88:91]\n"
      "s_waitcnt lgkmcnt(4)\n"
      "v_pk_mul_f16 v100, v132, v164\n"
      "v_pk_mul_f16 v101, v133, v165\n"
      "v_mfma_f32_16x16x32_f16 v[88:91], a[40:43], v[120:123], v[88:91]\n"
      "v_pk_mul_f16 v102, v134, v166\n"
      "v_fma_f32 v193, |v84|, v52, v193\n"
      "v_mfma_f32_16x16x32_f16 v[88:91], a[44:47], v[124:127], v[88:91]\n"
      "v_fma_f32 v193, |v85|, v53, v193\n"
      "v_pk_mul_f16 v103, v135, v167\n"
      "v_mfma_f32_16x16x32_f16 v[92:95], a[60:63], v[124:127], v[28:31]\n"
      "v_fma_f32 v193, |v86|, v54, v193\n"
      "v_fma_f32 v193, |v87|, v55, v193\n"
      "v_mfma_f32_16x16x32_f16 v[92:95], a[56:59], v[120:123], v[92:95]\n"
      "s_waitcnt lgkmcnt(3)\n"
      "v_pk_mul_f16 v104, v136, v168\n"
      "v_pk_mul_f16 v105, v137, v169\n"
      "v_mfma_f32_16x16x32_f16 v[92:95], a[52:55], v[116:119], v[92:95]\n"
      "v_fma_f32 v193, |v88|, v56, v193\n"
      "v_fma_f32 v193, |v89|, v57, v193\n"
      "v_mfma_f32_16x16x32_f16 v[92:95], a[48:51], v[112:115], v[92:95]\n"
      "ds_read_b128 v[172:175], v203 offset:4800\n"
      "v_fma_f32 v193, |v90|, v58, v193\n"
      "v_mfma_f32_16x16x32_f16 v[80:83], a[64:67], v[112:115], v[32:35]\n"
      "v_fma_f32 v193, |v91|, v59, v193\n"
      "v_pk_mul_f16 v106, v138, v170\n"
      "v_mfma_f32_16x16x32_f16 v[80:83], a[68:71], v[116:119], v[80:83]\n"
      "s_waitcnt lgkmcnt(3)\n"
      "v_pk_mul_f16 v107, v139, v171\n"
      "v_cmp_ne_u32 vcc, 13, v224\n"
      "v_mfma_f32_16x16x32_f16 v[80:83], a[72:75], v[120:123], v[80:83]\n"
      "v_fma_f32 v193, |v92|, v60, v193\n"
      "v_fma_f32 v193, |v93|, v61, v193\n"
      "v_mfma_f32_16x16x32_f16 v[80:83], a[76:79], v[124:127], v[80:83]\n"
      "s_or_b32 s45, vcc_lo, s30\n"
      "v_fma_f32 v193, |v94|, v62, v193\n"
      "v_fma_f32 v193, |v95|, v63, v193\n"
      "v_mfma_f32_16x16x32_f16 v[84:87], a[92:95], v[124:127], v[36:39]\n"
      "s_waitcnt lgkmcnt(1)\n"
      "v_max_f16_sdwa v226, |v226|, |v226| dst_sel:DWORD dst_unused:UNUSED_PAD src0_sel:WORD_0 src1_sel:WORD_1\n"
      "v_cmp_le_f16 vcc, 0x6400, v226\n"
      "v_mfma_f32_16x16x32_f16 v[84:87], a[88:91], v[120:123], v[84:87]\n"
      "v_add_f32 v190, v190, v192\n"
      "v_mfma_f32_16x16x32_f16 v[84:87], a[84:87], v[116:119], v[84:87]\n"
      "s_or_b32 vcc_lo, vcc_lo, s45\n"
      "v_fma_f32 v193, |v80|, v64, v193\n"
      "v_fma_f32 v193, |v81|, v65, v193\n"
      "v_mfma_f32_16x16x32_f16 v[84:87], a[80:83], v[112:115], v[84:87]\n"
      "s_cmp_lg_u64 vcc, 0\n"
      "v_permlane16_swap_b32 v189, v190\n"
      "v_mfma_f32_16x16x32_f16 v[88:91], a[96:99], v[112:115], v[40:43]\n"
      "s_cbranch_scc1 5f\n"
      "4:\n"
      "v_mfma_f32_16x16x32_f16 v[88:91], a[100:103], v[116:119], v[88:91]\n"
      "s_waitcnt lgkmcnt(0)\n"
      "v_pk_mul_f16 v108, v140, v172\n"
      "v_fma_f32 v193, |v82|, v66, v193\n"
      "v_mfma_f32_16x16x32_f16 v[88:91], a[104:107], v[120:123], v[88:91]\n"
      "v_fma_f32 v193, |v83|, v67, v193\n"
      "v_pk_mul_f16 v109, v141, v173\n"
      "v_mfma_f32_16x16x32_f16 v[88:91], a[108:111], v[124:127], v[88:91]\n"
      "v_pk_mul_f16 v110, v142, v174\n"
      "v_pk_mul_f16 v111, v143, v175\n"
      "v_mfma_f32_16x16x32_f16 v[92:95], a[124:127], v[124:127], v[44:47]\n"
      "v_fma_f32 v193, |v84|, v68, v193\n"
      "v_fma_f32 v193, |v85|, v69, v193\n"
      "v_mfma_f32_16x16x32_f16 v[92:95], a[120:123], v[120:123], v[92:95]\n"
      "v_fma_f32 v193, |v86|, v70, v193\n"
      "v_fma_f32 v193, |v87|, v71, v193\n"
      "v_mfma_f32_16x16x32_f16 v[92:95], a[116:119], v[116:119], v[92:95]\n"
      "v_cvt_pk_f16_f32 v176, v176, v177\n"
      "v_fma_f32 v193, |v88|, v72, v193\n"
      "v_mfma_f32_16x16x32_f16 v[92:95], a[112:115], v[112:115], v[92:95]\n"
      "s_lshl_b32 s49, s40, 1\n"
      "ds_write_b32 v213, v176 offset:11904\n"
      "v_mfma_f32_16x16x32_f16 v[80:83], a[0:3], v[96:99], v[16:19]\n"
      "s_add_u32 s51, s57, s49\n"
      "v_fma_f32 v193, |v89|, v73, v193\n"
      "v_fma_f32 v193, |v90|, v74, v193\n"
      "v_mfma_f32_16x16x32_f16 v[80:83], a[4:7], v[100:103], v[80:83]\n"
      "s_add_u32 s51, s51, s58\n"
      "v_fma_f32 v193, |v91|, v75, v193\n"
      "v_pk_mul_f16 v112, v144, v160\n"
      "v_mfma_f32_16x16x32_f16 v[80:83], a[8:11], v[104:107], v[80:83]\n"
      "s_add_u32 s51, s51, s56\n"
      "v_fma_f32 v193, |v92|, v76, v193\n"
      "v_fma_f32 v193, |v93|, v77, v193\n"
      "v_mfma_f32_16x16x32_f16 v[80:83], a[12:15], v[108:111], v[80:83]\n"
      "s_sub_u32 s51, 0, s51\n"
      "v_pk_mul_f16 v113, v145, v161\n"
      "v_fma_f32 v193, |v94|, v78, v193\n"
      "v_mfma_f32_16x16x32_f16 v[84:87], a[28:31], v[108:111], v[20:23]\n"
      "s_add_u32 s49, s59, s49\n"
      "v_fma_f32 v193, |v95|, v79, v193\n"
      "v_pk_mul_f16 v114, v146, v162\n"
      "v_mfma_f32_16x16x32_f16 v[84:87], a[24:27], v[104:107], v[84:87]\n"
      "s_sub_u32 s39, 5, s49\n"
      "v_pk_mul_f16 v115, v147, v163\n"
      "v_pk_mul_f16 v116, v148, v164\n"
      "v_mfma_f32_16x16x32_f16 v[84:87], a[20:23], v[100:103], v[84:87]\n"
      "v_permlane32_swap_b32 v188, v193\n"
      "v_mfma_f32_16x16x32_f16 v[84:87], a[16:19], v[96:99], v[84:87]\n"
      "v_mul_f32 v194, |v80|, v48\n"
      "v_fma_f32 v194, |v81|, v49, v194\n"
      "v_mfma_f32_16x16x32_f16 v[88:91], a[32:35], v[96:99], v[24:27]\n"
      "v_fma_f32 v194, |v82|, v50, v194\n"
      "v_fma_f32 v194, |v83|, v51, v194\n"
      "v_mfma_f32_16x16x32_f16 v[88:91], a[36:39], v[100:103], v[88:91]\n"
      "41:\n"
      "s_cbranch_execz 44b\n"
      "v_pk_mul_f16 v117, v149, v165\n"
      "v_mfma_f32_16x16x32_f16 v[88:91], a[40:43], v[104:107], v[88:91]\n"
      "v_add_f32 v189, v189, v190\n"
      "v_pk_mul_f16 v118, v150, v166\n"
      "v_mfma_f32_16x16x32_f16 v[88:91], a[44:47], v[108:111], v[88:91]\n"
      "v_fma_f32 v194, |v84|, v52, v194\n"
      "v_fma_f32 v194, |v85|, v53, v194\n"
      "v_mfma_f32_16x16x32_f16 v[92:95], a[60:63], v[108:111], v[28:31]\n"
      "v_pk_mul_f16 v119, v151, v167\n"
      "v_ldexp_f32 v200, v200, s51\n"
      "v_mfma_f32_16x16x32_f16 v[92:95], a[56:59], v[104:107], v[92:95]\n"
      "v_fma_f32 v194, |v86|, v54, v194\n"
      "v_fma_f32 v194, |v87|, v55, v194\n"
      "v_mfma_f32_16x16x32_f16 v[92:95], a[52:55], v[100:103], v[92:95]\n"
      "v_pk_mul_f16 v120, v152, v168\n"
      "v_pk_mul_f16 v121, v153, v169\n"
      "v_mfma_f32_16x16x32_f16 v[92:95], a[48:51], v[96:99], v[92:95]\n"
      "v_fma_f32 v194, |v88|, v56, v194\n"
      "v_fma_f32 v194, |v89|, v57, v194\n"
      "v_mfma_f32_16x16x32_f16 v[80:83], a[64:67], v[96:99], v[32:35]\n"
      "v_pk_mul_f16 v122, v154, v170\n"
      "v_fma_f32 v194, |v90|, v58, v194\n"
      "v_mfma_f32_16x16x32_f16 v[80:83], a[68:71], v[100:103], v[80:83]\n"
      "v_fma_f32 v194, |v91|, v59, v194\n"
      "v_pk_mul_f16 v123, v155, v171\n"
      "v_mfma_f32_16x16x32_f16 v[80:83], a[72:75], v[104:107], v[80:83]\n"
      "v_fma_f32 v194, |v92|, v60, v194\n"
      "v_fma_f32 v194, |v93|, v61, v194\n"
      "v_mfma_f32_16x16x32_f16 v[80:83], a[76:79], v[108:111], v[80:83]\n"
      "v_fma_f32 v194, |v94|, v62, v194\n"
      "v_fma_f32 v194, |v95|, v63, v194\n"
      "v_mfma_f32_16x16x32_f16 v[84:87], a[92:95], v[108:111], v[36:39]\n"
      "v_pk_mul_f16 v124, v156, v172\n"
      "v_pk_mul_f16 v125, v157, v173\n"
      "v_mfma_f32_16x16x32_f16 v[84:87], a[88:91], v[104:107], v[84:87]\n"
      "v_pk_mul_f16 v126, v158, v174\n"
      "v_pk_mul_f16 v127, v159, v175\n"
      "v_mfma_f32_16x16x32_f16 v[84:87], a[84:87], v[100:103], v[84:87]\n"
      "v_fma_f32 v194, |v80|, v64, v194\n"
      "v_fma_f32 v194, |v81|, v65, v194\n"
      "v_mfma_f32_16x16x32_f16 v[84:87], a[80:83], v[96:99], v[84:87]\n"
      "v_fma_f32 v194, |v82|, v66, v194\n"
      "v_fma_f32 v194, |v83|, v67, v194\n"
      "v_mfma_f32_4x4x4_16b_bf16 v[176:179], a[216:217], v[248:249], 0\n"
      "ds_read_b128 v[160:163], v203 offset:5184\n"
      "v_mfma_f32_16x16x32_f16 v[88:91], a[96:99], v[96:99], v[40:43]\n"
      "v_add_f32 v188, v188, v193\n"
      "v_add_u32_sdwa v10, s99, v199 dst_sel:DWORD dst_unused:UNUSED_PAD src0_sel:DWORD src1_sel:WORD_0\n"
      "v_mfma_f32_16x16x32_f16 v[88:91], a[100:103], v[100:103], v[88:91]\n"
      "v_fma_f32 v194, |v84|, v68, v194\n"
      "v_fma_f32 v194, |v85|, v69, v194\n"
      "v_mfma_f32_16x16x32_f16 v[88:91], a[104:107], v[104:107], v[88:91]\n"
      "v_fma_f32 v194, |v86|, v70, v194\n"
      "v_fma_f32 v194, |v87|, v71, v194\n"
      "v_mfma_f32_16x16x32_f16 v[88:91], a[108:111], v[108:111], v[88:91]\n"
      "ds_read_b128 v[164:167], v203 offset:5248\n"
      "v_cvt_pk_f16_f32 v184, v176, v177\n"
      "v_mfma_f32_4x4x4_16b_bf16 v[180:183], a[216:217], v[250:251], 0\n"
      "v_cvt_pk_f16_f32 v185, v178, v179\n"
      "v_mfma_f32_16x16x32_f16 v[92:95], a[124:127], v[108:111], v[44:47]\n"
      "v_add_u32_sdwa v11, s99, v199 dst_sel:DWORD dst_unused:UNUSED_PAD src0_sel:DWORD src1_sel:WORD_1\n"
      "v_mov_b32 v7, 0xf149f2ca\n"
      "v_mfma_f32_16x16x32_f16 v[92:95], a[120:123], v[104:107], v[92:95]\n"
      "v_fma_f32 v194, |v88|, v72, v194\n"
      "v_fma_f32 v194, |v89|, v73, v194\n"
      "v_mfma_f32_16x16x32_f16 v[92:95], a[116:119], v[100:103], v[92:95]\n"
      "v_fma_f32 v194, |v90|, v74, v194\n"
      "v_fma_f32 v194, |v91|, v75, v194\n"
      "v_mfma_f32_16x16x32_f16 v[92:95], a[112:115], v[96:99], v[92:95]\n"
      "s_waitcnt lgkmcnt(1)\n"
      "v_pk_mul_f16 v96, v128, v160\n"
      "v_pk_mul_f16 v97, v129, v161\n"
      "v_mfma_f32_16x16x32_f16 v[80:83], a[0:3], v[112:115], v[16:19]\n"
      "v_pk_mul_f16 v98, v130, v162\n"
      "v_pk_mul_f16 v99, v131, v163\n"
      "v_mfma_f32_16x16x32_f16 v[80:83], a[4:7], v[116:119], v[80:83]\n"
      "ds_read_b128 v[168:171], v203 offset:5312\n"
      "v_cvt_pk_f16_f32 v186, v180, v181\n"
      "v_mfma_f32_16x16x32_f16 v[80:83], a[8:11], v[120:123], v[80:83]\n"
      "v_fma_f32 v194, |v92|, v76, v194\n"
      "v_fma_f32 v194, |v93|, v77, v194\n"
      "v_mfma_f32_16x16x32_f16 v[80:83], a[12:15], v[124:127], v[80:83]\n"
      "v_fma_f32 v194, |v94|, v78, v194\n"
      "v_fma_f32 v194, |v95|, v79, v194\n"
      "v_mfma_f32_16x16x32_f16 v[84:87], a[28:31], v[124:127], v[20:23]\n"
      "s_waitcnt lgkmcnt(1)\n"
      "v_pk_mul_f16 v100, v132, v164\n"
      "v_pk_mul_f16 v101, v133, v165\n"
      "v_mfma_f32_16x16x32_f16 v[84:87], a[24:27], v[120:123], v[84:87]\n"
      "v_pk_mul_f16 v102, v134, v166\n"
      "v_pk_mul_f16 v103, v135, v167\n"
      "v_mfma_f32_16x16x32_f16 v[84:87], a[20:23], v[116:119], v[84:87]\n"
      "v_mul_f32 v195, |v80|, v48\n"
      "v_fma_f32 v195, |v81|, v49, v195\n"
      "v_mfma_f32_16x16x32_f16 v[84:87], a[16:19], v[112:115], v[84:87]\n"
      "v_fma_f32 v195, |v82|, v50, v195\n"
      "v_fma_f32 v195, |v83|, v51, v195\n"
      "v_mfma_f32_16x16x32_f16 v[88:91], a[32:35], v[112:115], v[24:27]\n"
      "v_cvt_pk_f16_f32 v187, v182, v183\n"
      "v_add_u32 v12, s98, v1\n"
      "v_mfma_f32_16x16x32_f16 v[88:91], a[36:39], v[116:119], v[88:91]\n"
      "s_and_b32 s100, s100, 0x37373737\n"
      "ds_write_b128 v1, v[184:187] offset:5760\n"
      "v_mfma_f32_16x16x32_f16 v[88:91], a[40:43], v[120:123], v[88:91]\n"
      "s_xor_b32 s100, s100, 0x370037\n"
      "v_fma_f32 v195, |v84|, v52, v195\n"
      "v_fma_f32 v195, |v85|, v53, v195\n"
      "v_mfma_f32_16x16x32_f16 v[88:91], a[44:47], v[124:127], v[88:91]\n"
      "s_mov_b32 s101, s100\n"
      "v_fma_f32 v195, |v86|, v54, v195\n"
      "v_fma_f32 v195, |v87|, v55, v195\n"
      "v_mfma_f32_16x16x32_f16 v[92:95], a[60:63], v[124:127], v[28:31]\n"
      "s_waitcnt lgkmcnt(1)\n"
      "v_pk_mul_f16 v104, v136, v168\n"
      "v_pk_mul_f16 v105, v137, v169\n"
      "v_mfma_f32_16x16x32_f16 v[92:95], a[56:59], v[120:123], v[92:95]\n"
      "v_pk_mul_f16 v106, v138, v170\n"
      "v_pk_mul_f16 v107, v139, v171\n"
      "v_mfma_f32_16x16x32_f16 v[92:95], a[52:55], v[116:119], v[92:95]\n"
      "v_fma_f32 v195, |v88|, v56, v195\n"
      "v_fma_f32 v195, |v89|, v57, v195\n"
      "v_mfma_f32_16x16x32_f16 v[92:95], a[48:51], v[112:115], v[92:95]\n"
      "v_fma_f32 v195, |v90|, v58, v195\n"
      "v_fma_f32 v195, |v91|, v59, v195\n"
      "v_mfma_f32_16x16x32_f16 v[80:83], a[64:67], v[112:115], v[32:35]\n"
      "ds_read_b128 v[172:175], v203 offset:5376\n"
      "v_mov_b32 v8, 0xf149f2ca\n"
      "v_mfma_f32_16x16x32_f16 v[80:83], a[68:71], v[116:119], v[80:83]\n"
      "ds_read_b128 a[200:203], v215 offset:11904\n"
      "ds_read_b128 a[204:207], v215 offset:11968\n"
      "v_mfma_f32_16x16x32_f16 v[80:83], a[72:75], v[120:123], v[80:83]\n"
      "v_fma_f32 v195, |v92|, v60, v195\n"
      "v_fma_f32 v195, |v93|, v61, v195\n"
      "v_mfma_f32_16x16x32_f16 v[80:83], a[76:79], v[124:127], v[80:83]\n"
      "v_fma_f32 v195, |v94|, v62, v195\n"
      "v_fma_f32 v195, |v95|, v63, v195\n"
      "v_mfma_f32_16x16x32_f16 v[84:87], a[92:95], v[124:127], v[36:39]\n"
      "ds_read_b128 a[208:211], v215 offset:12032\n"
      "ds_read_b128 a[212:215], v215 offset:12096\n"
      "v_mfma_f32_16x16x32_f16 v[84:87], a[88:91], v[120:123], v[84:87]\n"
      "v_mfma_f32_16x16x32_f16 v[84:87], a[84:87], v[116:119], v[84:87]\n"
      "s_nop 0\n"
      "v_fma_f32 v195, |v80|, v64, v195\n"
      "v_fma_f32 v195, |v81|, v65, v195\n"
      "v_mfma_f32_16x16x32_f16 v[84:87], a[80:83], v[112:115], v[84:87]\n"
      "v_fma_f32 v195, |v82|, v66, v195\n"
      "v_fma_f32 v195, |v83|, v67, v195\n"
      "v_mfma_f32_16x16x32_f16 v[88:91], a[96:99], v[112:115], v[40:43]\n"
      "s_waitcnt lgkmcnt(4)\n"
      "v_pk_mul_f16 v108, v140, v172\n"
      "v_pk_mul_f16 v109, v141, v173\n"
      "v_mfma_f32_16x16x32_f16 v[88:91], a[100:103], v[116:119], v[88:91]\n"
      "v_pk_mul_f16 v110, v142, v174\n"
      "v_pk_mul_f16 v111, v143, v175\n"
      "v_mfma_f32_16x16x32_f16 v[88:91], a[104:107], v[120:123], v[88:91]\n"
      "v_fma_f32 v195, |v84|, v68, v195\n"
      "v_fma_f32 v195, |v85|, v69, v195\n"
      "v_mfma_f32_16x16x32_f16 v[88:91], a[108:111], v[124:127], v[88:91]\n"
      "v_fma_f32 v195, |v86|, v70, v195\n"
      "v_fma_f32 v195, |v87|, v71, v195\n"
      "v_mfma_f32_4x4x4_16b_bf16 v[176:179], a[216:217], v[252:253], 0\n"
      "v_mfma_f32_16x16x32_f16 v[92:95], a[124:127], v[124:127], v[44:47]\n"
      "v_pk_mul_f16 v124, v156, v172\n"
      "v_pk_mul_f16 v125, v157, v173\n"
      "v_mfma_f32_16x16x32_f16 v[92:95], a[120:123], v[120:123], v[92:95]\n"
      "s_nop 0\n"
      "v_fma_f32 v195, |v88|, v72, v195\n"
      "v_fma_f32 v195, |v89|, v73, v195\n"
      "v_mfma_f32_16x16x32_f16 v[92:95], a[116:119], v[116:119], v[92:95]\n"
      "v_fma_f32 v195, |v90|, v74, v195\n"
      "v_fma_f32 v195, |v91|, v75, v195\n"
      "v_mfma_f32_16x16x32_f16 v[92:95], a[112:115], v[112:115], v[92:95]\n"
      "v_pk_mul_f16 v112, v144, v160\n"
      "v_pk_mul_f16 v113, v145, v161\n"
      "v_mfma_f32_4x4x4_16b_bf16 v[180:183], a[216:217], v[254:255], 0\n"
      "v_pk_mul_f16 v114, v146, v162\n"
      "v_mfma_f32_16x16x32_f16 v[80:83], a[0:3], v[96:99], v[16:19]\n"
      "v_pk_mul_f16 v115, v147, v163\n"
      "v_pk_mul_f16 v116, v148, v164\n"
      "v_mfma_f32_16x16x32_f16 v[80:83], a[4:7], v[100:103], v[80:83]\n"
      "v_fma_f32 v195, |v92|, v76, v195\n"
      "v_fma_f32 v195, |v93|, v77, v195\n"
      "v_mfma_f32_16x16x32_f16 v[80:83], a[8:11], v[104:107], v[80:83]\n"
      "v_fma_f32 v195, |v94|, v78, v195\n"
      "v_fma_f32 v195, |v95|, v79, v195\n"
      "v_mfma_f32_16x16x32_f16 v[80:83], a[12:15], v[108:111], v[80:83]\n"
      "v_pk_mul_f16 v117, v149, v165\n"
      "v_pk_mul_f16 v118, v150, v166\n"
      "v_mfma_f32_16x16x32_f16 v[84:87], a[28:31], v[108:111], v[20:23]\n"
      "v_pk_mul_f16 v119, v151, v167\n"
      "v_pk_mul_f16 v120, v152, v168\n"
      "v_mfma_f32_16x16x32_f16 v[84:87], a[24:27], v[104:107], v[84:87]\n"
      "v_pk_mul_f16 v121, v153, v169\n"
      "v_pk_mul_f16 v122, v154, v170\n"
      "v_mfma_f32_16x16x32_f16 v[84:87], a[20:23], v[100:103], v[84:87]\n"
      "v_mul_f32 v190, |v80|, v48\n"
      "v_fma_f32 v190, |v81|, v49, v190\n"
      "v_mfma_f32_16x16x32_f16 v[84:87], a[16:19], v[96:99], v[84:87]\n"
      "v_fma_f32 v190, |v82|, v50, v190\n"
      "v_fma_f32 v190, |v83|, v51, v190\n"
      "v_mfma_f32_16x16x32_f16 v[88:91], a[32:35], v[96:99], v[24:27]\n"
      "v_pk_mul_f16 v123, v155, v171\n"
      "v_pk_mul_f16 v126, v158, v174\n"
      "v_mfma_f32_16x16x32_f16 v[88:91], a[36:39], v[100:103], v[88:91]\n"
      "v_pk_mul_f16 v127, v159, v175\n"
      "v_cvt_pk_f16_f32 v184, v176, v177\n"
      "v_mfma_f32_16x16x32_f16 v[88:91], a[40:43], v[104:107], v[88:91]\n"
      "v_fma_f32 v190, |v84|, v52, v190\n"
      "v_fma_f32 v190, |v85|, v53, v190\n"
      "v_mfma_f32_16x16x32_f16 v[88:91], a[44:47], v[108:111], v[88:91]\n"
      "v_fma_f32 v190, |v86|, v54, v190\n"
      "v_fma_f32 v190, |v87|, v55, v190\n"
      "v_mfma_f32_16x16x32_f16 v[92:95], a[60:63], v[108:111], v[28:31]\n"
      "v_cvt_pk_f16_f32 v185, v178, v179\n"
      "v_cvt_pk_f16_f32 v186, v180, v181\n"
      "v_mfma_f32_16x16x32_f16 v[92:95], a[56:59], v[104:107], v[92:95]\n"
      "v_cvt_pk_f16_f32 v187, v182, v183\n"
      "v_mfma_f32_16x16x32_f16 v[92:95], a[52:55], v[100:103], v[92:95]\n"
      "s_or_b32 s44, s55, s41\n"
      "ds_write_b128 v1, v[184:187] offset:6912\n"
      "v_mfma_f32_16x16x32_f16 v[92:95], a[48:51], v[96:99], v[92:95]\n"
      "s_cselect_b32 s44, 2, 1\n"
      "v_fma_f32 v190, |v88|, v56, v190\n"
      "v_fma_f32 v190, |v89|, v57, v190\n"
      "v_mfma_f32_16x16x32_f16 v[80:83], a[64:67], v[96:99], v[32:35]\n"
      "v_fma_f32 v190, |v90|, v58, v190\n"
      "v_fma_f32 v190, |v91|, v59, v190\n"
      "v_mfma_f32_16x16x32_f16 v[80:83], a[68:71], v[100:103], v[80:83]\n"
      "ds_read_b128 v[176:179], v214 offset:11904\n"
      "ds_read_b128 a[152:155], v219 offset:0\n"
      "v_mfma_f32_16x16x32_f16 v[80:83], a[72:75], v[104:107], v[80:83]\n"
      "v_fma_f32 v190, |v92|, v60, v190\n"
      "v_fma_f32 v190, |v93|, v61, v190\n"
      "v_mfma_f32_16x16x32_f16 v[80:83], a[76:79], v[108:111], v[80:83]\n"
      "v_fma_f32 v190, |v94|, v62, v190\n"
      "v_fma_f32 v190, |v95|, v63, v190\n"
      "v_mfma_f32_16x16x32_f16 v[84:87], a[92:95], v[108:111], v[36:39]\n"
      "ds_read_b128 v[160:163], v203 offset:5760\n"
      "ds_read_b128 a[168:171], v220 offset:0\n"
      "v_mfma_f32_16x16x32_f16 v[84:87], a[88:91], v[104:107], v[84:87]\n"
      "ds_read_b128 a[184:187], v221 offset:0\n"
      "ds_read_b128 a[156:159], v219 offset:64\n"
      "v_mfma_f32_16x16x32_f16 v[84:87], a[84:87], v[100:103], v[84:87]\n"
      "v_fma_f32 v190, |v80|, v64, v190\n"
      "v_fma_f32 v190, |v81|, v65, v190\n"
      "v_mfma_f32_16x16x32_f16 v[84:87], a[80:83], v[96:99], v[84:87]\n"
      "v_fma_f32 v190, |v82|, v66, v190\n"
      "v_fma_f32 v190, |v83|, v67, v190\n"
      "v_mfma_f32_16x16x32_f16 v[88:91], a[96:99], v[96:99], v[40:43]\n"
      "ds_read_b128 a[172:175], v220 offset:64\n"
      "ds_read_b128 a[188:191], v221 offset:64\n"
      "v_mfma_f32_16x16x32_f16 v[88:91], a[100:103], v[100:103], v[88:91]\n"
      "s_waitcnt lgkmcnt(7)\n"
      "v_pk_mul_f16 v228, v128, v176\n"
      "v_pk_mul_f16 v229, v129, v177\n"
      "v_mfma_f32_16x16x32_f16 v[88:91], a[104:107], v[104:107], v[88:91]\n"
      "v_fma_f32 v190, |v84|, v68, v190\n"
      "v_fma_f32 v190, |v85|, v69, v190\n"
      "v_mfma_f32_16x16x32_f16 v[88:91], a[108:111], v[108:111], v[88:91]\n"
      "v_fma_f32 v190, |v86|, v70, v190\n"
      "v_fma_f32 v190, |v87|, v71, v190\n"
      "v_mfma_f32_16x16x32_f16 v[92:95], a[124:127], v[108:111], v[44:47]\n"
      "ds_read_b128 v[164:167], v203 offset:5824\n"
      "v_pk_mul_f16 v230, v130, v178\n"
      "v_mfma_f32_16x16x32_f16 v[92:95], a[120:123], v[104:107], v[92:95]\n"
      "v_pk_mul_f16 v231, v131, v179\n"
      "v_pk_mul_f16 v232, v144, v176\n"
      "v_mfma_f32_16x16x32_f16 v[92:95], a[116:119], v[100:103], v[92:95]\n"
      "v_fma_f32 v190, |v88|, v72, v190\n"
      "v_fma_f32 v190, |v89|, v73, v190\n"
      "v_mfma_f32_16x16x32_f16 v[92:95], a[112:115], v[96:99], v[92:95]\n"
      "v_fma_f32 v190, |v90|, v74, v190\n"
      "v_fma_f32 v190, |v91|, v75, v190\n"
      "v_mfma_f32_16x16x32_f16 v[80:83], a[0:3], v[112:115], v[16:19]\n"
      "s_waitcnt lgkmcnt(6)\n"
      "v_pk_mul_f16 v96, v128, v160\n"
      "v_pk_mul_f16 v97, v129, v161\n"
      "v_mfma_f32_16x16x32_f16 v[80:83], a[4:7], v[116:119], v[80:83]\n"
      "v_pk_mul_f16 v98, v130, v162\n"
      "v_pk_mul_f16 v99, v131, v163\n"
      "v_mfma_f32_16x16x32_f16 v[80:83], a[8:11], v[120:123], v[80:83]\n"
      "v_fma_f32 v190, |v92|, v76, v190\n"
      "v_fma_f32 v190, |v93|, v77, v190\n"
      "v_mfma_f32_16x16x32_f16 v[80:83], a[12:15], v[124:127], v[80:83]\n"
      "v_fma_f32 v190, |v94|, v78, v190\n"
      "v_fma_f32 v190, |v95|, v79, v190\n"
      "v_mfma_f32_16x16x32_f16 v[84:87], a[28:31], v[124:127], v[20:23]\n"
      "s_waitcnt lgkmcnt(0)\n"
      "ds_read_b128 v[168:171], v203 offset:5888\n"
      "v_pk_mul_f16 v100, v132, v164\n"
      "v_mfma_f32_16x16x32_f16 v[84:87], a[24:27], v[120:123], v[84:87]\n"
      "v_pk_mul_f16 v101, v133, v165\n"
      "v_pk_mul_f16 v102, v134, v166\n"
      "v_mfma_f32_16x16x32_f16 v[84:87], a[20:23], v[116:119], v[84:87]\n"
      "v_mul_f32 v191, |v80|, v48\n"
      "v_fma_f32 v191, |v81|, v49, v191\n"
      "v_mfma_f32_16x16x32_f16 v[84:87], a[16:19], v[112:115], v[84:87]\n"
      "v_fma_f32 v191, |v82|, v50, v191\n"
      "v_fma_f32 v191, |v83|, v51, v191\n"
      "v_mfma_f32_16x16x32_f16 v[88:91], a[32:35], v[112:115], v[24:27]\n"
      "ds_read_b128 v[240:243], v201 offset:4608\n"
      "v_pk_mul_f16 v103, v135, v167\n"
      "v_mfma_f32_16x16x32_f16 v[88:91], a[36:39], v[116:119], v[88:91]\n"
      "ds_read_b128 v[172:175], v203 offset:5952\n"
      "ds_read_b128 v[244:247], v201 offset:4672\n"
      "v_mfma_f32_16x16x32_f16 v[88:91], a[40:43], v[120:123], v[88:91]\n"
      "v_fma_f32 v191, |v84|, v52, v191\n"
      "v_fma_f32 v191, |v85|, v53, v191\n"
      "v_mfma_f32_16x16x32_f16 v[88:91], a[44:47], v[124:127], v[88:91]\n"
      "v_fma_f32 v191, |v86|, v54, v191\n"
      "v_fma_f32 v191, |v87|, v55, v191\n"
      "v_mfma_f32_16x16x32_f16 v[92:95], a[60:63], v[124:127], v[28:31]\n"
      "s_waitcnt lgkmcnt(3)\n"
      "v_pk_mul_f16 v104, v136, v168\n"
      "v_pk_mul_f16 v105, v137, v169\n"
      "v_mfma_f32_16x16x32_f16 v[92:95], a[56:59], v[120:123], v[92:95]\n"
      "v_pk_mul_f16 v106, v138, v170\n"
      "v_pk_mul_f16 v107, v139, v171\n"
      "v_mfma_f32_16x16x32_f16 v[92:95], a[52:55], v[116:119], v[92:95]\n"
      "v_fma_f32 v191, |v88|, v56, v191\n"
      "v_fma_f32 v191, |v89|, v57, v191\n"
      "v_mfma_f32_16x16x32_f16 v[92:95], a[48:51], v[112:115], v[92:95]\n"
      "v_fma_f32 v191, |v90|, v58, v191\n"
      "v_fma_f32 v191, |v91|, v59, v191\n"
      "v_mfma_f32_16x16x32_f16 v[80:83], a[64:67], v[112:115], v[32:35]\n"
      "ds_read_b128 v[248:251], v201 offset:4736\n"
      "ds_read_b128 v[252:255], v201 offset:4800\n"
      "v_mfma_f32_16x16x32_f16 v[80:83], a[68:71], v[116:119], v[80:83]\n"
      "s_waitcnt lgkmcnt(3)\n"
      "v_pk_mul_f16 v108, v140, v172\n"
      "v_pk_mul_f16 v109, v141, v173\n"
      "v_mfma_f32_16x16x32_f16 v[80:83], a[72:75], v[120:123], v[80:83]\n"
      "v_fma_f32 v191, |v92|, v60, v191\n"
      "v_fma_f32 v191, |v93|, v61, v191\n"
      "v_mfma_f32_16x16x32_f16 v[80:83], a[76:79], v[124:127], v[80:83]\n"
      "v_fma_f32 v191, |v94|, v62, v191\n"
      "v_fma_f32 v191, |v95|, v63, v191\n"
      "v_mfma_f32_16x16x32_f16 v[84:87], a[92:95], v[124:127], v[36:39]\n"
      "v_pk_mul_f16 v110, v142, v174\n"
      "v_pk_mul_f16 v111, v143, v175\n"
      "v_mfma_f32_16x16x32_f16 v[84:87], a[88:91], v[120:123], v[84:87]\n"
      "v_pk_mul_f16 v233, v145, v177\n"
      "v_pk_mul_f16 v234, v146, v178\n"
      "v_mfma_f32_16x16x32_f16 v[84:87], a[84:87], v[116:119], v[84:87]\n"
      "v_fma_f32 v191, |v80|, v64, v191\n"
      "v_fma_f32 v191, |v81|, v65, v191\n"
      "v_mfma_f32_16x16x32_f16 v[84:87], a[80:83], v[112:115], v[84:87]\n"
      "v_fma_f32 v191, |v82|, v66, v191\n"
      "v_fma_f32 v191, |v83|, v67, v191\n"
      "v_mfma_f32_16x16x32_f16 v[88:91], a[96:99], v[112:115], v[40:43]\n"
      "v_pk_mul_f16 v235, v147, v179\n"
      "v_pk_mul_f16 v236, v240, v176\n"
      "v_mfma_f32_16x16x32_f16 v[88:91], a[100:103], v[116:119], v[88:91]\n"
      "v_pk_mul_f16 v237, v241, v177\n"
      "v_pk_mul_f16 v238, v242, v178\n"
      "v_mfma_f32_16x16x32_f16 v[88:91], a[104:107], v[120:123], v[88:91]\n"
      "v_fma_f32 v191, |v84|, v68, v191\n"
      "v_fma_f32 v191, |v85|, v69, v191\n"
      "v_mfma_f32_16x16x32_f16 v[88:91], a[108:111], v[124:127], v[88:91]\n"
      "v_fma_f32 v191, |v86|, v70, v191\n"
      "v_fma_f32 v191, |v87|, v71, v191\n"
      "v_mfma_f32_16x16x32_f16 v[92:95], a[124:127], v[124:127], v[44:47]\n"
      "v_pk_mul_f16 v239, v243, v179\n"
      "v_pk_mul_f16 v124, v156, v172\n"
      "v_mfma_f32_16x16x32_f16 v[92:95], a[120:123], v[120:123], v[92:95]\n"
      "v_pk_mul_f16 v120, v152, v168\n"
      "v_pk_mul_f16 v121, v153, v169\n"
      "v_mfma_f32_16x16x32_f16 v[92:95], a[116:119], v[116:119], v[92:95]\n"
      "v_fma_f32 v191, |v88|, v72, v191\n"
      "v_fma_f32 v191, |v89|, v73, v191\n"
      "v_mfma_f32_16x16x32_f16 v[92:95], a[112:115], v[112:115], v[92:95]\n"
      "v_fma_f32 v191, |v90|, v74, v191\n"
      "v_fma_f32 v191, |v91|, v75, v191\n"
      "v_mfma_f32_16x16x32_f16 v[80:83], a[0:3], v[96:99], v[16:19]\n"
      "v_pk_mul_f16 v112, v144, v160\n"
      "v_pk_mul_f16 v113, v145, v161\n"
      "v_mfma_f32_16x16x32_f16 v[80:83], a[4:7], v[100:103], v[80:83]\n"
      "v_pk_mul_f16 v114, v146, v162\n"
      "v_pk_mul_f16 v115, v147, v163\n"
      "v_mfma_f32_16x16x32_f16 v[80:83], a[8:11], v[104:107], v[80:83]\n"
      "v_fma_f32 v191, |v92|, v76, v191\n"
      "v_fma_f32 v191, |v93|, v77, v191\n"
      "v_mfma_f32_16x16x32_f16 v[80:83], a[12:15], v[108:111], v[80:83]\n"
      "v_fma_f32 v191, |v94|, v78, v191\n"
      "v_fma_f32 v191, |v95|, v79, v191\n"
      "v_mfma_f32_16x16x32_f16 v[84:87], a[28:31], v[108:111], v[20:23]\n"
      "v_pk_mul_f16 v116, v148, v164\n"
      "v_pk_mul_f16 v117, v149, v165\n"
      "v_mfma_f32_16x16x32_f16 v[84:87], a[24:27], v[104:107], v[84:87]\n"
      "v_pk_mul_f16 v118, v150, v166\n"
      "v_pk_mul_f16 v119, v151, v167\n"
      "v_mfma_f32_16x16x32_f16 v[84:87], a[20:23], v[100:103], v[84:87]\n"
      "v_mul_f32 v192, |v80|, v48\n"
      "v_fma_f32 v192, |v81|, v49, v192\n"
      "v_mfma_f32_16x16x32_f16 v[84:87], a[16:19], v[96:99], v[84:87]\n"
      "v_fma_f32 v192, |v82|, v50, v192\n"
      "v_fma_f32 v192, |v83|, v51, v192\n"
      "v_mfma_f32_16x16x32_f16 v[88:91], a[32:35], v[96:99], v[24:27]\n"
      "v_pk_mul_f16 v122, v154, v170\n"
      "v_pk_mul_f16 v123, v155, v171\n"
      "v_mfma_f32_16x16x32_f16 v[88:91], a[36:39], v[100:103], v[88:91]\n"
      "v_pk_mul_f16 v125, v157, v173\n"
      "v_pk_mul_f16 v126, v158, v174\n"
      "v_mfma_f32_16x16x32_f16 v[88:91], a[40:43], v[104:107], v[88:91]\n"
      "v_fma_f32 v192, |v84|, v52, v192\n"
      "v_fma_f32 v192, |v85|, v53, v192\n"
      "v_mfma_f32_16x16x32_f16 v[88:91], a[44:47], v[108:111], v[88:91]\n"
      "v_fma_f32 v192, |v86|, v54, v192\n"
      "v_fma_f32 v192, |v87|, v55, v192\n"
      "v_mfma_f32_16x16x32_f16 v[92:95], a[60:63], v[108:111], v[28:31]\n"
      "ds_read_b128 v[176:179], v214 offset:11968\n"
      "v_pk_mul_f16 v127, v159, v175\n"
      "v_mfma_f32_16x16x32_f16 v[92:95], a[56:59], v[104:107], v[92:95]\n"
      "ds_read_b128 a[160:163], v219 offset:128\n"
      "ds_read_b128 a[176:179], v220 offset:128\n"
      "v_mfma_f32_16x16x32_f16 v[92:95], a[52:55], v[100:103], v[92:95]\n"
      "v_fma_f32 v192, |v88|, v56, v192\n"
      "v_fma_f32 v192, |v89|, v57, v192\n"
      "v_mfma_f32_16x16x32_f16 v[92:95], a[48:51], v[96:99], v[92:95]\n"
      "v_fma_f32 v192, |v90|, v58, v192\n"
      "v_fma_f32 v192, |v91|, v59, v192\n"
      "v_mfma_f32_16x16x32_f16 a[128:131], v[228:231], a[152:155], 0\n"
      "ds_read_b128 a[192:195], v221 offset:128\n"
      "v_mov_b32 v223, s44\n"
      "v_mfma_f32_16x16x32_f16 v[80:83], a[64:67], v[96:99], v[32:35]\n"
      "ds_write_b32 v2, v223 offset:8\n"
      "v_mfma_f32_16x16x32_f16 v[80:83], a[68:71], v[100:103], v[80:83]\n"
      "v_fma_f32 v192, |v92|, v60, v192\n"
      "v_fma_f32 v192, |v93|, v61, v192\n"
      "v_mfma_f32_16x16x32_f16 v[80:83], a[72:75], v[104:107], v[80:83]\n"
      "v_fma_f32 v192, |v94|, v62, v192\n"
      "v_fma_f32 v192, |v95|, v63, v192\n"
      "v_mfma_f32_16x16x32_f16 v[80:83], a[76:79], v[108:111], v[80:83]\n"
      "s_waitcnt lgkmcnt(4)\n"
      "v_pk_mul_f16 v228, v132, v176\n"
      "v_pk_mul_f16 v229, v133, v177\n"
      "v_mfma_f32_16x16x32_f16 v[84:87], a[92:95], v[108:111], v[36:39]\n"
      "v_pk_mul_f16 v230, v134, v178\n"
      "v_pk_mul_f16 v231, v135, v179\n"
      "v_mfma_f32_16x16x32_f16 v[84:87], a[88:91], v[104:107], v[84:87]\n"
      "ds_read_b128 a[164:167], v219 offset:192\n"
      "ds_read_b128 a[180:183], v220 offset:192\n"
      "v_mfma_f32_16x16x32_f16 v[84:87], a[84:87], v[100:103], v[84:87]\n"
      "v_fma_f32 v192, |v80|, v64, v192\n"
      "v_fma_f32 v192, |v81|, v65, v192\n"
      "v_mfma_f32_16x16x32_f16 v[84:87], a[80:83], v[96:99], v[84:87]\n"
      "v_fma_f32 v192, |v82|, v66, v192\n"
      "v_fma_f32 v192, |v83|, v67, v192\n"
      "v_mfma_f32_16x16x32_f16 a[132:135], v[232:235], a[168:171], 0\n"
      "ds_read_b128 v[160:163], v203 offset:6336\n"
      "v_pk_mul_f16 v232, v148, v176\n"
      "v_mfma_f32_16x16x32_f16 v[88:91], a[96:99], v[96:99], v[40:43]\n"
      "v_pk_mul_f16 v233, v149, v177\n"
      "v_pk_mul_f16 v234, v150, v178\n"
      "v_mfma_f32_16x16x32_f16 v[88:91], a[100:103], v[100:103], v[88:91]\n"
      "v_fma_f32 v192, |v84|, v68, v192\n"
      "v_fma_f32 v192, |v85|, v69, v192\n"
      "v_mfma_f32_16x16x32_f16 v[88:91], a[104:107], v[104:107], v[88:91]\n"
      "v_fma_f32 v192, |v86|, v70, v192\n"
      "v_fma_f32 v192, |v87|, v71, v192\n"
      "v_mfma_f32_16x16x32_f16 v[88:91], a[108:111], v[108:111], v[88:91]\n"
      "ds_read_b128 v[164:167], v203 offset:6400\n"
      "v_pk_mul_f16 v235, v151, v179\n"
      "v_mfma_f32_16x16x32_f16 v[92:95], a[124:127], v[108:111], v[44:47]\n"
      "ds_read_b128 a[196:199], v221 offset:192\n"
      "v_mfma_f32_16x16x32_f16 v[92:95], a[120:123], v[104:107], v[92:95]\n"
      "v_mfma_f32_16x16x32_f16 v[92:95], a[116:119], v[100:103], v[92:95]\n"
      "s_nop 1\n"
      "v_fma_f32 v192, |v88|, v72, v192\n"
      "v_fma_f32 v192, |v89|, v73, v192\n"
      "v_mfma_f32_16x16x32_f16 v[92:95], a[112:115], v[96:99], v[92:95]\n"
      "v_fma_f32 v192, |v90|, v74, v192\n"
      "v_fma_f32 v192, |v91|, v75, v192\n"
      "v_mfma_f32_16x16x32_f16 a[136:139], v[236:239], a[184:187], 0\n"
      "s_waitcnt lgkmcnt(2)\n"
      "v_pk_mul_f16 v96, v128, v160\n"
      "v_pk_mul_f16 v97, v129, v161\n"
      "v_mfma_f32_16x16x32_f16 v[80:83], a[0:3], v[112:115], v[16:19]\n"
      "v_pk_mul_f16 v98, v130, v162\n"
      "v_pk_mul_f16 v99, v131, v163\n"
      "v_mfma_f32_16x16x32_f16 v[80:83], a[4:7], v[116:119], v[80:83]\n"
      "v_fma_f32 v192, |v92|, v76, v192\n"
      "v_fma_f32 v192, |v93|, v77, v192\n"
      "v_mfma_f32_16x16x32_f16 v[80:83], a[8:11], v[120:123], v[80:83]\n"
      "v_fma_f32 v192, |v94|, v78, v192\n"
      "v_fma_f32 v192, |v95|, v79, v192\n"
      "v_mfma_f32_16x16x32_f16 v[80:83], a[12:15], v[124:127], v[80:83]\n"
      "s_waitcnt lgkmcnt(1)\n"
      "ds_read_b128 v[168:171], v203 offset:6464\n"
      "v_pk_mul_f16 v100, v132, v164\n"
      "v_mfma_f32_16x16x32_f16 v[84:87], a[28:31], v[124:127], v[20:23]\n"
      "v_permlane32_swap_b32 v194, v192\n"
      "v_mfma_f32_16x16x32_f16 v[84:87], a[24:27], v[120:123], v[84:87]\n"
      "v_pk_mul_f16 v101, v133, v165\n"
      "v_pk_mul_f16 v102, v134, v166\n"
      "v_mfma_f32_16x16x32_f16 v[84:87], a[20:23], v[116:119], v[84:87]\n"
      "v_mul_f32 v193, |v80|, v48\n"
      "v_fma_f32 v193, |v81|, v49, v193\n"
      "v_mfma_f32_16x16x32_f16 v[84:87], a[16:19], v[112:115], v[84:87]\n"
      "v_fma_f32 v193, |v82|, v50, v193\n"
      "v_fma_f32 v193, |v83|, v51, v193\n"
      "v_mfma_f32_16x16x32_f16 v[88:91], a[32:35], v[112:115], v[24:27]\n"
      "v_pk_mul_f16 v103, v135, v167\n"
      "v_pk_mul_f16 v236, v244, v176\n"
      "v_mfma_f32_16x16x32_f16 v[88:91], a[36:39], v[116:119], v[88:91]\n"
      "v_pk_mul_f16 v237, v245, v177\n"
      "v_pk_mul_f16 v238, v246, v178\n"
      "v_mfma_f32_16x16x32_f16 v[88:91], a[40:43], v[120:123], v[88:91]\n"
      "v_fma_f32 v193, |v84|, v52, v193\n"
      "v_fma_f32 v193, |v85|, v53, v193\n"
      "v_mfma_f32_16x16x32_f16 v[88:91], a[44:47], v[124:127], v[88:91]\n"
      "v_fma_f32 v193, |v86|, v54, v193\n"
      "v_fma_f32 v193, |v87|, v55, v193\n"
      "v_mfma_f32_16x16x32_f16 v[92:95], a[60:63], v[124:127], v[28:31]\n"
      "s_waitcnt lgkmcnt(0)\n"
      "v_pk_mul_f16 v104, v136, v168\n"
      "v_pk_mul_f16 v105, v137, v169\n"
      "v_mfma_f32_16x16x32_f16 v[92:95], a[56:59], v[120:123], v[92:95]\n"
      "v_pk_mul_f16 v106, v138, v170\n"
      "v_pk_mul_f16 v107, v139, v171\n"
      "v_mfma_f32_16x16x32_f16 v[92:95], a[52:55], v[116:119], v[92:95]\n"
      "v_fma_f32 v193, |v88|, v56, v193\n"
      "v_fma_f32 v193, |v89|, v57, v193\n"
      "v_mfma_f32_16x16x32_f16 v[92:95], a[48:51], v[112:115], v[92:95]\n"
      "42:\n"
      "s_cbranch_execz 44b\n"
      "v_fma_f32 v193, |v90|, v58, v193\n"
      "v_mfma_f32_16x16x32_f16 a[128:131], v[228:231], a[156:159], a[128:131]\n"
      "ds_read_b128 v[172:175], v203 offset:6528\n"
      "v_fma_f32 v193, |v91|, v59, v193\n"
      "v_mfma_f32_16x16x32_f16 v[80:83], a[64:67], v[112:115], v[32:35]\n"
      "v_pk_mul_f16 v239, v247, v179\n"
      "v_add_f32 v194, v194, v192\n"
      "v_mfma_f32_16x16x32_f16 v[80:83], a[68:71], v[116:119], v[80:83]\n"
      "v_fma_f32 v193, |v92|, v60, v193\n"
      "v_fma_f32 v193, |v93|, v61, v193\n"
      "v_mfma_f32_16x16x32_f16 v[80:83], a[72:75], v[120:123], v[80:83]\n"
      "v_fma_f32 v193, |v94|, v62, v193\n"
      "v_fma_f32 v193, |v95|, v63, v193\n"
      "v_mfma_f32_16x16x32_f16 v[80:83], a[76:79], v[124:127], v[80:83]\n"
      "ds_read_b128 v[176:179], v214 offset:12032\n"
      "v_mfma_f32_16x16x32_f16 v[84:87], a[92:95], v[124:127], v[36:39]\n"
      "v_mfma_f32_16x16x32_f16 v[84:87], a[88:91], v[120:123], v[84:87]\n"
      "v_mfma_f32_16x16x32_f16 v[84:87], a[84:87], v[116:119], v[84:87]\n"
      "s_nop 3\n"
      "v_fma_f32 v193, |v80|, v64, v193\n"
      "v_fma_f32 v193, |v81|, v65, v193\n"
      "v_mfma_f32_16x16x32_f16 v[84:87], a[80:83], v[112:115], v[84:87]\n"
      "s_waitcnt lgkmcnt(1)\n"
      "v_pk_mul_f16 v108, v140, v172\n"
      "v_fma_f32 v193, |v82|, v66, v193\n"
      "v_mfma_f32_16x16x32_f16 a[132:135], v[232:235], a[172:175], a[132:135]\n"
      "v_fma_f32 v193, |v83|, v67, v193\n"
      "v_pk_mul_f16 v109, v141, v173\n"
      "v_mfma_f32_16x16x32_f16 v[88:91], a[96:99], v[112:115], v[40:43]\n"
      "v_pk_mul_f16 v110, v142, v174\n"
      "v_pk_mul_f16 v111, v143, v175\n"
      "v_mfma_f32_16x16x32_f16 v[88:91], a[100:103], v[116:119], v[88:91]\n"
      "v_fma_f32 v193, |v84|, v68, v193\n"
      "v_fma_f32 v193, |v85|, v69, v193\n"
      "v_mfma_f32_16x16x32_f16 v[88:91], a[104:107], v[120:123], v[88:91]\n"
      "v_fma_f32 v193, |v86|, v70, v193\n"
      "v_fma_f32 v193, |v87|, v71, v193\n"
      "v_mfma_f32_16x16x32_f16 v[88:91], a[108:111], v[124:127], v[88:91]\n"
      "s_waitcnt lgkmcnt(0)\n"
      "v_pk_mul_f16 v228, v136, v176\n"
      "v_pk_mul_f16 v229, v137, v177\n"
      "v_mfma_f32_16x16x32_f16 v[92:95], a[124:127], v[124:127], v[44:47]\n"
      "v_pk_mul_f16 v230, v138, v178\n"
      "v_pk_mul_f16 v231, v139, v179\n"
      "v_mfma_f32_16x16x32_f16 v[92:95], a[120:123], v[120:123], v[92:95]\n"
      "v_pk_mul_f16 v232, v152, v176\n"
      "v_pk_mul_f16 v120, v152, v168\n"
      "v_mfma_f32_16x16x32_f16 v[92:95], a[116:119], v[116:119], v[92:95]\n"
      "v_fma_f32 v193, |v88|, v72, v193\n"
      "v_fma_f32 v193, |v89|, v73, v193\n"
      "v_mfma_f32_16x16x32_f16 v[92:95], a[112:115], v[112:115], v[92:95]\n"
      "v_fma_f32 v193, |v90|, v74, v193\n"
      "v_fma_f32 v193, |v91|, v75, v193\n"
      "v_mfma_f32_16x16x32_f16 a[136:139], v[236:239], a[188:191], a[136:139]\n"
      "v_pk_mul_f16 v112, v144, v160\n"
      "v_pk_mul_f16 v113, v145, v161\n"
      "v_mfma_f32_16x16x32_f16 v[80:83], a[0:3], v[96:99], v[16:19]\n"
      "v_pk_mul_f16 v114, v146, v162\n"
      "v_pk_mul_f16 v115, v147, v163\n"
      "v_mfma_f32_16x16x32_f16 v[80:83], a[4:7], v[100:103], v[80:83]\n"
      "v_fma_f32 v193, |v92|, v76, v193\n"
      "v_fma_f32 v193, |v93|, v77, v193\n"
      "v_mfma_f32_16x16x32_f16 v[80:83], a[8:11], v[104:107], v[80:83]\n"
      "v_fma_f32 v193, |v94|, v78, v193\n"
      "v_fma_f32 v193, |v95|, v79, v193\n"
      "v_mfma_f32_16x16x32_f16 v[80:83], a[12:15], v[108:111], v[80:83]\n"
      "v_pk_mul_f16 v116, v148, v164\n"
      "v_pk_mul_f16 v117, v149, v165\n"
      "v_mfma_f32_16x16x32_f16 v[84:87], a[28:31], v[108:111], v[20:23]\n"
      "v_permlane32_swap_b32 v195, v193\n"
      "v_mfma_f32_16x16x32_f16 v[84:87], a[24:27], v[104:107], v[84:87]\n"
      "v_pk_mul_f16 v118, v150, v166\n"
      "v_pk_mul_f16 v119, v151, v167\n"
      "v_mfma_f32_16x16x32_f16 v[84:87], a[20:23], v[100:103], v[84:87]\n"
      "v_mul_f32 v196, |v80|, v48\n"
      "v_fma_f32 v196, |v81|, v49, v196\n"
      "v_mfma_f32_16x16x32_f16 v[84:87], a[16:19], v[96:99], v[84:87]\n"
      "v_fma_f32 v196, |v82|, v50, v196\n"
      "v_fma_f32 v196, |v83|, v51, v196\n"
      "v_mfma_f32_16x16x32_f16 v[88:91], a[32:35], v[96:99], v[24:27]\n"
      "v_pk_mul_f16 v233, v153, v177\n"
      "v_pk_mul_f16 v121, v153, v169\n"
      "v_mfma_f32_16x16x32_f16 v[88:91], a[36:39], v[100:103], v[88:91]\n"
      "v_pk_mul_f16 v234, v154, v178\n"
      "v_pk_mul_f16 v122, v154, v170\n"
      "v_mfma_f32_16x16x32_f16 v[88:91], a[40:43], v[104:107], v[88:91]\n"
      "v_fma_f32 v196, |v84|, v52, v196\n"
      "v_fma_f32 v196, |v85|, v53, v196\n"
      "v_mfma_f32_16x16x32_f16 v[88:91], a[44:47], v[108:111], v[88:91]\n"
      "v_fma_f32 v196, |v86|, v54, v196\n"
      "v_fma_f32 v196, |v87|, v55, v196\n"
      "v_mfma_f32_16x16x32_f16 v[92:95], a[60:63], v[108:111], v[28:31]\n"
      "v_pk_mul_f16 v235, v155, v179\n"
      "v_pk_mul_f16 v123, v155, v171\n"
      "v_mfma_f32_16x16x32_f16 v[92:95], a[56:59], v[104:107], v[92:95]\n"
      "v_pk_mul_f16 v236, v248, v176\n"
      "v_pk_mul_f16 v237, v249, v177\n"
      "v_mfma_f32_16x16x32_f16 v[92:95], a[52:55], v[100:103], v[92:95]\n"
      "v_fma_f32 v196, |v88|, v56, v196\n"
      "v_fma_f32 v196, |v89|, v57, v196\n"
      "v_mfma_f32_16x16x32_f16 v[92:95], a[48:51], v[96:99], v[92:95]\n"
      "v_fma_f32 v196, |v90|, v58, v196\n"
      "v_fma_f32 v196, |v91|, v59, v196\n"
      "v_mfma_f32_16x16x32_f16 a[128:131], v[228:231], a[160:163], a[128:131]\n"
      "v_pk_mul_f16 v238, v250, v178\n"
      "v_pk_mul_f16 v239, v251, v179\n"
      "v_mfma_f32_16x16x32_f16 v[80:83], a[64:67], v[96:99], v[32:35]\n"
      "v_pk_mul_f16 v124, v156, v172\n"
      "v_pk_mul_f16 v125, v157, v173\n"
      "v_mfma_f32_16x16x32_f16 v[80:83], a[68:71], v[100:103], v[80:83]\n"
      "v_fma_f32 v196, |v92|, v60, v196\n"
      "v_fma_f32 v196, |v93|, v61, v196\n"
      "v_mfma_f32_16x16x32_f16 v[80:83], a[72:75], v[104:107], v[80:83]\n"
      "v_fma_f32 v196, |v94|, v62, v196\n"
      "v_fma_f32 v196, |v95|, v63, v196\n"
      "v_mfma_f32_16x16x32_f16 v[80:83], a[76:79], v[108:111], v[80:83]\n"
      "ds_read_b128 v[176:179], v214 offset:12096\n"
      "v_pk_mul_f16 v126, v158, v174\n"
      "v_mfma_f32_16x16x32_f16 v[84:87], a[92:95], v[108:111], v[36:39]\n"
      "v_pk_mul_f16 v127, v159, v175\n"
      "v_add_f32 v195, v195, v193\n"
      "v_mfma_f32_16x16x32_f16 v[84:87], a[88:91], v[104:107], v[84:87]\n"
      "v_mfma_f32_16x16x32_f16 v[84:87], a[84:87], v[100:103], v[84:87]\n"
      "s_nop 0\n"
      "v_fma_f32 v196, |v80|, v64, v196\n"
      "v_fma_f32 v196, |v81|, v65, v196\n"
      "v_mfma_f32_16x16x32_f16 v[84:87], a[80:83], v[96:99], v[84:87]\n"
      "v_fma_f32 v196, |v82|, v66, v196\n"
      "v_fma_f32 v196, |v83|, v67, v196\n"
      "v_mfma_f32_16x16x32_f16 a[132:135], v[232:235], a[176:179], a[132:135]\n"
      "ds_read_b128 v[160:163], v202 offset:4608\n"
      "v_mfma_f32_16x16x32_f16 v[88:91], a[96:99], v[96:99], v[40:43]\n"
      "v_mfma_f32_16x16x32_f16 v[88:91], a[100:103], v[100:103], v[88:91]\n"
      "s_nop 1\n"
      "v_fma_f32 v196, |v84|, v68, v196\n"
      "v_fma_f32 v196, |v85|, v69, v196\n"
      "v_mfma_f32_16x16x32_f16 v[88:91], a[104:107], v[104:107], v[88:91]\n"
      "v_fma_f32 v196, |v86|, v70, v196\n"
      "v_fma_f32 v196, |v87|, v71, v196\n"
      "v_mfma_f32_16x16x32_f16 v[88:91], a[108:111], v[108:111], v[88:91]\n"
      "s_waitcnt lgkmcnt(1)\n"
      "ds_read_b128 v[164:167], v202 offset:4672\n"
      "v_pk_mul_f16 v228, v140, v176\n"
      "v_mfma_f32_16x16x32_f16 v[92:95], a[124:127], v[108:111], v[44:47]\n"
      "v_pk_mul_f16 v229, v141, v177\n"
      "v_pk_mul_f16 v230, v142, v178\n"
      "v_mfma_f32_16x16x32_f16 v[92:95], a[120:123], v[104:107], v[92:95]\n"
      "v_pk_mul_f16 v231, v143, v179\n"
      "v_pk_mul_f16 v232, v156, v176\n"
      "v_mfma_f32_16x16x32_f16 v[92:95], a[116:119], v[100:103], v[92:95]\n"
      "v_fma_f32 v196, |v88|, v72, v196\n"
      "v_fma_f32 v196, |v89|, v73, v196\n"
      "v_mfma_f32_16x16x32_f16 v[92:95], a[112:115], v[96:99], v[92:95]\n"
      "v_fma_f32 v196, |v90|, v74, v196\n"
      "v_fma_f32 v196, |v91|, v75, v196\n"
      "v_mfma_f32_16x16x32_f16 a[136:139], v[236:239], a[192:195], a[136:139]\n"
      "s_waitcnt lgkmcnt(1)\n"
      "v_pk_mul_f16 v96, v240, v160\n"
      "v_pk_mul_f16 v97, v241, v161\n"
      "v_mfma_f32_16x16x32_f16 v[80:83], a[0:3], v[112:115], v[16:19]\n"
      "v_pk_mul_f16 v98, v242, v162\n"
      "v_pk_mul_f16 v99, v243, v163\n"
      "v_mfma_f32_16x16x32_f16 v[80:83], a[4:7], v[116:119], v[80:83]\n"
      "v_fma_f32 v196, |v92|, v76, v196\n"
      "v_fma_f32 v196, |v93|, v77, v196\n"
      "v_mfma_f32_16x16x32_f16 v[80:83], a[8:11], v[120:123], v[80:83]\n"
      "v_fma_f32 v196, |v94|, v78, v196\n"
      "v_fma_f32 v196, |v95|, v79, v196\n"
      "v_mfma_f32_16x16x32_f16 v[80:83], a[12:15], v[124:127], v[80:83]\n"
      "s_waitcnt lgkmcnt(0)\n"
      "ds_read_b128 v[168:171], v202 offset:4736\n"
      "v_pk_mul_f16 v100, v244, v164\n"
      "v_mfma_f32_16x16x32_f16 v[84:87], a[28:31], v[124:127], v[20:23]\n"
      "v_permlane32_swap_b32 v190, v196\n"
      "v_mfma_f32_16x16x32_f16 v[84:87], a[24:27], v[120:123], v[84:87]\n"
      "v_pk_mul_f16 v101, v245, v165\n"
      "v_pk_mul_f16 v102, v246, v166\n"
      "v_mfma_f32_16x16x32_f16 v[84:87], a[20:23], v[116:119], v[84:87]\n"
      "v_mul_f32 v197, |v80|, v48\n"
      "v_fma_f32 v197, |v81|, v49, v197\n"
      "v_mfma_f32_16x16x32_f16 v[84:87], a[16:19], v[112:115], v[84:87]\n"
      "v_fma_f32 v197, |v82|, v50, v197\n"
      "v_fma_f32 v197, |v83|, v51, v197\n"
      "v_mfma_f32_16x16x32_f16 v[88:91], a[32:35], v[112:115], v[24:27]\n"
      "v_pk_mul_f16 v103, v247, v167\n"
      "v_pk_mul_f16 v233, v157, v177\n"
      "v_mfma_f32_16x16x32_f16 v[88:91], a[36:39], v[116:119], v[88:91]\n"
      "v_pk_mul_f16 v234, v158, v178\n"
      "v_pk_mul_f16 v235, v159, v179\n"
      "v_mfma_f32_16x16x32_f16 v[88:91], a[40:43], v[120:123], v[88:91]\n"
      "v_fma_f32 v197, |v84|, v52, v197\n"
      "v_fma_f32 v197, |v85|, v53, v197\n"
      "v_mfma_f32_16x16x32_f16 v[88:91], a[44:47], v[124:127], v[88:91]\n"
      "v_fma_f32 v197, |v86|, v54, v197\n"
      "v_fma_f32 v197, |v87|, v55, v197\n"
      "v_mfma_f32_16x16x32_f16 v[92:95], a[60:63], v[124:127], v[28:31]\n"
      "s_waitcnt lgkmcnt(0)\n"
      "v_pk_mul_f16 v104, v248, v168\n"
      "v_pk_mul_f16 v105, v249, v169\n"
      "v_mfma_f32_16x16x32_f16 v[92:95], a[56:59], v[120:123], v[92:95]\n"
      "v_pk_mul_f16 v106, v250, v170\n"
      "v_pk_mul_f16 v107, v251, v171\n"
      "v_mfma_f32_16x16x32_f16 v[92:95], a[52:55], v[116:119], v[92:95]\n"
      "v_fma_f32 v197, |v88|, v56, v197\n"
      "v_fma_f32 v197, |v89|, v57, v197\n"
      "v_mfma_f32_16x16x32_f16 v[92:95], a[48:51], v[112:115], v[92:95]\n"
      "v_fma_f32 v197, |v90|, v58, v197\n"
      "v_fma_f32 v197, |v91|, v59, v197\n"
      "v_mfma_f32_16x16x32_f16 a[128:131], v[228:231], a[164:167], a[128:131]\n"
      "ds_read_b128 v[172:175], v202 offset:4800\n"
      "v_pk_mul_f16 v236, v252, v176\n"
      "v_mfma_f32_16x16x32_f16 v[80:83], a[64:67], v[112:115], v[32:35]\n"
      "v_pk_mul_f16 v237, v253, v177\n"
      "v_pk_mul_f16 v238, v254, v178\n"
      "v_mfma_f32_16x16x32_f16 v[80:83], a[68:71], v[116:119], v[80:83]\n"
      "v_fma_f32 v197, |v92|, v60, v197\n"
      "v_fma_f32 v197, |v93|, v61, v197\n"
      "v_mfma_f32_16x16x32_f16 v[80:83], a[72:75], v[120:123], v[80:83]\n"
      "ds_write_b64 v218, a[128:129] offset:0\n"
      "v_mfma_f32_16x16x32_f16 v[80:83], a[76:79], v[124:127], v[80:83]\n"
      "v_fma_f32 v197, |v94|, v62, v197\n"
      "v_fma_f32 v197, |v95|, v63, v197\n"
      "v_mfma_f32_16x16x32_f16 v[84:87], a[92:95], v[124:127], v[36:39]\n"
      "ds_write_b64 v218, a[130:131] offset:8\n"
      "v_mfma_f32_16x16x32_f16 v[84:87], a[88:91], v[120:123], v[84:87]\n"
      "v_pk_mul_f16 v239, v255, v179\n"
      "v_add_f32 v190, v190, v196\n"
      "v_mfma_f32_16x16x32_f16 v[84:87], a[84:87], v[116:119], v[84:87]\n"
      "v_fma_f32 v197, |v80|, v64, v197\n"
      "v_fma_f32 v197, |v81|, v65, v197\n"
      "v_mfma_f32_16x16x32_f16 v[84:87], a[80:83], v[112:115], v[84:87]\n"
      "v_permlane16_swap_b32 v194, v190\n"
      "v_mfma_f32_16x16x32_f16 a[132:135], v[232:235], a[180:183], a[132:135]\n"
      "s_waitcnt lgkmcnt(2)\n"
      "v_pk_mul_f16 v108, v252, v172\n"
      "v_fma_f32 v197, |v82|, v66, v197\n"
      "v_mfma_f32_16x16x32_f16 v[88:91], a[96:99], v[112:115], v[40:43]\n"
      "v_fma_f32 v197, |v83|, v67, v197\n"
      "v_pk_mul_f16 v109, v253, v173\n"
      "v_mfma_f32_16x16x32_f16 v[88:91], a[100:103], v[116:119], v[88:91]\n"
      "v_pk_mul_f16 v110, v254, v174\n"
      "v_pk_mul_f16 v111, v255, v175\n"
      "v_mfma_f32_16x16x32_f16 v[88:91], a[104:107], v[120:123], v[88:91]\n"
      "ds_write_b64 v218, a[132:133] offset:1280\n"
      "v_mfma_f32_16x16x32_f16 v[88:91], a[108:111], v[124:127], v[88:91]\n"
      "v_fma_f32 v197, |v84|, v68, v197\n"
      "v_fma_f32 v197, |v85|, v69, v197\n"
      "v_mfma_f32_16x16x32_f16 v[92:95], a[124:127], v[124:127], v[44:47]\n"
      "ds_write_b64 v218, a[134:135] offset:1288\n"
      "v_mfma_f32_16x16x32_f16 v[92:95], a[120:123], v[120:123], v[92:95]\n"
      "v_fma_f32 v197, |v86|, v70, v197\n"
      "v_fma_f32 v197, |v87|, v71, v197\n"
      "v_mfma_f32_16x16x32_f16 v[92:95], a[116:119], v[116:119], v[92:95]\n"
      "ds_read_b128 v[160:163], v203 offset:6912\n"
      "v_fma_f32 v197, |v88|, v72, v197\n"
      "v_mfma_f32_16x16x32_f16 v[92:95], a[112:115], v[112:115], v[92:95]\n"
      "v_fma_f32 v197, |v89|, v73, v197\n"
      "v_fma_f32 v197, |v90|, v74, v197\n"
      "v_mfma_f32_16x16x32_f16 a[136:139], v[236:239], a[196:199], a[136:139]\n"
      "ds_read_b128 v[164:167], v203 offset:6976\n"
      "v_fma_f32 v197, |v91|, v75, v197\n"
      "v_mfma_f32_16x16x32_f16 v[80:83], a[0:3], v[96:99], v[16:19]\n"
      "ds_read_b128 v[168:171], v203 offset:7040\n"
      "v_add_f32 v194, v194, v190\n"
      "v_mfma_f32_16x16x32_f16 v[80:83], a[4:7], v[100:103], v[80:83]\n"
      "v_fma_f32 v197, |v92|, v76, v197\n"
      "v_fma_f32 v197, |v93|, v77, v197\n"
      "v_mfma_f32_16x16x32_f16 v[80:83], a[8:11], v[104:107], v[80:83]\n"
      "ds_write_b128 v218, a[136:139] offset:2560\n"
      "v_mfma_f32_16x16x32_f16 v[80:83], a[12:15], v[108:111], v[80:83]\n"
      "v_fma_f32 v197, |v94|, v78, v197\n"
      "v_fma_f32 v197, |v95|, v79, v197\n"
      "v_mfma_f32_16x16x32_f16 v[84:87], a[28:31], v[108:111], v[20:23]\n"
      "v_mfma_f32_16x16x32_f16 v[84:87], a[24:27], v[104:107], v[84:87]\n"
      "s_waitcnt lgkmcnt(3)\n"
      "v_pk_mul_f16 v112, v128, v160\n"
      "v_pk_mul_f16 v113, v129, v161\n"
      "v_mfma_f32_16x16x32_f16 v[84:87], a[20:23], v[100:103], v[84:87]\n"
      "v_permlane32_swap_b32 v191, v197\n"
      "v_mfma_f32_16x16x32_f16 v[84:87], a[16:19], v[96:99], v[84:87]\n"
      "v_pk_mul_f16 v114, v130, v162\n"
      "v_pk_mul_f16 v115, v131, v163\n"
      "v_mfma_f32_16x16x32_f16 v[88:91], a[32:35], v[96:99], v[24:27]\n"
      "v_mul_f32 v198, |v80|, v48\n"
      "v_fma_f32 v198, |v81|, v49, v198\n"
      "v_mfma_f32_16x16x32_f16 v[88:91], a[36:39], v[100:103], v[88:91]\n"
      "v_fma_f32 v198, |v82|, v50, v198\n"
      "v_fma_f32 v198, |v83|, v51, v198\n"
      "v_mfma_f32_16x16x32_f16 v[88:91], a[40:43], v[104:107], v[88:91]\n"
      "s_waitcnt lgkmcnt(2)\n"
      "v_pk_mul_f16 v116, v132, v164\n"
      "v_pk_mul_f16 v117, v133, v165\n"
      "v_mfma_f32_16x16x32_f16 v[88:91], a[44:47], v[108:111], v[88:91]\n"
      "v_pk_mul_f16 v118, v134, v166\n"
      "v_fma_f32 v198, |v84|, v52, v198\n"
      "v_mfma_f32_16x16x32_f16 v[92:95], a[60:63], v[108:111], v[28:31]\n"
      "v_fma_f32 v198, |v85|, v53, v198\n"
      "v_pk_mul_f16 v119, v135, v167\n"
      "v_mfma_f32_16x16x32_f16 v[92:95], a[56:59], v[104:107], v[92:95]\n"
      "v_fma_f32 v198, |v86|, v54, v198\n"
      "v_fma_f32 v198, |v87|, v55, v198\n"
      "v_mfma_f32_16x16x32_f16 v[92:95], a[52:55], v[100:103], v[92:95]\n"
      "s_waitcnt lgkmcnt(1)\n"
      "v_pk_mul_f16 v120, v136, v168\n"
      "v_fma_f32 v198, |v88|, v56, v198\n"
      "v_mfma_f32_16x16x32_f16 v[92:95], a[48:51], v[96:99], v[92:95]\n"
      "v_fma_f32 v198, |v89|, v57, v198\n"
      "v_pk_mul_f16 v121, v137, v169\n"
      "v_mfma_f32_16x16x32_f16 v[80:83], a[64:67], v[96:99], v[32:35]\n"
      "ds_read_b128 v[172:175], v203 offset:7104\n"
      "v_fma_f32 v198, |v90|, v58, v198\n"
      "v_mfma_f32_16x16x32_f16 v[80:83], a[68:71], v[100:103], v[80:83]\n"
      "v_fma_f32 v198, |v91|, v59, v198\n"
      "v_pk_mul_f16 v122, v138, v170\n"
      "v_mfma_f32_16x16x32_f16 v[80:83], a[72:75], v[104:107], v[80:83]\n"
      "v_pk_mul_f16 v123, v139, v171\n"
      "v_fma_f32 v198, |v92|, v60, v198\n"
      "v_mfma_f32_16x16x32_f16 v[80:83], a[76:79], v[108:111], v[80:83]\n"
      "v_fma_f32 v198, |v93|, v61, v198\n"
      "v_fma_f32 v198, |v94|, v62, v198\n"
      "v_mfma_f32_16x16x32_f16 v[84:87], a[92:95], v[108:111], v[36:39]\n"
      "v_fma_f32 v198, |v95|, v63, v198\n"
      "v_add_f32 v191, v191, v197\n"
      "v_mfma_f32_16x16x32_f16 v[84:87], a[88:91], v[104:107], v[84:87]\n"
      "ds_read2_b32 v[228:229], v204 offset0:0 offset1:8\n"
      "ds_read2_b32 v[230:231], v205 offset0:0 offset1:8\n"
      "v_mfma_f32_16x16x32_f16 v[84:87], a[84:87], v[100:103], v[84:87]\n"
      "v_permlane16_swap_b32 v195, v191\n"
      "v_mfma_f32_16x16x32_f16 v[84:87], a[80:83], v[96:99], v[84:87]\n"
      "v_fma_f32 v198, |v80|, v64, v198\n"
      "v_fma_f32 v198, |v81|, v65, v198\n"
      "v_mfma_f32_16x16x32_f16 v[88:91], a[96:99], v[96:99], v[40:43]\n"
      "s_waitcnt lgkmcnt(2)\n"
      "v_pk_mul_f16 v124, v140, v172\n"
      "v_fma_f32 v198, |v82|, v66, v198\n"
      "v_mfma_f32_16x16x32_f16 v[88:91], a[100:103], v[100:103], v[88:91]\n"
      "v_fma_f32 v198, |v83|, v67, v198\n"
      "v_pk_mul_f16 v125, v141, v173\n"
      "v_mfma_f32_16x16x32_f16 v[88:91], a[104:107], v[104:107], v[88:91]\n"
      "v_pk_mul_f16 v126, v142, v174\n"
      "v_pk_mul_f16 v127, v143, v175\n"
      "v_mfma_f32_16x16x32_f16 v[88:91], a[108:111], v[108:111], v[88:91]\n"
      "v_fma_f32 v198, |v84|, v68, v198\n"
      "v_fma_f32 v198, |v85|, v69, v198\n"
      "v_mfma_f32_16x16x32_f16 v[92:95], a[124:127], v[108:111], v[44:47]\n"
      "v_fma_f32 v198, |v86|, v70, v198\n"
      "v_fma_f32 v198, |v87|, v71, v198\n"
      "v_mfma_f32_16x16x32_f16 v[92:95], a[120:123], v[104:107], v[92:95]\n"
      "v_add_f32 v195, v195, v191\n"
      "v_pk_mul_f16 v104, v152, v168\n"
      "v_mfma_f32_16x16x32_f16 v[92:95], a[116:119], v[100:103], v[92:95]\n"
      "v_fma_f32 v198, |v88|, v72, v198\n"
      "v_fma_f32 v198, |v89|, v73, v198\n"
      "v_mfma_f32_16x16x32_f16 v[92:95], a[112:115], v[96:99], v[92:95]\n"
      "v_fma_f32 v198, |v90|, v74, v198\n"
      "v_fma_f32 v198, |v91|, v75, v198\n"
      "v_mfma_f32_16x16x32_f16 v[80:83], a[0:3], v[112:115], v[16:19]\n"
      "v_pk_mul_f16 v96, v144, v160\n"
      "v_pk_mul_f16 v97, v145, v161\n"
      "v_mfma_f32_16x16x32_f16 v[80:83], a[4:7], v[116:119], v[80:83]\n"
      "v_pk_mul_f16 v98, v146, v162\n"
      "v_pk_mul_f16 v99, v147, v163\n"
      "v_mfma_f32_16x16x32_f16 v[80:83], a[8:11], v[120:123], v[80:83]\n"
      "v_fma_f32 v198, |v92|, v76, v198\n"
      "v_fma_f32 v198, |v93|, v77, v198\n"
      "v_mfma_f32_16x16x32_f16 v[80:83], a[12:15], v[124:127], v[80:83]\n"
      "v_fma_f32 v198, |v94|, v78, v198\n"
      "v_fma_f32 v198, |v95|, v79, v198\n"
      "v_mfma_f32_16x16x32_f16 v[84:87], a[28:31], v[124:127], v[20:23]\n"
      "v_pk_mul_f16 v100, v148, v164\n"
      "v_pk_mul_f16 v101, v149, v165\n"
      "v_mfma_f32_16x16x32_f16 v[84:87], a[24:27], v[120:123], v[84:87]\n"
      "v_pk_mul_f16 v102, v150, v166\n"
      "v_pk_mul_f16 v103, v151, v167\n"
      "v_mfma_f32_16x16x32_f16 v[84:87], a[20:23], v[116:119], v[84:87]\n"
      "v_mul_f32 v192, |v80|, v48\n"
      "v_fma_f32 v192, |v81|, v49, v192\n"
      "v_mfma_f32_16x16x32_f16 v[84:87], a[16:19], v[112:115], v[84:87]\n"
      "v_fma_f32 v192, |v82|, v50, v192\n"
      "v_fma_f32 v192, |v83|, v51, v192\n"
      "v_mfma_f32_16x16x32_f16 v[88:91], a[32:35], v[112:115], v[24:27]\n"
      "v_pk_mul_f16 v105, v153, v169\n"
      "v_pk_mul_f16 v106, v154, v170\n"
      "v_mfma_f32_16x16x32_f16 v[88:91], a[36:39], v[116:119], v[88:91]\n"
      "ds_read_b32 v224, v227 offset:37640\n"
      "v_pk_mul_f16 v107, v155, v171\n"
      "v_mfma_f32_16x16x32_f16 v[88:91], a[40:43], v[120:123], v[88:91]\n"
      "v_fma_f32 v192, |v84|, v52, v192\n"
      "v_fma_f32 v192, |v85|, v53, v192\n"
      "v_mfma_f32_16x16x32_f16 v[88:91], a[44:47], v[124:127], v[88:91]\n"
      "v_fma_f32 v192, |v86|, v54, v192\n"
      "v_fma_f32 v192, |v87|, v55, v192\n"
      "v_mfma_f32_16x16x32_f16 v[92:95], a[60:63], v[124:127], v[28:31]\n"
      "ds_read2_b32 v[232:233], v206 offset0:0 offset1:8\n"
      "v_pk_mul_f16 v108, v156, v172\n"
      "v_mfma_f32_16x16x32_f16 v[92:95], a[56:59], v[120:123], v[92:95]\n"
      "ds_read2_b32 v[234:235], v207 offset0:0 offset1:8\n"
      "v_pk_mul_f16 v109, v157, v173\n"
      "v_mfma_f32_16x16x32_f16 v[92:95], a[52:55], v[116:119], v[92:95]\n"
      "v_fma_f32 v192, |v88|, v56, v192\n"
      "v_fma_f32 v192, |v89|, v57, v192\n"
      "v_mfma_f32_16x16x32_f16 v[92:95], a[48:51], v[112:115], v[92:95]\n"
      "v_fma_f32 v192, |v90|, v58, v192\n"
      "v_fma_f32 v192, |v91|, v59, v192\n"
      "v_mfma_f32_16x16x32_f16 v[80:83], a[64:67], v[112:115], v[32:35]\n"
      "ds_read2_b32 v[236:237], v208 offset0:0 offset1:8\n"
      "v_pk_mul_f16 v110, v158, v174\n"
      "v_mfma_f32_16x16x32_f16 v[80:83], a[68:71], v[116:119], v[80:83]\n"
      "ds_read2_b32 v[238:239], v209 offset0:0 offset1:8\n"
      "v_pk_mul_f16 v111, v159, v175\n"
      "v_mfma_f32_16x16x32_f16 v[80:83], a[72:75], v[120:123], v[80:83]\n"
      "v_fma_f32 v192, |v92|, v60, v192\n"
      "v_fma_f32 v192, |v93|, v61, v192\n"
      "v_mfma_f32_16x16x32_f16 v[80:83], a[76:79], v[124:127], v[80:83]\n"
      "v_fma_f32 v192, |v94|, v62, v192\n"
      "v_fma_f32 v192, |v95|, v63, v192\n"
      "v_mfma_f32_16x16x32_f16 v[84:87], a[92:95], v[124:127], v[36:39]\n"
      "s_waitcnt lgkmcnt(4)\n"
      "ds_read_b128 v[128:131], v210 offset:0\n"
      "v_cmp_eq_u32 vcc, 1, v224\n"
      "v_mfma_f32_16x16x32_f16 v[84:87], a[88:91], v[120:123], v[84:87]\n"
      "ds_read_b128 v[144:147], v211 offset:0\n"
      "ds_read_b128 v[132:135], v210 offset:64\n"
      "v_mfma_f32_16x16x32_f16 v[84:87], a[84:87], v[116:119], v[84:87]\n"
      "s_and_b32 s44, vcc_lo, s54\n"
      "v_fma_f32 v192, |v80|, v64, v192\n"
      "v_fma_f32 v192, |v81|, v65, v192\n"
      "v_mfma_f32_16x16x32_f16 v[84:87], a[80:83], v[112:115], v[84:87]\n"
      "s_cmp_lg_u32 s44, s54\n"
      "v_fma_f32 v192, |v82|, v66, v192\n"
      "v_fma_f32 v192, |v83|, v67, v192\n"
      "v_mfma_f32_16x16x32_f16 v[88:91], a[96:99], v[112:115], v[40:43]\n"
      "s_cbranch_scc1 80f\n"
      "81:\n"
      "v_mfma_f32_16x16x32_f16 v[88:91], a[100:103], v[116:119], v[88:91]\n"
      "ds_read_b128 v[148:151], v211 offset:64\n"
      "ds_read_b128 v[136:139], v210 offset:128\n"
      "v_mfma_f32_16x16x32_f16 v[88:91], a[104:107], v[120:123], v[88:91]\n"
      "v_fma_f32 v192, |v84|, v68, v192\n"
      "v_fma_f32 v192, |v85|, v69, v192\n"
      "v_mfma_f32_16x16x32_f16 v[88:91], a[108:111], v[124:127], v[88:91]\n"
      "v_fma_f32 v192, |v86|, v70, v192\n"
      "v_fma_f32 v192, |v87|, v71, v192\n"
      "v_mfma_f32_16x16x32_f16 v[92:95], a[124:127], v[124:127], v[44:47]\n"
      "v_pk_mul_f16 v124, v252, v172\n"
      "v_pk_mul_f16 v125, v253, v173\n"
      "v_mfma_f32_16x16x32_f16 v[92:95], a[120:123], v[120:123], v[92:95]\n"
      "v_pk_mul_f16 v120, v248, v168\n"
      "v_pk_mul_f16 v121, v249, v169\n"
      "v_mfma_f32_16x16x32_f16 v[92:95], a[116:119], v[116:119], v[92:95]\n"
      "v_fma_f32 v192, |v88|, v72, v192\n"
      "v_fma_f32 v192, |v89|, v73, v192\n"
      "v_mfma_f32_16x16x32_f16 v[92:95], a[112:115], v[112:115], v[92:95]\n"
      "v_fma_f32 v192, |v90|, v74, v192\n"
      "v_fma_f32 v192, |v91|, v75, v192\n"
      "v_mfma_f32_16x16x32_f16 v[80:83], a[0:3], v[96:99], v[16:19]\n"
      "v_pk_mul_f16 v112, v240, v160\n"
      "v_pk_mul_f16 v113, v241, v161\n"
      "v_mfma_f32_16x16x32_f16 v[80:83], a[4:7], v[100:103], v[80:83]\n"
      "v_pk_mul_f16 v114, v242, v162\n"
      "v_pk_mul_f16 v115, v243, v163\n"
      "v_mfma_f32_16x16x32_f16 v[80:83], a[8:11], v[104:107], v[80:83]\n"
      "v_fma_f32 v192, |v92|, v76, v192\n"
      "v_fma_f32 v192, |v93|, v77, v192\n"
      "v_mfma_f32_16x16x32_f16 v[80:83], a[12:15], v[108:111], v[80:83]\n"
      "v_fma_f32 v192, |v94|, v78, v192\n"
      "v_fma_f32 v192, |v95|, v79, v192\n"
      "v_mfma_f32_16x16x32_f16 v[84:87], a[28:31], v[108:111], v[20:23]\n"
      "v_pk_mul_f16 v116, v244, v164\n"
      "v_pk_mul_f16 v117, v245, v165\n"
      "v_mfma_f32_16x16x32_f16 v[84:87], a[24:27], v[104:107], v[84:87]\n"
      "v_pk_mul_f16 v118, v246, v166\n"
      "v_pk_mul_f16 v119, v247, v167\n"
      "v_mfma_f32_16x16x32_f16 v[84:87], a[20:23], v[100:103], v[84:87]\n"
      "v_mul_f32 v193, |v80|, v48\n"
      "v_fma_f32 v193, |v81|, v49, v193\n"
      "v_mfma_f32_16x16x32_f16 v[84:87], a[16:19], v[96:99], v[84:87]\n"
      "v_fma_f32 v193, |v82|, v50, v193\n"
      "v_fma_f32 v193, |v83|, v51, v193\n"
      "v_mfma_f32_16x16x32_f16 v[88:91], a[32:35], v[96:99], v[24:27]\n"
      "v_pk_mul_f16 v122, v250, v170\n"
      "v_pk_mul_f16 v123, v251, v171\n"
      "v_mfma_f32_16x16x32_f16 v[88:91], a[36:39], v[100:103], v[88:91]\n"
      "v_pk_mul_f16 v126, v254, v174\n"
      "v_pk_mul_f16 v127, v255, v175\n"
      "v_mfma_f32_16x16x32_f16 v[88:91], a[40:43], v[104:107], v[88:91]\n"
      "v_fma_f32 v193, |v84|, v52, v193\n"
      "v_fma_f32 v193, |v85|, v53, v193\n"
      "v_mfma_f32_16x16x32_f16 v[88:91], a[44:47], v[108:111], v[88:91]\n"
      "v_fma_f32 v193, |v86|, v54, v193\n"
      "v_fma_f32 v193, |v87|, v55, v193\n"
      "v_mfma_f32_16x16x32_f16 v[92:95], a[60:63], v[108:111], v[28:31]\n"
      "ds_read_b128 v[160:163], v10 offset:0\n"
      "ds_read_b128 v[240:243], v11 offset:0\n"
      "v_mfma_f32_16x16x32_f16 v[92:95], a[56:59], v[104:107], v[92:95]\n"
      "ds_read_b128 v[164:167], v10 offset:64\n"
      "ds_read_b128 v[244:247], v11 offset:64\n"
      "v_mfma_f32_16x16x32_f16 v[92:95], a[52:55], v[100:103], v[92:95]\n"
      "v_fma_f32 v193, |v88|, v56, v193\n"
      "v_fma_f32 v193, |v89|, v57, v193\n"
      "v_mfma_f32_16x16x32_f16 v[92:95], a[48:51], v[96:99], v[92:95]\n"
      "v_fma_f32 v193, |v90|, v58, v193\n"
      "v_fma_f32 v193, |v91|, v59, v193\n"
      "v_mfma_f32_16x16x32_f16 v[80:83], a[64:67], v[96:99], v[32:35]\n"
      "ds_read_b128 v[168:171], v10 offset:128\n"
      "ds_read_b128 v[248:251], v11 offset:128\n"
      "v_mfma_f32_16x16x32_f16 v[80:83], a[68:71], v[100:103], v[80:83]\n"
      "ds_read_b128 v[172:175], v10 offset:192\n"
      "ds_read_b128 v[252:255], v11 offset:192\n"
      "v_mfma_f32_16x16x32_f16 v[80:83], a[72:75], v[104:107], v[80:83]\n"
      "v_fma_f32 v193, |v92|, v60, v193\n"
      "v_fma_f32 v193, |v93|, v61, v193\n"
      "v_mfma_f32_16x16x32_f16 v[80:83], a[76:79], v[108:111], v[80:83]\n"
      "v_fma_f32 v193, |v94|, v62, v193\n"
      "v_fma_f32 v193, |v95|, v63, v193\n"
      "v_mfma_f32_16x16x32_f16 v[84:87], a[92:95], v[108:111], v[36:39]\n"
      "s_waitcnt lgkmcnt(6)\n"
      "v_pk_mul_f16 v160, v160, v240\n"
      "v_pk_mul_f16 v161, v161, v241\n"
      "v_mfma_f32_16x16x32_f16 v[84:87], a[88:91], v[104:107], v[84:87]\n"
      "v_pk_mul_f16 v162, v162, v242\n"
      "v_pk_mul_f16 v163, v163, v243\n"
      "v_mfma_f32_16x16x32_f16 v[84:87], a[84:87], v[100:103], v[84:87]\n"
      "v_fma_f32 v193, |v80|, v64, v193\n"
      "v_fma_f32 v193, |v81|, v65, v193\n"
      "v_mfma_f32_16x16x32_f16 v[84:87], a[80:83], v[96:99], v[84:87]\n"
      "v_fma_f32 v193, |v82|, v66, v193\n"
      "v_fma_f32 v193, |v83|, v67, v193\n"
      "v_mfma_f32_16x16x32_f16 v[88:91], a[96:99], v[96:99], v[40:43]\n"
      "43:\n"
      "s_cbranch_execz 44b\n"
      "s_waitcnt lgkmcnt(4)\n"
      "v_pk_mul_f16 v164, v164, v244\n"
      "v_mfma_f32_16x16x32_f16 v[88:91], a[100:103], v[100:103], v[88:91]\n"
      "v_pk_mul_f16 v165, v165, v245\n"
      "v_pk_mul_f16 v166, v166, v246\n"
      "v_mfma_f32_16x16x32_f16 v[88:91], a[104:107], v[104:107], v[88:91]\n"
      "v_fma_f32 v193, |v84|, v68, v193\n"
      "v_fma_f32 v193, |v85|, v69, v193\n"
      "v_mfma_f32_16x16x32_f16 v[88:91], a[108:111], v[108:111], v[88:91]\n"
      "v_fma_f32 v193, |v86|, v70, v193\n"
      "v_fma_f32 v193, |v87|, v71, v193\n"
      "v_mfma_f32_16x16x32_f16 v[92:95], a[124:127], v[108:111], v[44:47]\n"
      "s_waitcnt lgkmcnt(2)\n"
      "v_pk_mul_f16 v167, v167, v247\n"
      "v_pk_mul_f16 v168, v168, v248\n"
      "v_mfma_f32_16x16x32_f16 v[92:95], a[120:123], v[104:107], v[92:95]\n"
      "v_pk_mul_f16 v169, v169, v249\n"
      "v_pk_mul_f16 v170, v170, v250\n"
      "v_mfma_f32_16x16x32_f16 v[92:95], a[116:119], v[100:103], v[92:95]\n"
      "v_fma_f32 v193, |v88|, v72, v193\n"
      "v_fma_f32 v193, |v89|, v73, v193\n"
      "v_mfma_f32_16x16x32_f16 v[92:95], a[112:115], v[96:99], v[92:95]\n"
      "v_fma_f32 v193, |v90|, v74, v193\n"
      "v_fma_f32 v193, |v91|, v75, v193\n"
      "v_mfma_f32_16x16x32_f16 v[80:83], a[0:3], v[112:115], v[16:19]\n"
      "v_pk_mul_f16 v128, v128, v144\n"
      "v_pk_mul_f16 v171, v171, v251\n"
      "v_mfma_f32_16x16x32_f16 v[80:83], a[4:7], v[116:119], v[80:83]\n"
      "v_pk_mul_f16 v129, v129, v145\n"
      "v_pk_mul_f16 v130, v130, v146\n"
      "v_mfma_f32_16x16x32_f16 v[80:83], a[8:11], v[120:123], v[80:83]\n"
      "v_fma_f32 v193, |v92|, v76, v193\n"
      "v_fma_f32 v193, |v93|, v77, v193\n"
      "v_mfma_f32_16x16x32_f16 v[80:83], a[12:15], v[124:127], v[80:83]\n"
      "v_fma_f32 v193, |v94|, v78, v193\n"
      "v_fma_f32 v193, |v95|, v79, v193\n"
      "v_mfma_f32_16x16x32_f16 v[84:87], a[28:31], v[124:127], v[20:23]\n"
      "s_waitcnt lgkmcnt(0)\n"
      "v_pk_mul_f16 v131, v131, v147\n"
      "v_pk_mul_f16 v172, v172, v252\n"
      "v_mfma_f32_16x16x32_f16 v[84:87], a[24:27], v[120:123], v[84:87]\n"
      "ds_read_b128 v[152:155], v211 offset:128\n"
      "v_pk_mul_f16 v173, v173, v253\n"
      "v_mfma_f32_16x16x32_f16 v[84:87], a[20:23], v[116:119], v[84:87]\n"
      "v_mul_f32 v190, |v80|, v48\n"
      "v_fma_f32 v190, |v81|, v49, v190\n"
      "v_mfma_f32_16x16x32_f16 v[84:87], a[16:19], v[112:115], v[84:87]\n"
      "v_fma_f32 v190, |v82|, v50, v190\n"
      "v_fma_f32 v190, |v83|, v51, v190\n"
      "v_mfma_f32_16x16x32_f16 v[88:91], a[32:35], v[112:115], v[24:27]\n"
      "v_pk_mul_f16 v174, v174, v254\n"
      "v_pk_mul_f16 v132, v132, v148\n"
      "v_mfma_f32_16x16x32_f16 v[88:91], a[36:39], v[116:119], v[88:91]\n"
      "v_pk_mul_f16 v175, v175, v255\n"
      "v_pk_mul_f16 v133, v133, v149\n"
      "v_mfma_f32_16x16x32_f16 v[88:91], a[40:43], v[120:123], v[88:91]\n"
      "v_fma_f32 v190, |v84|, v52, v190\n"
      "v_fma_f32 v190, |v85|, v53, v190\n"
      "v_mfma_f32_16x16x32_f16 v[88:91], a[44:47], v[124:127], v[88:91]\n"
      "v_fma_f32 v190, |v86|, v54, v190\n"
      "v_fma_f32 v190, |v87|, v55, v190\n"
      "v_mfma_f32_16x16x32_f16 v[92:95], a[60:63], v[124:127], v[28:31]\n"
      "v_fmac_f32 v189, v200, v228\n"
      "v_pk_mul_f16 v134, v134, v150\n"
      "v_mfma_f32_16x16x32_f16 v[92:95], a[56:59], v[120:123], v[92:95]\n"
      "v_permlane32_swap_b32 v198, v192\n"
      "v_mfma_f32_16x16x32_f16 v[92:95], a[52:55], v[116:119], v[92:95]\n"
      "v_fma_f32 v190, |v88|, v56, v190\n"
      "v_fma_f32 v190, |v89|, v57, v190\n"
      "v_mfma_f32_16x16x32_f16 v[92:95], a[48:51], v[112:115], v[92:95]\n"
      "v_fma_f32 v190, |v90|, v58, v190\n"
      "v_fma_f32 v190, |v91|, v59, v190\n"
      "v_mfma_f32_16x16x32_f16 v[80:83], a[64:67], v[112:115], v[32:35]\n"
      "v_fmac_f32 v194, v200, v230\n"
      "v_pk_mul_f16 v135, v135, v151\n"
      "v_mfma_f32_16x16x32_f16 v[80:83], a[68:71], v[116:119], v[80:83]\n"
      "v_fmac_f32 v195, v200, v232\n"
      "v_max3_f32 v219, v189, v194, v195\n"
      "v_mfma_f32_16x16x32_f16 v[80:83], a[72:75], v[120:123], v[80:83]\n"
      "v_fma_f32 v190, |v92|, v60, v190\n"
      "v_fma_f32 v190, |v93|, v61, v190\n"
      "v_mfma_f32_16x16x32_f16 v[80:83], a[76:79], v[124:127], v[80:83]\n"
      "v_fma_f32 v190, |v94|, v62, v190\n"
      "v_fma_f32 v190, |v95|, v63, v190\n"
      "v_mfma_f32_16x16x32_f16 v[84:87], a[92:95], v[124:127], v[36:39]\n"
      "ds_read_b128 v[140:143], v210 offset:192\n"
      "v_min3_f32 v226, v189, v194, v195\n"
      "v_mfma_f32_16x16x32_f16 v[84:87], a[88:91], v[120:123], v[84:87]\n"
      "ds_read_b128 v[156:159], v211 offset:192\n"
      "v_sub_f32 v189, v189, v219\n"
      "v_mfma_f32_16x16x32_f16 v[84:87], a[84:87], v[116:119], v[84:87]\n"
      "v_fma_f32 v190, |v80|, v64, v190\n"
      "v_fma_f32 v190, |v81|, v65, v190\n"
      "v_mfma_f32_16x16x32_f16 v[84:87], a[80:83], v[112:115], v[84:87]\n"
      "v_fma_f32 v190, |v82|, v66, v190\n"
      "v_fma_f32 v190, |v83|, v67, v190\n"
      "v_mfma_f32_16x16x32_f16 v[88:91], a[96:99], v[112:115], v[40:43]\n"
      "s_waitcnt lgkmcnt(2)\n"
      "v_pk_mul_f16 v136, v136, v152\n"
      "v_sub_f32 v194, v194, v219\n"
      "v_mfma_f32_16x16x32_f16 v[88:91], a[100:103], v[116:119], v[88:91]\n"
      "v_pk_mul_f16 v137, v137, v153\n"
      "v_sub_f32 v195, v195, v219\n"
      "v_mfma_f32_16x16x32_f16 v[88:91], a[104:107], v[120:123], v[88:91]\n"
      "v_exp_f32 v189, v189\n"
      "v_mfma_f32_16x16x32_f16 v[88:91], a[108:111], v[124:127], v[88:91]\n"
      "v_exp_f32 v194, v194\n"
      "v_mfma_f32_16x16x32_f16 v[92:95], a[124:127], v[124:127], v[44:47]\n"
      "v_exp_f32 v195, v195\n"
      "v_mfma_f32_16x16x32_f16 v[92:95], a[120:123], v[120:123], v[92:95]\n"
      "v_fma_f32 v190, |v84|, v68, v190\n"
      "v_fma_f32 v190, |v85|, v69, v190\n"
      "v_mfma_f32_16x16x32_f16 v[92:95], a[116:119], v[116:119], v[92:95]\n"
      "v_fma_f32 v190, |v86|, v70, v190\n"
      "v_fma_f32 v190, |v87|, v71, v190\n"
      "v_mfma_f32_16x16x32_f16 v[92:95], a[112:115], v[112:115], v[92:95]\n"
      "v_fma_f32 v190, |v88|, v72, v190\n"
      "v_fma_f32 v190, |v89|, v73, v190\n"
      "v_mfma_f32_16x16x32_f16 v[80:83], a[0:3], v[160:163], v[16:19]\n"
      "v_fma_f32 v190, |v90|, v74, v190\n"
      "v_fma_f32 v190, |v91|, v75, v190\n"
      "v_mfma_f32_16x16x32_f16 v[80:83], a[4:7], v[164:167], v[80:83]\n"
      "v_pk_mul_f16 v138, v138, v154\n"
      "v_pk_mul_f16 v139, v139, v155\n"
      "v_mfma_f32_16x16x32_f16 v[80:83], a[8:11], v[168:171], v[80:83]\n"
      "v_fma_f32 v190, |v92|, v76, v190\n"
      "v_fma_f32 v190, |v93|, v77, v190\n"
      "v_mfma_f32_16x16x32_f16 v[80:83], a[12:15], v[172:175], v[80:83]\n"
      "v_fma_f32 v190, |v94|, v78, v190\n"
      "v_fma_f32 v190, |v95|, v79, v190\n"
      "v_mfma_f32_16x16x32_f16 v[84:87], a[28:31], v[172:175], v[20:23]\n"
      "v_mul_f32 v221, v189, v229\n"
      "v_add_f32 v198, v198, v192\n"
      "v_mfma_f32_16x16x32_f16 v[84:87], a[24:27], v[168:171], v[84:87]\n"
      "v_permlane32_swap_b32 v193, v190\n"
      "v_mfma_f32_16x16x32_f16 v[84:87], a[20:23], v[164:167], v[84:87]\n"
      "v_permlane16_swap_b32 v188, v198\n"
      "v_mfma_f32_16x16x32_f16 v[84:87], a[16:19], v[160:163], v[84:87]\n"
      "v_add_f32 v220, v189, v194\n"
      "v_mul_f32 v199, |v80|, v48\n"
      "v_mfma_f32_16x16x32_f16 v[88:91], a[32:35], v[160:163], v[24:27]\n"
      "s_waitcnt lgkmcnt(0)\n"
      "v_fma_f32 v199, |v81|, v49, v199\n"
      "v_pk_mul_f16 v140, v140, v156\n"
      "v_mfma_f32_16x16x32_f16 v[88:91], a[36:39], v[164:167], v[88:91]\n"
      "v_fmac_f32 v221, v194, v231\n"
      "v_fma_f32 v199, |v82|, v50, v199\n"
      "v_mfma_f32_16x16x32_f16 v[88:91], a[40:43], v[168:171], v[88:91]\n"
      "v_fma_f32 v199, |v83|, v51, v199\n"
      "v_pk_mul_f16 v141, v141, v157\n"
      "v_mfma_f32_16x16x32_f16 v[88:91], a[44:47], v[172:175], v[88:91]\n"
      "v_add_f32 v220, v220, v195\n"
      "v_add_f32 v188, v188, v198\n"
      "v_mfma_f32_16x16x32_f16 v[92:95], a[60:63], v[172:175], v[28:31]\n"
      "v_pk_mul_f16 v142, v142, v158\n"
      "v_fmac_f32 v221, v195, v233\n"
      "v_mfma_f32_16x16x32_f16 v[92:95], a[56:59], v[168:171], v[92:95]\n"
      "v_fmac_f32 v188, v200, v234\n"
      "v_pk_mul_f16 v143, v143, v159\n"
      "v_mfma_f32_16x16x32_f16 v[92:95], a[52:55], v[164:167], v[92:95]\n"
      "v_fma_f32 v199, |v84|, v52, v199\n"
      "v_fma_f32 v199, |v85|, v53, v199\n"
      "v_mfma_f32_16x16x32_f16 v[92:95], a[48:51], v[160:163], v[92:95]\n"
      "v_fma_f32 v199, |v86|, v54, v199\n"
      "v_fma_f32 v199, |v87|, v55, v199\n"
      "v_mfma_f32_16x16x32_f16 v[80:83], a[0:3], v[128:131], v[16:19]\n"
      "v_fma_f32 v199, |v88|, v56, v199\n"
      "v_fma_f32 v199, |v89|, v57, v199\n"
      "v_mfma_f32_16x16x32_f16 v[80:83], a[4:7], v[132:135], v[80:83]\n"
      "v_fma_f32 v199, |v90|, v58, v199\n"
      "v_fma_f32 v199, |v91|, v59, v199\n"
      "v_mfma_f32_16x16x32_f16 v[80:83], a[8:11], v[136:139], v[80:83]\n"
      "ds_read_b128 v[112:115], v216 offset:0\n"
      "ds_read_b128 v[144:147], v217 offset:0\n"
      "v_mfma_f32_16x16x32_f16 v[80:83], a[12:15], v[140:143], v[80:83]\n"
      "v_fma_f32 v199, |v92|, v60, v199\n"
      "v_fma_f32 v199, |v93|, v61, v199\n"
      "v_mfma_f32_16x16x32_f16 v[84:87], a[28:31], v[140:143], v[20:23]\n"
      "v_fma_f32 v199, |v94|, v62, v199\n"
      "v_fma_f32 v199, |v95|, v63, v199\n"
      "v_mfma_f32_16x16x32_f16 v[84:87], a[24:27], v[136:139], v[84:87]\n"
      "ds_write_b32 v1, v199 offset:12992\n"
      "v_mfma_f32_16x16x32_f16 v[84:87], a[20:23], v[132:135], v[84:87]\n"
      "ds_read_b128 v[116:119], v216 offset:64\n"
      "ds_read_b128 v[148:151], v217 offset:64\n"
      "v_mfma_f32_16x16x32_f16 v[84:87], a[16:19], v[128:131], v[84:87]\n"
      "v_mul_f32 v191, |v80|, v48\n"
      "v_fma_f32 v191, |v81|, v49, v191\n"
      "v_mfma_f32_16x16x32_f16 v[180:183], a[200:203], v[160:163], 0\n"
      "v_fma_f32 v191, |v82|, v50, v191\n"
      "v_fma_f32 v191, |v83|, v51, v191\n"
      "v_mfma_f32_16x16x32_f16 v[88:91], a[32:35], v[128:131], v[24:27]\n"
      "ds_read_b128 v[120:123], v216 offset:128\n"
      "ds_read_b128 v[152:155], v217 offset:128\n"
      "v_mfma_f32_16x16x32_f16 v[88:91], a[36:39], v[132:135], v[88:91]\n"
      "s_waitcnt lgkmcnt(5)\n"
      "v_pk_mul_f16 v112, v112, v144\n"
      "v_pk_mul_f16 v113, v113, v145\n"
      "v_mfma_f32_16x16x32_f16 v[88:91], a[40:43], v[136:139], v[88:91]\n"
      "v_pk_mul_f16 v114, v114, v146\n"
      "v_fma_f32 v191, |v84|, v52, v191\n"
      "v_mfma_f32_16x16x32_f16 v[88:91], a[44:47], v[140:143], v[88:91]\n"
      "v_fma_f32 v191, |v85|, v53, v191\n"
      "v_pk_mul_f16 v115, v115, v147\n"
      "v_mfma_f32_16x16x32_f16 v[92:95], a[60:63], v[140:143], v[28:31]\n"
      "v_fma_f32 v191, |v86|, v54, v191\n"
      "v_fma_f32 v191, |v87|, v55, v191\n"
      "v_mfma_f32_16x16x32_f16 v[92:95], a[56:59], v[136:139], v[92:95]\n"
      "s_waitcnt lgkmcnt(2)\n"
      "v_pk_mul_f16 v116, v116, v148\n"
      "v_pk_mul_f16 v117, v117, v149\n"
      "v_mfma_f32_16x16x32_f16 v[92:95], a[52:55], v[132:135], v[92:95]\n"
      "v_fma_f32 v191, |v88|, v56, v191\n"
      "v_fma_f32 v191, |v89|, v57, v191\n"
      "v_mfma_f32_16x16x32_f16 v[92:95], a[48:51], v[128:131], v[92:95]\n"
      "ds_read_b128 v[124:127], v216 offset:192\n"
      "v_pk_mul_f16 v118, v118, v150\n"
      "v_mfma_f32_16x16x32_f16 v[180:183], a[204:207], v[164:167], v[180:183]\n"
      "ds_read_b128 v[156:159], v217 offset:192\n"
      "v_fma_f32 v191, |v90|, v58, v191\n"
      "v_mfma_f32_16x16x32_f16 v[80:83], a[64:67], v[128:131], v[32:35]\n"
      "v_fma_f32 v191, |v91|, v59, v191\n"
      "v_pk_mul_f16 v119, v119, v151\n"
      "v_mfma_f32_16x16x32_f16 v[80:83], a[68:71], v[132:135], v[80:83]\n"
      "s_waitcnt lgkmcnt(2)\n"
      "v_pk_mul_f16 v120, v120, v152\n"
      "v_fma_f32 v191, |v92|, v60, v191\n"
      "v_mfma_f32_16x16x32_f16 v[80:83], a[72:75], v[136:139], v[80:83]\n"
      "v_fma_f32 v191, |v93|, v61, v191\n"
      "v_pk_mul_f16 v121, v121, v153\n"
      "v_mfma_f32_16x16x32_f16 v[80:83], a[76:79], v[140:143], v[80:83]\n"
      "v_fma_f32 v191, |v94|, v62, v191\n"
      "v_fma_f32 v191, |v95|, v63, v191\n"
      "v_mfma_f32_16x16x32_f16 v[84:87], a[92:95], v[140:143], v[36:39]\n"
      "v_pk_mul_f16 v122, v122, v154\n"
      "v_pk_mul_f16 v123, v123, v155\n"
      "v_mfma_f32_16x16x32_f16 v[84:87], a[88:91], v[136:139], v[84:87]\n"
      "ds_read_b32 v224, v12 offset:12992\n"
      "v_add_f32 v193, v193, v190\n"
      "v_mfma_f32_16x16x32_f16 v[84:87], a[84:87], v[132:135], v[84:87]\n"
      "v_fma_f32 v191, |v80|, v64, v191\n"
      "v_fma_f32 v191, |v81|, v65, v191\n"
      "v_mfma_f32_16x16x32_f16 v[84:87], a[80:83], v[128:131], v[84:87]\n"
      "v_permlane16_swap_b32 v193, v7\n"
      "v_mfma_f32_16x16x32_f16 v[88:91], a[96:99], v[128:131], v[40:43]\n"
      "s_waitcnt lgkmcnt(1)\n"
      "v_pk_mul_f16 v124, v124, v156\n"
      "v_fma_f32 v191, |v82|, v66, v191\n"
      "v_mfma_f32_16x16x32_f16 v[88:91], a[100:103], v[132:135], v[88:91]\n"
      "v_fma_f32 v191, |v83|, v67, v191\n"
      "v_pk_mul_f16 v125, v125, v157\n"
      "v_mfma_f32_16x16x32_f16 v[88:91], a[104:107], v[136:139], v[88:91]\n"
      "v_pk_mul_f16 v126, v126, v158\n"
      "v_pk_mul_f16 v127, v127, v159\n"
      "v_mfma_f32_16x16x32_f16 v[88:91], a[108:111], v[140:143], v[88:91]\n"
      "v_fma_f32 v191, |v84|, v68, v191\n"
      "v_fma_f32 v191, |v85|, v69, v191\n"
      "v_mfma_f32_16x16x32_f16 v[92:95], a[124:127], v[140:143], v[44:47]\n"
      "v_fma_f32 v191, |v86|, v70, v191\n"
      "v_fma_f32 v191, |v87|, v71, v191\n"
      "v_mfma_f32_16x16x32_f16 v[92:95], a[120:123], v[136:139], v[92:95]\n"
      "v_add_f32 v193, v193, v7\n"
      "v_fmac_f32 v193, v200, v236\n"
      "v_mfma_f32_16x16x32_f16 v[92:95], a[116:119], v[132:135], v[92:95]\n"
      "v_fma_f32 v191, |v88|, v72, v191\n"
      "v_fma_f32 v191, |v89|, v73, v191\n"
      "v_mfma_f32_16x16x32_f16 v[92:95], a[112:115], v[128:131], v[92:95]\n"
      "v_fma_f32 v191, |v90|, v74, v191\n"
      "v_fma_f32 v191, |v91|, v75, v191\n"
      "v_mfma_f32_16x16x32_f16 v[80:83], a[0:3], v[112:115], v[16:19]\n"
      "v_max3_f32 v222, v219, v188, v193\n"
      "v_min3_f32 v226, v226, v188, v193\n"
      "v_mfma_f32_16x16x32_f16 v[80:83], a[4:7], v[116:119], v[80:83]\n"
      "v_sub_f32 v219, v219, v222\n"
      "v_sub_f32 v188, v188, v222\n"
      "v_mfma_f32_16x16x32_f16 v[80:83], a[8:11], v[120:123], v[80:83]\n"
      "v_fma_f32 v191, |v92|, v76, v191\n"
      "v_fma_f32 v191, |v93|, v77, v191\n"
      "v_mfma_f32_16x16x32_f16 v[80:83], a[12:15], v[124:127], v[80:83]\n"
      "v_fma_f32 v191, |v94|, v78, v191\n"
      "v_fma_f32 v191, |v95|, v79, v191\n"
      "v_mfma_f32_16x16x32_f16 v[84:87], a[28:31], v[124:127], v[20:23]\n"
      "s_waitcnt lgkmcnt(0)\n"
      "v_sub_f32 v193, v193, v222\n"
      "v_cmp_eq_u32 vcc, s37, v224\n"
      "v_mfma_f32_16x16x32_f16 v[84:87], a[24:27], v[120:123], v[84:87]\n"
      "v_exp_f32 v219, v219\n"
      "v_mfma_f32_16x16x32_f16 v[84:87], a[20:23], v[116:119], v[84:87]\n"
      "s_or_b32 vcc_lo, vcc_lo, s36\n"
      "v_exp_f32 v188, v188\n"
      "v_mfma_f32_16x16x32_f16 v[84:87], a[16:19], v[112:115], v[84:87]\n"
      "s_cmp_lg_u64 vcc, 0\n"
      "v_exp_f32 v193, v193\n"
      "v_mfma_f32_16x16x32_f16 v[88:91], a[32:35], v[112:115], v[24:27]\n"
      "s_cbranch_scc1 82f\n"
      "83:\n"
      "v_mfma_f32_16x16x32_f16 v[88:91], a[36:39], v[116:119], v[88:91]\n"
      "v_mul_f32 v196, |v80|, v48\n"
      "v_fma_f32 v196, |v81|, v49, v196\n"
      "v_mfma_f32_16x16x32_f16 v[88:91], a[40:43], v[120:123], v[88:91]\n"
      "v_fma_f32 v196, |v82|, v50, v196\n"
      "v_fma_f32 v196, |v83|, v51, v196\n"
      "v_mfma_f32_16x16x32_f16 v[88:91], a[44:47], v[124:127], v[88:91]\n"
      "v_fma_f32 v196, |v84|, v52, v196\n"
      "v_fma_f32 v196, |v85|, v53, v196\n"
      "v_mfma_f32_16x16x32_f16 v[92:95], a[60:63], v[124:127], v[28:31]\n"
      "v_fma_f32 v196, |v86|, v54, v196\n"
      "v_fma_f32 v196, |v87|, v55, v196\n"
      "v_mfma_f32_16x16x32_f16 v[92:95], a[56:59], v[120:123], v[92:95]\n"
      "v_mul_f32 v221, v221, v219\n"
      "v_fma_f32 v220, v220, v219, v188\n"
      "v_mfma_f32_16x16x32_f16 v[92:95], a[52:55], v[116:119], v[92:95]\n"
      "v_fma_f32 v196, |v88|, v56, v196\n"
      "v_fma_f32 v196, |v89|, v57, v196\n"
      "v_mfma_f32_16x16x32_f16 v[92:95], a[48:51], v[112:115], v[92:95]\n"
      "v_fma_f32 v196, |v90|, v58, v196\n"
      "v_fma_f32 v196, |v91|, v59, v196\n"
      "v_mfma_f32_16x16x32_f16 v[80:83], a[64:67], v[112:115], v[32:35]\n"
      "v_fmac_f32 v221, v188, v235\n"
      "v_add_f32 v220, v220, v193\n"
      "v_mfma_f32_16x16x32_f16 v[80:83], a[68:71], v[116:119], v[80:83]\n"
      "v_fmac_f32 v221, v193, v237\n"
      "v_add_f32 v199, v199, v224\n"
      "v_mfma_f32_16x16x32_f16 v[80:83], a[72:75], v[120:123], v[80:83]\n"
      "v_fma_f32 v196, |v92|, v60, v196\n"
      "v_fma_f32 v196, |v93|, v61, v196\n"
      "v_mfma_f32_16x16x32_f16 v[80:83], a[76:79], v[124:127], v[80:83]\n"
      "v_fma_f32 v196, |v94|, v62, v196\n"
      "v_fma_f32 v196, |v95|, v63, v196\n"
      "v_mfma_f32_16x16x32_f16 v[84:87], a[92:95], v[124:127], v[36:39]\n"
      "v_fma_f32 v220, v226, 0, v220\n"
      "v_cndmask_b32 v199, v8, v199, s[100:101]\n"
      "v_mfma_f32_16x16x32_f16 v[84:87], a[88:91], v[120:123], v[84:87]\n"
      "v_mfma_f32_16x16x32_f16 v[84:87], a[84:87], v[116:119], v[84:87]\n"
      "s_nop 0\n"
      "v_fma_f32 v196, |v80|, v64, v196\n"
      "v_fma_f32 v196, |v81|, v65, v196\n"
      "v_mfma_f32_16x16x32_f16 v[84:87], a[80:83], v[112:115], v[84:87]\n"
      "v_fma_f32 v196, |v82|, v66, v196\n"
      "v_fma_f32 v196, |v83|, v67, v196\n"
      "v_mfma_f32_16x16x32_f16 v[88:91], a[96:99], v[112:115], v[40:43]\n"
      "v_permlane32_swap_b32 v199, v191\n"
      "v_mfma_f32_16x16x32_f16 v[88:91], a[100:103], v[116:119], v[88:91]\n"
      "v_add_f32 v199, v199, v191\n"
      "v_mfma_f32_16x16x32_f16 v[88:91], a[104:107], v[120:123], v[88:91]\n"
      "s_nop 0\n"
      "v_fma_f32 v196, |v84|, v68, v196\n"
      "v_fma_f32 v196, |v85|, v69, v196\n"
      "v_mfma_f32_16x16x32_f16 v[88:91], a[108:111], v[124:127], v[88:91]\n"
      "v_fma_f32 v196, |v86|, v70, v196\n"
      "v_fma_f32 v196, |v87|, v71, v196\n"
      "v_mfma_f32_16x16x32_f16 v[92:95], a[124:127], v[124:127], v[44:47]\n"
      "v_mfma_f32_16x16x32_f16 v[92:95], a[120:123], v[120:123], v[92:95]\n"
      "v_mfma_f32_16x16x32_f16 v[92:95], a[116:119], v[116:119], v[92:95]\n"
      "s_nop 2\n"
      "v_fma_f32 v196, |v88|, v72, v196\n"
      "v_fma_f32 v196, |v89|, v73, v196\n"
      "v_mfma_f32_16x16x32_f16 v[92:95], a[112:115], v[112:115], v[92:95]\n"
      "v_fma_f32 v196, |v90|, v74, v196\n"
      "v_fma_f32 v196, |v91|, v75, v196\n"
      "v_mfma_f32_16x16x32_f16 v[180:183], a[208:211], v[168:171], v[180:183]\n"
      "v_mfma_f32_16x16x32_f16 v[180:183], a[212:215], v[172:175], v[180:183]\n"
      "s_nop 3\n"
      "v_fma_f32 v196, |v92|, v76, v196\n"
      "v_fma_f32 v196, |v93|, v77, v196\n"
      "v_fma_f32 v196, |v94|, v78, v196\n"
      "v_fma_f32 v196, |v95|, v79, v196\n"
      "v_mov_b32_dpp v238, v180 quad_perm:[0,1,2,3] row_mask:0x1 bank_mask:0xf\n"
      "v_mov_b32_dpp v239, v181 quad_perm:[0,1,2,3] row_mask:0x1 bank_mask:0xf\n"
      "v_permlane32_swap_b32 v196, v8\n"
      "v_add_f32 v196, v196, v8\n"
      "s_movk_i32 s45, 0x207\n"
      "s_nop 0\n"
      "v_permlane16_swap_b32 v199, v196\n"
      "v_add_f32 v199, v199, v196\n"
      "v_fmac_f32 v199, v200, v238\n"
      "v_max_f32 v225, v222, v199\n"
      "v_fma_f32 v220, v199, 0, v220\n"
      "s_nop 0\n"
      "v_max_f32_dpp v223, v225, v225 row_ror:4 row_mask:0xf bank_mask:0xf\n"
      "v_max_f32_dpp v223, v225, v223 row_ror:8 row_mask:0xf bank_mask:0xf\n"
      "v_max_f32_dpp v223, v225, v223 row_ror:12 row_mask:0xf bank_mask:0xf\n"
      "s_nop 1\n"
      "v_max_f32_dpp v225, v223, v223 row_ror:1 row_mask:0xf bank_mask:0xf\n"
      "v_max_f32_dpp v225, v223, v225 row_ror:2 row_mask:0xf bank_mask:0xf\n"
      "v_max_f32_dpp v225, v223, v225 row_ror:3 row_mask:0xf bank_mask:0xf\n"
      "s_nop 1\n"
      "v_max_f32_dpp v225, v225, v225 row_bcast:15 row_mask:0xa bank_mask:0xf\n"
      "s_nop 1\n"
      "v_max_f32_dpp v225, v225, v225 row_bcast:31 row_mask:0xc bank_mask:0xf\n"
      "s_nop 0\n"
      "v_readlane_b32 s44, v225, 63\n"
      "s_nop 1\n"
      "v_subrev_f32 v223, s44, v222\n"
      "v_subrev_f32 v199, s44, v199\n"
      "v_exp_f32 v223, v223\n"
      "v_exp_f32 v199, v199\n"
      "v_mul_f32 v221, v221, v223\n"
      "v_fma_f32 v220, v220, v223, v199\n"
      "v_fmac_f32 v221, v199, v239\n"
      "s_nop 0\n"
      "v_add_f32_dpp v220, v220, v220 row_ror:8 row_mask:0xf bank_mask:0xf\n"
      "v_add_f32_dpp v221, v221, v221 row_ror:8 row_mask:0xf bank_mask:0xf\n"
      "s_nop 0\n"
      "v_add_f32_dpp v220, v220, v220 row_ror:4 row_mask:0xf bank_mask:0xf\n"
      "v_add_f32_dpp v221, v221, v221 row_ror:4 row_mask:0xf bank_mask:0xf\n"
      "s_nop 0\n"
      "v_add_f32_dpp v220, v220, v220 row_ror:2 row_mask:0xf bank_mask:0xf\n"
      "v_add_f32_dpp v221, v221, v221 row_ror:2 row_mask:0xf bank_mask:0xf\n"
      "s_nop 0\n"
      "v_add_f32_dpp v220, v220, v220 row_ror:1 row_mask:0xf bank_mask:0xf\n"
      "v_add_f32_dpp v221, v221, v221 row_ror:1 row_mask:0xf bank_mask:0xf\n"
      "s_nop 0\n"
      "v_add_f32_dpp v220, v220, v220 row_bcast:15 row_mask:0xa bank_mask:0xf\n"
      "v_add_f32_dpp v221, v221, v221 row_bcast:15 row_mask:0xa bank_mask:0xf\n"
      "s_nop 0\n"
      "v_add_f32_dpp v220, v220, v220 row_bcast:31 row_mask:0xc bank_mask:0xf\n"
      "v_add_f32_dpp v221, v221, v221 row_bcast:31 row_mask:0xc bank_mask:0xf\n"
      "s_mov_b32 exec_lo, 0\n"
      "v_rcp_f32 v220, v220\n"
      "v_ldexp_f32 v221, v221, s39\n"
      "s_mov_b32 exec_hi, 0x80000000\n"
      "v_mul_f32 v221, v221, v220\n"
      "v_cvt_pk_bf16_f32 v220, v221, v221\n"
      "global_store_short v3, v220, s[24:25]\n"
      "v_cmp_class_f32 vcc, v221, s45\n"
      "s_nop 0\n"
      "s_cmp_lt_i32 vcc_hi, 0\n"
      "s_cbranch_scc1 7f\n"
      "s_endpgm\n"
      "s_mov_b64 exec, -1\n"
      "s_branch 9f\n"
      "80:\n"
      "s_waitcnt vmcnt(0) lgkmcnt(0)\n"
      "ds_read_b32 v224, v227 offset:37640\n"
      "s_waitcnt lgkmcnt(0)\n"
      "v_cmp_eq_u32 vcc, 0, v224\n"
      "s_nop 4\n"
      "s_and_b32 s44, vcc_lo, s54\n"
      "s_cbranch_scc1 80b\n"
      "v_cmp_eq_u32 vcc, 1, v224\n"
      "s_nop 4\n"
      "s_and_b32 s44, vcc_lo, s54\n"
      "s_cmp_eq_u32 s44, s54\n"
      "s_cbranch_scc1 81b\n"
      "s_mov_b32 s36, 1\n"
      "s_add_u32 s44, s38, 1152\n"
      "v_cndmask_b32 v10, v201, v10, s[100:101]\n"
      "v_cndmask_b32 v11, v201, v11, s[100:101]\n"
      "v_mov_b32 v1, s44\n"
      "s_nop 1\n"
      "s_branch 81b\n"
      "82:\n"
      "s_cmp_lg_u32 s36, 0\n"
      "s_cbranch_scc1 84f\n"
      "85:\n"
      "ds_read_b32 v224, v12 offset:12992\n"
      "s_waitcnt lgkmcnt(0)\n"
      "v_cmp_eq_u32 vcc, s37, v224\n"
      "s_nop 4\n"
      "s_cmp_lg_u64 vcc, 0\n"
      "s_cbranch_scc1 85b\n"
      "s_branch 83b\n"
      "84:\n"
      "s_waitcnt lgkmcnt(0)\n"
      "v_mfma_f32_16x16x32_f16 v[240:243], a[64:67], v[160:163], v[32:35]\n"
      "v_mfma_f32_16x16x32_f16 v[240:243], a[68:71], v[164:167], v[240:243]\n"
      "v_mfma_f32_16x16x32_f16 v[240:243], a[72:75], v[168:171], v[240:243]\n"
      "v_mfma_f32_16x16x32_f16 v[240:243], a[76:79], v[172:175], v[240:243]\n"
      "v_mfma_f32_16x16x32_f16 v[244:247], a[80:83], v[160:163], v[36:39]\n"
      "v_mfma_f32_16x16x32_f16 v[244:247], a[84:87], v[164:167], v[244:247]\n"
      "v_mfma_f32_16x16x32_f16 v[244:247], a[88:91], v[168:171], v[244:247]\n"
      "v_mfma_f32_16x16x32_f16 v[244:247], a[92:95], v[172:175], v[244:247]\n"
      "v_mfma_f32_16x16x32_f16 v[248:251], a[96:99], v[160:163], v[40:43]\n"
      "v_mfma_f32_16x16x32_f16 v[248:251], a[100:103], v[164:167], v[248:251]\n"
      "v_mfma_f32_16x16x32_f16 v[248:251], a[104:107], v[168:171], v[248:251]\n"
      "v_mfma_f32_16x16x32_f16 v[248:251], a[108:111], v[172:175], v[248:251]\n"
      "v_mfma_f32_16x16x32_f16 v[252:255], a[112:115], v[160:163], v[44:47]\n"
      "v_mfma_f32_16x16x32_f16 v[252:255], a[116:119], v[164:167], v[252:255]\n"
      "v_mfma_f32_16x16x32_f16 v[252:255], a[120:123], v[168:171], v[252:255]\n"
      "v_mfma_f32_16x16x32_f16 v[252:255], a[124:127], v[172:175], v[252:255]\n"
      "s_nop 7\n"
      "v_mul_f32 v224, |v240|, v64\n"
      "v_fma_f32 v224, |v241|, v65, v224\n"
      "v_fma_f32 v224, |v242|, v66, v224\n"
      "v_fma_f32 v224, |v243|, v67, v224\n"
      "v_fma_f32 v224, |v244|, v68, v224\n"
      "v_fma_f32 v224, |v245|, v69, v224\n"
      "v_fma_f32 v224, |v246|, v70, v224\n"
      "v_fma_f32 v224, |v247|, v71, v224\n"
      "v_fma_f32 v224, |v248|, v72, v224\n"
      "v_fma_f32 v224, |v249|, v73, v224\n"
      "v_fma_f32 v224, |v250|, v74, v224\n"
      "v_fma_f32 v224, |v251|, v75, v224\n"
      "v_fma_f32 v224, |v252|, v76, v224\n"
      "v_fma_f32 v224, |v253|, v77, v224\n"
      "v_fma_f32 v224, |v254|, v78, v224\n"
      "v_fma_f32 v224, |v255|, v79, v224\n"
      "s_nop 1\n"
      "s_branch 83b\n"
      "5:\n"
      "s_waitcnt lgkmcnt(0)\n"
      "ds_read_b32 v224, v227 offset:37632\n"
      "s_waitcnt lgkmcnt(0)\n"
      "v_cmp_eq_u32 vcc, 0, v224\n"
      "s_nop 4\n"
      "s_cmp_lg_u32 vcc_lo, 0\n"
      "s_cbranch_scc1 5b\n"
      "v_cmp_gt_u32 vcc, 4, v224\n"
      "s_nop 4\n"
      "s_cmp_lg_u32 vcc_lo, 0\n"
      "s_cbranch_scc1 8f\n"
      "s_cmp_lg_u32 s58, 0\n"
      "s_cbranch_scc1 52f\n"
      "ds_read_b32 v226, v213 offset:12480\n"
      "v_cmp_eq_u32 s[44:45], 5, v224\n"
      "s_waitcnt lgkmcnt(0)\n"
      "v_max_f16_sdwa v226, |v226|, |v226| dst_sel:DWORD dst_unused:UNUSED_PAD src0_sel:WORD_0 src1_sel:WORD_1\n"
      "s_nop 1\n"
      "v_cmp_le_f16 vcc, 0x6400, v226\n"
      "s_nop 4\n"
      "s_or_b64 vcc, vcc, s[44:45]\n"
      "s_cbranch_scc0 52f\n"
      "v_and_b32 v224, 63, %3\n"
      "v_lshlrev_b32 v224, 9, v224\n"
      "global_load_dwordx4 v[176:179], v224, s[16:17]\n"
      "global_load_dwordx4 v[180:183], v224, s[16:17] offset:336\n"
      "s_waitcnt vmcnt(0)\n"
      "v_max3_f32 v224, |v176|, |v177|, |v178|\n"
      "v_max3_f32 v224, |v179|, |v180|, v224\n"
      "v_max3_f32 v224, |v181|, |v182|, v224\n"
      "v_max_f32 v224, |v183|, v224\n"
      "s_nop 1\n"
      "v_max_f32_dpp v224, v224, v224 row_ror:8 row_mask:0xf bank_mask:0xf\n"
      "s_nop 1\n"
      "v_max_f32_dpp v224, v224, v224 row_ror:4 row_mask:0xf bank_mask:0xf\n"
      "s_nop 1\n"
      "v_max_f32_dpp v224, v224, v224 row_ror:2 row_mask:0xf bank_mask:0xf\n"
      "s_nop 1\n"
      "v_max_f32_dpp v224, v224, v224 row_ror:1 row_mask:0xf bank_mask:0xf\n"
      "s_nop 0\n"
      "v_readlane_b32 s44, v224, 0\n"
      "v_readlane_b32 s45, v224, 16\n"
      "v_readlane_b32 s46, v224, 32\n"
      "v_readlane_b32 s47, v224, 48\n"
      "s_max_u32 s44, s44, s45\n"
      "s_max_u32 s46, s46, s47\n"
      "s_max_u32 s44, s44, s46\n"
      "s_lshr_b32 s44, s44, 23\n"
      "s_sub_i32 s58, 135, s44\n"
      "s_cmp_eq_u32 s44, 0\n"
      "s_cselect_b32 s58, 0, s58\n"
      "s_max_i32 s58, s58, 11\n"
      "s_cmp_gt_u32 s58, 51\n"
      "s_cbranch_scc1 8f\n"
      "s_mov_b32 s41, 1\n"
      "s_mov_b32 s35, 0\n"
      "s_waitcnt vmcnt(0) lgkmcnt(0)\n"
      "s_barrier\n"
      "s_branch 0b\n"
      "52:\n"
      "ds_read_b64 v[176:177], v5 offset:37120\n"
      "v_cmp_eq_u32 vcc, 9, v224\n"
      "v_cmp_eq_u32 s[50:51], 5, v224\n"
      "s_waitcnt lgkmcnt(0)\n"
      "v_max_f32 v225, |v176|, |v177|\n"
      "s_nop 1\n"
      "v_max_f32_dpp v225, v225, v225 row_ror:8 row_mask:0xf bank_mask:0xf\n"
      "s_nop 1\n"
      "v_max_f32_dpp v225, v225, v225 row_ror:4 row_mask:0xf bank_mask:0xf\n"
      "s_nop 1\n"
      "v_max_f32_dpp v225, v225, v225 row_ror:2 row_mask:0xf bank_mask:0xf\n"
      "s_nop 1\n"
      "v_max_f32_dpp v225, v225, v225 row_ror:1 row_mask:0xf bank_mask:0xf\n"
      "s_nop 0\n"
      "v_readlane_b32 s44, v225, 0\n"
      "v_readlane_b32 s45, v225, 16\n"
      "v_readlane_b32 s46, v225, 32\n"
      "v_readlane_b32 s47, v225, 48\n"
      "s_max_u32 s44, s44, s45\n"
      "s_max_u32 s46, s46, s47\n"
      "s_max_u32 s44, s44, s46\n"
      "v_frexp_exp_i32_f32 v224, s44\n"
      "v_sub_u32 v224, 0, v224\n"
      "s_or_b32 s30, s30, vcc_lo\n"
      "s_cmp_lg_u32 s55, 0\n"
      "s_cselect_b32 s30, 0, s30\n"
      "v_readfirstlane_b32 s51, v224\n"
      "s_cmp_lg_u32 s30, 0\n"
      "s_cbranch_scc1 6f\n"
      "v_ldexp_f32 v176, v176, v224\n"
      "v_ldexp_f32 v177, v177, v224\n"
      "v_readfirstlane_b32 s56, v224\n"
      "s_nop 1\n"
      "s_branch 4b\n"
      "22:\n"
      "s_nop 7\n"
      "v_lshlrev_b32 v176, 16, v192\n"
      "v_and_b32 v177, s43, v192\n"
      "v_lshlrev_b32 v178, 16, v193\n"
      "v_and_b32 v179, s43, v193\n"
      "v_lshlrev_b32 v180, 16, v194\n"
      "v_and_b32 v181, s43, v194\n"
      "v_lshlrev_b32 v182, 16, v195\n"
      "v_and_b32 v183, s43, v195\n"
      "v_max3_f32 v222, |v176|, |v177|, |v178|\n"
      "v_max3_f32 v222, |v179|, |v180|, v222\n"
      "v_max3_f32 v222, |v181|, |v182|, v222\n"
      "v_max_f32 v222, |v183|, v222\n"
      "s_nop 1\n"
      "v_max_f32_dpp v222, v222, v222 row_ror:8 row_mask:0xf bank_mask:0xf\n"
      "s_nop 1\n"
      "v_max_f32_dpp v222, v222, v222 row_ror:4 row_mask:0xf bank_mask:0xf\n"
      "s_nop 1\n"
      "v_max_f32_dpp v222, v222, v222 row_ror:2 row_mask:0xf bank_mask:0xf\n"
      "s_nop 1\n"
      "v_max_f32_dpp v222, v222, v222 row_ror:1 row_mask:0xf bank_mask:0xf\n"
      "v_frexp_exp_i32_f32 v90, v222\n"
      "v_sub_u32 v90, 0, v90\n"
      "v_ldexp_f32 v176, v176, v90\n"
      "v_ldexp_f32 v177, v177, v90\n"
      "v_ldexp_f32 v178, v178, v90\n"
      "v_ldexp_f32 v179, v179, v90\n"
      "v_ldexp_f32 v180, v180, v90\n"
      "v_ldexp_f32 v181, v181, v90\n"
      "v_ldexp_f32 v182, v182, v90\n"
      "v_ldexp_f32 v183, v183, v90\n"
      "v_ldexp_f32 v176, v176, 9\n"
      "v_ldexp_f32 v177, v177, 9\n"
      "v_ldexp_f32 v178, v178, 9\n"
      "v_ldexp_f32 v179, v179, 9\n"
      "v_ldexp_f32 v180, v180, 9\n"
      "v_ldexp_f32 v181, v181, 9\n"
      "v_ldexp_f32 v182, v182, 9\n"
      "v_ldexp_f32 v183, v183, 9\n"
      "v_readlane_b32 s59, v90, 0\n"
      "v_readlane_b32 s57, v90, 32\n"
      "s_lshl_b64 exec, -1, 48\n"
      "v_xor_b32 v176, 0x80000000, v176\n"
      "v_xor_b32 v177, 0x80000000, v177\n"
      "v_xor_b32 v178, 0x80000000, v178\n"
      "v_xor_b32 v179, 0x80000000, v179\n"
      "v_xor_b32 v180, 0x80000000, v180\n"
      "v_xor_b32 v181, 0x80000000, v181\n"
      "v_xor_b32 v182, 0x80000000, v182\n"
      "v_xor_b32 v183, 0x80000000, v183\n"
      "s_mov_b64 exec, -1\n"
      "s_cmp_lt_i32 s57, 0\n"
      "s_cselect_b32 s41, 1, s41\n"
      "s_branch 23b\n"
      "70:\n"
      "s_mov_b32 s49, 0\n"
      "s_add_u32 s44, s58, 127\n"
      "s_lshl_b32 s44, s44, 23\n"
      "s_waitcnt vmcnt(0) lgkmcnt(0)\n"
      "s_nop 7\n"
      "v_lshlrev_b32 v128, 16, v96\n"
      "v_and_b32 v129, s43, v96\n"
      "v_mul_f32 v128, s44, v128\n"
      "v_mul_f32 v129, s44, v129\n"
      "v_cvt_pk_bf16_f32 v96, v128, v129\n"
      "v_lshlrev_b32 v128, 16, v97\n"
      "v_and_b32 v129, s43, v97\n"
      "v_mul_f32 v128, s44, v128\n"
      "v_mul_f32 v129, s44, v129\n"
      "v_cvt_pk_bf16_f32 v97, v128, v129\n"
      "v_lshlrev_b32 v128, 16, v98\n"
      "v_and_b32 v129, s43, v98\n"
      "v_mul_f32 v128, s44, v128\n"
      "v_mul_f32 v129, s44, v129\n"
      "v_cvt_pk_bf16_f32 v98, v128, v129\n"
      "v_lshlrev_b32 v128, 16, v99\n"
      "v_and_b32 v129, s43, v99\n"
      "v_mul_f32 v128, s44, v128\n"
      "v_mul_f32 v129, s44, v129\n"
      "v_cvt_pk_bf16_f32 v99, v128, v129\n"
      "v_lshlrev_b32 v128, 16, v100\n"
      "v_and_b32 v129, s43, v100\n"
      "v_mul_f32 v128, s44, v128\n"
      "v_mul_f32 v129, s44, v129\n"
      "v_cvt_pk_bf16_f32 v100, v128, v129\n"
      "v_lshlrev_b32 v128, 16, v101\n"
      "v_and_b32 v129, s43, v101\n"
      "v_mul_f32 v128, s44, v128\n"
      "v_mul_f32 v129, s44, v129\n"
      "v_cvt_pk_bf16_f32 v101, v128, v129\n"
      "v_lshlrev_b32 v128, 16, v102\n"
      "v_and_b32 v129, s43, v102\n"
      "v_mul_f32 v128, s44, v128\n"
      "v_mul_f32 v129, s44, v129\n"
      "v_cvt_pk_bf16_f32 v102, v128, v129\n"
      "v_lshlrev_b32 v128, 16, v103\n"
      "v_and_b32 v129, s43, v103\n"
      "v_mul_f32 v128, s44, v128\n"
      "v_mul_f32 v129, s44, v129\n"
      "v_cvt_pk_bf16_f32 v103, v128, v129\n"
      "v_lshlrev_b32 v128, 16, v104\n"
      "v_and_b32 v129, s43, v104\n"
      "v_mul_f32 v128, s44, v128\n"
      "v_mul_f32 v129, s44, v129\n"
      "v_cvt_pk_bf16_f32 v104, v128, v129\n"
      "v_lshlrev_b32 v128, 16, v105\n"
      "v_and_b32 v129, s43, v105\n"
      "v_mul_f32 v128, s44, v128\n"
      "v_mul_f32 v129, s44, v129\n"
      "v_cvt_pk_bf16_f32 v105, v128, v129\n"
      "v_lshlrev_b32 v128, 16, v106\n"
      "v_and_b32 v129, s43, v106\n"
      "v_mul_f32 v128, s44, v128\n"
      "v_mul_f32 v129, s44, v129\n"
      "v_cvt_pk_bf16_f32 v106, v128, v129\n"
      "v_lshlrev_b32 v128, 16, v107\n"
      "v_and_b32 v129, s43, v107\n"
      "v_mul_f32 v128, s44, v128\n"
      "v_mul_f32 v129, s44, v129\n"
      "v_cvt_pk_bf16_f32 v107, v128, v129\n"
      "v_lshlrev_b32 v128, 16, v108\n"
      "v_and_b32 v129, s43, v108\n"
      "v_mul_f32 v128, s44, v128\n"
      "v_mul_f32 v129, s44, v129\n"
      "v_cvt_pk_bf16_f32 v108, v128, v129\n"
      "v_lshlrev_b32 v128, 16, v109\n"
      "v_and_b32 v129, s43, v109\n"
      "v_mul_f32 v128, s44, v128\n"
      "v_mul_f32 v129, s44, v129\n"
      "v_cvt_pk_bf16_f32 v109, v128, v129\n"
      "v_lshlrev_b32 v128, 16, v110\n"
      "v_and_b32 v129, s43, v110\n"
      "v_mul_f32 v128, s44, v128\n"
      "v_mul_f32 v129, s44, v129\n"
      "v_cvt_pk_bf16_f32 v110, v128, v129\n"
      "v_lshlrev_b32 v128, 16, v111\n"
      "v_and_b32 v129, s43, v111\n"
      "v_mul_f32 v128, s44, v128\n"
      "v_mul_f32 v129, s44, v129\n"
      "v_cvt_pk_bf16_f32 v111, v128, v129\n"
      "v_lshlrev_b32 v128, 16, v112\n"
      "v_and_b32 v129, s43, v112\n"
      "v_mul_f32 v128, s44, v128\n"
      "v_mul_f32 v129, s44, v129\n"
      "v_cvt_pk_bf16_f32 v112, v128, v129\n"
      "v_lshlrev_b32 v128, 16, v113\n"
      "v_and_b32 v129, s43, v113\n"
      "v_mul_f32 v128, s44, v128\n"
      "v_mul_f32 v129, s44, v129\n"
      "v_cvt_pk_bf16_f32 v113, v128, v129\n"
      "v_lshlrev_b32 v128, 16, v114\n"
      "v_and_b32 v129, s43, v114\n"
      "v_mul_f32 v128, s44, v128\n"
      "v_mul_f32 v129, s44, v129\n"
      "v_cvt_pk_bf16_f32 v114, v128, v129\n"
      "v_lshlrev_b32 v128, 16, v115\n"
      "v_and_b32 v129, s43, v115\n"
      "v_mul_f32 v128, s44, v128\n"
      "v_mul_f32 v129, s44, v129\n"
      "v_cvt_pk_bf16_f32 v115, v128, v129\n"
      "v_lshlrev_b32 v128, 16, v116\n"
      "v_and_b32 v129, s43, v116\n"
      "v_mul_f32 v128, s44, v128\n"
      "v_mul_f32 v129, s44, v129\n"
      "v_cvt_pk_bf16_f32 v116, v128, v129\n"
      "v_lshlrev_b32 v128, 16, v117\n"
      "v_and_b32 v129, s43, v117\n"
      "v_mul_f32 v128, s44, v128\n"
      "v_mul_f32 v129, s44, v129\n"
      "v_cvt_pk_bf16_f32 v117, v128, v129\n"
      "v_lshlrev_b32 v128, 16, v118\n"
      "v_and_b32 v129, s43, v118\n"
      "v_mul_f32 v128, s44, v128\n"
      "v_mul_f32 v129, s44, v129\n"
      "v_cvt_pk_bf16_f32 v118, v128, v129\n"
      "v_lshlrev_b32 v128, 16, v119\n"
      "v_and_b32 v129, s43, v119\n"
      "v_mul_f32 v128, s44, v128\n"
      "v_mul_f32 v129, s44, v129\n"
      "v_cvt_pk_bf16_f32 v119, v128, v129\n"
      "v_lshlrev_b32 v128, 16, v120\n"
      "v_and_b32 v129, s43, v120\n"
      "v_mul_f32 v128, s44, v128\n"
      "v_mul_f32 v129, s44, v129\n"
      "v_cvt_pk_bf16_f32 v120, v128, v129\n"
      "v_lshlrev_b32 v128, 16, v121\n"
      "v_and_b32 v129, s43, v121\n"
      "v_mul_f32 v128, s44, v128\n"
      "v_mul_f32 v129, s44, v129\n"
      "v_cvt_pk_bf16_f32 v121, v128, v129\n"
      "v_lshlrev_b32 v128, 16, v122\n"
      "v_and_b32 v129, s43, v122\n"
      "v_mul_f32 v128, s44, v128\n"
      "v_mul_f32 v129, s44, v129\n"
      "v_cvt_pk_bf16_f32 v122, v128, v129\n"
      "v_lshlrev_b32 v128, 16, v123\n"
      "v_and_b32 v129, s43, v123\n"
      "v_mul_f32 v128, s44, v128\n"
      "v_mul_f32 v129, s44, v129\n"
      "v_cvt_pk_bf16_f32 v123, v128, v129\n"
      "v_lshlrev_b32 v128, 16, v124\n"
      "v_and_b32 v129, s43, v124\n"
      "v_mul_f32 v128, s44, v128\n"
      "v_mul_f32 v129, s44, v129\n"
      "v_cvt_pk_bf16_f32 v124, v128, v129\n"
      "v_lshlrev_b32 v128, 16, v125\n"
      "v_and_b32 v129, s43, v125\n"
      "v_mul_f32 v128, s44, v128\n"
      "v_mul_f32 v129, s44, v129\n"
      "v_cvt_pk_bf16_f32 v125, v128, v129\n"
      "v_lshlrev_b32 v128, 16, v126\n"
      "v_and_b32 v129, s43, v126\n"
      "v_mul_f32 v128, s44, v128\n"
      "v_mul_f32 v129, s44, v129\n"
      "v_cvt_pk_bf16_f32 v126, v128, v129\n"
      "v_lshlrev_b32 v128, 16, v127\n"
      "v_and_b32 v129, s43, v127\n"
      "v_mul_f32 v128, s44, v128\n"
      "v_mul_f32 v129, s44, v129\n"
      "v_cvt_pk_bf16_f32 v127, v128, v129\n"
      "s_nop 1\n"
      "s_branch 71b\n"
      "72:\n"
      "s_nop 7\n"
      "s_sub_u32 s44, 0, s58\n"
      "v_ldexp_f32 v16, v16, s58\n"
      "v_ldexp_f32 v48, v48, s44\n"
      "v_ldexp_f32 v17, v17, s58\n"
      "v_ldexp_f32 v49, v49, s44\n"
      "v_ldexp_f32 v18, v18, s58\n"
      "v_ldexp_f32 v50, v50, s44\n"
      "v_ldexp_f32 v19, v19, s58\n"
      "v_ldexp_f32 v51, v51, s44\n"
      "v_ldexp_f32 v20, v20, s58\n"
      "v_ldexp_f32 v52, v52, s44\n"
      "v_ldexp_f32 v21, v21, s58\n"
      "v_ldexp_f32 v53, v53, s44\n"
      "v_ldexp_f32 v22, v22, s58\n"
      "v_ldexp_f32 v54, v54, s44\n"
      "v_ldexp_f32 v23, v23, s58\n"
      "v_ldexp_f32 v55, v55, s44\n"
      "v_ldexp_f32 v24, v24, s58\n"
      "v_ldexp_f32 v56, v56, s44\n"
      "v_ldexp_f32 v25, v25, s58\n"
      "v_ldexp_f32 v57, v57, s44\n"
      "v_ldexp_f32 v26, v26, s58\n"
      "v_ldexp_f32 v58, v58, s44\n"
      "v_ldexp_f32 v27, v27, s58\n"
      "v_ldexp_f32 v59, v59, s44\n"
      "v_ldexp_f32 v28, v28, s58\n"
      "v_ldexp_f32 v60, v60, s44\n"
      "v_ldexp_f32 v29, v29, s58\n"
      "v_ldexp_f32 v61, v61, s44\n"
      "v_ldexp_f32 v30, v30, s58\n"
      "v_ldexp_f32 v62, v62, s44\n"
      "v_ldexp_f32 v31, v31, s58\n"
      "v_ldexp_f32 v63, v63, s44\n"
      "v_ldexp_f32 v32, v32, s58\n"
      "v_ldexp_f32 v64, v64, s44\n"
      "v_ldexp_f32 v33, v33, s58\n"
      "v_ldexp_f32 v65, v65, s44\n"
      "v_ldexp_f32 v34, v34, s58\n"
      "v_ldexp_f32 v66, v66, s44\n"
      "v_ldexp_f32 v35, v35, s58\n"
      "v_ldexp_f32 v67, v67, s44\n"
      "v_ldexp_f32 v36, v36, s58\n"
      "v_ldexp_f32 v68, v68, s44\n"
      "v_ldexp_f32 v37, v37, s58\n"
      "v_ldexp_f32 v69, v69, s44\n"
      "v_ldexp_f32 v38, v38, s58\n"
      "v_ldexp_f32 v70, v70, s44\n"
      "v_ldexp_f32 v39, v39, s58\n"
      "v_ldexp_f32 v71, v71, s44\n"
      "v_ldexp_f32 v40, v40, s58\n"
      "v_ldexp_f32 v72, v72, s44\n"
      "v_ldexp_f32 v41, v41, s58\n"
      "v_ldexp_f32 v73, v73, s44\n"
      "v_ldexp_f32 v42, v42, s58\n"
      "v_ldexp_f32 v74, v74, s44\n"
      "v_ldexp_f32 v43, v43, s58\n"
      "v_ldexp_f32 v75, v75, s44\n"
      "v_ldexp_f32 v44, v44, s58\n"
      "v_ldexp_f32 v76, v76, s44\n"
      "v_ldexp_f32 v45, v45, s58\n"
      "v_ldexp_f32 v77, v77, s44\n"
      "v_ldexp_f32 v46, v46, s58\n"
      "v_ldexp_f32 v78, v78, s44\n"
      "v_ldexp_f32 v47, v47, s58\n"
      "v_ldexp_f32 v79, v79, s44\n"
      "s_branch 73b\n"
      "6:\n"
      "s_waitcnt vmcnt(0) lgkmcnt(0)\n"
      "s_nop 7\n"
      "s_mov_b32 s55, 1\n"
      "s_mov_b32 s41, 1\n"
      "s_mov_b32 s45, 0x7fff7fff\n"
      "v_mov_b32 v226, 0\n"
      "v_and_b32 v176, s45, v228\n"
      "v_pk_max_u16 v226, v226, v176\n"
      "v_and_b32 v176, s45, v229\n"
      "v_pk_max_u16 v226, v226, v176\n"
      "v_and_b32 v176, s45, v230\n"
      "v_pk_max_u16 v226, v226, v176\n"
      "v_and_b32 v176, s45, v231\n"
      "v_pk_max_u16 v226, v226, v176\n"
      "v_and_b32 v176, s45, v232\n"
      "v_pk_max_u16 v226, v226, v176\n"
      "v_and_b32 v176, s45, v233\n"
      "v_pk_max_u16 v226, v226, v176\n"
      "v_and_b32 v176, s45, v234\n"
      "v_pk_max_u16 v226, v226, v176\n"
      "v_and_b32 v176, s45, v235\n"
      "v_pk_max_u16 v226, v226, v176\n"
      "v_and_b32 v176, s45, v236\n"
      "v_pk_max_u16 v226, v226, v176\n"
      "v_and_b32 v176, s45, v237\n"
      "v_pk_max_u16 v226, v226, v176\n"
      "v_and_b32 v176, s45, v238\n"
      "v_pk_max_u16 v226, v226, v176\n"
      "v_and_b32 v176, s45, v239\n"
      "v_pk_max_u16 v226, v226, v176\n"
      "v_and_b32 v176, s45, v240\n"
      "v_pk_max_u16 v226, v226, v176\n"
      "v_and_b32 v176, s45, v241\n"
      "v_pk_max_u16 v226, v226, v176\n"
      "v_and_b32 v176, s45, v242\n"
      "v_pk_max_u16 v226, v226, v176\n"
      "v_and_b32 v176, s45, v243\n"
      "v_pk_max_u16 v226, v226, v176\n"
      "v_and_b32 v176, s45, v244\n"
      "v_pk_max_u16 v226, v226, v176\n"
      "v_and_b32 v176, s45, v245\n"
      "v_pk_max_u16 v226, v226, v176\n"
      "v_and_b32 v176, s45, v246\n"
      "v_pk_max_u16 v226, v226, v176\n"
      "v_and_b32 v176, s45, v247\n"
      "v_pk_max_u16 v226, v226, v176\n"
      "v_and_b32 v176, s45, v248\n"
      "v_pk_max_u16 v226, v226, v176\n"
      "v_and_b32 v176, s45, v249\n"
      "v_pk_max_u16 v226, v226, v176\n"
      "v_and_b32 v176, s45, v250\n"
      "v_pk_max_u16 v226, v226, v176\n"
      "v_and_b32 v176, s45, v251\n"
      "v_pk_max_u16 v226, v226, v176\n"
      "v_and_b32 v176, s45, v252\n"
      "v_pk_max_u16 v226, v226, v176\n"
      "v_and_b32 v176, s45, v253\n"
      "v_pk_max_u16 v226, v226, v176\n"
      "v_and_b32 v176, s45, v254\n"
      "v_pk_max_u16 v226, v226, v176\n"
      "v_and_b32 v176, s45, v255\n"
      "v_pk_max_u16 v226, v226, v176\n"
      "v_lshlrev_b32 v176, 16, v226\n"
      "v_max_u32 v226, v226, v176\n"
      "s_nop 1\n"
      "v_max_u32_dpp v226, v226, v226 row_ror:8 row_mask:0xf bank_mask:0xf\n"
      "s_nop 1\n"
      "v_max_u32_dpp v226, v226, v226 row_ror:4 row_mask:0xf bank_mask:0xf\n"
      "s_nop 1\n"
      "v_max_u32_dpp v226, v226, v226 row_ror:2 row_mask:0xf bank_mask:0xf\n"
      "s_nop 1\n"
      "v_max_u32_dpp v226, v226, v226 row_ror:1 row_mask:0xf bank_mask:0xf\n"
      "s_nop 0\n"
      "v_readlane_b32 s44, v226, 0\n"
      "v_readlane_b32 s45, v226, 16\n"
      "v_readlane_b32 s46, v226, 32\n"
      "v_readlane_b32 s47, v226, 48\n"
      "s_max_u32 s44, s44, s45\n"
      "s_max_u32 s46, s46, s47\n"
      "s_max_u32 s44, s44, s46\n"
      "s_lshr_b32 s44, s44, 23\n"
      "s_sub_i32 s40, 137, s44\n"
      "s_cmp_eq_u32 s44, 0\n"
      "s_cselect_b32 s40, 0, s40\n"
      "s_add_i32 s45, s40, 33\n"
      "s_cmp_gt_u32 s45, 80\n"
      "s_cbranch_scc1 8f\n"
      "s_lshl1_add_u32 s44, s40, s58\n"
      "s_add_i32 s44, s44, s57\n"
      "s_add_i32 s45, s44, s51\n"
      "s_cmp_lt_i32 s45, 1\n"
      "s_cbranch_scc1 8f\n"
      "s_cmp_lg_u32 s50, 0\n"
      "s_cselect_b32 s44, s44, 64\n"
      "s_cmp_lt_i32 s44, 24\n"
      "s_cbranch_scc1 8f\n"
      "s_add_u32 s39, s40, 120\n"
      "s_lshl_b32 s39, s39, 23\n"
      "s_lshl_b32 s44, s40, 1\n"
      "s_sub_u32 s45, 127, s44\n"
      "s_add_u32 s44, s44, 127\n"
      "s_lshl_b32 s44, s44, 23\n"
      "s_lshl_b32 s45, s45, 23\n"
      "v_mul_f32 v16, s44, v16\n"
      "v_mul_f32 v48, s45, v48\n"
      "v_mul_f32 v17, s44, v17\n"
      "v_mul_f32 v49, s45, v49\n"
      "v_mul_f32 v18, s44, v18\n"
      "v_mul_f32 v50, s45, v50\n"
      "v_mul_f32 v19, s44, v19\n"
      "v_mul_f32 v51, s45, v51\n"
      "v_mul_f32 v20, s44, v20\n"
      "v_mul_f32 v52, s45, v52\n"
      "v_mul_f32 v21, s44, v21\n"
      "v_mul_f32 v53, s45, v53\n"
      "v_mul_f32 v22, s44, v22\n"
      "v_mul_f32 v54, s45, v54\n"
      "v_mul_f32 v23, s44, v23\n"
      "v_mul_f32 v55, s45, v55\n"
      "v_mul_f32 v24, s44, v24\n"
      "v_mul_f32 v56, s45, v56\n"
      "v_mul_f32 v25, s44, v25\n"
      "v_mul_f32 v57, s45, v57\n"
      "v_mul_f32 v26, s44, v26\n"
      "v_mul_f32 v58, s45, v58\n"
      "v_mul_f32 v27, s44, v27\n"
      "v_mul_f32 v59, s45, v59\n"
      "v_mul_f32 v28, s44, v28\n"
      "v_mul_f32 v60, s45, v60\n"
      "v_mul_f32 v29, s44, v29\n"
      "v_mul_f32 v61, s45, v61\n"
      "v_mul_f32 v30, s44, v30\n"
      "v_mul_f32 v62, s45, v62\n"
      "v_mul_f32 v31, s44, v31\n"
      "v_mul_f32 v63, s45, v63\n"
      "v_mul_f32 v32, s44, v32\n"
      "v_mul_f32 v64, s45, v64\n"
      "v_mul_f32 v33, s44, v33\n"
      "v_mul_f32 v65, s45, v65\n"
      "v_mul_f32 v34, s44, v34\n"
      "v_mul_f32 v66, s45, v66\n"
      "v_mul_f32 v35, s44, v35\n"
      "v_mul_f32 v67, s45, v67\n"
      "v_mul_f32 v36, s44, v36\n"
      "v_mul_f32 v68, s45, v68\n"
      "v_mul_f32 v37, s44, v37\n"
      "v_mul_f32 v69, s45, v69\n"
      "v_mul_f32 v38, s44, v38\n"
      "v_mul_f32 v70, s45, v70\n"
      "v_mul_f32 v39, s44, v39\n"
      "v_mul_f32 v71, s45, v71\n"
      "v_mul_f32 v40, s44, v40\n"
      "v_mul_f32 v72, s45, v72\n"
      "v_mul_f32 v41, s44, v41\n"
      "v_mul_f32 v73, s45, v73\n"
      "v_mul_f32 v42, s44, v42\n"
      "v_mul_f32 v74, s45, v74\n"
      "v_mul_f32 v43, s44, v43\n"
      "v_mul_f32 v75, s45, v75\n"
      "v_mul_f32 v44, s44, v44\n"
      "v_mul_f32 v76, s45, v76\n"
      "v_mul_f32 v45, s44, v45\n"
      "v_mul_f32 v77, s45, v77\n"
      "v_mul_f32 v46, s44, v46\n"
      "v_mul_f32 v78, s45, v78\n"
      "v_mul_f32 v47, s44, v47\n"
      "v_mul_f32 v79, s45, v79\n"
      "v_lshlrev_b32 v176, 16, v228\n"
      "v_and_b32 v177, s43, v228\n"
      "v_lshlrev_b32 v178, 16, v229\n"
      "v_and_b32 v179, s43, v229\n"
      "v_lshlrev_b32 v180, 16, v230\n"
      "v_and_b32 v181, s43, v230\n"
      "v_lshlrev_b32 v182, 16, v231\n"
      "v_and_b32 v183, s43, v231\n"
      "v_mul_f32 v176, s39, v176\n"
      "v_mul_f32 v177, s39, v177\n"
      "v_mul_f32 v178, s39, v178\n"
      "v_mul_f32 v179, s39, v179\n"
      "v_mul_f32 v180, s39, v180\n"
      "v_mul_f32 v181, s39, v181\n"
      "v_mul_f32 v182, s39, v182\n"
      "v_mul_f32 v183, s39, v183\n"
      "v_cvt_pk_f16_f32 v184, v176, v177\n"
      "v_cvt_pk_f16_f32 v185, v178, v179\n"
      "v_cvt_pk_f16_f32 v186, v180, v181\n"
      "v_cvt_pk_f16_f32 v187, v182, v183\n"
      "ds_write_b128 v1, v[184:187] offset:0\n"
      "v_lshlrev_b32 v176, 16, v232\n"
      "v_and_b32 v177, s43, v232\n"
      "v_lshlrev_b32 v178, 16, v233\n"
      "v_and_b32 v179, s43, v233\n"
      "v_lshlrev_b32 v180, 16, v234\n"
      "v_and_b32 v181, s43, v234\n"
      "v_lshlrev_b32 v182, 16, v235\n"
      "v_and_b32 v183, s43, v235\n"
      "v_mul_f32 v176, s39, v176\n"
      "v_mul_f32 v177, s39, v177\n"
      "v_mul_f32 v178, s39, v178\n"
      "v_mul_f32 v179, s39, v179\n"
      "v_mul_f32 v180, s39, v180\n"
      "v_mul_f32 v181, s39, v181\n"
      "v_mul_f32 v182, s39, v182\n"
      "v_mul_f32 v183, s39, v183\n"
      "v_cvt_pk_f16_f32 v184, v176, v177\n"
      "v_cvt_pk_f16_f32 v185, v178, v179\n"
      "v_cvt_pk_f16_f32 v186, v180, v181\n"
      "v_cvt_pk_f16_f32 v187, v182, v183\n"
      "ds_write_b128 v1, v[184:187] offset:1152\n"
      "v_lshlrev_b32 v176, 16, v236\n"
      "v_and_b32 v177, s43, v236\n"
      "v_lshlrev_b32 v178, 16, v237\n"
      "v_and_b32 v179, s43, v237\n"
      "v_lshlrev_b32 v180, 16, v238\n"
      "v_and_b32 v181, s43, v238\n"
      "v_lshlrev_b32 v182, 16, v239\n"
      "v_and_b32 v183, s43, v239\n"
      "v_mul_f32 v176, s39, v176\n"
      "v_mul_f32 v177, s39, v177\n"
      "v_mul_f32 v178, s39, v178\n"
      "v_mul_f32 v179, s39, v179\n"
      "v_mul_f32 v180, s39, v180\n"
      "v_mul_f32 v181, s39, v181\n"
      "v_mul_f32 v182, s39, v182\n"
      "v_mul_f32 v183, s39, v183\n"
      "v_cvt_pk_f16_f32 v184, v176, v177\n"
      "v_cvt_pk_f16_f32 v185, v178, v179\n"
      "v_cvt_pk_f16_f32 v186, v180, v181\n"
      "v_cvt_pk_f16_f32 v187, v182, v183\n"
      "ds_write_b128 v1, v[184:187] offset:2304\n"
      "v_lshlrev_b32 v176, 16, v240\n"
      "v_and_b32 v177, s43, v240\n"
      "v_lshlrev_b32 v178, 16, v241\n"
      "v_and_b32 v179, s43, v241\n"
      "v_lshlrev_b32 v180, 16, v242\n"
      "v_and_b32 v181, s43, v242\n"
      "v_lshlrev_b32 v182, 16, v243\n"
      "v_and_b32 v183, s43, v243\n"
      "v_mul_f32 v176, s39, v176\n"
      "v_mul_f32 v177, s39, v177\n"
      "v_mul_f32 v178, s39, v178\n"
      "v_mul_f32 v179, s39, v179\n"
      "v_mul_f32 v180, s39, v180\n"
      "v_mul_f32 v181, s39, v181\n"
      "v_mul_f32 v182, s39, v182\n"
      "v_mul_f32 v183, s39, v183\n"
      "v_cvt_pk_f16_f32 v184, v176, v177\n"
      "v_cvt_pk_f16_f32 v185, v178, v179\n"
      "v_cvt_pk_f16_f32 v186, v180, v181\n"
      "v_cvt_pk_f16_f32 v187, v182, v183\n"
      "ds_write_b128 v1, v[184:187] offset:3456\n"
      "v_lshlrev_b32 v176, 16, v244\n"
      "v_and_b32 v177, s43, v244\n"
      "v_lshlrev_b32 v178, 16, v245\n"
      "v_and_b32 v179, s43, v245\n"
      "v_lshlrev_b32 v180, 16, v246\n"
      "v_and_b32 v181, s43, v246\n"
      "v_lshlrev_b32 v182, 16, v247\n"
      "v_and_b32 v183, s43, v247\n"
      "v_mul_f32 v176, s39, v176\n"
      "v_mul_f32 v177, s39, v177\n"
      "v_mul_f32 v178, s39, v178\n"
      "v_mul_f32 v179, s39, v179\n"
      "v_mul_f32 v180, s39, v180\n"
      "v_mul_f32 v181, s39, v181\n"
      "v_mul_f32 v182, s39, v182\n"
      "v_mul_f32 v183, s39, v183\n"
      "v_cvt_pk_f16_f32 v184, v176, v177\n"
      "v_cvt_pk_f16_f32 v185, v178, v179\n"
      "v_cvt_pk_f16_f32 v186, v180, v181\n"
      "v_cvt_pk_f16_f32 v187, v182, v183\n"
      "ds_write_b128 v1, v[184:187] offset:4608\n"
      "v_lshlrev_b32 v176, 16, v248\n"
      "v_and_b32 v177, s43, v248\n"
      "v_lshlrev_b32 v178, 16, v249\n"
      "v_and_b32 v179, s43, v249\n"
      "v_lshlrev_b32 v180, 16, v250\n"
      "v_and_b32 v181, s43, v250\n"
      "v_lshlrev_b32 v182, 16, v251\n"
      "v_and_b32 v183, s43, v251\n"
      "v_mul_f32 v176, s39, v176\n"
      "v_mul_f32 v177, s39, v177\n"
      "v_mul_f32 v178, s39, v178\n"
      "v_mul_f32 v179, s39, v179\n"
      "v_mul_f32 v180, s39, v180\n"
      "v_mul_f32 v181, s39, v181\n"
      "v_mul_f32 v182, s39, v182\n"
      "v_mul_f32 v183, s39, v183\n"
      "v_cvt_pk_f16_f32 v184, v176, v177\n"
      "v_cvt_pk_f16_f32 v185, v178, v179\n"
      "v_cvt_pk_f16_f32 v186, v180, v181\n"
      "v_cvt_pk_f16_f32 v187, v182, v183\n"
      "ds_write_b128 v1, v[184:187] offset:5760\n"
      "v_lshlrev_b32 v176, 16, v252\n"
      "v_and_b32 v177, s43, v252\n"
      "v_lshlrev_b32 v178, 16, v253\n"
      "v_and_b32 v179, s43, v253\n"
      "v_lshlrev_b32 v180, 16, v254\n"
      "v_and_b32 v181, s43, v254\n"
      "v_lshlrev_b32 v182, 16, v255\n"
      "v_and_b32 v183, s43, v255\n"
      "v_mul_f32 v176, s39, v176\n"
      "v_mul_f32 v177, s39, v177\n"
      "v_mul_f32 v178, s39, v178\n"
      "v_mul_f32 v179, s39, v179\n"
      "v_mul_f32 v180, s39, v180\n"
      "v_mul_f32 v181, s39, v181\n"
      "v_mul_f32 v182, s39, v182\n"
      "v_mul_f32 v183, s39, v183\n"
      "v_cvt_pk_f16_f32 v184, v176, v177\n"
      "v_cvt_pk_f16_f32 v185, v178, v179\n"
      "v_cvt_pk_f16_f32 v186, v180, v181\n"
      "v_cvt_pk_f16_f32 v187, v182, v183\n"
      "ds_write_b128 v1, v[184:187] offset:6912\n"
      "v_mfma_f32_16x16x32_f16 v[192:195], a[184:187], a[152:155], 0\n"
      "v_mfma_f32_16x16x32_f16 v[192:195], a[188:191], a[156:159], v[192:195]\n"
      "v_mfma_f32_16x16x32_f16 v[192:195], a[192:195], a[160:163], v[192:195]\n"
      "v_mfma_f32_16x16x32_f16 v[192:195], a[196:199], a[164:167], v[192:195]\n"
      "v_mfma_f32_16x16x32_f16 v[252:255], a[184:187], a[168:171], 0\n"
      "v_mfma_f32_16x16x32_f16 v[252:255], a[188:191], a[172:175], v[252:255]\n"
      "v_mfma_f32_16x16x32_f16 v[252:255], a[192:195], a[176:179], v[252:255]\n"
      "v_mfma_f32_16x16x32_f16 v[252:255], a[196:199], a[180:183], v[252:255]\n"
      "s_sub_u32 s44, 96512, s38\n"
      "s_mov_b32 s98, 0\n"
      "v_add_u32 v1, s44, v1\n"
      "s_waitcnt lgkmcnt(0)\n"
      "ds_read_b128 v[128:131], v201 offset:0\n"
      "ds_read_b128 v[160:163], v202 offset:0\n"
      "ds_read_b128 v[132:135], v201 offset:64\n"
      "ds_read_b128 v[164:167], v202 offset:64\n"
      "ds_read_b128 v[136:139], v201 offset:128\n"
      "ds_read_b128 v[168:171], v202 offset:128\n"
      "ds_read_b128 v[140:143], v201 offset:192\n"
      "ds_read_b128 v[172:175], v202 offset:192\n"
      "s_waitcnt lgkmcnt(0)\n"
      "v_pk_mul_f16 v96, v128, v160\n"
      "v_pk_mul_f16 v97, v129, v161\n"
      "v_pk_mul_f16 v98, v130, v162\n"
      "v_pk_mul_f16 v99, v131, v163\n"
      "v_pk_mul_f16 v100, v132, v164\n"
      "v_pk_mul_f16 v101, v133, v165\n"
      "v_pk_mul_f16 v102, v134, v166\n"
      "v_pk_mul_f16 v103, v135, v167\n"
      "v_pk_mul_f16 v104, v136, v168\n"
      "v_pk_mul_f16 v105, v137, v169\n"
      "v_pk_mul_f16 v106, v138, v170\n"
      "v_pk_mul_f16 v107, v139, v171\n"
      "s_nop 1\n"
      "s_branch 3b\n"
      "7:\n"
      "s_mov_b64 exec, -1\n"
      "s_cmp_lg_u32 s41, 0\n"
      "s_cbranch_scc1 8f\n"
      "s_mov_b32 s41, 1\n"
      "s_waitcnt vmcnt(0)\n"
      "s_branch 0b\n"
      "8:\n"
      "v_mov_b32 v222, 2\n"
      "s_mov_b32 s42, 1\n"
      "ds_write_b32 v2, v222 offset:8\n"
      "9:\n"
      "s_nop 7\n"
      "s_waitcnt vmcnt(0) lgkmcnt(0)\n"
      "s_mov_b32 %0, s42\n"
      : "=s"(flag)
      : "s"(__builtin_amdgcn_kernarg_segment_ptr()), "s"(blockIdx.x), "v"(threadIdx.x), "s"(lbase)
      :
      "v1", "v2", "v3", "v4", "v5", "v6", "v7", "v8", "v9", "v10", "v11", "v12", "v13", "v14", "v15", "v16", "v17", "v18", "v19", "v20", "v21", "v22", "v23", "v24",
      "v25", "v26", "v27", "v28", "v29", "v30", "v31", "v32", "v33", "v34", "v35", "v36", "v37", "v38", "v39", "v40", "v41", "v42", "v43", "v44", "v45", "v46", "v47", "v48",
      "v49", "v50", "v51", "v52", "v53", "v54", "v55", "v56", "v57", "v58", "v59", "v60", "v61", "v62", "v63", "v64", "v65", "v66", "v67", "v68", "v69", "v70", "v71", "v72",
      "v73", "v74", "v75", "v76", "v77", "v78", "v79", "v80", "v81", "v82", "v83", "v84", "v85", "v86", "v87", "v88", "v89", "v90", "v91", "v92", "v93", "v94", "v95", "v96",
      "v97", "v98", "v99", "v100", "v101", "v102", "v103", "v104", "v105", "v106", "v107", "v108", "v109", "v110", "v111", "v112", "v113", "v114", "v115", "v116", "v117", "v118", "v119", "v120",
      "v121", "v122", "v123", "v124", "v125", "v126", "v127", "v128", "v129", "v130", "v131", "v132", "v133", "v134", "v135", "v136", "v137", "v138", "v139", "v140", "v141", "v142", "v143", "v144",
      "v145", "v146", "v147", "v148", "v149", "v150", "v151", "v152", "v153", "v154", "v155", "v156", "v157", "v158", "v159", "v160", "v161", "v162", "v163", "v164", "v165", "v166", "v167", "v168",
      "v169", "v170", "v171", "v172", "v173", "v174", "v175", "v176", "v177", "v178", "v179", "v180", "v181", "v182", "v183", "v184", "v185", "v186", "v187", "v188", "v189", "v190", "v191", "v192",
      "v193", "v194", "v195", "v196", "v197", "v198", "v199", "v200", "v201", "v202", "v203", "v204", "v205", "v206", "v207", "v208", "v209", "v210", "v211", "v212", "v213", "v214", "v215", "v216",
      "v217", "v218", "v219", "v220", "v221", "v222", "v223", "v224", "v225", "v226", "v227", "v228", "v229", "v230", "v231", "v232", "v233", "v234", "v235", "v236", "v237", "v238", "v239", "v240",
      "v241", "v242", "v243", "v244", "v245", "v246", "v247", "v248", "v249", "v250", "v251", "v252", "v253", "v254", "v255", "a0", "a1", "a2", "a3", "a4", "a5", "a6", "a7", "a8",
      "a9", "a10", "a11", "a12", "a13", "a14", "a15", "a16", "a17", "a18", "a19", "a20", "a21", "a22", "a23", "a24", "a25", "a26", "a27", "a28", "a29", "a30", "a31", "a32",
      "a33", "a34", "a35", "a36", "a37", "a38", "a39", "a40", "a41", "a42", "a43", "a44", "a45", "a46", "a47", "a48", "a49", "a50", "a51", "a52", "a53", "a54", "a55", "a56",
      "a57", "a58", "a59", "a60", "a61", "a62", "a63", "a64", "a65", "a66", "a67", "a68", "a69", "a70", "a71", "a72", "a73", "a74", "a75", "a76", "a77", "a78", "a79", "a80",
      "a81", "a82", "a83", "a84", "a85", "a86", "a87", "a88", "a89", "a90", "a91", "a92", "a93", "a94", "a95", "a96", "a97", "a98", "a99", "a100", "a101", "a102", "a103", "a104",
      "a105", "a106", "a107", "a108", "a109", "a110", "a111", "a112", "a113", "a114", "a115", "a116", "a117", "a118", "a119", "a120", "a121", "a122", "a123", "a124", "a125", "a126", "a127", "a128",
      "a129", "a130", "a131", "a132", "a133", "a134", "a135", "a136", "a137", "a138", "a139", "a140", "a141", "a142", "a143", "a144", "a145", "a146", "a147", "a148", "a149", "a150", "a151", "a152",
      "a153", "a154", "a155", "a156", "a157", "a158", "a159", "a160", "a161", "a162", "a163", "a164", "a165", "a166", "a167", "a168", "a169", "a170", "a171", "a172", "a173", "a174", "a175", "a176",
      "a177", "a178", "a179", "a180", "a181", "a182", "a183", "a184", "a185", "a186", "a187", "a188", "a189", "a190", "a191", "a192", "a193", "a194", "a195", "a196", "a197", "a198", "a199", "a200",
      "a201", "a202", "a203", "a204", "a205", "a206", "a207", "a208", "a209", "a210", "a211", "a212", "a213", "a214", "a215", "a216", "a217", "s8", "s9", "s10", "s11", "s12", "s13", "s14",
      "s15", "s16", "s17", "s18", "s19", "s20", "s21", "s22", "s23", "s24", "s25", "s26", "s27", "s28", "s29", "s30", "s31", "s33", "s34", "s35", "s36", "s37", "s38", "s39",
      "s40", "s41", "s42", "s43", "s44", "s45", "s46", "s47", "s48", "s49", "s50", "s51", "s52", "s53", "s54", "s55", "s56", "s57", "s58", "s59", "s60", "s61", "s62", "s63",
      "s64", "s65", "s66", "s67", "s68", "s69", "s70", "s71", "s72", "s73", "s74", "s75", "s76", "s77", "s78", "s79", "s80", "s81", "s82", "s83", "s84", "s85", "s86", "s87",
      "s88", "s89", "s90", "s91", "s92", "s93", "s94", "s95", "s96", "s97", "s98", "s99", "s100", "s101", "vcc", "scc", "memory");
  if (flag | lbase) {
    const int F = 26, P = 325;
    const int lane = threadIdx.x & 63;
    const int b = (int)(min((blockIdx.x & 7) * kx + ((blockIdx.x >> 3) * 4 + (threadIdx.x >> 6)) * 104u, lim) / 104u);
    {
      float m = -INFINITY, z = 0.f, n = 0.f;
      for (int p = lane; p < P; p += 64) {
        const unsigned short* ei = table + (size_t)idx[b * F + ii[p]] * 128;
        const unsigned short* ej = table + (size_t)idx[b * F + jj[p]] * 128;
        float e = 0.f, s = 0.f;
        for (int a = 0; a < 128; ++a) {
          float acc = 0.f;
          for (int d = 0; d < 128; ++d) acc = fmaf(rbf(bf2f(ei[d]) * bf2f(ej[d])), bf2f(W[d * 128 + a]), acc);
          float x = acc + bf2f(bvec[a]);
          x = 0.5f * (x + fabsf(x));
          e = fmaf(x, bf2f(hvec[a]), e);
        }
        for (int d = 0; d < 128; ++d) s = fmaf(rbf(bf2f(ei[d]) * bf2f(ej[d])), bf2f(pvec[d]), s);
        float mn = fmaxf(m, e);
        float fa = __expf(m - mn), fb = __expf(e - mn);
        z = z * fa + fb; n = n * fa + fb * s; m = mn;
      }
      float mx = m;
      for (int o = 1; o < 64; o <<= 1) mx = fmaxf(mx, __shfl_xor(mx, o));
      float f = (m == -INFINITY) ? 0.f : __expf(m - mx);
      z *= f; n *= f;
      for (int o = 1; o < 64; o <<= 1) { z += __shfl_xor(z, o); n += __shfl_xor(n, o); }
      if (lane == 0) out[b] = f2bf(n / z);
    }
  }
}

extern "C" __global__ __launch_bounds__(256) void afm_any(const int* __restrict__ idx, const unsigned short* __restrict__ table,
                                   const int* __restrict__ ii, const int* __restrict__ jj,
                                   const unsigned short* __restrict__ W, const unsigned short* __restrict__ bvec,
                                   const unsigned short* __restrict__ hvec, const unsigned short* __restrict__ pvec,
                                   unsigned short* __restrict__ out, float* __restrict__ ws,
                                   int B, int F, int P, int D, int A) {
  const int b = blockIdx.x;
  float* ebuf = ws + (size_t)b * 2 * P;
  float* sbuf = ebuf + P;
  for (int p = threadIdx.x; p < P; p += 256) {
    const unsigned short* ei = table + (size_t)idx[b * F + ii[p]] * D;
    const unsigned short* ej = table + (size_t)idx[b * F + jj[p]] * D;
    float e = 0.f, sacc = 0.f;
    for (int a = 0; a < A; ++a) {
      float acc = 0.f;
      for (int d = 0; d < D; ++d) acc = fmaf(rbf(bf2f(ei[d]) * bf2f(ej[d])), bf2f(W[(size_t)d * A + a]), acc);
      float x = acc + bf2f(bvec[a]);
      x = 0.5f * (x + fabsf(x));
      e = fmaf(x, bf2f(hvec[a]), e);
    }
    for (int d = 0; d < D; ++d) sacc = fmaf(rbf(bf2f(ei[d]) * bf2f(ej[d])), bf2f(pvec[d]), sacc);
    ebuf[p] = e; sbuf[p] = sacc;
  }
  __syncthreads();
  if (threadIdx.x == 0) {
    float mx = -INFINITY;
    for (int p = 0; p < P; ++p) mx = fmaxf(mx, ebuf[p]);
    float z = 0.f, n = 0.f;
    for (int p = 0; p < P; ++p) { float w = __expf(ebuf[p] - mx); z += w; n += w * sbuf[p]; }
    out[b] = f2bf(n / z);
  }
}

extern "C" void kernel_launch(void* const* d_in, const int* in_sizes, int n_in,
                              void* d_out, int out_size, void* d_ws, size_t ws_size,
                              hipStream_t stream) {
  const int* idx = (const int*)d_in[0];
  const unsigned short* table = (const unsigned short*)d_in[1];
  const int* ii = (const int*)d_in[2];
  const int* jj = (const int*)d_in[3];
  const unsigned short* W = (const unsigned short*)d_in[4];
  const unsigned short* bvec = (const unsigned short*)d_in[5];
  const unsigned short* hvec = (const unsigned short*)d_in[6];
  const unsigned short* pvec = (const unsigned short*)d_in[7];
  unsigned short* out = (unsigned short*)d_out;
  const int B = out_size;
  const int P = in_sizes[2];
  const int D = in_sizes[7];
  const int A = in_sizes[5];
  if (B <= 0) return;
  const int F = in_sizes[0] / B;
  if (D == 128 && A == 128 && F == 26 && P == 325 && B < (1 << 24)) {
    const int g8 = (B + 31) / 32;
    afm_fast<<<8 * g8, 256, 0, stream>>>(idx, table, ii, jj, W, bvec, hvec, pvec, out, 104u * (unsigned)(B - 1), 416u * (unsigned)g8
        );
  } else {
    afm_any<<<B, 256, 0, stream>>>(idx, table, ii, jj, W, bvec, hvec, pvec, out, (float*)d_ws, B, F, P, D, A);
  }
}
```

```cpp
#include <hip/hip_runtime.h>
#include <hip/hip_bf16.h>
#include <stdint.h>

#define AFM_HAS_STAMPS 0

typedef float  f32x2  __attribute__((ext_vector_type(2)));
typedef __bf16 bf16x2 __attribute__((ext_vector_type(2)));

__device__ __forceinline__ float bf2f(unsigned short u) { return __builtin_bit_cast(float, (unsigned)u << 16); }
__device__ __forceinline__ unsigned short f2bf(float x) {
  f32x2 v = {x, 0.f}; bf16x2 r = __builtin_convertvector(v, bf16x2);
  return (unsigned short)(__builtin_bit_cast(unsigned, r) & 0xffffu);
}
__device__ __forceinline__ float rbf(float x) { return bf2f(f2bf(x)); }

extern "C" __device__ __attribute__((used, aligned(512))) const unsigned afm_lane_tab[804] = {
  738206208u, 533735168u, 697050480u, 864u, 743449092u, 539240196u, 719070580u, 20972672u, 748691976u, 539764488u, 554968440u, 1440u, 753934860u, 550250252u, 717235580u, 20973248u,
  759177744u, 555755280u, 613426560u, 2016u, 764420628u, 561260308u, 540550532u, 20971520u, 769663512u, 561784600u, 549463432u, 288u, 774906396u, 530327324u, 722740620u, 20972096u,
  738468432u, 538978128u, 618931888u, 3168u, 743711316u, 534521684u, 727983796u, 20974976u, 748954200u, 549988184u, 708585144u, 3744u, 754197084u, 555493212u, 723003068u, 20975552u,
  759439968u, 560998240u, 728508096u, 4320u, 764682852u, 556541796u, 624436932u, 20973824u, 769925736u, 530065256u, 702555848u, 2592u, 775168620u, 535570284u, 697575116u, 20974400u,
  738730656u, 701507488u, 697050480u, 1049456u, 743973540u, 707012516u, 719070580u, 22021264u, 749216424u, 707536808u, 554968440u, 1050032u, 754459308u, 718022572u, 717235580u, 22021840u,
  759702192u, 723527600u, 613426560u, 1050608u, 764945076u, 729032628u, 540550532u, 22020112u, 770187960u, 729556920u, 549463432u, 1048880u, 775430844u, 698099644u, 722740620u, 22020688u,
  738992880u, 706750448u, 618931888u, 1051760u, 744235764u, 702294004u, 727983796u, 22023568u, 749478648u, 717760504u, 708585144u, 1052336u, 754721532u, 723265532u, 723003068u, 22024144u,
  759964416u, 728770560u, 728508096u, 1052912u, 765207300u, 724314116u, 624436932u, 22022416u, 770450184u, 697837576u, 702555848u, 1051184u, 775693068u, 703342604u, 697575116u, 22022992u,
  739255104u, 617621568u, 529279600u, 2098048u, 744497988u, 623126596u, 549726836u, 23069856u, 749740872u, 623650888u, 555231864u, 2098624u, 754983756u, 634136652u, 560736892u, 23070432u,
  760226640u, 639641680u, 551299712u, 2099200u, 765469524u, 645146708u, 529803908u, 23068704u, 770712408u, 645671000u, 535308936u, 2097472u, 775955292u, 614213724u, 540813964u, 23069280u,
  739517328u, 622864528u, 613166000u, 2100352u, 744760212u, 618408084u, 633613236u, 23072160u, 750003096u, 633874584u, 639118264u, 2100928u, 755245980u, 639379612u, 644623292u, 23072736u,
  760488864u, 644884640u, 635186112u, 2101504u, 765731748u, 640428196u, 613690308u, 23071008u, 770974632u, 613951656u, 619195336u, 2099776u, 776217516u, 619456684u, 624700364u, 23071584u,
  739779552u, 544221408u, 529279600u, 3146640u, 745022436u, 544483556u, 549726836u, 24118448u, 750265320u, 544745704u, 555231864u, 3147216u, 755508204u, 545007852u, 560736892u, 24119024u,
  760751088u, 545270000u, 551299712u, 3147792u, 765993972u, 545532148u, 529803908u, 24117296u, 771236856u, 545794296u, 535308936u, 3146064u, 776479740u, 546056444u, 540813964u, 24117872u,
  740041776u, 565193008u, 613166000u, 3148944u, 745284660u, 565455156u, 633613236u, 24120752u, 750527544u, 565717304u, 639118264u, 3149520u, 755770428u, 565979452u, 644623292u, 24121328u,
  761013312u, 566241600u, 635186112u, 3150096u, 766256196u, 566503748u, 613690308u, 24119600u, 771499080u, 566765896u, 619195336u, 3148368u, 776741964u, 567028044u, 624700364u, 24120176u,
  358617600u, 8064u, 301992192u, 132121440u, 434116224u, 18882512u, 320866848u, 358619616u, 94372128u, 37756960u, 339741504u, 415241280u, 377491968u, 452993136u, 452991744u, 358619616u,
  150998400u, 75505856u, 377490816u, 452992032u, 37750464u, 94380304u, 396365472u, 207622368u, 75497472u, 113254752u, 415240128u, 207622368u, 396366624u, 471867824u, 471866400u, 415241280u,
  169873056u, 151003648u, 4608u, 1075854240u, 415240992u, 169878096u, 18879264u, 1283477760u, 434115648u, 188752544u, 37753920u, 1396724832u, 396366912u, 207626992u, 56628576u, 1283477760u,
  415241568u, 226501440u, 75503232u, 1377848736u, 188747712u, 245375888u, 94377888u, 1151355168u, 320870016u, 264250336u, 113252544u, 1151355168u, 301995936u, 283124784u, 132127200u, 1283477760u,
  359666192u, 1056656u, 303040784u, 133170032u, 435164816u, 19931104u, 321915440u, 359668208u, 95420720u, 38805552u, 340790096u, 416289872u, 378540560u, 454041728u, 454040336u, 359668208u,
  152046992u, 76554448u, 378539408u, 454040624u, 38799056u, 95428896u, 397414064u, 208670960u, 76546064u, 114303344u, 416288720u, 208670960u, 397415216u, 472916416u, 472914992u, 416289872u,
  170921648u, 152052240u, 1053200u, 1076902832u, 416289584u, 170926688u, 19927856u, 1284526352u, 435164240u, 189801136u, 38802512u, 1397773424u, 397415504u, 208675584u, 57677168u, 1284526352u,
  416290160u, 227550032u, 76551824u, 1378897328u, 189796304u, 246424480u, 95426480u, 1152403760u, 321918608u, 265298928u, 114301136u, 1152403760u, 303044528u, 284173376u, 133175792u, 1284526352u,
  360714784u, 2105248u, 304089376u, 134218624u, 436213408u, 20979696u, 322964032u, 360716800u, 96469312u, 39854144u, 341838688u, 417338464u, 379589152u, 455090320u, 455088928u, 360716800u,
  153095584u, 77603040u, 379588000u, 455089216u, 39847648u, 96477488u, 398462656u, 209719552u, 77594656u, 115351936u, 417337312u, 209719552u, 398463808u, 473965008u, 473963584u, 417338464u,
  171970240u, 153100832u, 2101792u, 1077951424u, 417338176u, 171975280u, 20976448u, 1285574944u, 436212832u, 190849728u, 39851104u, 1398822016u, 398464096u, 209724176u, 58725760u, 1285574944u,
  417338752u, 228598624u, 77600416u, 1379945920u, 190844896u, 247473072u, 96475072u, 1153452352u, 322967200u, 266347520u, 115349728u, 1153452352u, 304093120u, 285221968u, 134224384u, 1285574944u,
  361763376u, 3153840u, 305137968u, 135267216u, 437262000u, 22028288u, 324012624u, 361765392u, 97517904u, 40902736u, 342887280u, 418387056u, 380637744u, 456138912u, 456137520u, 361765392u,
  154144176u, 78651632u, 380636592u, 456137808u, 40896240u, 97526080u, 399511248u, 210768144u, 78643248u, 116400528u, 418385904u, 210768144u, 399512400u, 475013600u, 475012176u, 418387056u,
  173018832u, 154149424u, 3150384u, 1079000016u, 418386768u, 173023872u, 22025040u, 1286623536u, 437261424u, 191898320u, 40899696u, 1399870608u, 399512688u, 210772768u, 59774352u, 1286623536u,
  418387344u, 229647216u, 78649008u, 1380994512u, 191893488u, 248521664u, 97523664u, 1154500944u, 324015792u, 267396112u, 116398320u, 1154500944u, 305141712u, 286270560u, 135272976u, 1286623536u,
  0u, 18874368u, 524304u, 37748740u, 75497504u, 56623112u, 76021808u, 75497484u, 304087104u, 94371856u, 304611408u, 113246228u, 379584608u, 132120600u, 380108912u, 28u,
  608174208u, 37748864u, 608698512u, 56623236u, 683671712u, 75497608u, 684196016u, 94371980u, 912261312u, 113246352u, 912785616u, 132120724u, 987758816u, 152u, 988283120u, 18874524u,
  1053216u, 19923744u, 1577520u, 38798116u, 76550720u, 57672488u, 77075024u, 76546860u, 305140320u, 95421232u, 305664624u, 114295604u, 380637824u, 133169976u, 381162128u, 1049404u,
  609227424u, 38798240u, 609751728u, 57672612u, 684724928u, 76546984u, 685249232u, 95421356u, 913314528u, 114295728u, 913838832u, 133170100u, 988812032u, 1049528u, 989336336u, 19923900u,
  2106432u, 20972352u, 2630736u, 39846724u, 77603936u, 58721096u, 78128240u, 77595468u, 306193536u, 96469840u, 306717840u, 115344212u, 381691040u, 134218584u, 382215344u, 2098012u,
  610280640u, 39846848u, 610804944u, 58721220u, 685778144u, 77595592u, 686302448u, 96469964u, 914367744u, 115344336u, 914892048u, 134218708u, 989865248u, 2098136u, 990389552u, 20972508u,
  3159648u, 22020960u, 3683952u, 40895332u, 78657152u, 59769704u, 79181456u, 78644076u, 307246752u, 97518448u, 307771056u, 116392820u, 382744256u, 135267192u, 383268560u, 3146620u,
  611333856u, 40895456u, 611858160u, 59769828u, 686831360u, 78644200u, 687355664u, 97518572u, 915420960u, 116392944u, 915945264u, 135267316u, 990918464u, 3146744u, 991442768u, 22021116u,
  0u, 0u, 0u, 0u, 0u, 0u, 16843008u, 16843009u, 16843009u, 16843009u, 16843009u, 16843009u, 33686017u, 33686018u, 33686018u, 33686018u,
  33686018u, 33686018u, 50529027u, 50529027u, 50529027u, 50529027u, 50529027u, 67371779u, 67372036u, 67372036u, 67372036u, 67372036u, 84149252u, 84215045u, 84215045u, 84215045u,
  84215045u, 100992261u, 101058054u, 101058054u, 101058054u, 101058054u, 117900806u, 117901063u, 117901063u, 117901063u, 117901063u, 134744072u, 134744072u, 134744072u, 134744072u, 151587080u,
  151587081u, 151587081u, 151587081u, 168430089u, 168430090u, 168430090u, 168430090u, 185273099u, 185273099u, 185273099u, 202115851u, 202116108u, 202116108u, 218893324u, 218959117u, 218959117u,
  235736333u, 235802126u, 235802126u, 252644878u, 252645135u, 252645135u, 269488144u, 269488144u, 286331152u, 286331153u, 303174161u, 303174162u, 320017171u, 336859923u, 353637396u, 370480405u,
  387388950u, 404166422u, 67305985u, 134678021u, 202050057u, 269422093u, 336794129u, 404166165u, 67306009u, 134678021u, 202050057u, 269422093u, 336794129u, 404166165u, 84149017u, 151521030u,
  218893066u, 286265102u, 353637138u, 421009174u, 117835012u, 185207048u, 252579084u, 319951120u, 387323156u, 100997400u, 168364039u, 235736075u, 303108111u, 370480147u, 102307863u, 168364039u,
  235736075u, 303108111u, 370480147u, 119085079u, 185207048u, 252579084u, 319951120u, 387323156u, 151525656u, 218893066u, 286265102u, 353637138u, 421009174u, 202050057u, 269422093u, 336794129u,
  404166165u, 202050073u, 269422093u, 336794129u, 404166165u, 218893081u, 286265102u, 353637138u, 421009174u, 252579084u, 319951120u, 387323156u, 235739416u, 303108111u, 370480147u, 236525591u,
  303108111u, 370480147u, 253302807u, 319951120u, 387323156u, 286267672u, 353637138u, 421009174u, 336794129u, 404166165u, 336794137u, 404166165u, 353637145u, 421009174u, 387323156u, 370481432u,
  370743319u, 387520535u, 421009688u, 421074969u
};

extern "C" __global__ __launch_bounds__(256, 1) __attribute__((aligned(4096)))
void afm_fast(const int* __restrict__ idx, const unsigned short* __restrict__ table,
                     const int* __restrict__ ii, const int* __restrict__ jj,
                     const unsigned short* __restrict__ W, const unsigned short* __restrict__ bvec,
                     const unsigned short* __restrict__ hvec, const unsigned short* __restrict__ pvec,
                     unsigned short* __restrict__ out, unsigned lim, unsigned kx
                     ) {
  __shared__ __attribute__((aligned(16))) char smem[110912];
  const unsigned lbase = (unsigned)(uintptr_t)(__attribute__((address_space(3))) char*)smem;
  unsigned flag;
  asm volatile(
      "s_load_dwordx16 s[8:23], %1, 0x0\n"
      "s_load_dwordx4 s[24:27], %1, 0x40\n"
      "s_mov_b32 s41, 0\n"
      "s_mov_b32 s42, 0\n"
      "s_mov_b32 s58, 0\n"
      "s_mov_b32 s35, 0\n"
      "0:\n"
      "s_getpc_b64 s[56:57]\n"
      "s_add_u32 s98, s56, afm_lane_tab@rel32@lo+4\n"
      "s_addc_u32 s99, s57, afm_lane_tab@rel32@hi+12\n"
      "v_and_b32 v255, 63, %3\n"
      "v_and_b32 v254, 127, %3\n"
      "v_lshrrev_b32 v253, 7, %3\n"
      "v_min_u32 v254, 81, v254\n"
      "v_mul_u32_u24 v253, 82, v253\n"
      "v_lshlrev_b32 v14, 4, v255\n"
      "v_lshlrev_b32 v252, 3, v255\n"
      "v_add_lshl_u32 v253, v253, v254, 2\n"
      "global_load_dwordx2 v[22:23], v252, s[98:99] offset:2048\n"
      "global_load_dword v20, v253, s[98:99] offset:2560\n"
      "global_load_dwordx4 v[204:207], v14, s[98:99]\n"
      "global_load_dwordx4 v[196:199], v14, s[98:99] offset:1024\n"
      "v_lshrrev_b32 v2, 6, %3\n"
      "s_bfe_u32 s44, %2, 0x30003\n"
      "s_mul_i32 s44, s44, 24\n"
      "v_mad_u32_u24 v255, v2, 6, v255\n"
      "s_mov_b64 exec, 0x3f\n"
      "v_add_lshl_u32 v255, v255, s44, 7\n"
      "global_load_dword v255, v255, s[56:57]\n"
      "s_mov_b64 exec, -1\n"
      "v_bfe_u32 v4, %3, 4, 2\n"
      "v_and_b32 v3, 15, %3\n"
      "v_readfirstlane_b32 s53, v2\n"
      "s_and_b32 s50, %2, 7\n"
      "s_lshr_b32 s51, %2, 3\n"
      "s_lshl2_add_u32 s51, s51, s53\n"
      "s_mul_i32 s51, s51, 104\n"
      "v_lshlrev_b32 v13, 4, v3\n"
      "v_lshlrev_b32 v225, 4, v254\n"
      "v_min_u32 v225, 1284, v225\n"
      "v_lshl_add_u32 v80, v2, 11, v14\n"
      "v_add_u32 v81, 8192, v80\n"
      "v_add_u32 v82, 16384, v80\n"
      "v_add_u32 v26, 24576, v80\n"
      "v_lshlrev_b32 v2, 4, v2\n"
      "s_mul_i32 s38, s53, 14400\n"
      "v_add_u32 v2, 37632, v2\n"
      "s_add_u32 s38, s38, 38912\n"
      "v_mul_u32_u24 v222, 288, v4\n"
      "v_add_u32 v213, s38, v13\n"
      "v_mov_b32 v5, 0\n"
      "v_add_u32 v1, v222, v213\n"
      "v_mov_b32 v222, 0x7fa5a5a5\n"
      "s_cmp_lg_u32 s35, 0\n"
      "s_cbranch_scc1 2f\n"
      "ds_write2_b32 v2, v5, v5 offset0:0 offset1:1\n"
      "ds_write_b32 v2, v5 offset:8\n"
      "ds_write_b32 v1, v222 offset:12992\n"
      "2:\n"
      "s_mov_b32 s43, 0xffff0000\n"
      "s_mov_b32 s56, 0\n"
      "s_mov_b32 s40, 0\n"
      "s_mov_b32 s55, 0\n"
      "s_and_b32 s46, s53, 2\n"
      "s_lshl_b32 s54, 3, s46\n"
      "s_bitcmp1_b32 s53, 0\n"
      "s_cselect_b64 s[100:101], -1, 0\n"
      "s_movk_i32 s98, 14400\n"
      "s_cselect_b32 s99, s98, 0\n"
      "s_cmovk_i32 s98, -14400\n"
      "s_cselect_b32 s46, 0, 18560\n"
      "s_cselect_b32 s47, 18560, 0\n"
      "v_add_u32 v212, s46, v14\n"
      "v_add_u32 v14, s47, v14\n"
      "s_sub_u32 s99, s38, s99\n"
      "v_cmp_eq_u32 vcc, 1, v4\n"
      "v_mov_b32 v222, 0x3300\n"
      "s_mov_b32 s46, 0\n"
      "v_cndmask_b32 v94, 0, v222, vcc\n"
      "s_mov_b32 s47, 0xc72c4bb9\n"
      "v_lshlrev_b32 v223, 4, v4\n"
      "v_lshrrev_b64 v[146:147], v223, s[46:47]\n"
      "v_lshlrev_b32 v138, 16, v146\n"
      "v_lshrrev_b32 v130, 16, v138\n"
      "v_mov_b32 v200, 0x4738aa3b\n"
      "v_lshlrev_b32 v224, 16, v94\n"
      "v_accvgpr_write_b32 a128, v130\n"
      "v_accvgpr_write_b32 a129, 0\n"
      "v_accvgpr_write_b32 a130, v138\n"
      "v_accvgpr_write_b32 a131, 0\n"
      "v_accvgpr_write_b32 a132, 0\n"
      "v_accvgpr_write_b32 a133, v130\n"
      "v_accvgpr_write_b32 a134, 0\n"
      "v_accvgpr_write_b32 a135, v138\n"
      "v_accvgpr_write_b32 a208, v94\n"
      "v_accvgpr_write_b32 a209, 0\n"
      "v_accvgpr_write_b32 a210, v224\n"
      "v_accvgpr_write_b32 a211, 0\n"
      "v_accvgpr_write_b32 a212, 0\n"
      "v_accvgpr_write_b32 a213, v94\n"
      "v_accvgpr_write_b32 a214, 0\n"
      "v_accvgpr_write_b32 a215, v224\n"
      "v_add_u32 v95, s38, v223\n"
      "v_and_b32 v227, 3, %3\n"
      "v_bfe_u32 v224, %3, 2, 2\n"
      "v_cmp_eq_u32 vcc, v227, v4\n"
      "v_mov_b32 v222, 0x3a00\n"
      "v_mov_b64 v[84:85], 0\n"
      "s_waitcnt lgkmcnt(0)\n"
      "s_mul_i32 s50, s50, s27\n"
      "s_add_u32 s50, s50, s51\n"
      "s_min_u32 s34, s50, s26\n"
      "s_load_dwordx16 s[60:75], s[8:9], s34\n"
      "s_load_dwordx8 s[76:83], s[8:9], s34 offset:0x40\n"
      "s_load_dwordx2 s[84:85], s[8:9], s34 offset:0x60\n"
      "global_load_dwordx4 v[96:99], v80, s[16:17] offset:0\n"
      "global_load_dwordx4 v[100:103], v80, s[16:17] offset:1024\n"
      "global_load_dwordx4 v[104:107], v81, s[16:17] offset:0\n"
      "global_load_dwordx4 v[108:111], v81, s[16:17] offset:1024\n"
      "v_mov_b64 v[24:25], s[20:21]\n"
      "s_mov_b32 exec_hi, 0\n"
      "v_mov_b64 v[24:25], s[18:19]\n"
      "s_mov_b32 exec_lo, 0xffff\n"
      "v_mov_b64 v[24:25], s[22:23]\n"
      "s_mov_b64 exec, -1\n"
      "v_add_co_u32 v24, s[28:29], v24, v13\n"
      "s_cmp_lt_u32 s53, 2\n"
      "s_cselect_b64 s[12:13], s[12:13], s[14:15]\n"
      "v_addc_co_u32 v25, s[28:29], 0, v25, s[28:29]\n"
      "global_load_dwordx4 v[192:195], v[24:25], off\n"
      "global_load_dwordx4 v[112:115], v82, s[16:17] offset:0\n"
      "global_load_dwordx4 v[116:119], v82, s[16:17] offset:1024\n"
      "global_load_dwordx4 v[120:123], v26, s[16:17] offset:0\n"
      "global_load_dwordx4 v[124:127], v26, s[16:17] offset:1024\n"
      "s_cmp_lg_u32 s35, 0\n"
      "s_cbranch_scc1 21f\n"
      "s_barrier\n"
      "21:\n"
      "v_mov_b64 v[88:89], 0\n"
      "v_cndmask_b32 v84, 0, v222, vcc\n"
      "v_lshlrev_b32 v222, 4, v224\n"
      "v_lshlrev_b64 v[86:87], v222, v[84:85]\n"
      "s_mov_b32 s46, 0xd905\n"
      "v_lshlrev_b32 v222, 2, v4\n"
      "v_mov_b32 v84, 0\n"
      "v_bfe_u32 v222, s46, v222, 4\n"
      "v_lshl_add_u32 v91, v222, 6, v213\n"
      "v_lshlrev_b32 v227, 4, v227\n"
      "s_mov_b64 s[46:47], 0x3c00\n"
      "v_lshlrev_b32 v5, 5, v3\n"
      "v_lshl_add_u32 v213, v4, 2, v213\n"
      "v_lshl_add_u32 v5, v4, 3, v5\n"
      "v_lshlrev_b64 v[222:223], v227, s[46:47]\n"
      "s_movk_i32 s46, 0x4400\n"
      "v_accvgpr_write_b32 a216, v222\n"
      "v_lshlrev_b64 v[172:173], v227, s[46:47]\n"
      "s_lshl_b64 exec, -1, 48\n"
      "s_bitset1_b32 s46, 15\n"
      "v_lshlrev_b64 v[172:173], v227, s[46:47]\n"
      "s_mov_b64 exec, -1\n"
      "s_mov_b32 s49, 0x03000000\n"
      "v_accvgpr_write_b32 a217, v223\n"
      "s_movk_i32 s46, 288\n"
      "v_and_b32 v222, 7, %3\n"
      "v_bfe_u32 v223, %3, 3, 1\n"
      "v_mad_u32_u24 v201, v222, s46, v95\n"
      "v_mad_u32_u24 v203, v223, s46, v95\n"
      "s_movk_i32 s47, 320\n"
      "v_mad_u32_u24 v210, v3, s46, v95\n"
      "v_mad_u32_u24 v214, v223, s47, v95\n"
      "s_mov_b32 s59, 0\n"
      "s_mov_b32 s57, 0\n"
      "v_and_b32 v222, v3, v4\n"
      "v_lshlrev_b32 v223, 6, v3\n"
      "v_bfe_u32 v222, 6, v222, 1\n"
      "s_movk_i32 s46, 0x100\n"
      "v_lshl_add_u32 v44, v222, 8, v95\n"
      "v_bitop3_b32 v45, v44, v223, s46 bitop3:0x78\n"
      "v_bitop3_b32 v46, v44, v5, s46 bitop3:0x78\n"
      "v_bitop3_b32 v47, v45, v5, s46 bitop3:0x78\n"
      "s_lshr_b32 s33, s34, 2\n"
      "s_mul_i32 s33, s33, 0xc4ec4ec5\n"
      "s_mov_b32 s36, 0\n"
      "s_mov_b32 s37, 0x7fa5a5a5\n"
      "global_load_dwordx4 v[80:83], v225, s[12:13]\n"
      "s_mov_b64 exec, 0\n"
      "s_cmp_eq_u32 s53, 0\n"
      "s_cbranch_scc1 40f\n"
      "s_cmp_eq_u32 s53, 1\n"
      "s_cbranch_scc1 41f\n"
      "s_cmp_eq_u32 s53, 2\n"
      "s_cbranch_scc1 42f\n"
      "s_branch 43f\n"
      "44:\n"
      "s_mov_b64 exec, -1\n"
      "s_waitcnt lgkmcnt(0)\n"
      "v_lshl_add_u32 v6, s60, 8, v13\n"
      "v_lshl_add_u32 v93, s64, 8, v13\n"
      "s_mov_b32 exec_lo, 0xffff0000\n"
      "v_lshl_add_u32 v6, s61, 8, v13\n"
      "v_lshl_add_u32 v93, s65, 8, v13\n"
      "s_mov_b32 exec_lo, 0\n"
      "v_lshl_add_u32 v6, s62, 8, v13\n"
      "v_lshl_add_u32 v93, s66, 8, v13\n"
      "s_mov_b32 exec_hi, 0xffff0000\n"
      "v_lshl_add_u32 v6, s63, 8, v13\n"
      "v_lshl_add_u32 v93, s67, 8, v13\n"
      "s_mov_b64 exec, -1\n"
      "global_load_dwordx4 v[228:231], v6, s[10:11] nt\n"
      "global_load_dwordx4 v[232:235], v93, s[10:11] nt\n"
      "v_lshl_add_u32 v8, s68, 8, v13\n"
      "v_lshl_add_u32 v9, s72, 8, v13\n"
      "v_lshl_add_u32 v10, s76, 8, v13\n"
      "v_lshl_add_u32 v11, s80, 8, v13\n"
      "v_lshl_add_u32 v12, s84, 8, v13\n"
      "s_mov_b32 exec_lo, 0xffff0000\n"
      "v_lshl_add_u32 v8, s69, 8, v13\n"
      "v_lshl_add_u32 v9, s73, 8, v13\n"
      "v_lshl_add_u32 v10, s77, 8, v13\n"
      "v_lshl_add_u32 v11, s81, 8, v13\n"
      "v_lshl_add_u32 v12, s85, 8, v13\n"
      "s_mov_b32 exec_lo, 0\n"
      "v_lshl_add_u32 v8, s70, 8, v13\n"
      "v_lshl_add_u32 v9, s74, 8, v13\n"
      "v_lshl_add_u32 v10, s78, 8, v13\n"
      "v_lshl_add_u32 v11, s82, 8, v13\n"
      "s_mov_b32 exec_hi, 0xffff0000\n"
      "v_lshl_add_u32 v8, s71, 8, v13\n"
      "v_lshl_add_u32 v9, s75, 8, v13\n"
      "v_lshl_add_u32 v10, s79, 8, v13\n"
      "v_lshl_add_u32 v11, s83, 8, v13\n"
      "s_mov_b64 exec, -1\n"
      "s_lshl_b32 s46, s53, 8\n"
      "s_lshl_b32 s47, s53, 5\n"
      "s_waitcnt vmcnt(16)\n"
      "v_add_u32_sdwa v15, s46, v22 dst_sel:DWORD dst_unused:UNUSED_PAD src0_sel:DWORD src1_sel:WORD_0\n"
      "v_add_u32_sdwa v92, s46, v22 dst_sel:DWORD dst_unused:UNUSED_PAD src0_sel:DWORD src1_sel:WORD_1\n"
      "v_add_u32_sdwa v7, s47, v23 dst_sel:DWORD dst_unused:UNUSED_PAD src0_sel:DWORD src1_sel:WORD_0\n"
      "v_add_u32_sdwa v202, s38, v23 dst_sel:DWORD dst_unused:UNUSED_PAD src0_sel:DWORD src1_sel:WORD_1\n"
      "s_waitcnt vmcnt(15)\n"
      "v_and_b32 v160, 0xff, v20\n"
      "v_bfe_u32 v161, v20, 8, 8\n"
      "v_bfe_u32 v162, v20, 16, 8\n"
      "v_lshrrev_b32 v163, 24, v20\n"
      "s_cmp_lg_u32 s58, 0\n"
      "s_cbranch_scc1 70f\n"
      "71:\n"
      "s_waitcnt vmcnt(11)\n"
      "v_mfma_f32_16x16x32_bf16 v[128:131], v[86:89], v[96:99], 0\n"
      "s_waitcnt vmcnt(10)\n"
      "v_mfma_f32_16x16x32_bf16 v[132:135], v[86:89], v[100:103], 0\n"
      "v_mfma_f32_16x16x32_bf16 v[136:139], v[84:87], v[96:99], 0\n"
      "v_mfma_f32_16x16x32_bf16 v[140:143], v[84:87], v[100:103], 0\n"
      "s_waitcnt vmcnt(9)\n"
      "v_mfma_f32_16x16x32_bf16 v[144:147], v[86:89], v[104:107], 0\n"
      "s_nop 1\n"
      "v_cvt_pk_f16_f32 v164, v128, v129\n"
      "v_cvt_pk_f16_f32 v165, v130, v131\n"
      "s_waitcnt vmcnt(8)\n"
      "v_mfma_f32_16x16x32_bf16 v[148:151], v[86:89], v[108:111], 0\n"
      "v_cvt_pk_f16_f32 v166, v132, v133\n"
      "v_cvt_pk_f16_f32 v167, v134, v135\n"
      "v_mfma_f32_16x16x32_bf16 v[152:155], v[84:87], v[104:107], 0\n"
      "ds_write_b128 v15, v[164:167] offset:0\n"
      "v_cvt_pk_f16_f32 v168, v136, v137\n"
      "v_cvt_pk_f16_f32 v169, v138, v139\n"
      "v_mfma_f32_16x16x32_bf16 v[156:159], v[84:87], v[108:111], 0\n"
      "v_cvt_pk_f16_f32 v170, v140, v141\n"
      "v_cvt_pk_f16_f32 v171, v142, v143\n"
      "global_load_dwordx4 v[236:239], v8, s[10:11] nt\n"
      "s_waitcnt vmcnt(7)\n"
      "v_mfma_f32_16x16x32_bf16 v[128:131], v[86:89], v[112:115], 0\n"
      "ds_write_b128 v15, v[168:171] offset:18560\n"
      "v_cvt_pk_f16_f32 v164, v144, v145\n"
      "v_cvt_pk_f16_f32 v165, v146, v147\n"
      "s_waitcnt vmcnt(6)\n"
      "v_mfma_f32_16x16x32_bf16 v[132:135], v[86:89], v[116:119], 0\n"
      "v_cvt_pk_f16_f32 v166, v148, v149\n"
      "v_cvt_pk_f16_f32 v167, v150, v151\n"
      "v_mfma_f32_16x16x32_bf16 v[136:139], v[84:87], v[112:115], 0\n"
      "ds_write_b128 v15, v[164:167] offset:1152\n"
      "v_cvt_pk_f16_f32 v168, v152, v153\n"
      "v_cvt_pk_f16_f32 v169, v154, v155\n"
      "v_mfma_f32_16x16x32_bf16 v[140:143], v[84:87], v[116:119], 0\n"
      "v_cvt_pk_f16_f32 v170, v156, v157\n"
      "v_cvt_pk_f16_f32 v171, v158, v159\n"
      "s_waitcnt vmcnt(5)\n"
      "v_mfma_f32_16x16x32_bf16 v[144:147], v[86:89], v[120:123], 0\n"
      "ds_write_b128 v15, v[168:171] offset:19712\n"
      "v_cvt_pk_f16_f32 v164, v128, v129\n"
      "v_cvt_pk_f16_f32 v165, v130, v131\n"
      "s_waitcnt vmcnt(4)\n"
      "v_mfma_f32_16x16x32_bf16 v[148:151], v[86:89], v[124:127], 0\n"
      "v_cvt_pk_f16_f32 v166, v132, v133\n"
      "v_cvt_pk_f16_f32 v167, v134, v135\n"
      "v_mfma_f32_16x16x32_bf16 v[152:155], v[84:87], v[120:123], 0\n"
      "ds_write_b128 v15, v[164:167] offset:2304\n"
      "v_cvt_pk_f16_f32 v168, v136, v137\n"
      "v_cvt_pk_f16_f32 v169, v138, v139\n"
      "v_mfma_f32_16x16x32_bf16 v[156:159], v[84:87], v[124:127], 0\n"
      "v_cvt_pk_f16_f32 v170, v140, v141\n"
      "v_cvt_pk_f16_f32 v171, v142, v143\n"
      "ds_write_b128 v15, v[168:171] offset:20864\n"
      "v_mfma_f32_4x4x4_16b_bf16 v[176:179], v[172:173], v[192:193], 0\n"
      "v_cndmask_b32 v188, v192, v194, s[100:101]\n"
      "v_cvt_pk_f16_f32 v164, v144, v145\n"
      "v_cndmask_b32 v189, v193, v195, s[100:101]\n"
      "v_max3_f32 v222, |v192|, |v193|, |v195|\n"
      "v_cvt_pk_f16_f32 v165, v146, v147\n"
      "v_mfma_f32_4x4x4_16b_bf16 v[180:183], v[172:173], v[194:195], 0\n"
      "v_subrev_u32 v222, 0x3d800000, v222\n"
      "v_cvt_pk_f16_f32 v166, v148, v149\n"
      "v_mfma_f32_16x16x16_bf16 v[48:51], v[188:189], a[128:129], 0\n"
      "v_cmp_gt_u32 vcc, s49, v222\n"
      "v_cvt_pk_f16_f32 v167, v150, v151\n"
      "v_mfma_f32_16x16x16_bf16 v[52:55], v[188:189], a[130:131], 0\n"
      "v_cndmask_b32 v190, v194, v192, s[100:101]\n"
      "ds_write_b128 v15, v[164:167] offset:3456\n"
      "v_cndmask_b32 v191, v195, v193, s[100:101]\n"
      "v_mfma_f32_16x16x16_bf16 v[56:59], v[188:189], a[132:133], 0\n"
      "v_cvt_pk_f16_f32 v168, v152, v153\n"
      "s_and_b32 s44, vcc_lo, 0xffff\n"
      "s_cselect_b32 s44, vcc_hi, 0\n"
      "v_cvt_pk_f16_f32 v169, v154, v155\n"
      "v_mfma_f32_16x16x16_bf16 v[60:63], v[188:189], a[134:135], 0\n"
      "s_cmp_eq_u32 s44, 0\n"
      "v_cvt_pk_f16_f32 v170, v156, v157\n"
      "s_cbranch_scc1 22f\n"
      "23:\n"
      "v_mfma_f32_16x16x16_bf16 v[64:67], v[190:191], a[128:129], 0\n"
      "v_cvt_pk_f16_f32 v171, v158, v159\n"
      "v_cvt_pk_f16_f32 v184, v176, v177\n"
      "v_cvt_pk_f16_f32 v185, v178, v179\n"
      "ds_write_b128 v15, v[168:171] offset:22016\n"
      "v_mfma_f32_16x16x16_bf16 v[68:71], v[190:191], a[130:131], 0\n"
      "v_cvt_pk_f16_f32 v186, v180, v181\n"
      "ds_write_b32 v2, v2 offset:4\n"
      "v_cvt_pk_f16_f32 v187, v182, v183\n"
      "ds_write_b128 v91, v[184:187] offset:11904\n"
      "v_mfma_f32_16x16x16_bf16 v[72:75], v[190:191], a[132:133], 0\n"
      "v_mfma_f32_16x16x16_bf16 v[76:79], v[190:191], a[134:135], 0\n"
      "v_mov_b32 v3, s33\n"
      "global_load_ushort v250, v3, s[24:25]\n"
      "v_mfma_f32_16x16x16_bf16 v[16:19], v[188:189], a[208:209], 0\n"
      "ds_read_b128 a[184:187], v44 offset:12480\n"
      "ds_read_b128 a[188:191], v45 offset:12544\n"
      "v_mfma_f32_16x16x16_bf16 v[20:23], v[188:189], a[210:211], 0\n"
      "ds_read_b128 a[192:195], v46 offset:12608\n"
      "ds_read_b128 a[196:199], v47 offset:12672\n"
      "v_mfma_f32_16x16x16_bf16 v[24:27], v[188:189], a[212:213], 0\n"
      "ds_read_b64_tr_b16 a[152:153], v92 offset:0\n"
      "ds_read_b64_tr_b16 a[154:155], v92 offset:18560\n"
      "ds_read_b64_tr_b16 a[156:157], v92 offset:64\n"
      "v_mfma_f32_16x16x16_bf16 v[28:31], v[188:189], a[214:215], 0\n"
      "ds_read_b64_tr_b16 a[158:159], v92 offset:18624\n"
      "ds_read_b64_tr_b16 a[160:161], v92 offset:128\n"
      "ds_read_b64_tr_b16 a[162:163], v92 offset:18688\n"
      "v_mfma_f32_16x16x16_bf16 v[32:35], v[190:191], a[208:209], 0\n"
      "ds_read_b64_tr_b16 a[164:165], v92 offset:192\n"
      "ds_read_b64_tr_b16 a[166:167], v92 offset:18752\n"
      "ds_read_b64_tr_b16 a[168:169], v92 offset:2304\n"
      "v_mfma_f32_16x16x16_bf16 v[36:39], v[190:191], a[210:211], 0\n"
      "ds_read_b64_tr_b16 a[170:171], v92 offset:20864\n"
      "ds_read_b64_tr_b16 a[172:173], v92 offset:2368\n"
      "ds_read_b64_tr_b16 a[174:175], v92 offset:20928\n"
      "v_mfma_f32_16x16x16_bf16 v[40:43], v[190:191], a[212:213], 0\n"
      "ds_read_b64_tr_b16 a[176:177], v92 offset:2432\n"
      "ds_read_b64_tr_b16 a[178:179], v92 offset:20992\n"
      "ds_read_b64_tr_b16 a[180:181], v92 offset:2496\n"
      "v_mfma_f32_16x16x16_bf16 v[44:47], v[190:191], a[214:215], 0\n"
      "ds_read_b64_tr_b16 a[182:183], v92 offset:21056\n"
      "ds_read_b32 v224, v227 offset:37636\n"
      "s_waitcnt lgkmcnt(15)\n"
      "v_mfma_f32_16x16x32_f16 v[192:195], a[184:187], a[152:155], 0\n"
      "s_waitcnt vmcnt(4)\n"
      "v_cmp_eq_u64 vcc, v[80:81], v[160:161]\n"
      "s_waitcnt lgkmcnt(13)\n"
      "v_mfma_f32_16x16x32_f16 v[192:195], a[188:191], a[156:159], v[192:195]\n"
      "v_cmp_eq_u64 s[28:29], v[82:83], v[162:163]\n"
      "s_waitcnt lgkmcnt(11)\n"
      "v_mfma_f32_16x16x32_f16 v[192:195], a[192:195], a[160:163], v[192:195]\n"
      "s_and_b64 vcc, vcc, s[28:29]\n"
      "s_cmp_eq_u64 vcc, -1\n"
      "s_waitcnt lgkmcnt(9)\n"
      "v_mfma_f32_16x16x32_f16 v[192:195], a[196:199], a[164:167], v[192:195]\n"
      "s_cselect_b32 s52, 13, 1\n"
      "s_waitcnt lgkmcnt(0)\n"
      "v_cmp_eq_u32 vcc, 0, v224\n"
      "s_cbranch_vccz 13f\n"
      "11:\n"
      "ds_read_b32 v224, v227 offset:37636\n"
      "s_waitcnt lgkmcnt(0)\n"
      "v_cmp_eq_u32 vcc, 0, v224\n"
      "s_cbranch_vccnz 11b\n"
      "13:\n"
      "v_mfma_f32_16x16x32_f16 v[252:255], a[184:187], a[168:171], 0\n"
      "ds_read_b128 a[0:3], v14 offset:0\n"
      "ds_read_b128 a[4:7], v14 offset:1152\n"
      "v_mfma_f32_16x16x32_f16 v[252:255], a[188:191], a[172:175], v[252:255]\n"
      "ds_read_b128 a[8:11], v14 offset:2304\n"
      "ds_read_b128 a[12:15], v14 offset:3456\n"
      "v_mfma_f32_16x16x32_f16 v[252:255], a[192:195], a[176:179], v[252:255]\n"
      "ds_read_b128 a[16:19], v14 offset:4640\n"
      "ds_read_b128 a[20:23], v14 offset:5792\n"
      "v_mfma_f32_16x16x32_f16 v[252:255], a[196:199], a[180:183], v[252:255]\n"
      "ds_read_b128 a[24:27], v14 offset:6944\n"
      "ds_read_b128 a[28:31], v14 offset:8096\n"
      "s_cmp_lg_u32 s58, 0\n"
      "s_cbranch_scc1 72f\n"
      "73:\n"
      "global_load_dwordx4 v[240:243], v9, s[10:11] nt\n"
      "s_waitcnt vmcnt(4)\n"
      "v_mfma_f32_4x4x4_16b_bf16 v[176:179], a[216:217], v[228:229], 0\n"
      "v_mfma_f32_4x4x4_16b_bf16 v[180:183], a[216:217], v[230:231], 0\n"
      "ds_read_b128 a[32:35], v14 offset:9280\n"
      "s_waitcnt vmcnt(3)\n"
      "v_mfma_f32_4x4x4_16b_bf16 v[144:147], a[216:217], v[232:233], 0\n"
      "ds_read_b128 a[36:39], v14 offset:10432\n"
      "v_mfma_f32_4x4x4_16b_bf16 v[148:151], a[216:217], v[234:235], 0\n"
      "ds_read_b128 a[40:43], v14 offset:11584\n"
      "ds_read_b128 a[44:47], v14 offset:12736\n"
      "v_cvt_pk_f16_f32 v184, v176, v177\n"
      "v_cvt_pk_f16_f32 v185, v178, v179\n"
      "v_cvt_pk_f16_f32 v186, v180, v181\n"
      "v_cvt_pk_f16_f32 v187, v182, v183\n"
      "ds_write_b128 v1, v[184:187] offset:0\n"
      "v_cvt_pk_f16_f32 v152, v144, v145\n"
      "v_cvt_pk_f16_f32 v153, v146, v147\n"
      "v_cvt_pk_f16_f32 v154, v148, v149\n"
      "v_cvt_pk_f16_f32 v155, v150, v151\n"
      "ds_write_b128 v1, v[152:155] offset:1152\n"
      "ds_read_b128 v[128:131], v201 offset:0\n"
      "ds_read_b128 v[160:163], v202 offset:0\n"
      "ds_read_b128 v[132:135], v201 offset:64\n"
      "ds_read_b128 v[164:167], v202 offset:64\n"
      "ds_read_b128 v[136:139], v201 offset:128\n"
      "ds_read_b128 v[168:171], v202 offset:128\n"
      "ds_read_b128 v[140:143], v201 offset:192\n"
      "ds_read_b128 v[172:175], v202 offset:192\n"
      "s_waitcnt vmcnt(2)\n"
      "v_mfma_f32_4x4x4_16b_bf16 v[112:115], a[216:217], v[236:237], 0\n"
      "v_mfma_f32_4x4x4_16b_bf16 v[116:119], a[216:217], v[238:239], 0\n"
      "s_nop 3\n"
      "v_cvt_pk_f16_f32 v120, v112, v113\n"
      "v_cvt_pk_f16_f32 v121, v114, v115\n"
      "v_cvt_pk_f16_f32 v122, v116, v117\n"
      "v_cvt_pk_f16_f32 v123, v118, v119\n"
      "ds_write_b128 v1, v[120:123] offset:2304\n"
      "s_waitcnt lgkmcnt(7)\n"
      "v_pk_mul_f16 v96, v128, v160\n"
      "v_pk_mul_f16 v97, v129, v161\n"
      "v_pk_mul_f16 v98, v130, v162\n"
      "v_pk_mul_f16 v99, v131, v163\n"
      "s_waitcnt lgkmcnt(5)\n"
      "v_pk_mul_f16 v100, v132, v164\n"
      "v_pk_mul_f16 v101, v133, v165\n"
      "v_pk_mul_f16 v102, v134, v166\n"
      "v_pk_mul_f16 v103, v135, v167\n"
      "s_waitcnt lgkmcnt(3)\n"
      "v_pk_mul_f16 v104, v136, v168\n"
      "v_pk_mul_f16 v105, v137, v169\n"
      "v_pk_mul_f16 v106, v138, v170\n"
      "v_pk_mul_f16 v107, v139, v171\n"
      "3:\n"
      "v_mfma_f32_16x16x32_f16 v[80:83], a[0:3], v[96:99], v[16:19]\n"
      "s_waitcnt lgkmcnt(1)\n"
      "v_pk_mul_f16 v108, v140, v172\n"
      "v_pk_mul_f16 v109, v141, v173\n"
      "v_mfma_f32_16x16x32_f16 v[80:83], a[4:7], v[100:103], v[80:83]\n"
      "v_pk_mul_f16 v110, v142, v174\n"
      "v_pk_mul_f16 v111, v143, v175\n"
      "v_mfma_f32_16x16x32_f16 v[80:83], a[8:11], v[104:107], v[80:83]\n"
      "ds_read_b128 a[60:63], v14 offset:17376\n"
      "ds_read_b128 a[56:59], v14 offset:16224\n"
      "v_mfma_f32_16x16x32_f16 v[80:83], a[12:15], v[108:111], v[80:83]\n"
      "ds_read_b128 a[52:55], v14 offset:15072\n"
      "ds_read_b128 a[48:51], v14 offset:13920\n"
      "v_mfma_f32_16x16x32_f16 v[84:87], a[28:31], v[108:111], v[20:23]\n"
      "ds_read_b128 a[64:67], v212 offset:0\n"
      "ds_read_b128 v[144:147], v203 offset:2304\n"
      "v_mfma_f32_16x16x32_f16 v[84:87], a[24:27], v[104:107], v[84:87]\n"
      "ds_read_b128 a[68:71], v212 offset:1152\n"
      "ds_read_b128 a[72:75], v212 offset:2304\n"
      "v_mfma_f32_16x16x32_f16 v[84:87], a[20:23], v[100:103], v[84:87]\n"
      "ds_read_b128 a[76:79], v212 offset:3456\n"
      "ds_read_b128 a[92:95], v212 offset:8096\n"
      "v_mfma_f32_16x16x32_f16 v[84:87], a[16:19], v[96:99], v[84:87]\n"
      "ds_read_b128 a[88:91], v212 offset:6944\n"
      "ds_read_b128 v[148:151], v203 offset:2368\n"
      "v_mfma_f32_16x16x32_f16 v[88:91], a[32:35], v[96:99], v[24:27]\n"
      "ds_read_b128 a[84:87], v212 offset:5792\n"
      "ds_read_b128 a[80:83], v212 offset:4640\n"
      "v_mfma_f32_16x16x32_f16 v[88:91], a[36:39], v[100:103], v[88:91]\n"
      "v_fma_f32 v188, |v80|, v48, 4.0\n"
      "v_fma_f32 v188, |v81|, v49, v188\n"
      "v_mfma_f32_16x16x32_f16 v[88:91], a[40:43], v[104:107], v[88:91]\n"
      "ds_read_b128 a[96:99], v212 offset:9280\n"
      "v_fma_f32 v188, |v82|, v50, v188\n"
      "v_mfma_f32_16x16x32_f16 v[88:91], a[44:47], v[108:111], v[88:91]\n"
      "s_waitcnt lgkmcnt(13)\n"
      "v_fma_f32 v188, |v83|, v51, v188\n"
      "v_fma_f32 v188, |v84|, v52, v188\n"
      "v_mfma_f32_16x16x32_f16 v[92:95], a[60:63], v[108:111], v[28:31]\n"
      "s_waitcnt lgkmcnt(9)\n"
      "v_pk_mul_f16 v112, v128, v144\n"
      "v_pk_mul_f16 v113, v129, v145\n"
      "v_mfma_f32_16x16x32_f16 v[92:95], a[56:59], v[104:107], v[92:95]\n"
      "ds_read_b128 a[100:103], v212 offset:10432\n"
      "ds_read_b128 a[104:107], v212 offset:11584\n"
      "v_mfma_f32_16x16x32_f16 v[92:95], a[52:55], v[100:103], v[92:95]\n"
      "v_pk_mul_f16 v114, v130, v146\n"
      "v_pk_mul_f16 v115, v131, v147\n"
      "v_mfma_f32_16x16x32_f16 v[92:95], a[48:51], v[96:99], v[92:95]\n"
      "s_waitcnt lgkmcnt(10)\n"
      "ds_read_b128 v[152:155], v203 offset:2432\n"
      "ds_read_b128 a[108:111], v212 offset:12736\n"
      "v_mfma_f32_16x16x32_f16 v[80:83], a[64:67], v[96:99], v[32:35]\n"
      "ds_read_b128 a[124:127], v212 offset:17376\n"
      "v_fma_f32 v188, |v85|, v53, v188\n"
      "v_mfma_f32_16x16x32_f16 v[80:83], a[68:71], v[100:103], v[80:83]\n"
      "s_waitcnt lgkmcnt(11)\n"
      "v_fma_f32 v188, |v86|, v54, v188\n"
      "v_fma_f32 v188, |v87|, v55, v188\n"
      "v_mfma_f32_16x16x32_f16 v[80:83], a[72:75], v[104:107], v[80:83]\n"
      "s_waitcnt lgkmcnt(8)\n"
      "ds_read_b128 a[120:123], v212 offset:16224\n"
      "v_pk_mul_f16 v116, v132, v148\n"
      "v_mfma_f32_16x16x32_f16 v[80:83], a[76:79], v[108:111], v[80:83]\n"
      "ds_read_b128 a[116:119], v212 offset:15072\n"
      "v_pk_mul_f16 v117, v133, v149\n"
      "v_mfma_f32_16x16x32_f16 v[84:87], a[92:95], v[108:111], v[36:39]\n"
      "ds_read_b128 a[112:115], v212 offset:13920\n"
      "v_fma_f32 v188, |v88|, v56, v188\n"
      "v_mfma_f32_16x16x32_f16 v[84:87], a[88:91], v[104:107], v[84:87]\n"
      "s_waitcnt lgkmcnt(9)\n"
      "v_fma_f32 v188, |v89|, v57, v188\n"
      "v_pk_mul_f16 v118, v134, v150\n"
      "v_mfma_f32_16x16x32_f16 v[84:87], a[84:87], v[100:103], v[84:87]\n"
      "ds_read_b128 v[156:159], v203 offset:2496\n"
      "v_fma_f32 v188, |v90|, v58, v188\n"
      "v_mfma_f32_16x16x32_f16 v[84:87], a[80:83], v[96:99], v[84:87]\n"
      "s_waitcnt lgkmcnt(8)\n"
      "v_fma_f32 v188, |v91|, v59, v188\n"
      "v_pk_mul_f16 v119, v135, v151\n"
      "v_mfma_f32_16x16x32_f16 v[88:91], a[96:99], v[96:99], v[40:43]\n"
      "s_waitcnt lgkmcnt(6)\n"
      "v_pk_mul_f16 v120, v136, v152\n"
      "v_fma_f32 v188, |v92|, v60, v188\n"
      "v_mfma_f32_16x16x32_f16 v[88:91], a[100:103], v[100:103], v[88:91]\n"
      "s_waitcnt lgkmcnt(5)\n"
      "v_fma_f32 v188, |v93|, v61, v188\n"
      "v_pk_mul_f16 v121, v137, v153\n"
      "v_mfma_f32_16x16x32_f16 v[88:91], a[104:107], v[104:107], v[88:91]\n"
      "v_fma_f32 v188, |v94|, v62, v188\n"
      "v_fma_f32 v188, |v95|, v63, v188\n"
      "v_mfma_f32_16x16x32_f16 v[88:91], a[108:111], v[108:111], v[88:91]\n"
      "s_waitcnt lgkmcnt(3)\n"
      "v_pk_mul_f16 v122, v138, v154\n"
      "v_pk_mul_f16 v123, v139, v155\n"
      "v_mfma_f32_16x16x32_f16 v[92:95], a[124:127], v[108:111], v[44:47]\n"
      "global_load_dwordx4 v[244:247], v10, s[10:11] nt\n"
      "v_fma_f32 v188, |v80|, v64, v188\n"
      "v_mfma_f32_16x16x32_f16 v[92:95], a[120:123], v[104:107], v[92:95]\n"
      "s_waitcnt lgkmcnt(1)\n"
      "v_fma_f32 v188, |v81|, v65, v188\n"
      "v_fma_f32 v188, |v82|, v66, v188\n"
      "v_mfma_f32_16x16x32_f16 v[92:95], a[116:119], v[100:103], v[92:95]\n"
      "s_waitcnt lgkmcnt(0)\n"
      "v_pk_mul_f16 v124, v140, v156\n"
      "v_fma_f32 v188, |v83|, v67, v188\n"
      "v_mfma_f32_16x16x32_f16 v[92:95], a[112:115], v[96:99], v[92:95]\n"
      "v_pk_mul_f16 v125, v141, v157\n"
      "v_pk_mul_f16 v126, v142, v158\n"
      "v_mfma_f32_16x16x32_f16 v[80:83], a[0:3], v[112:115], v[16:19]\n"
      "ds_read_b128 v[160:163], v203 offset:2880\n"
      "v_pk_mul_f16 v127, v143, v159\n"
      "v_mfma_f32_16x16x32_f16 v[80:83], a[4:7], v[116:119], v[80:83]\n"
      "v_fma_f32 v188, |v84|, v68, v188\n"
      "v_fma_f32 v188, |v85|, v69, v188\n"
      "v_mfma_f32_16x16x32_f16 v[80:83], a[8:11], v[120:123], v[80:83]\n"
      "v_fma_f32 v188, |v86|, v70, v188\n"
      "v_fma_f32 v188, |v87|, v71, v188\n"
      "v_mfma_f32_16x16x32_f16 v[80:83], a[12:15], v[124:127], v[80:83]\n"
      "v_fma_f32 v188, |v88|, v72, v188\n"
      "v_fma_f32 v188, |v89|, v73, v188\n"
      "v_mfma_f32_16x16x32_f16 v[84:87], a[28:31], v[124:127], v[20:23]\n"
      "v_fma_f32 v188, |v90|, v74, v188\n"
      "v_fma_f32 v188, |v91|, v75, v188\n"
      "v_mfma_f32_16x16x32_f16 v[84:87], a[24:27], v[120:123], v[84:87]\n"
      "ds_read_b128 v[164:167], v203 offset:2944\n"
      "v_fma_f32 v188, |v92|, v76, v188\n"
      "v_mfma_f32_16x16x32_f16 v[84:87], a[20:23], v[116:119], v[84:87]\n"
      "v_fma_f32 v188, |v93|, v77, v188\n"
      "v_fma_f32 v188, |v94|, v78, v188\n"
      "v_mfma_f32_16x16x32_f16 v[84:87], a[16:19], v[112:115], v[84:87]\n"
      "v_fma_f32 v188, |v95|, v79, v188\n"
      "v_fma_f32 v189, |v80|, v48, 4.0\n"
      "v_mfma_f32_16x16x32_f16 v[88:91], a[32:35], v[112:115], v[24:27]\n"
      "s_waitcnt lgkmcnt(1)\n"
      "v_pk_mul_f16 v96, v128, v160\n"
      "v_pk_mul_f16 v97, v129, v161\n"
      "v_mfma_f32_16x16x32_f16 v[88:91], a[36:39], v[116:119], v[88:91]\n"
      "40:\n"
      "s_cbranch_execz 44b\n"
      "v_pk_mul_f16 v98, v130, v162\n"
      "v_mfma_f32_16x16x32_f16 v[88:91], a[40:43], v[120:123], v[88:91]\n"
      "ds_read_b128 v[168:171], v203 offset:3008\n"
      "v_pk_mul_f16 v99, v131, v163\n"
      "v_mfma_f32_16x16x32_f16 v[88:91], a[44:47], v[124:127], v[88:91]\n"
      "s_waitcnt vmcnt(1)\n"
      "v_fma_f32 v189, |v81|, v49, v189\n"
      "v_fma_f32 v189, |v82|, v50, v189\n"
      "v_mfma_f32_4x4x4_16b_bf16 v[176:179], a[216:217], v[240:241], 0\n"
      "v_fma_f32 v189, |v83|, v51, v189\n"
      "v_mfma_f32_4x4x4_16b_bf16 v[180:183], a[216:217], v[242:243], 0\n"
      "s_waitcnt lgkmcnt(1)\n"
      "v_pk_mul_f16 v100, v132, v164\n"
      "v_pk_mul_f16 v101, v133, v165\n"
      "v_mfma_f32_16x16x32_f16 v[92:95], a[60:63], v[124:127], v[28:31]\n"
      "v_pk_mul_f16 v102, v134, v166\n"
      "v_pk_mul_f16 v103, v135, v167\n"
      "v_mfma_f32_16x16x32_f16 v[92:95], a[56:59], v[120:123], v[92:95]\n"
      "v_fma_f32 v189, |v84|, v52, v189\n"
      "v_fma_f32 v189, |v85|, v53, v189\n"
      "v_mfma_f32_16x16x32_f16 v[92:95], a[52:55], v[116:119], v[92:95]\n"
      "v_fma_f32 v189, |v86|, v54, v189\n"
      "v_fma_f32 v189, |v87|, v55, v189\n"
      "v_mfma_f32_16x16x32_f16 v[92:95], a[48:51], v[112:115], v[92:95]\n"
      "v_fma_f32 v189, |v88|, v56, v189\n"
      "v_fma_f32 v189, |v89|, v57, v189\n"
      "v_mfma_f32_16x16x32_f16 v[80:83], a[64:67], v[112:115], v[32:35]\n"
      "s_waitcnt lgkmcnt(0)\n"
      "v_pk_mul_f16 v104, v136, v168\n"
      "v_pk_mul_f16 v105, v137, v169\n"
      "v_mfma_f32_16x16x32_f16 v[80:83], a[68:71], v[116:119], v[80:83]\n"
      "v_pk_mul_f16 v106, v138, v170\n"
      "v_pk_mul_f16 v107, v139, v171\n"
      "v_mfma_f32_16x16x32_f16 v[80:83], a[72:75], v[120:123], v[80:83]\n"
      "ds_read_b128 v[172:175], v203 offset:3072\n"
      "v_fma_f32 v189, |v90|, v58, v189\n"
      "v_mfma_f32_16x16x32_f16 v[80:83], a[76:79], v[124:127], v[80:83]\n"
      "v_fma_f32 v189, |v91|, v59, v189\n"
      "v_fma_f32 v189, |v92|, v60, v189\n"
      "v_mfma_f32_16x16x32_f16 v[84:87], a[92:95], v[124:127], v[36:39]\n"
      "v_cvt_pk_f16_f32 v184, v176, v177\n"
      "v_cvt_pk_f16_f32 v185, v178, v179\n"
      "v_mfma_f32_16x16x32_f16 v[84:87], a[88:91], v[120:123], v[84:87]\n"
      "v_fma_f32 v189, |v93|, v61, v189\n"
      "v_fma_f32 v189, |v94|, v62, v189\n"
      "v_mfma_f32_16x16x32_f16 v[84:87], a[84:87], v[116:119], v[84:87]\n"
      "v_fma_f32 v189, |v95|, v63, v189\n"
      "v_fma_f32 v189, |v80|, v64, v189\n"
      "v_mfma_f32_16x16x32_f16 v[84:87], a[80:83], v[112:115], v[84:87]\n"
      "v_cvt_pk_f16_f32 v186, v180, v181\n"
      "v_cvt_pk_f16_f32 v187, v182, v183\n"
      "v_mfma_f32_16x16x32_f16 v[88:91], a[96:99], v[112:115], v[40:43]\n"
      "ds_write_b128 v1, v[184:187] offset:3456\n"
      "v_mfma_f32_16x16x32_f16 v[88:91], a[100:103], v[116:119], v[88:91]\n"
      "v_fma_f32 v189, |v81|, v65, v189\n"
      "v_fma_f32 v189, |v82|, v66, v189\n"
      "v_mfma_f32_16x16x32_f16 v[88:91], a[104:107], v[120:123], v[88:91]\n"
      "s_waitcnt lgkmcnt(1)\n"
      "v_pk_mul_f16 v108, v140, v172\n"
      "v_pk_mul_f16 v109, v141, v173\n"
      "v_mfma_f32_16x16x32_f16 v[88:91], a[108:111], v[124:127], v[88:91]\n"
      "v_fma_f32 v189, |v83|, v67, v189\n"
      "v_fma_f32 v189, |v84|, v68, v189\n"
      "v_mfma_f32_16x16x32_f16 v[92:95], a[124:127], v[124:127], v[44:47]\n"
      "v_pk_mul_f16 v110, v142, v174\n"
      "v_pk_mul_f16 v111, v143, v175\n"
      "v_mfma_f32_16x16x32_f16 v[92:95], a[120:123], v[120:123], v[92:95]\n"
      "ds_read_b128 v[160:163], v203 offset:3456\n"
      "v_fma_f32 v189, |v85|, v69, v189\n"
      "v_mfma_f32_16x16x32_f16 v[92:95], a[116:119], v[116:119], v[92:95]\n"
      "v_fma_f32 v189, |v86|, v70, v189\n"
      "v_fma_f32 v189, |v87|, v71, v189\n"
      "v_mfma_f32_16x16x32_f16 v[92:95], a[112:115], v[112:115], v[92:95]\n"
      "v_fma_f32 v189, |v88|, v72, v189\n"
      "v_fma_f32 v189, |v89|, v73, v189\n"
      "v_mfma_f32_16x16x32_f16 v[80:83], a[0:3], v[96:99], v[16:19]\n"
      "v_fma_f32 v189, |v90|, v74, v189\n"
      "v_fma_f32 v189, |v91|, v75, v189\n"
      "v_mfma_f32_16x16x32_f16 v[80:83], a[4:7], v[100:103], v[80:83]\n"
      "ds_read_b128 v[164:167], v203 offset:3520\n"
      "ds_read_b128 v[168:171], v203 offset:3584\n"
      "v_mfma_f32_16x16x32_f16 v[80:83], a[8:11], v[104:107], v[80:83]\n"
      "v_fma_f32 v189, |v92|, v76, v189\n"
      "v_fma_f32 v189, |v93|, v77, v189\n"
      "v_mfma_f32_16x16x32_f16 v[80:83], a[12:15], v[108:111], v[80:83]\n"
      "v_fma_f32 v189, |v94|, v78, v189\n"
      "v_fma_f32 v189, |v95|, v79, v189\n"
      "v_mfma_f32_16x16x32_f16 v[84:87], a[28:31], v[108:111], v[20:23]\n"
      "ds_write_b32 v7, v252 offset:37376\n"
      "v_mfma_f32_16x16x32_f16 v[84:87], a[24:27], v[104:107], v[84:87]\n"
      "s_waitcnt lgkmcnt(3)\n"
      "v_pk_mul_f16 v112, v128, v160\n"
      "v_pk_mul_f16 v113, v129, v161\n"
      "v_mfma_f32_16x16x32_f16 v[84:87], a[20:23], v[100:103], v[84:87]\n"
      "ds_write_b32 v7, v192 offset:37120\n"
      "v_mfma_f32_16x16x32_f16 v[84:87], a[16:19], v[96:99], v[84:87]\n"
      "v_pk_mul_f16 v114, v130, v162\n"
      "v_pk_mul_f16 v115, v131, v163\n"
      "v_mfma_f32_16x16x32_f16 v[88:91], a[32:35], v[96:99], v[24:27]\n"
      "v_fma_f32 v190, |v80|, v48, 4.0\n"
      "v_fma_f32 v190, |v81|, v49, v190\n"
      "v_mfma_f32_16x16x32_f16 v[88:91], a[36:39], v[100:103], v[88:91]\n"
      "v_fma_f32 v190, |v82|, v50, v190\n"
      "v_fma_f32 v190, |v83|, v51, v190\n"
      "v_mfma_f32_16x16x32_f16 v[88:91], a[40:43], v[104:107], v[88:91]\n"
      "s_waitcnt lgkmcnt(3)\n"
      "v_pk_mul_f16 v116, v132, v164\n"
      "v_pk_mul_f16 v117, v133, v165\n"
      "v_mfma_f32_16x16x32_f16 v[88:91], a[44:47], v[108:111], v[88:91]\n"
      "v_pk_mul_f16 v118, v134, v166\n"
      "v_pk_mul_f16 v119, v135, v167\n"
      "v_mfma_f32_16x16x32_f16 v[92:95], a[60:63], v[108:111], v[28:31]\n"
      "v_fma_f32 v190, |v84|, v52, v190\n"
      "v_fma_f32 v190, |v85|, v53, v190\n"
      "v_mfma_f32_16x16x32_f16 v[92:95], a[56:59], v[104:107], v[92:95]\n"
      "v_fma_f32 v190, |v86|, v54, v190\n"
      "v_fma_f32 v190, |v87|, v55, v190\n"
      "v_mfma_f32_16x16x32_f16 v[92:95], a[52:55], v[100:103], v[92:95]\n"
      "s_waitcnt lgkmcnt(2)\n"
      "v_pk_mul_f16 v120, v136, v168\n"
      "v_pk_mul_f16 v121, v137, v169\n"
      "v_mfma_f32_16x16x32_f16 v[92:95], a[48:51], v[96:99], v[92:95]\n"
      "v_fma_f32 v190, |v88|, v56, v190\n"
      "v_fma_f32 v190, |v89|, v57, v190\n"
      "v_mfma_f32_16x16x32_f16 v[80:83], a[64:67], v[96:99], v[32:35]\n"
      "ds_read_b128 v[172:175], v203 offset:3648\n"
      "v_fma_f32 v190, |v90|, v58, v190\n"
      "v_mfma_f32_16x16x32_f16 v[80:83], a[68:71], v[100:103], v[80:83]\n"
      "v_fma_f32 v190, |v91|, v59, v190\n"
      "v_pk_mul_f16 v122, v138, v170\n"
      "v_mfma_f32_16x16x32_f16 v[80:83], a[72:75], v[104:107], v[80:83]\n"
      "global_load_dwordx4 v[248:251], v11, s[10:11] nt\n"
      "v_pk_mul_f16 v123, v139, v171\n"
      "v_mfma_f32_16x16x32_f16 v[80:83], a[76:79], v[108:111], v[80:83]\n"
      "v_fma_f32 v190, |v92|, v60, v190\n"
      "v_fma_f32 v190, |v93|, v61, v190\n"
      "v_mfma_f32_16x16x32_f16 v[84:87], a[92:95], v[108:111], v[36:39]\n"
      "v_fma_f32 v190, |v94|, v62, v190\n"
      "v_fma_f32 v190, |v95|, v63, v190\n"
      "v_mfma_f32_16x16x32_f16 v[84:87], a[88:91], v[104:107], v[84:87]\n"
      "v_max3_f32 v195, |v193|, |v194|, |v195|\n"
      "v_max3_f32 v194, |v253|, |v254|, |v255|\n"
      "v_mfma_f32_16x16x32_f16 v[84:87], a[84:87], v[100:103], v[84:87]\n"
      "v_fma_f32 v190, |v80|, v64, v190\n"
      "v_fma_f32 v190, |v81|, v65, v190\n"
      "v_mfma_f32_16x16x32_f16 v[84:87], a[80:83], v[96:99], v[84:87]\n"
      "v_fma_f32 v190, |v82|, v66, v190\n"
      "v_fma_f32 v190, |v83|, v67, v190\n"
      "v_mfma_f32_16x16x32_f16 v[88:91], a[96:99], v[96:99], v[40:43]\n"
      "s_waitcnt lgkmcnt(0)\n"
      "v_pk_mul_f16 v124, v140, v172\n"
      "v_pk_mul_f16 v125, v141, v173\n"
      "v_mfma_f32_16x16x32_f16 v[88:91], a[100:103], v[100:103], v[88:91]\n"
      "v_max_f32 v194, |v252|, v194\n"
      "v_max_f32 v195, |v192|, v195\n"
      "v_mfma_f32_16x16x32_f16 v[88:91], a[104:107], v[104:107], v[88:91]\n"
      "v_pk_mul_f16 v126, v142, v174\n"
      "v_pk_mul_f16 v127, v143, v175\n"
      "v_mfma_f32_16x16x32_f16 v[88:91], a[108:111], v[108:111], v[88:91]\n"
      "ds_read_b128 v[160:163], v203 offset:4032\n"
      "v_fma_f32 v190, |v84|, v68, v190\n"
      "v_mfma_f32_16x16x32_f16 v[92:95], a[124:127], v[108:111], v[44:47]\n"
      "v_fma_f32 v190, |v85|, v69, v190\n"
      "v_fma_f32 v190, |v86|, v70, v190\n"
      "v_mfma_f32_16x16x32_f16 v[92:95], a[120:123], v[104:107], v[92:95]\n"
      "global_load_dwordx4 v[252:255], v12, s[10:11] nt\n"
      "v_cmp_le_f32 vcc, 0x3ca3d70a, v194\n"
      "v_mfma_f32_16x16x32_f16 v[92:95], a[116:119], v[100:103], v[92:95]\n"
      "v_fma_f32 v190, |v87|, v71, v190\n"
      "v_fma_f32 v190, |v88|, v72, v190\n"
      "v_mfma_f32_16x16x32_f16 v[92:95], a[112:115], v[96:99], v[92:95]\n"
      "s_or_b32 s44, vcc_lo, vcc_hi\n"
      "v_fma_f32 v190, |v89|, v73, v190\n"
      "v_fma_f32 v190, |v90|, v74, v190\n"
      "v_mfma_f32_16x16x32_f16 v[80:83], a[0:3], v[112:115], v[16:19]\n"
      "v_cmp_le_f32 vcc, 0x3ca3d70a, v195\n"
      "v_max_f32 v195, v195, v194\n"
      "v_mfma_f32_16x16x32_f16 v[80:83], a[4:7], v[116:119], v[80:83]\n"
      "ds_read_b128 v[164:167], v203 offset:4096\n"
      "v_fma_f32 v190, |v91|, v75, v190\n"
      "v_mfma_f32_16x16x32_f16 v[80:83], a[8:11], v[120:123], v[80:83]\n"
      "s_or_b32 s45, vcc_lo, vcc_hi\n"
      "v_fma_f32 v190, |v92|, v76, v190\n"
      "v_fma_f32 v190, |v93|, v77, v190\n"
      "v_mfma_f32_16x16x32_f16 v[80:83], a[12:15], v[124:127], v[80:83]\n"
      "s_waitcnt lgkmcnt(1)\n"
      "v_cmp_le_f32 vcc, 0x3f000000, v195\n"
      "v_pk_mul_f16 v96, v128, v160\n"
      "v_mfma_f32_16x16x32_f16 v[84:87], a[28:31], v[124:127], v[20:23]\n"
      "s_pack_ll_b32_b16 s46, s44, s45\n"
      "v_pk_mul_f16 v97, v129, v161\n"
      "v_pk_mul_f16 v98, v130, v162\n"
      "v_mfma_f32_16x16x32_f16 v[84:87], a[24:27], v[120:123], v[84:87]\n"
      "s_pack_hh_b32_b16 s44, s44, s45\n"
      "v_fma_f32 v190, |v94|, v78, v190\n"
      "v_fma_f32 v190, |v95|, v79, v190\n"
      "v_mfma_f32_16x16x32_f16 v[84:87], a[20:23], v[116:119], v[84:87]\n"
      "s_or_b32 s44, s44, s46\n"
      "ds_read_b128 v[168:171], v203 offset:4160\n"
      "v_pk_mul_f16 v99, v131, v163\n"
      "v_mfma_f32_16x16x32_f16 v[84:87], a[16:19], v[112:115], v[84:87]\n"
      "s_cmp_eq_u64 vcc, 0\n"
      "v_fma_f32 v191, |v80|, v48, 4.0\n"
      "v_fma_f32 v191, |v81|, v49, v191\n"
      "v_mfma_f32_16x16x32_f16 v[88:91], a[32:35], v[112:115], v[24:27]\n"
      "s_cselect_b32 s45, 13, 9\n"
      "v_fma_f32 v191, |v82|, v50, v191\n"
      "v_fma_f32 v191, |v83|, v51, v191\n"
      "v_mfma_f32_16x16x32_f16 v[88:91], a[36:39], v[116:119], v[88:91]\n"
      "s_waitcnt lgkmcnt(1)\n"
      "v_pk_mul_f16 v100, v132, v164\n"
      "v_pk_mul_f16 v101, v133, v165\n"
      "v_mfma_f32_16x16x32_f16 v[88:91], a[40:43], v[120:123], v[88:91]\n"
      "s_cmp_eq_u32 s44, -1\n"
      "v_pk_mul_f16 v102, v134, v166\n"
      "v_pk_mul_f16 v103, v135, v167\n"
      "v_mfma_f32_16x16x32_f16 v[88:91], a[44:47], v[124:127], v[88:91]\n"
      "s_cselect_b32 s44, s45, 5\n"
      "v_fma_f32 v191, |v84|, v52, v191\n"
      "v_fma_f32 v191, |v85|, v53, v191\n"
      "v_mfma_f32_16x16x32_f16 v[92:95], a[60:63], v[124:127], v[28:31]\n"
      "s_min_u32 s44, s44, s52\n"
      "v_fma_f32 v191, |v86|, v54, v191\n"
      "v_fma_f32 v191, |v87|, v55, v191\n"
      "v_mfma_f32_16x16x32_f16 v[92:95], a[56:59], v[120:123], v[92:95]\n"
      "s_movk_i32 s31, 0x2000\n"
      "ds_read_b128 v[172:175], v203 offset:4224\n"
      "v_mov_b32 v223, s44\n"
      "v_mfma_f32_16x16x32_f16 v[92:95], a[52:55], v[116:119], v[92:95]\n"
      "s_mov_b32 s35, 1\n"
      "ds_write_b32 v2, v223\n"
      "v_mfma_f32_16x16x32_f16 v[92:95], a[48:51], v[112:115], v[92:95]\n"
      "s_waitcnt lgkmcnt(2)\n"
      "v_pk_mul_f16 v104, v136, v168\n"
      "v_pk_mul_f16 v105, v137, v169\n"
      "v_mfma_f32_16x16x32_f16 v[80:83], a[64:67], v[112:115], v[32:35]\n"
      "v_fma_f32 v191, |v88|, v56, v191\n"
      "v_fma_f32 v191, |v89|, v57, v191\n"
      "v_mfma_f32_16x16x32_f16 v[80:83], a[68:71], v[116:119], v[80:83]\n"
      "v_fma_f32 v191, |v90|, v58, v191\n"
      "v_fma_f32 v191, |v91|, v59, v191\n"
      "v_mfma_f32_16x16x32_f16 v[80:83], a[72:75], v[120:123], v[80:83]\n"
      "v_pk_mul_f16 v106, v138, v170\n"
      "v_pk_mul_f16 v107, v139, v171\n"
      "v_mfma_f32_16x16x32_f16 v[80:83], a[76:79], v[124:127], v[80:83]\n"
      "v_fma_f32 v191, |v92|, v60, v191\n"
      "v_fma_f32 v191, |v93|, v61, v191\n"
      "v_mfma_f32_16x16x32_f16 v[84:87], a[92:95], v[124:127], v[36:39]\n"
      "v_fma_f32 v191, |v94|, v62, v191\n"
      "v_fma_f32 v191, |v95|, v63, v191\n"
      "v_mfma_f32_16x16x32_f16 v[84:87], a[88:91], v[120:123], v[84:87]\n"
      "s_waitcnt lgkmcnt(1)\n"
      "v_pk_mul_f16 v108, v140, v172\n"
      "v_pk_mul_f16 v109, v141, v173\n"
      "v_mfma_f32_16x16x32_f16 v[84:87], a[84:87], v[116:119], v[84:87]\n"
      "v_fma_f32 v191, |v80|, v64, v191\n"
      "v_fma_f32 v191, |v81|, v65, v191\n"
      "v_mfma_f32_16x16x32_f16 v[84:87], a[80:83], v[112:115], v[84:87]\n"
      "v_fma_f32 v191, |v82|, v66, v191\n"
      "v_fma_f32 v191, |v83|, v67, v191\n"
      "v_mfma_f32_16x16x32_f16 v[88:91], a[96:99], v[112:115], v[40:43]\n"
      "ds_read_b128 v[144:147], v201 offset:2304\n"
      "v_pk_mul_f16 v110, v142, v174\n"
      "v_mfma_f32_16x16x32_f16 v[88:91], a[100:103], v[116:119], v[88:91]\n"
      "ds_read_b128 v[160:163], v202 offset:2304\n"
      "v_pk_mul_f16 v111, v143, v175\n"
      "v_mfma_f32_16x16x32_f16 v[88:91], a[104:107], v[120:123], v[88:91]\n"
      "v_fma_f32 v191, |v84|, v68, v191\n"
      "v_fma_f32 v191, |v85|, v69, v191\n"
      "v_mfma_f32_16x16x32_f16 v[88:91], a[108:111], v[124:127], v[88:91]\n"
      "v_fma_f32 v191, |v86|, v70, v191\n"
      "v_fma_f32 v191, |v87|, v71, v191\n"
      "v_mfma_f32_16x16x32_f16 v[92:95], a[124:127], v[124:127], v[44:47]\n"
      "ds_read_b128 v[148:151], v201 offset:2368\n"
      "ds_read_b128 v[164:167], v202 offset:2368\n"
      "v_mfma_f32_16x16x32_f16 v[92:95], a[120:123], v[120:123], v[92:95]\n"
      "s_waitcnt vmcnt(2)\n"
      "ds_read_b128 v[152:155], v201 offset:2432\n"
      "v_lshlrev_b32 v176, 16, v244\n"
      "v_mfma_f32_16x16x32_f16 v[92:95], a[116:119], v[116:119], v[92:95]\n"
      "v_fma_f32 v191, |v88|, v72, v191\n"
      "v_fma_f32 v191, |v89|, v73, v191\n"
      "v_mfma_f32_16x16x32_f16 v[92:95], a[112:115], v[112:115], v[92:95]\n"
      "v_fma_f32 v191, |v90|, v74, v191\n"
      "v_fma_f32 v191, |v91|, v75, v191\n"
      "v_mfma_f32_16x16x32_f16 v[80:83], a[0:3], v[96:99], v[16:19]\n"
      "ds_read_b128 v[168:171], v202 offset:2432\n"
      "v_and_b32 v177, s43, v244\n"
      "v_mfma_f32_16x16x32_f16 v[80:83], a[4:7], v[100:103], v[80:83]\n"
      "s_waitcnt lgkmcnt(4)\n"
      "v_pk_mul_f16 v112, v144, v160\n"
      "v_pk_mul_f16 v113, v145, v161\n"
      "v_mfma_f32_16x16x32_f16 v[80:83], a[8:11], v[104:107], v[80:83]\n"
      "v_fma_f32 v191, |v92|, v76, v191\n"
      "v_fma_f32 v191, |v93|, v77, v191\n"
      "v_mfma_f32_16x16x32_f16 v[80:83], a[12:15], v[108:111], v[80:83]\n"
      "v_fma_f32 v191, |v94|, v78, v191\n"
      "v_fma_f32 v191, |v95|, v79, v191\n"
      "v_mfma_f32_16x16x32_f16 v[84:87], a[28:31], v[108:111], v[20:23]\n"
      "v_pk_mul_f16 v114, v146, v162\n"
      "v_pk_mul_f16 v115, v147, v163\n"
      "v_mfma_f32_16x16x32_f16 v[84:87], a[24:27], v[104:107], v[84:87]\n"
      "v_permlane32_swap_b32 v189, v191\n"
      "v_mfma_f32_16x16x32_f16 v[84:87], a[20:23], v[100:103], v[84:87]\n"
      "v_fma_f32 v192, |v80|, v48, 4.0\n"
      "v_fma_f32 v192, |v81|, v49, v192\n"
      "v_mfma_f32_16x16x32_f16 v[84:87], a[16:19], v[96:99], v[84:87]\n"
      "v_fma_f32 v192, |v82|, v50, v192\n"
      "v_fma_f32 v192, |v83|, v51, v192\n"
      "v_mfma_f32_16x16x32_f16 v[88:91], a[32:35], v[96:99], v[24:27]\n"
      "s_waitcnt lgkmcnt(2)\n"
      "v_lshlrev_b32 v178, 16, v245\n"
      "v_pk_mul_f16 v116, v148, v164\n"
      "v_mfma_f32_16x16x32_f16 v[88:91], a[36:39], v[100:103], v[88:91]\n"
      "v_and_b32 v179, s43, v245\n"
      "v_pk_mul_f16 v117, v149, v165\n"
      "v_mfma_f32_16x16x32_f16 v[88:91], a[40:43], v[104:107], v[88:91]\n"
      "ds_read_b128 v[156:159], v201 offset:2496\n"
      "v_lshlrev_b32 v180, 16, v246\n"
      "v_mfma_f32_16x16x32_f16 v[88:91], a[44:47], v[108:111], v[88:91]\n"
      "v_pk_mul_f16 v118, v150, v166\n"
      "v_pk_mul_f16 v119, v151, v167\n"
      "v_mfma_f32_16x16x32_f16 v[92:95], a[60:63], v[108:111], v[28:31]\n"
      "v_fma_f32 v192, |v84|, v52, v192\n"
      "v_fma_f32 v192, |v85|, v53, v192\n"
      "v_mfma_f32_16x16x32_f16 v[92:95], a[56:59], v[104:107], v[92:95]\n"
      "v_and_b32 v181, s43, v246\n"
      "v_fma_f32 v192, |v86|, v54, v192\n"
      "v_mfma_f32_16x16x32_f16 v[92:95], a[52:55], v[100:103], v[92:95]\n"
      "v_fma_f32 v192, |v87|, v55, v192\n"
      "v_fma_f32 v192, |v88|, v56, v192\n"
      "v_mfma_f32_16x16x32_f16 v[92:95], a[48:51], v[96:99], v[92:95]\n"
      "v_lshlrev_b32 v182, 16, v247\n"
      "v_and_b32 v183, s43, v247\n"
      "v_mfma_f32_16x16x32_f16 v[80:83], a[64:67], v[96:99], v[32:35]\n"
      "v_cvt_pk_f16_f32 v184, v176, v177\n"
      "v_cvt_pk_f16_f32 v185, v178, v179\n"
      "v_mfma_f32_16x16x32_f16 v[80:83], a[68:71], v[100:103], v[80:83]\n"
      "s_waitcnt lgkmcnt(1)\n"
      "v_pk_mul_f16 v120, v152, v168\n"
      "v_pk_mul_f16 v121, v153, v169\n"
      "v_mfma_f32_16x16x32_f16 v[80:83], a[72:75], v[104:107], v[80:83]\n"
      "v_fma_f32 v192, |v89|, v57, v192\n"
      "v_fma_f32 v192, |v90|, v58, v192\n"
      "v_mfma_f32_16x16x32_f16 v[80:83], a[76:79], v[108:111], v[80:83]\n"
      "ds_read_b128 v[172:175], v202 offset:2496\n"
      "v_fma_f32 v192, |v91|, v59, v192\n"
      "v_mfma_f32_16x16x32_f16 v[84:87], a[92:95], v[108:111], v[36:39]\n"
      "v_cvt_pk_f16_f32 v186, v180, v181\n"
      "v_cvt_pk_f16_f32 v187, v182, v183\n"
      "v_mfma_f32_16x16x32_f16 v[84:87], a[88:91], v[104:107], v[84:87]\n"
      "v_pk_mul_f16 v122, v154, v170\n"
      "v_pk_mul_f16 v123, v155, v171\n"
      "v_mfma_f32_16x16x32_f16 v[84:87], a[84:87], v[100:103], v[84:87]\n"
      "v_pk_mul_f16 v184, v184, s31 op_sel_hi:[1,0]\n"
      "v_pk_mul_f16 v185, v185, s31 op_sel_hi:[1,0]\n"
      "v_mfma_f32_16x16x32_f16 v[84:87], a[80:83], v[96:99], v[84:87]\n"
      "v_fma_f32 v192, |v92|, v60, v192\n"
      "v_fma_f32 v192, |v93|, v61, v192\n"
      "v_mfma_f32_16x16x32_f16 v[88:91], a[96:99], v[96:99], v[40:43]\n"
      "v_fma_f32 v192, |v94|, v62, v192\n"
      "v_fma_f32 v192, |v95|, v63, v192\n"
      "v_mfma_f32_16x16x32_f16 v[88:91], a[100:103], v[100:103], v[88:91]\n"
      "v_pk_mul_f16 v186, v186, s31 op_sel_hi:[1,0]\n"
      "v_pk_mul_f16 v187, v187, s31 op_sel_hi:[1,0]\n"
      "v_mfma_f32_16x16x32_f16 v[88:91], a[104:107], v[104:107], v[88:91]\n"
      "ds_write_b128 v1, v[184:187] offset:4608\n"
      "v_mfma_f32_16x16x32_f16 v[88:91], a[108:111], v[108:111], v[88:91]\n"
      "v_fma_f32 v192, |v80|, v64, v192\n"
      "v_fma_f32 v192, |v81|, v65, v192\n"
      "v_mfma_f32_16x16x32_f16 v[92:95], a[124:127], v[108:111], v[44:47]\n"
      "s_waitcnt lgkmcnt(1)\n"
      "v_pk_mul_f16 v124, v156, v172\n"
      "v_pk_mul_f16 v125, v157, v173\n"
      "v_mfma_f32_16x16x32_f16 v[92:95], a[120:123], v[104:107], v[92:95]\n"
      "v_fma_f32 v192, |v82|, v66, v192\n"
      "v_fma_f32 v192, |v83|, v67, v192\n"
      "v_mfma_f32_16x16x32_f16 v[92:95], a[116:119], v[100:103], v[92:95]\n"
      "v_pk_mul_f16 v126, v158, v174\n"
      "v_pk_mul_f16 v127, v159, v175\n"
      "v_mfma_f32_16x16x32_f16 v[92:95], a[112:115], v[96:99], v[92:95]\n"
      "ds_read_b128 v[160:163], v203 offset:4608\n"
      "v_fma_f32 v192, |v84|, v68, v192\n"
      "v_mfma_f32_16x16x32_f16 v[80:83], a[0:3], v[112:115], v[16:19]\n"
      "v_fma_f32 v192, |v85|, v69, v192\n"
      "v_fma_f32 v192, |v86|, v70, v192\n"
      "v_mfma_f32_16x16x32_f16 v[80:83], a[4:7], v[116:119], v[80:83]\n"
      "v_fma_f32 v192, |v87|, v71, v192\n"
      "v_fma_f32 v192, |v88|, v72, v192\n"
      "v_mfma_f32_16x16x32_f16 v[80:83], a[8:11], v[120:123], v[80:83]\n"
      "v_fma_f32 v192, |v89|, v73, v192\n"
      "v_fma_f32 v192, |v90|, v74, v192\n"
      "v_mfma_f32_16x16x32_f16 v[80:83], a[12:15], v[124:127], v[80:83]\n"
      "v_fma_f32 v192, |v91|, v75, v192\n"
      "v_fma_f32 v192, |v92|, v76, v192\n"
      "v_mfma_f32_16x16x32_f16 v[84:87], a[28:31], v[124:127], v[20:23]\n"
      "ds_read_b128 v[164:167], v203 offset:4672\n"
      "v_fma_f32 v192, |v93|, v77, v192\n"
      "v_mfma_f32_16x16x32_f16 v[84:87], a[24:27], v[120:123], v[84:87]\n"
      "v_fma_f32 v192, |v94|, v78, v192\n"
      "v_fma_f32 v192, |v95|, v79, v192\n"
      "v_mfma_f32_16x16x32_f16 v[84:87], a[20:23], v[116:119], v[84:87]\n"
      "ds_read_b128 v[168:171], v203 offset:4736\n"
      "v_min3_f32 v226, |v228|, |v233|, |v238|\n"
      "v_mfma_f32_16x16x32_f16 v[84:87], a[16:19], v[112:115], v[84:87]\n"
      "s_waitcnt lgkmcnt(2)\n"
      "v_pk_mul_f16 v96, v128, v160\n"
      "v_pk_mul_f16 v97, v129, v161\n"
      "v_mfma_f32_16x16x32_f16 v[88:91], a[32:35], v[112:115], v[24:27]\n"
      "v_pk_mul_f16 v98, v130, v162\n"
      "v_pk_mul_f16 v99, v131, v163\n"
      "v_mfma_f32_16x16x32_f16 v[88:91], a[36:39], v[116:119], v[88:91]\n"
      "v_fma_f32 v193, |v80|, v48, 4.0\n"
      "v_fma_f32 v193, |v81|, v49, v193\n"
      "v_mfma_f32_16x16x32_f16 v[88:91], a[40:43], v[120:123], v[88:91]\n"
      "s_waitcnt vmcnt(0)\n"
      "v_min3_f32 v226, |v243|, |v244|, v226\n"
      "v_min3_f32 v226, |v249|, |v254|, v226\n"
      "v_mfma_f32_16x16x32_f16 v[88:91], a[44:47], v[124:127], v[88:91]\n"
      "v_fma_f32 v193, |v82|, v50, v193\n"
      "v_fma_f32 v193, |v83|, v51, v193\n"
      "v_mfma_f32_16x16x32_f16 v[92:95], a[60:63], v[124:127], v[28:31]\n"
      "s_waitcnt lgkmcnt(1)\n"
      "v_pk_mul_f16 v100, v132, v164\n"
      "v_pk_mul_f16 v101, v133, v165\n"
      "v_mfma_f32_16x16x32_f16 v[92:95], a[56:59], v[120:123], v[92:95]\n"
      "v_cmp_le_f32 vcc, 0x3d800000, v226\n"
      "v_pk_mul_f16 v102, v134, v166\n"
      "v_mfma_f32_16x16x32_f16 v[92:95], a[52:55], v[116:119], v[92:95]\n"
      "v_fma_f32 v193, |v84|, v52, v193\n"
      "v_fma_f32 v193, |v85|, v53, v193\n"
      "v_mfma_f32_16x16x32_f16 v[92:95], a[48:51], v[112:115], v[92:95]\n"
      "s_waitcnt lgkmcnt(0)\n"
      "v_pk_mul_f16 v103, v135, v167\n"
      "v_pk_mul_f16 v104, v136, v168\n"
      "v_mfma_f32_16x16x32_f16 v[80:83], a[64:67], v[112:115], v[32:35]\n"
      "s_quadmask_b64 s[44:45], vcc\n"
      "v_fma_f32 v193, |v86|, v54, v193\n"
      "v_fma_f32 v193, |v87|, v55, v193\n"
      "v_mfma_f32_16x16x32_f16 v[80:83], a[68:71], v[116:119], v[80:83]\n"
      "s_quadmask_b32 s30, s44\n"
      "v_fma_f32 v193, |v88|, v56, v193\n"
      "v_fma_f32 v193, |v89|, v57, v193\n"
      "v_mfma_f32_16x16x32_f16 v[80:83], a[72:75], v[120:123], v[80:83]\n"
      "s_xor_b32 s30, s30, 15\n"
      "v_pk_mul_f16 v105, v137, v169\n"
      "v_pk_mul_f16 v106, v138, v170\n"
      "v_mfma_f32_16x16x32_f16 v[80:83], a[76:79], v[124:127], v[80:83]\n"
      "s_or_b32 s30, s30, s41\n"
      "ds_read_b128 v[172:175], v203 offset:4800\n"
      "v_fma_f32 v193, |v90|, v58, v193\n"
      "v_mfma_f32_16x16x32_f16 v[84:87], a[92:95], v[124:127], v[36:39]\n"
      "s_cmp_lg_u32 s55, 0\n"
      "v_fma_f32 v193, |v91|, v59, v193\n"
      "v_fma_f32 v193, |v92|, v60, v193\n"
      "v_mfma_f32_16x16x32_f16 v[84:87], a[88:91], v[120:123], v[84:87]\n"
      "s_cselect_b32 s30, 0, s30\n"
      "v_pk_mul_f16 v107, v139, v171\n"
      "v_fma_f32 v193, |v93|, v61, v193\n"
      "v_mfma_f32_16x16x32_f16 v[84:87], a[84:87], v[116:119], v[84:87]\n"
      "v_fma_f32 v193, |v94|, v62, v193\n"
      "v_fma_f32 v193, |v95|, v63, v193\n"
      "v_mfma_f32_16x16x32_f16 v[84:87], a[80:83], v[112:115], v[84:87]\n"
      "v_fma_f32 v193, |v80|, v64, v193\n"
      "v_fma_f32 v193, |v81|, v65, v193\n"
      "v_mfma_f32_16x16x32_f16 v[88:91], a[96:99], v[112:115], v[40:43]\n"
      "v_fma_f32 v193, |v82|, v66, v193\n"
      "v_fma_f32 v193, |v83|, v67, v193\n"
      "v_mfma_f32_16x16x32_f16 v[88:91], a[100:103], v[116:119], v[88:91]\n"
      "ds_read_b32 v224, v227 offset:37632\n"
      "v_add_f32 v189, v189, v191\n"
      "v_mfma_f32_16x16x32_f16 v[88:91], a[104:107], v[120:123], v[88:91]\n"
      "v_permlane32_swap_b32 v190, v192\n"
      "v_mfma_f32_16x16x32_f16 v[88:91], a[108:111], v[124:127], v[88:91]\n"
      "s_waitcnt lgkmcnt(1)\n"
      "v_pk_mul_f16 v108, v140, v172\n"
      "v_pk_mul_f16 v109, v141, v173\n"
      "v_mfma_f32_16x16x32_f16 v[92:95], a[124:127], v[124:127], v[44:47]\n"
      "v_pk_mul_f16 v110, v142, v174\n"
      "v_pk_mul_f16 v111, v143, v175\n"
      "v_mfma_f32_16x16x32_f16 v[92:95], a[120:123], v[120:123], v[92:95]\n"
      "v_fma_f32 v193, |v84|, v68, v193\n"
      "v_fma_f32 v193, |v85|, v69, v193\n"
      "v_mfma_f32_16x16x32_f16 v[92:95], a[116:119], v[116:119], v[92:95]\n"
      "v_fma_f32 v193, |v86|, v70, v193\n"
      "v_fma_f32 v193, |v87|, v71, v193\n"
      "v_mfma_f32_16x16x32_f16 v[92:95], a[112:115], v[112:115], v[92:95]\n"
      "v_fma_f32 v193, |v88|, v72, v193\n"
      "v_fma_f32 v193, |v89|, v73, v193\n"
      "v_mfma_f32_16x16x32_f16 v[80:83], a[0:3], v[96:99], v[16:19]\n"
      "v_fma_f32 v193, |v90|, v74, v193\n"
      "v_fma_f32 v193, |v91|, v75, v193\n"
      "v_mfma_f32_16x16x32_f16 v[80:83], a[4:7], v[100:103], v[80:83]\n"
      "ds_read_b64 v[176:177], v5 offset:37120\n"
      "v_pk_mul_f16 v112, v144, v160\n"
      "v_mfma_f32_16x16x32_f16 v[80:83], a[8:11], v[104:107], v[80:83]\n"
      "ds_read_b32 v226, v213 offset:12480\n"
      "v_fma_f32 v193, |v92|, v76, v193\n"
      "v_mfma_f32_16x16x32_f16 v[80:83], a[12:15], v[108:111], v[80:83]\n"
      "v_fma_f32 v193, |v93|, v77, v193\n"
      "v_fma_f32 v193, |v94|, v78, v193\n"
      "v_mfma_f32_16x16x32_f16 v[84:87], a[28:31], v[108:111], v[20:23]\n"
      "v_pk_mul_f16 v113, v145, v161\n"
      "v_pk_mul_f16 v114, v146, v162\n"
      "v_mfma_f32_16x16x32_f16 v[84:87], a[24:27], v[104:107], v[84:87]\n"
      "s_waitcnt lgkmcnt(2)\n"
      "v_cmp_ne_u32 vcc, 13, v224\n"
      "v_fma_f32 v193, |v95|, v79, v193\n"
      "v_mfma_f32_16x16x32_f16 v[84:87], a[20:23], v[100:103], v[84:87]\n"
      "v_pk_mul_f16 v115, v147, v163\n"
      "v_pk_mul_f16 v116, v148, v164\n"
      "v_mfma_f32_16x16x32_f16 v[84:87], a[16:19], v[96:99], v[84:87]\n"
      "s_or_b32 s45, vcc_lo, s30\n"
      "v_permlane32_swap_b32 v188, v193\n"
      "v_mfma_f32_16x16x32_f16 v[88:91], a[32:35], v[96:99], v[24:27]\n"
      "v_add_f32 v190, v190, v192\n"
      "v_add_f32 v188, v188, v193\n"
      "v_mfma_f32_16x16x32_f16 v[88:91], a[36:39], v[100:103], v[88:91]\n"
      "v_fma_f32 v194, |v80|, v48, 4.0\n"
      "v_fma_f32 v194, |v81|, v49, v194\n"
      "v_mfma_f32_16x16x32_f16 v[88:91], a[40:43], v[104:107], v[88:91]\n"
      "s_waitcnt lgkmcnt(0)\n"
      "v_max_f16_sdwa v226, |v226|, |v226| dst_sel:DWORD dst_unused:UNUSED_PAD src0_sel:WORD_0 src1_sel:WORD_1\n"
      "v_cmp_le_f16 vcc, 0x6400, v226\n"
      "v_mfma_f32_16x16x32_f16 v[88:91], a[44:47], v[108:111], v[88:91]\n"
      "v_permlane16_swap_b32 v189, v190\n"
      "v_mfma_f32_16x16x32_f16 v[92:95], a[60:63], v[108:111], v[28:31]\n"
      "s_or_b32 vcc_lo, vcc_lo, s45\n"
      "v_fma_f32 v194, |v82|, v50, v194\n"
      "v_fma_f32 v194, |v83|, v51, v194\n"
      "v_mfma_f32_16x16x32_f16 v[92:95], a[56:59], v[104:107], v[92:95]\n"
      "s_cmp_lg_u64 vcc, 0\n"
      "v_pk_mul_f16 v117, v149, v165\n"
      "v_pk_mul_f16 v118, v150, v166\n"
      "v_mfma_f32_16x16x32_f16 v[92:95], a[52:55], v[100:103], v[92:95]\n"
      "v_add_f32 v189, v189, v190\n"
      "v_fma_f32 v194, |v84|, v52, v194\n"
      "v_mfma_f32_16x16x32_f16 v[92:95], a[48:51], v[96:99], v[92:95]\n"
      "s_cbranch_scc1 5f\n"
      "4:\n"
      "v_mfma_f32_16x16x32_f16 v[80:83], a[64:67], v[96:99], v[32:35]\n"
      "v_fma_f32 v194, |v85|, v53, v194\n"
      "v_pk_mul_f16 v119, v151, v167\n"
      "v_mfma_f32_16x16x32_f16 v[80:83], a[68:71], v[100:103], v[80:83]\n"
      "v_cvt_pk_f16_f32 v176, v176, v177\n"
      "v_fma_f32 v194, |v86|, v54, v194\n"
      "v_mfma_f32_16x16x32_f16 v[80:83], a[72:75], v[104:107], v[80:83]\n"
      "41:\n"
      "s_cbranch_execz 44b\n"
      "v_fma_f32 v194, |v87|, v55, v194\n"
      "v_mfma_f32_16x16x32_f16 v[80:83], a[76:79], v[108:111], v[80:83]\n"
      "s_lshl_b32 s49, s40, 1\n"
      "ds_write_b32 v213, v176 offset:11904\n"
      "v_mfma_f32_16x16x32_f16 v[84:87], a[92:95], v[108:111], v[36:39]\n"
      "s_add_u32 s51, s57, s49\n"
      "v_pk_mul_f16 v120, v152, v168\n"
      "v_fma_f32 v194, |v88|, v56, v194\n"
      "v_mfma_f32_16x16x32_f16 v[84:87], a[88:91], v[104:107], v[84:87]\n"
      "s_add_u32 s51, s51, s58\n"
      "v_fma_f32 v194, |v89|, v57, v194\n"
      "v_pk_mul_f16 v121, v153, v169\n"
      "v_mfma_f32_16x16x32_f16 v[84:87], a[84:87], v[100:103], v[84:87]\n"
      "s_add_u32 s51, s51, s56\n"
      "v_fma_f32 v194, |v90|, v58, v194\n"
      "v_fma_f32 v194, |v91|, v59, v194\n"
      "v_mfma_f32_16x16x32_f16 v[84:87], a[80:83], v[96:99], v[84:87]\n"
      "s_sub_u32 s51, 0, s51\n"
      "v_pk_mul_f16 v122, v154, v170\n"
      "v_pk_mul_f16 v123, v155, v171\n"
      "v_mfma_f32_16x16x32_f16 v[88:91], a[96:99], v[96:99], v[40:43]\n"
      "s_add_u32 s49, s59, s49\n"
      "v_fma_f32 v194, |v92|, v60, v194\n"
      "v_fma_f32 v194, |v93|, v61, v194\n"
      "v_mfma_f32_16x16x32_f16 v[88:91], a[100:103], v[100:103], v[88:91]\n"
      "s_sub_u32 s39, 5, s49\n"
      "v_fma_f32 v194, |v94|, v62, v194\n"
      "v_fma_f32 v194, |v95|, v63, v194\n"
      "v_mfma_f32_16x16x32_f16 v[88:91], a[104:107], v[104:107], v[88:91]\n"
      "v_fma_f32 v194, |v80|, v64, v194\n"
      "v_fma_f32 v194, |v81|, v65, v194\n"
      "v_mfma_f32_16x16x32_f16 v[88:91], a[108:111], v[108:111], v[88:91]\n"
      "v_pk_mul_f16 v124, v156, v172\n"
      "v_pk_mul_f16 v125, v157, v173\n"
      "v_mfma_f32_16x16x32_f16 v[92:95], a[124:127], v[108:111], v[44:47]\n"
      "v_fma_f32 v194, |v82|, v66, v194\n"
      "v_fma_f32 v194, |v83|, v67, v194\n"
      "v_mfma_f32_16x16x32_f16 v[92:95], a[120:123], v[104:107], v[92:95]\n"
      "v_pk_mul_f16 v126, v158, v174\n"
      "v_pk_mul_f16 v127, v159, v175\n"
      "v_mfma_f32_16x16x32_f16 v[92:95], a[116:119], v[100:103], v[92:95]\n"
      "ds_read_b128 v[160:163], v203 offset:5184\n"
      "v_ldexp_f32 v200, v200, s51\n"
      "v_mfma_f32_16x16x32_f16 v[92:95], a[112:115], v[96:99], v[92:95]\n"
      "v_fma_f32 v194, |v84|, v68, v194\n"
      "v_fma_f32 v194, |v85|, v69, v194\n"
      "v_mfma_f32_16x16x32_f16 v[80:83], a[0:3], v[112:115], v[16:19]\n"
      "v_fma_f32 v194, |v86|, v70, v194\n"
      "v_fma_f32 v194, |v87|, v71, v194\n"
      "v_mfma_f32_16x16x32_f16 v[80:83], a[4:7], v[116:119], v[80:83]\n"
      "ds_read_b128 v[164:167], v203 offset:5248\n"
      "v_fma_f32 v194, |v88|, v72, v194\n"
      "v_mfma_f32_16x16x32_f16 v[80:83], a[8:11], v[120:123], v[80:83]\n"
      "v_fma_f32 v194, |v89|, v73, v194\n"
      "v_fma_f32 v194, |v90|, v74, v194\n"
      "v_mfma_f32_16x16x32_f16 v[80:83], a[12:15], v[124:127], v[80:83]\n"
      "v_fma_f32 v194, |v91|, v75, v194\n"
      "v_fma_f32 v194, |v92|, v76, v194\n"
      "v_mfma_f32_4x4x4_16b_bf16 v[176:179], a[216:217], v[248:249], 0\n"
      "v_fma_f32 v194, |v93|, v77, v194\n"
      "v_mfma_f32_4x4x4_16b_bf16 v[180:183], a[216:217], v[250:251], 0\n"
      "v_fma_f32 v194, |v94|, v78, v194\n"
      "v_mfma_f32_4x4x4_16b_bf16 v[228:231], a[216:217], v[252:253], 0\n"
      "v_fma_f32 v194, |v95|, v79, v194\n"
      "v_mfma_f32_4x4x4_16b_bf16 v[232:235], a[216:217], v[254:255], 0\n"
      "s_waitcnt lgkmcnt(1)\n"
      "v_pk_mul_f16 v96, v128, v160\n"
      "v_pk_mul_f16 v97, v129, v161\n"
      "v_mfma_f32_16x16x32_f16 v[84:87], a[28:31], v[124:127], v[20:23]\n"
      "v_pk_mul_f16 v98, v130, v162\n"
      "v_pk_mul_f16 v99, v131, v163\n"
      "v_mfma_f32_16x16x32_f16 v[84:87], a[24:27], v[120:123], v[84:87]\n"
      "s_waitcnt lgkmcnt(0)\n"
      "ds_read_b128 v[168:171], v203 offset:5312\n"
      "v_pk_mul_f16 v100, v132, v164\n"
      "v_mfma_f32_16x16x32_f16 v[84:87], a[20:23], v[116:119], v[84:87]\n"
      "v_pk_mul_f16 v101, v133, v165\n"
      "v_pk_mul_f16 v102, v134, v166\n"
      "v_mfma_f32_16x16x32_f16 v[84:87], a[16:19], v[112:115], v[84:87]\n"
      "v_fma_f32 v195, |v80|, v48, 4.0\n"
      "v_fma_f32 v195, |v81|, v49, v195\n"
      "v_mfma_f32_16x16x32_f16 v[88:91], a[32:35], v[112:115], v[24:27]\n"
      "v_pk_mul_f16 v103, v135, v167\n"
      "v_fma_f32 v195, |v82|, v50, v195\n"
      "v_mfma_f32_16x16x32_f16 v[88:91], a[36:39], v[116:119], v[88:91]\n"
      "ds_read_b128 v[172:175], v203 offset:5376\n"
      "v_fma_f32 v195, |v83|, v51, v195\n"
      "v_mfma_f32_16x16x32_f16 v[88:91], a[40:43], v[120:123], v[88:91]\n"
      "v_fma_f32 v195, |v84|, v52, v195\n"
      "v_fma_f32 v195, |v85|, v53, v195\n"
      "v_mfma_f32_16x16x32_f16 v[88:91], a[44:47], v[124:127], v[88:91]\n"
      "v_fma_f32 v195, |v86|, v54, v195\n"
      "v_fma_f32 v195, |v87|, v55, v195\n"
      "v_mfma_f32_16x16x32_f16 v[92:95], a[60:63], v[124:127], v[28:31]\n"
      "v_add_u32_sdwa v219, s38, v197 dst_sel:DWORD dst_unused:UNUSED_PAD src0_sel:DWORD src1_sel:WORD_1\n"
      "v_add_u32_sdwa v220, s38, v198 dst_sel:DWORD dst_unused:UNUSED_PAD src0_sel:DWORD src1_sel:WORD_0\n"
      "v_mfma_f32_16x16x32_f16 v[92:95], a[56:59], v[120:123], v[92:95]\n"
      "s_waitcnt lgkmcnt(1)\n"
      "v_pk_mul_f16 v104, v136, v168\n"
      "v_pk_mul_f16 v105, v137, v169\n"
      "v_mfma_f32_16x16x32_f16 v[92:95], a[52:55], v[116:119], v[92:95]\n"
      "v_pk_mul_f16 v106, v138, v170\n"
      "v_pk_mul_f16 v107, v139, v171\n"
      "v_mfma_f32_16x16x32_f16 v[92:95], a[48:51], v[112:115], v[92:95]\n"
      "v_fma_f32 v195, |v88|, v56, v195\n"
      "v_fma_f32 v195, |v89|, v57, v195\n"
      "v_mfma_f32_16x16x32_f16 v[80:83], a[64:67], v[112:115], v[32:35]\n"
      "v_fma_f32 v195, |v90|, v58, v195\n"
      "v_fma_f32 v195, |v91|, v59, v195\n"
      "v_mfma_f32_16x16x32_f16 v[80:83], a[68:71], v[116:119], v[80:83]\n"
      "s_waitcnt lgkmcnt(0)\n"
      "v_pk_mul_f16 v108, v140, v172\n"
      "v_pk_mul_f16 v109, v141, v173\n"
      "v_mfma_f32_16x16x32_f16 v[80:83], a[72:75], v[120:123], v[80:83]\n"
      "v_fma_f32 v195, |v92|, v60, v195\n"
      "v_fma_f32 v195, |v93|, v61, v195\n"
      "v_mfma_f32_16x16x32_f16 v[80:83], a[76:79], v[124:127], v[80:83]\n"
      "v_fma_f32 v195, |v94|, v62, v195\n"
      "v_fma_f32 v195, |v95|, v63, v195\n"
      "v_mfma_f32_16x16x32_f16 v[84:87], a[92:95], v[124:127], v[36:39]\n"
      "v_pk_mul_f16 v110, v142, v174\n"
      "v_pk_mul_f16 v111, v143, v175\n"
      "v_mfma_f32_16x16x32_f16 v[84:87], a[88:91], v[120:123], v[84:87]\n"
      "v_add_u32_sdwa v221, s38, v198 dst_sel:DWORD dst_unused:UNUSED_PAD src0_sel:DWORD src1_sel:WORD_1\n"
      "v_add_u32_sdwa v216, s38, v196 dst_sel:DWORD dst_unused:UNUSED_PAD src0_sel:DWORD src1_sel:WORD_0\n"
      "v_mfma_f32_16x16x32_f16 v[84:87], a[84:87], v[116:119], v[84:87]\n"
      "v_fma_f32 v195, |v80|, v64, v195\n"
      "v_fma_f32 v195, |v81|, v65, v195\n"
      "v_mfma_f32_16x16x32_f16 v[84:87], a[80:83], v[112:115], v[84:87]\n"
      "v_fma_f32 v195, |v82|, v66, v195\n"
      "v_fma_f32 v195, |v83|, v67, v195\n"
      "v_mfma_f32_16x16x32_f16 v[88:91], a[96:99], v[112:115], v[40:43]\n"
      "v_cvt_pk_f16_f32 v184, v176, v177\n"
      "v_cvt_pk_f16_f32 v185, v178, v179\n"
      "v_mfma_f32_16x16x32_f16 v[88:91], a[100:103], v[116:119], v[88:91]\n"
      "v_cvt_pk_f16_f32 v186, v180, v181\n"
      "v_cvt_pk_f16_f32 v187, v182, v183\n"
      "v_mfma_f32_16x16x32_f16 v[88:91], a[104:107], v[120:123], v[88:91]\n"
      "ds_write_b128 v1, v[184:187] offset:5760\n"
      "v_mfma_f32_16x16x32_f16 v[88:91], a[108:111], v[124:127], v[88:91]\n"
      "v_fma_f32 v195, |v84|, v68, v195\n"
      "v_fma_f32 v195, |v85|, v69, v195\n"
      "v_mfma_f32_16x16x32_f16 v[92:95], a[124:127], v[124:127], v[44:47]\n"
      "v_fma_f32 v195, |v86|, v70, v195\n"
      "v_fma_f32 v195, |v87|, v71, v195\n"
      "v_mfma_f32_16x16x32_f16 v[92:95], a[120:123], v[120:123], v[92:95]\n"
      "v_pk_mul_f16 v120, v152, v168\n"
      "v_pk_mul_f16 v121, v153, v169\n"
      "v_mfma_f32_16x16x32_f16 v[92:95], a[116:119], v[116:119], v[92:95]\n"
      "v_fma_f32 v195, |v88|, v72, v195\n"
      "v_fma_f32 v195, |v89|, v73, v195\n"
      "v_mfma_f32_16x16x32_f16 v[92:95], a[112:115], v[112:115], v[92:95]\n"
      "v_fma_f32 v195, |v90|, v74, v195\n"
      "v_fma_f32 v195, |v91|, v75, v195\n"
      "v_mfma_f32_16x16x32_f16 v[80:83], a[0:3], v[96:99], v[16:19]\n"
      "v_pk_mul_f16 v112, v144, v160\n"
      "v_pk_mul_f16 v113, v145, v161\n"
      "v_mfma_f32_16x16x32_f16 v[80:83], a[4:7], v[100:103], v[80:83]\n"
      "v_pk_mul_f16 v114, v146, v162\n"
      "v_pk_mul_f16 v115, v147, v163\n"
      "v_mfma_f32_16x16x32_f16 v[80:83], a[8:11], v[104:107], v[80:83]\n"
      "v_fma_f32 v195, |v92|, v76, v195\n"
      "v_fma_f32 v195, |v93|, v77, v195\n"
      "v_mfma_f32_16x16x32_f16 v[80:83], a[12:15], v[108:111], v[80:83]\n"
      "v_fma_f32 v195, |v94|, v78, v195\n"
      "v_fma_f32 v195, |v95|, v79, v195\n"
      "v_mfma_f32_16x16x32_f16 v[84:87], a[28:31], v[108:111], v[20:23]\n"
      "v_pk_mul_f16 v116, v148, v164\n"
      "v_pk_mul_f16 v117, v149, v165\n"
      "v_mfma_f32_16x16x32_f16 v[84:87], a[24:27], v[104:107], v[84:87]\n"
      "v_pk_mul_f16 v118, v150, v166\n"
      "v_pk_mul_f16 v119, v151, v167\n"
      "v_mfma_f32_16x16x32_f16 v[84:87], a[20:23], v[100:103], v[84:87]\n"
      "v_fma_f32 v190, |v80|, v48, 4.0\n"
      "v_fma_f32 v190, |v81|, v49, v190\n"
      "v_mfma_f32_16x16x32_f16 v[84:87], a[16:19], v[96:99], v[84:87]\n"
      "v_fma_f32 v190, |v82|, v50, v190\n"
      "v_fma_f32 v190, |v83|, v51, v190\n"
      "v_mfma_f32_16x16x32_f16 v[88:91], a[32:35], v[96:99], v[24:27]\n"
      "v_pk_mul_f16 v122, v154, v170\n"
      "v_pk_mul_f16 v123, v155, v171\n"
      "v_mfma_f32_16x16x32_f16 v[88:91], a[36:39], v[100:103], v[88:91]\n"
      "v_pk_mul_f16 v124, v156, v172\n"
      "v_pk_mul_f16 v125, v157, v173\n"
      "v_mfma_f32_16x16x32_f16 v[88:91], a[40:43], v[104:107], v[88:91]\n"
      "v_fma_f32 v190, |v84|, v52, v190\n"
      "v_fma_f32 v190, |v85|, v53, v190\n"
      "v_mfma_f32_16x16x32_f16 v[88:91], a[44:47], v[108:111], v[88:91]\n"
      "v_fma_f32 v190, |v86|, v54, v190\n"
      "v_fma_f32 v190, |v87|, v55, v190\n"
      "v_mfma_f32_16x16x32_f16 v[92:95], a[60:63], v[108:111], v[28:31]\n"
      "v_pk_mul_f16 v126, v158, v174\n"
      "v_pk_mul_f16 v127, v159, v175\n"
      "v_mfma_f32_16x16x32_f16 v[92:95], a[56:59], v[104:107], v[92:95]\n"
      "v_cvt_pk_f16_f32 v184, v228, v229\n"
      "v_cvt_pk_f16_f32 v185, v230, v231\n"
      "v_mfma_f32_16x16x32_f16 v[92:95], a[52:55], v[100:103], v[92:95]\n"
      "v_fma_f32 v190, |v88|, v56, v190\n"
      "v_fma_f32 v190, |v89|, v57, v190\n"
      "v_mfma_f32_16x16x32_f16 v[92:95], a[48:51], v[96:99], v[92:95]\n"
      "v_fma_f32 v190, |v90|, v58, v190\n"
      "v_fma_f32 v190, |v91|, v59, v190\n"
      "v_mfma_f32_16x16x32_f16 v[80:83], a[64:67], v[96:99], v[32:35]\n"
      "v_cvt_pk_f16_f32 v186, v232, v233\n"
      "v_cvt_pk_f16_f32 v187, v234, v235\n"
      "v_mfma_f32_16x16x32_f16 v[80:83], a[68:71], v[100:103], v[80:83]\n"
      "ds_write_b128 v1, v[184:187] offset:6912\n"
      "v_mfma_f32_16x16x32_f16 v[80:83], a[72:75], v[104:107], v[80:83]\n"
      "v_fma_f32 v190, |v92|, v60, v190\n"
      "v_fma_f32 v190, |v93|, v61, v190\n"
      "v_mfma_f32_16x16x32_f16 v[80:83], a[76:79], v[108:111], v[80:83]\n"
      "v_fma_f32 v190, |v94|, v62, v190\n"
      "v_fma_f32 v190, |v95|, v63, v190\n"
      "v_mfma_f32_16x16x32_f16 v[84:87], a[92:95], v[108:111], v[36:39]\n"
      "ds_read_b128 v[160:163], v203 offset:5760\n"
      "ds_read_b128 v[176:179], v214 offset:11904\n"
      "v_mfma_f32_16x16x32_f16 v[84:87], a[88:91], v[104:107], v[84:87]\n"
      "ds_read_b128 a[152:155], v219 offset:0\n"
      "ds_read_b128 a[168:171], v220 offset:0\n"
      "v_mfma_f32_16x16x32_f16 v[84:87], a[84:87], v[100:103], v[84:87]\n"
      "v_fma_f32 v190, |v80|, v64, v190\n"
      "v_fma_f32 v190, |v81|, v65, v190\n"
      "v_mfma_f32_16x16x32_f16 v[84:87], a[80:83], v[96:99], v[84:87]\n"
      "v_fma_f32 v190, |v82|, v66, v190\n"
      "v_fma_f32 v190, |v83|, v67, v190\n"
      "v_mfma_f32_16x16x32_f16 v[88:91], a[96:99], v[96:99], v[40:43]\n"
      "ds_read_b128 a[184:187], v221 offset:0\n"
      "ds_read_b128 a[156:159], v219 offset:64\n"
      "v_mfma_f32_16x16x32_f16 v[88:91], a[100:103], v[100:103], v[88:91]\n"
      "v_add_u32_sdwa v217, s38, v196 dst_sel:DWORD dst_unused:UNUSED_PAD src0_sel:DWORD src1_sel:WORD_1\n"
      "v_add_u32_sdwa v218, s38, v197 dst_sel:DWORD dst_unused:UNUSED_PAD src0_sel:DWORD src1_sel:WORD_0\n"
      "v_mfma_f32_16x16x32_f16 v[88:91], a[104:107], v[104:107], v[88:91]\n"
      "v_fma_f32 v190, |v84|, v68, v190\n"
      "v_fma_f32 v190, |v85|, v69, v190\n"
      "v_mfma_f32_16x16x32_f16 v[88:91], a[108:111], v[108:111], v[88:91]\n"
      "v_fma_f32 v190, |v86|, v70, v190\n"
      "v_fma_f32 v190, |v87|, v71, v190\n"
      "v_mfma_f32_16x16x32_f16 v[92:95], a[124:127], v[108:111], v[44:47]\n"
      "s_waitcnt lgkmcnt(4)\n"
      "ds_read_b128 v[164:167], v203 offset:5824\n"
      "v_pk_mul_f16 v228, v128, v176\n"
      "v_mfma_f32_16x16x32_f16 v[92:95], a[120:123], v[104:107], v[92:95]\n"
      "v_pk_mul_f16 v229, v129, v177\n"
      "v_pk_mul_f16 v230, v130, v178\n"
      "v_mfma_f32_16x16x32_f16 v[92:95], a[116:119], v[100:103], v[92:95]\n"
      "v_fma_f32 v190, |v88|, v72, v190\n"
      "v_fma_f32 v190, |v89|, v73, v190\n"
      "v_mfma_f32_16x16x32_f16 v[92:95], a[112:115], v[96:99], v[92:95]\n"
      "v_fma_f32 v190, |v90|, v74, v190\n"
      "v_fma_f32 v190, |v91|, v75, v190\n"
      "v_mfma_f32_16x16x32_f16 v[80:83], a[0:3], v[112:115], v[16:19]\n"
      "v_pk_mul_f16 v96, v128, v160\n"
      "v_pk_mul_f16 v97, v129, v161\n"
      "v_mfma_f32_16x16x32_f16 v[80:83], a[4:7], v[116:119], v[80:83]\n"
      "v_pk_mul_f16 v98, v130, v162\n"
      "v_pk_mul_f16 v99, v131, v163\n"
      "v_mfma_f32_16x16x32_f16 v[80:83], a[8:11], v[120:123], v[80:83]\n"
      "v_fma_f32 v190, |v92|, v76, v190\n"
      "v_fma_f32 v190, |v93|, v77, v190\n"
      "v_mfma_f32_16x16x32_f16 v[80:83], a[12:15], v[124:127], v[80:83]\n"
      "v_fma_f32 v190, |v94|, v78, v190\n"
      "v_fma_f32 v190, |v95|, v79, v190\n"
      "v_mfma_f32_16x16x32_f16 v[84:87], a[28:31], v[124:127], v[20:23]\n"
      "s_waitcnt lgkmcnt(0)\n"
      "ds_read_b128 v[168:171], v203 offset:5888\n"
      "v_pk_mul_f16 v100, v132, v164\n"
      "v_mfma_f32_16x16x32_f16 v[84:87], a[24:27], v[120:123], v[84:87]\n"
      "v_pk_mul_f16 v101, v133, v165\n"
      "v_pk_mul_f16 v102, v134, v166\n"
      "v_mfma_f32_16x16x32_f16 v[84:87], a[20:23], v[116:119], v[84:87]\n"
      "v_fma_f32 v191, |v80|, v48, 4.0\n"
      "v_fma_f32 v191, |v81|, v49, v191\n"
      "v_mfma_f32_16x16x32_f16 v[84:87], a[16:19], v[112:115], v[84:87]\n"
      "v_fma_f32 v191, |v82|, v50, v191\n"
      "v_fma_f32 v191, |v83|, v51, v191\n"
      "v_mfma_f32_16x16x32_f16 v[88:91], a[32:35], v[112:115], v[24:27]\n"
      "v_pk_mul_f16 v103, v135, v167\n"
      "v_pk_mul_f16 v231, v131, v179\n"
      "v_mfma_f32_16x16x32_f16 v[88:91], a[36:39], v[116:119], v[88:91]\n"
      "ds_read_b128 v[172:175], v203 offset:5952\n"
      "ds_read_b128 v[240:243], v201 offset:4608\n"
      "v_mfma_f32_16x16x32_f16 v[88:91], a[40:43], v[120:123], v[88:91]\n"
      "v_fma_f32 v191, |v84|, v52, v191\n"
      "v_fma_f32 v191, |v85|, v53, v191\n"
      "v_mfma_f32_16x16x32_f16 v[88:91], a[44:47], v[124:127], v[88:91]\n"
      "v_fma_f32 v191, |v86|, v54, v191\n"
      "v_fma_f32 v191, |v87|, v55, v191\n"
      "v_mfma_f32_16x16x32_f16 v[92:95], a[60:63], v[124:127], v[28:31]\n"
      "s_waitcnt lgkmcnt(2)\n"
      "v_pk_mul_f16 v104, v136, v168\n"
      "v_pk_mul_f16 v105, v137, v169\n"
      "v_mfma_f32_16x16x32_f16 v[92:95], a[56:59], v[120:123], v[92:95]\n"
      "v_pk_mul_f16 v106, v138, v170\n"
      "v_pk_mul_f16 v107, v139, v171\n"
      "v_mfma_f32_16x16x32_f16 v[92:95], a[52:55], v[116:119], v[92:95]\n"
      "v_fma_f32 v191, |v88|, v56, v191\n"
      "v_fma_f32 v191, |v89|, v57, v191\n"
      "v_mfma_f32_16x16x32_f16 v[92:95], a[48:51], v[112:115], v[92:95]\n"
      "v_fma_f32 v191, |v90|, v58, v191\n"
      "v_fma_f32 v191, |v91|, v59, v191\n"
      "v_mfma_f32_16x16x32_f16 v[80:83], a[64:67], v[112:115], v[32:35]\n"
      "ds_read_b128 v[244:247], v201 offset:4672\n"
      "ds_read_b128 v[248:251], v201 offset:4736\n"
      "v_mfma_f32_16x16x32_f16 v[80:83], a[68:71], v[116:119], v[80:83]\n"
      "s_waitcnt lgkmcnt(3)\n"
      "ds_read_b128 v[252:255], v201 offset:4800\n"
      "v_pk_mul_f16 v108, v140, v172\n"
      "v_mfma_f32_16x16x32_f16 v[80:83], a[72:75], v[120:123], v[80:83]\n"
      "v_fma_f32 v191, |v92|, v60, v191\n"
      "v_fma_f32 v191, |v93|, v61, v191\n"
      "v_mfma_f32_16x16x32_f16 v[80:83], a[76:79], v[124:127], v[80:83]\n"
      "v_fma_f32 v191, |v94|, v62, v191\n"
      "v_fma_f32 v191, |v95|, v63, v191\n"
      "v_mfma_f32_16x16x32_f16 v[84:87], a[92:95], v[124:127], v[36:39]\n"
      "v_pk_mul_f16 v109, v141, v173\n"
      "v_pk_mul_f16 v110, v142, v174\n"
      "v_mfma_f32_16x16x32_f16 v[84:87], a[88:91], v[120:123], v[84:87]\n"
      "v_pk_mul_f16 v111, v143, v175\n"
      "v_pk_mul_f16 v232, v144, v176\n"
      "v_mfma_f32_16x16x32_f16 v[84:87], a[84:87], v[116:119], v[84:87]\n"
      "v_fma_f32 v191, |v80|, v64, v191\n"
      "v_fma_f32 v191, |v81|, v65, v191\n"
      "v_mfma_f32_16x16x32_f16 v[84:87], a[80:83], v[112:115], v[84:87]\n"
      "v_fma_f32 v191, |v82|, v66, v191\n"
      "v_fma_f32 v191, |v83|, v67, v191\n"
      "v_mfma_f32_16x16x32_f16 v[88:91], a[96:99], v[112:115], v[40:43]\n"
      "v_pk_mul_f16 v233, v145, v177\n"
      "v_pk_mul_f16 v234, v146, v178\n"
      "v_mfma_f32_16x16x32_f16 v[88:91], a[100:103], v[116:119], v[88:91]\n"
      "s_waitcnt lgkmcnt(3)\n"
      "v_pk_mul_f16 v235, v147, v179\n"
      "v_pk_mul_f16 v236, v240, v176\n"
      "v_mfma_f32_16x16x32_f16 v[88:91], a[104:107], v[120:123], v[88:91]\n"
      "v_fma_f32 v191, |v84|, v68, v191\n"
      "v_fma_f32 v191, |v85|, v69, v191\n"
      "v_mfma_f32_16x16x32_f16 v[88:91], a[108:111], v[124:127], v[88:91]\n"
      "v_fma_f32 v191, |v86|, v70, v191\n"
      "v_fma_f32 v191, |v87|, v71, v191\n"
      "v_mfma_f32_16x16x32_f16 v[92:95], a[124:127], v[124:127], v[44:47]\n"
      "v_pk_mul_f16 v237, v241, v177\n"
      "v_pk_mul_f16 v238, v242, v178\n"
      "v_mfma_f32_16x16x32_f16 v[92:95], a[120:123], v[120:123], v[92:95]\n"
      "v_pk_mul_f16 v120, v152, v168\n"
      "v_pk_mul_f16 v121, v153, v169\n"
      "v_mfma_f32_16x16x32_f16 v[92:95], a[116:119], v[116:119], v[92:95]\n"
      "v_fma_f32 v191, |v88|, v72, v191\n"
      "v_fma_f32 v191, |v89|, v73, v191\n"
      "v_mfma_f32_16x16x32_f16 v[92:95], a[112:115], v[112:115], v[92:95]\n"
      "v_fma_f32 v191, |v90|, v74, v191\n"
      "v_fma_f32 v191, |v91|, v75, v191\n"
      "v_mfma_f32_16x16x32_f16 v[80:83], a[0:3], v[96:99], v[16:19]\n"
      "v_pk_mul_f16 v112, v144, v160\n"
      "v_pk_mul_f16 v113, v145, v161\n"
      "v_mfma_f32_16x16x32_f16 v[80:83], a[4:7], v[100:103], v[80:83]\n"
      "v_pk_mul_f16 v114, v146, v162\n"
      "v_pk_mul_f16 v115, v147, v163\n"
      "v_mfma_f32_16x16x32_f16 v[80:83], a[8:11], v[104:107], v[80:83]\n"
      "v_fma_f32 v191, |v92|, v76, v191\n"
      "v_fma_f32 v191, |v93|, v77, v191\n"
      "v_mfma_f32_16x16x32_f16 v[80:83], a[12:15], v[108:111], v[80:83]\n"
      "v_fma_f32 v191, |v94|, v78, v191\n"
      "v_fma_f32 v191, |v95|, v79, v191\n"
      "v_mfma_f32_16x16x32_f16 v[84:87], a[28:31], v[108:111], v[20:23]\n"
      "v_pk_mul_f16 v116, v148, v164\n"
      "v_pk_mul_f16 v117, v149, v165\n"
      "v_mfma_f32_16x16x32_f16 v[84:87], a[24:27], v[104:107], v[84:87]\n"
      "v_pk_mul_f16 v118, v150, v166\n"
      "v_pk_mul_f16 v119, v151, v167\n"
      "v_mfma_f32_16x16x32_f16 v[84:87], a[20:23], v[100:103], v[84:87]\n"
      "v_fma_f32 v192, |v80|, v48, 4.0\n"
      "v_fma_f32 v192, |v81|, v49, v192\n"
      "v_mfma_f32_16x16x32_f16 v[84:87], a[16:19], v[96:99], v[84:87]\n"
      "v_fma_f32 v192, |v82|, v50, v192\n"
      "v_fma_f32 v192, |v83|, v51, v192\n"
      "v_mfma_f32_16x16x32_f16 v[88:91], a[32:35], v[96:99], v[24:27]\n"
      "v_pk_mul_f16 v122, v154, v170\n"
      "v_pk_mul_f16 v123, v155, v171\n"
      "v_mfma_f32_16x16x32_f16 v[88:91], a[36:39], v[100:103], v[88:91]\n"
      "v_pk_mul_f16 v239, v243, v179\n"
      "v_pk_mul_f16 v124, v156, v172\n"
      "v_mfma_f32_16x16x32_f16 v[88:91], a[40:43], v[104:107], v[88:91]\n"
      "v_fma_f32 v192, |v84|, v52, v192\n"
      "v_fma_f32 v192, |v85|, v53, v192\n"
      "v_mfma_f32_16x16x32_f16 v[88:91], a[44:47], v[108:111], v[88:91]\n"
      "v_fma_f32 v192, |v86|, v54, v192\n"
      "v_fma_f32 v192, |v87|, v55, v192\n"
      "v_mfma_f32_16x16x32_f16 v[92:95], a[60:63], v[108:111], v[28:31]\n"
      "v_pk_mul_f16 v125, v157, v173\n"
      "v_pk_mul_f16 v126, v158, v174\n"
      "v_mfma_f32_16x16x32_f16 v[92:95], a[56:59], v[104:107], v[92:95]\n"
      "ds_read_b128 v[176:179], v214 offset:11968\n"
      "v_pk_mul_f16 v127, v159, v175\n"
      "v_mfma_f32_16x16x32_f16 v[92:95], a[52:55], v[100:103], v[92:95]\n"
      "v_fma_f32 v192, |v88|, v56, v192\n"
      "v_fma_f32 v192, |v89|, v57, v192\n"
      "v_mfma_f32_16x16x32_f16 v[92:95], a[48:51], v[96:99], v[92:95]\n"
      "v_fma_f32 v192, |v90|, v58, v192\n"
      "v_fma_f32 v192, |v91|, v59, v192\n"
      "v_mfma_f32_16x16x32_f16 a[128:131], v[228:231], a[152:155], 0\n"
      "v_add_u32_sdwa v215, s38, v207 dst_sel:DWORD dst_unused:UNUSED_PAD src0_sel:DWORD src1_sel:WORD_1\n"
      "v_add_u32_sdwa v211, s38, v207 dst_sel:DWORD dst_unused:UNUSED_PAD src0_sel:DWORD src1_sel:WORD_0\n"
      "v_mfma_f32_16x16x32_f16 v[80:83], a[64:67], v[96:99], v[32:35]\n"
      "v_add_u32_sdwa v209, s38, v206 dst_sel:DWORD dst_unused:UNUSED_PAD src0_sel:DWORD src1_sel:WORD_1\n"
      "v_add_u32_sdwa v208, s38, v206 dst_sel:DWORD dst_unused:UNUSED_PAD src0_sel:DWORD src1_sel:WORD_0\n"
      "v_mfma_f32_16x16x32_f16 v[80:83], a[68:71], v[100:103], v[80:83]\n"
      "v_fma_f32 v192, |v92|, v60, v192\n"
      "v_fma_f32 v192, |v93|, v61, v192\n"
      "v_mfma_f32_16x16x32_f16 v[80:83], a[72:75], v[104:107], v[80:83]\n"
      "v_fma_f32 v192, |v94|, v62, v192\n"
      "v_fma_f32 v192, |v95|, v63, v192\n"
      "v_mfma_f32_16x16x32_f16 v[80:83], a[76:79], v[108:111], v[80:83]\n"
      "v_add_u32_sdwa v207, s38, v205 dst_sel:DWORD dst_unused:UNUSED_PAD src0_sel:DWORD src1_sel:WORD_1\n"
      "v_add_u32_sdwa v206, s38, v205 dst_sel:DWORD dst_unused:UNUSED_PAD src0_sel:DWORD src1_sel:WORD_0\n"
      "v_mfma_f32_16x16x32_f16 v[84:87], a[92:95], v[108:111], v[36:39]\n"
      "s_waitcnt lgkmcnt(0)\n"
      "v_pk_mul_f16 v228, v132, v176\n"
      "v_pk_mul_f16 v229, v133, v177\n"
      "v_mfma_f32_16x16x32_f16 v[84:87], a[88:91], v[104:107], v[84:87]\n"
      "v_pk_mul_f16 v230, v134, v178\n"
      "v_pk_mul_f16 v231, v135, v179\n"
      "v_mfma_f32_16x16x32_f16 v[84:87], a[84:87], v[100:103], v[84:87]\n"
      "v_fma_f32 v192, |v80|, v64, v192\n"
      "v_fma_f32 v192, |v81|, v65, v192\n"
      "v_mfma_f32_16x16x32_f16 v[84:87], a[80:83], v[96:99], v[84:87]\n"
      "v_fma_f32 v192, |v82|, v66, v192\n"
      "v_fma_f32 v192, |v83|, v67, v192\n"
      "v_mfma_f32_16x16x32_f16 a[132:135], v[232:235], a[168:171], 0\n"
      "ds_read_b128 v[160:163], v203 offset:6336\n"
      "v_add_u32_sdwa v205, s38, v204 dst_sel:DWORD dst_unused:UNUSED_PAD src0_sel:DWORD src1_sel:WORD_1\n"
      "v_mfma_f32_16x16x32_f16 v[88:91], a[96:99], v[96:99], v[40:43]\n"
      "ds_read_b128 a[172:175], v220 offset:64\n"
      "v_add_u32_sdwa v204, s38, v204 dst_sel:DWORD dst_unused:UNUSED_PAD src0_sel:DWORD src1_sel:WORD_0\n"
      "v_mfma_f32_16x16x32_f16 v[88:91], a[100:103], v[100:103], v[88:91]\n"
      "v_fma_f32 v192, |v84|, v68, v192\n"
      "v_fma_f32 v192, |v85|, v69, v192\n"
      "v_mfma_f32_16x16x32_f16 v[88:91], a[104:107], v[104:107], v[88:91]\n"
      "v_fma_f32 v192, |v86|, v70, v192\n"
      "v_fma_f32 v192, |v87|, v71, v192\n"
      "v_mfma_f32_16x16x32_f16 v[88:91], a[108:111], v[108:111], v[88:91]\n"
      "ds_read_b128 v[164:167], v203 offset:6400\n"
      "v_pk_mul_f16 v232, v148, v176\n"
      "v_mfma_f32_16x16x32_f16 v[92:95], a[124:127], v[108:111], v[44:47]\n"
      "v_pk_mul_f16 v233, v149, v177\n"
      "v_pk_mul_f16 v234, v150, v178\n"
      "v_mfma_f32_16x16x32_f16 v[92:95], a[120:123], v[104:107], v[92:95]\n"
      "ds_read_b128 a[188:191], v221 offset:64\n"
      "v_pk_mul_f16 v235, v151, v179\n"
      "v_mfma_f32_16x16x32_f16 v[92:95], a[116:119], v[100:103], v[92:95]\n"
      "v_fma_f32 v192, |v88|, v72, v192\n"
      "v_fma_f32 v192, |v89|, v73, v192\n"
      "v_mfma_f32_16x16x32_f16 v[92:95], a[112:115], v[96:99], v[92:95]\n"
      "v_fma_f32 v192, |v90|, v74, v192\n"
      "v_fma_f32 v192, |v91|, v75, v192\n"
      "v_mfma_f32_16x16x32_f16 a[136:139], v[236:239], a[184:187], 0\n"
      "s_waitcnt lgkmcnt(3)\n"
      "v_pk_mul_f16 v96, v128, v160\n"
      "v_pk_mul_f16 v97, v129, v161\n"
      "v_mfma_f32_16x16x32_f16 v[80:83], a[0:3], v[112:115], v[16:19]\n"
      "v_pk_mul_f16 v98, v130, v162\n"
      "v_pk_mul_f16 v99, v131, v163\n"
      "v_mfma_f32_16x16x32_f16 v[80:83], a[4:7], v[116:119], v[80:83]\n"
      "v_fma_f32 v192, |v92|, v76, v192\n"
      "v_fma_f32 v192, |v93|, v77, v192\n"
      "v_mfma_f32_16x16x32_f16 v[80:83], a[8:11], v[120:123], v[80:83]\n"
      "v_fma_f32 v192, |v94|, v78, v192\n"
      "v_fma_f32 v192, |v95|, v79, v192\n"
      "v_mfma_f32_16x16x32_f16 v[80:83], a[12:15], v[124:127], v[80:83]\n"
      "s_waitcnt lgkmcnt(1)\n"
      "ds_read_b128 v[168:171], v203 offset:6464\n"
      "v_pk_mul_f16 v100, v132, v164\n"
      "v_mfma_f32_16x16x32_f16 v[84:87], a[28:31], v[124:127], v[20:23]\n"
      "v_permlane32_swap_b32 v194, v192\n"
      "v_mfma_f32_16x16x32_f16 v[84:87], a[24:27], v[120:123], v[84:87]\n"
      "v_pk_mul_f16 v101, v133, v165\n"
      "v_pk_mul_f16 v102, v134, v166\n"
      "v_mfma_f32_16x16x32_f16 v[84:87], a[20:23], v[116:119], v[84:87]\n"
      "v_fma_f32 v193, |v80|, v48, 4.0\n"
      "v_fma_f32 v193, |v81|, v49, v193\n"
      "v_mfma_f32_16x16x32_f16 v[84:87], a[16:19], v[112:115], v[84:87]\n"
      "v_fma_f32 v193, |v82|, v50, v193\n"
      "v_fma_f32 v193, |v83|, v51, v193\n"
      "v_mfma_f32_16x16x32_f16 v[88:91], a[32:35], v[112:115], v[24:27]\n"
      "v_pk_mul_f16 v103, v135, v167\n"
      "v_pk_mul_f16 v236, v244, v176\n"
      "v_mfma_f32_16x16x32_f16 v[88:91], a[36:39], v[116:119], v[88:91]\n"
      "v_pk_mul_f16 v237, v245, v177\n"
      "v_pk_mul_f16 v238, v246, v178\n"
      "v_mfma_f32_16x16x32_f16 v[88:91], a[40:43], v[120:123], v[88:91]\n"
      "v_fma_f32 v193, |v84|, v52, v193\n"
      "v_fma_f32 v193, |v85|, v53, v193\n"
      "v_mfma_f32_16x16x32_f16 v[88:91], a[44:47], v[124:127], v[88:91]\n"
      "v_fma_f32 v193, |v86|, v54, v193\n"
      "v_fma_f32 v193, |v87|, v55, v193\n"
      "v_mfma_f32_16x16x32_f16 v[92:95], a[60:63], v[124:127], v[28:31]\n"
      "s_waitcnt lgkmcnt(0)\n"
      "v_pk_mul_f16 v104, v136, v168\n"
      "v_pk_mul_f16 v105, v137, v169\n"
      "v_mfma_f32_16x16x32_f16 v[92:95], a[56:59], v[120:123], v[92:95]\n"
      "v_pk_mul_f16 v106, v138, v170\n"
      "v_pk_mul_f16 v107, v139, v171\n"
      "v_mfma_f32_16x16x32_f16 v[92:95], a[52:55], v[116:119], v[92:95]\n"
      "v_fma_f32 v193, |v88|, v56, v193\n"
      "v_fma_f32 v193, |v89|, v57, v193\n"
      "v_mfma_f32_16x16x32_f16 v[92:95], a[48:51], v[112:115], v[92:95]\n"
      "v_fma_f32 v193, |v90|, v58, v193\n"
      "v_fma_f32 v193, |v91|, v59, v193\n"
      "v_mfma_f32_16x16x32_f16 a[128:131], v[228:231], a[156:159], a[128:131]\n"
      "ds_read_b128 v[172:175], v203 offset:6528\n"
      "v_add_u32_sdwa v10, s99, v199 dst_sel:DWORD dst_unused:UNUSED_PAD src0_sel:DWORD src1_sel:WORD_0\n"
      "v_mfma_f32_16x16x32_f16 v[80:83], a[64:67], v[112:115], v[32:35]\n"
      "v_pk_mul_f16 v239, v247, v179\n"
      "v_add_u32_sdwa v11, s99, v199 dst_sel:DWORD dst_unused:UNUSED_PAD src0_sel:DWORD src1_sel:WORD_1\n"
      "v_mfma_f32_16x16x32_f16 v[80:83], a[68:71], v[116:119], v[80:83]\n"
      "v_fma_f32 v193, |v92|, v60, v193\n"
      "v_fma_f32 v193, |v93|, v61, v193\n"
      "v_mfma_f32_16x16x32_f16 v[80:83], a[72:75], v[120:123], v[80:83]\n"
      "v_fma_f32 v193, |v94|, v62, v193\n"
      "v_fma_f32 v193, |v95|, v63, v193\n"
      "v_mfma_f32_16x16x32_f16 v[80:83], a[76:79], v[124:127], v[80:83]\n"
      "v_mov_b32 v7, 0xf149f2ca\n"
      "v_mov_b32 v8, 0xf149f2ca\n"
      "v_mfma_f32_16x16x32_f16 v[84:87], a[92:95], v[124:127], v[36:39]\n"
      "42:\n"
      "s_cbranch_execz 44b\n"
      "v_add_u32 v12, s98, v1\n"
      "v_mfma_f32_16x16x32_f16 v[84:87], a[88:91], v[120:123], v[84:87]\n"
      "s_and_b32 s100, s100, 0x37373737\n"
      "ds_read_b128 v[176:179], v214 offset:12032\n"
      "ds_read_b128 a[160:163], v219 offset:128\n"
      "v_mfma_f32_16x16x32_f16 v[84:87], a[84:87], v[116:119], v[84:87]\n"
      "s_xor_b32 s100, s100, 0x370037\n"
      "v_fma_f32 v193, |v80|, v64, v193\n"
      "v_fma_f32 v193, |v81|, v65, v193\n"
      "v_mfma_f32_16x16x32_f16 v[84:87], a[80:83], v[112:115], v[84:87]\n"
      "s_waitcnt lgkmcnt(2)\n"
      "v_pk_mul_f16 v108, v140, v172\n"
      "v_pk_mul_f16 v109, v141, v173\n"
      "v_mfma_f32_16x16x32_f16 a[132:135], v[232:235], a[172:175], a[132:135]\n"
      "s_mov_b32 s101, s100\n"
      "v_fma_f32 v193, |v82|, v66, v193\n"
      "v_fma_f32 v193, |v83|, v67, v193\n"
      "v_mfma_f32_16x16x32_f16 v[88:91], a[96:99], v[112:115], v[40:43]\n"
      "s_or_b32 s44, s55, s41\n"
      "v_pk_mul_f16 v110, v142, v174\n"
      "v_pk_mul_f16 v111, v143, v175\n"
      "v_mfma_f32_16x16x32_f16 v[88:91], a[100:103], v[116:119], v[88:91]\n"
      "s_cselect_b32 s44, 2, 1\n"
      "v_fma_f32 v193, |v84|, v68, v193\n"
      "v_fma_f32 v193, |v85|, v69, v193\n"
      "v_mfma_f32_16x16x32_f16 v[88:91], a[104:107], v[120:123], v[88:91]\n"
      "v_fma_f32 v193, |v86|, v70, v193\n"
      "v_fma_f32 v193, |v87|, v71, v193\n"
      "v_mfma_f32_16x16x32_f16 v[88:91], a[108:111], v[124:127], v[88:91]\n"
      "ds_read_b128 a[176:179], v220 offset:128\n"
      "ds_read_b128 a[192:195], v221 offset:128\n"
      "v_mfma_f32_16x16x32_f16 v[92:95], a[124:127], v[124:127], v[44:47]\n"
      "s_waitcnt lgkmcnt(3)\n"
      "v_pk_mul_f16 v228, v136, v176\n"
      "v_pk_mul_f16 v229, v137, v177\n"
      "v_mfma_f32_16x16x32_f16 v[92:95], a[120:123], v[120:123], v[92:95]\n"
      "v_pk_mul_f16 v230, v138, v178\n"
      "v_pk_mul_f16 v231, v139, v179\n"
      "v_mfma_f32_16x16x32_f16 v[92:95], a[116:119], v[116:119], v[92:95]\n"
      "v_fma_f32 v193, |v88|, v72, v193\n"
      "v_fma_f32 v193, |v89|, v73, v193\n"
      "v_mfma_f32_16x16x32_f16 v[92:95], a[112:115], v[112:115], v[92:95]\n"
      "v_fma_f32 v193, |v90|, v74, v193\n"
      "v_fma_f32 v193, |v91|, v75, v193\n"
      "v_mfma_f32_16x16x32_f16 a[136:139], v[236:239], a[188:191], a[136:139]\n"
      "v_pk_mul_f16 v112, v144, v160\n"
      "v_pk_mul_f16 v113, v145, v161\n"
      "v_mfma_f32_16x16x32_f16 v[80:83], a[0:3], v[96:99], v[16:19]\n"
      "v_pk_mul_f16 v114, v146, v162\n"
      "v_pk_mul_f16 v115, v147, v163\n"
      "v_mfma_f32_16x16x32_f16 v[80:83], a[4:7], v[100:103], v[80:83]\n"
      "v_fma_f32 v193, |v92|, v76, v193\n"
      "v_fma_f32 v193, |v93|, v77, v193\n"
      "v_mfma_f32_16x16x32_f16 v[80:83], a[8:11], v[104:107], v[80:83]\n"
      "v_fma_f32 v193, |v94|, v78, v193\n"
      "v_fma_f32 v193, |v95|, v79, v193\n"
      "v_mfma_f32_16x16x32_f16 v[80:83], a[12:15], v[108:111], v[80:83]\n"
      "v_pk_mul_f16 v116, v148, v164\n"
      "v_pk_mul_f16 v117, v149, v165\n"
      "v_mfma_f32_16x16x32_f16 v[84:87], a[28:31], v[108:111], v[20:23]\n"
      "v_permlane32_swap_b32 v195, v193\n"
      "v_mfma_f32_16x16x32_f16 v[84:87], a[24:27], v[104:107], v[84:87]\n"
      "v_pk_mul_f16 v118, v150, v166\n"
      "v_pk_mul_f16 v119, v151, v167\n"
      "v_mfma_f32_16x16x32_f16 v[84:87], a[20:23], v[100:103], v[84:87]\n"
      "v_fma_f32 v196, |v80|, v48, 4.0\n"
      "v_fma_f32 v196, |v81|, v49, v196\n"
      "v_mfma_f32_16x16x32_f16 v[84:87], a[16:19], v[96:99], v[84:87]\n"
      "v_fma_f32 v196, |v82|, v50, v196\n"
      "v_fma_f32 v196, |v83|, v51, v196\n"
      "v_mfma_f32_16x16x32_f16 v[88:91], a[32:35], v[96:99], v[24:27]\n"
      "v_pk_mul_f16 v232, v152, v176\n"
      "v_pk_mul_f16 v120, v152, v168\n"
      "v_mfma_f32_16x16x32_f16 v[88:91], a[36:39], v[100:103], v[88:91]\n"
      "v_pk_mul_f16 v233, v153, v177\n"
      "v_pk_mul_f16 v121, v153, v169\n"
      "v_mfma_f32_16x16x32_f16 v[88:91], a[40:43], v[104:107], v[88:91]\n"
      "v_fma_f32 v196, |v84|, v52, v196\n"
      "v_fma_f32 v196, |v85|, v53, v196\n"
      "v_mfma_f32_16x16x32_f16 v[88:91], a[44:47], v[108:111], v[88:91]\n"
      "v_fma_f32 v196, |v86|, v54, v196\n"
      "v_fma_f32 v196, |v87|, v55, v196\n"
      "v_mfma_f32_16x16x32_f16 v[92:95], a[60:63], v[108:111], v[28:31]\n"
      "v_pk_mul_f16 v234, v154, v178\n"
      "v_pk_mul_f16 v122, v154, v170\n"
      "v_mfma_f32_16x16x32_f16 v[92:95], a[56:59], v[104:107], v[92:95]\n"
      "v_pk_mul_f16 v235, v155, v179\n"
      "v_pk_mul_f16 v123, v155, v171\n"
      "v_mfma_f32_16x16x32_f16 v[92:95], a[52:55], v[100:103], v[92:95]\n"
      "v_fma_f32 v196, |v88|, v56, v196\n"
      "v_fma_f32 v196, |v89|, v57, v196\n"
      "v_mfma_f32_16x16x32_f16 v[92:95], a[48:51], v[96:99], v[92:95]\n"
      "s_waitcnt lgkmcnt(2)\n"
      "v_fma_f32 v196, |v90|, v58, v196\n"
      "v_fma_f32 v196, |v91|, v59, v196\n"
      "v_mfma_f32_16x16x32_f16 a[128:131], v[228:231], a[160:163], a[128:131]\n"
      "v_pk_mul_f16 v236, v248, v176\n"
      "v_pk_mul_f16 v237, v249, v177\n"
      "v_mfma_f32_16x16x32_f16 v[80:83], a[64:67], v[96:99], v[32:35]\n"
      "v_pk_mul_f16 v238, v250, v178\n"
      "v_pk_mul_f16 v239, v251, v179\n"
      "v_mfma_f32_16x16x32_f16 v[80:83], a[68:71], v[100:103], v[80:83]\n"
      "v_fma_f32 v196, |v92|, v60, v196\n"
      "v_fma_f32 v196, |v93|, v61, v196\n"
      "v_mfma_f32_16x16x32_f16 v[80:83], a[72:75], v[104:107], v[80:83]\n"
      "v_fma_f32 v196, |v94|, v62, v196\n"
      "v_fma_f32 v196, |v95|, v63, v196\n"
      "v_mfma_f32_16x16x32_f16 v[80:83], a[76:79], v[108:111], v[80:83]\n"
      "v_pk_mul_f16 v124, v156, v172\n"
      "v_pk_mul_f16 v125, v157, v173\n"
      "v_mfma_f32_16x16x32_f16 v[84:87], a[92:95], v[108:111], v[36:39]\n"
      "v_mov_b32 v223, s44\n"
      "v_pk_mul_f16 v126, v158, v174\n"
      "v_mfma_f32_16x16x32_f16 v[84:87], a[88:91], v[104:107], v[84:87]\n"
      "ds_write_b32 v2, v223 offset:8\n"
      "v_mfma_f32_16x16x32_f16 v[84:87], a[84:87], v[100:103], v[84:87]\n"
      "v_fma_f32 v196, |v80|, v64, v196\n"
      "v_fma_f32 v196, |v81|, v65, v196\n"
      "v_mfma_f32_16x16x32_f16 v[84:87], a[80:83], v[96:99], v[84:87]\n"
      "s_waitcnt lgkmcnt(2)\n"
      "v_fma_f32 v196, |v82|, v66, v196\n"
      "v_fma_f32 v196, |v83|, v67, v196\n"
      "v_mfma_f32_16x16x32_f16 a[132:135], v[232:235], a[176:179], a[132:135]\n"
      "ds_read_b128 v[176:179], v214 offset:12096\n"
      "v_add_f32 v194, v194, v192\n"
      "v_mfma_f32_16x16x32_f16 v[88:91], a[96:99], v[96:99], v[40:43]\n"
      "ds_read_b128 v[160:163], v202 offset:4608\n"
      "v_pk_mul_f16 v127, v159, v175\n"
      "v_mfma_f32_16x16x32_f16 v[88:91], a[100:103], v[100:103], v[88:91]\n"
      "v_fma_f32 v196, |v84|, v68, v196\n"
      "v_fma_f32 v196, |v85|, v69, v196\n"
      "v_mfma_f32_16x16x32_f16 v[88:91], a[104:107], v[104:107], v[88:91]\n"
      "v_fma_f32 v196, |v86|, v70, v196\n"
      "v_fma_f32 v196, |v87|, v71, v196\n"
      "v_mfma_f32_16x16x32_f16 v[88:91], a[108:111], v[108:111], v[88:91]\n"
      "ds_read_b128 a[164:167], v219 offset:192\n"
      "ds_read_b128 v[164:167], v202 offset:4672\n"
      "v_mfma_f32_16x16x32_f16 v[92:95], a[124:127], v[108:111], v[44:47]\n"
      "ds_read_b128 a[180:183], v220 offset:192\n"
      "ds_read_b128 a[196:199], v221 offset:192\n"
      "v_mfma_f32_16x16x32_f16 v[92:95], a[120:123], v[104:107], v[92:95]\n"
      "ds_read_b128 a[200:203], v215 offset:11904\n"
      "v_add_f32 v195, v195, v193\n"
      "v_mfma_f32_16x16x32_f16 v[92:95], a[116:119], v[100:103], v[92:95]\n"
      "v_fma_f32 v196, |v88|, v72, v196\n"
      "v_fma_f32 v196, |v89|, v73, v196\n"
      "v_mfma_f32_16x16x32_f16 v[92:95], a[112:115], v[96:99], v[92:95]\n"
      "s_waitcnt lgkmcnt(8)\n"
      "v_fma_f32 v196, |v90|, v74, v196\n"
      "v_fma_f32 v196, |v91|, v75, v196\n"
      "v_mfma_f32_16x16x32_f16 a[136:139], v[236:239], a[192:195], a[136:139]\n"
      "s_waitcnt lgkmcnt(5)\n"
      "v_pk_mul_f16 v96, v240, v160\n"
      "v_pk_mul_f16 v97, v241, v161\n"
      "v_mfma_f32_16x16x32_f16 v[80:83], a[0:3], v[112:115], v[16:19]\n"
      "v_pk_mul_f16 v98, v242, v162\n"
      "v_pk_mul_f16 v228, v140, v176\n"
      "v_mfma_f32_16x16x32_f16 v[80:83], a[4:7], v[116:119], v[80:83]\n"
      "v_fma_f32 v196, |v92|, v76, v196\n"
      "v_fma_f32 v196, |v93|, v77, v196\n"
      "v_mfma_f32_16x16x32_f16 v[80:83], a[8:11], v[120:123], v[80:83]\n"
      "v_fma_f32 v196, |v94|, v78, v196\n"
      "v_fma_f32 v196, |v95|, v79, v196\n"
      "v_mfma_f32_16x16x32_f16 v[80:83], a[12:15], v[124:127], v[80:83]\n"
      "v_pk_mul_f16 v99, v243, v163\n"
      "v_pk_mul_f16 v229, v141, v177\n"
      "v_mfma_f32_16x16x32_f16 v[84:87], a[28:31], v[124:127], v[20:23]\n"
      "v_permlane32_swap_b32 v190, v196\n"
      "v_mfma_f32_16x16x32_f16 v[84:87], a[24:27], v[120:123], v[84:87]\n"
      "ds_read_b128 v[168:171], v202 offset:4736\n"
      "v_pk_mul_f16 v230, v142, v178\n"
      "v_mfma_f32_16x16x32_f16 v[84:87], a[20:23], v[116:119], v[84:87]\n"
      "s_waitcnt lgkmcnt(4)\n"
      "v_pk_mul_f16 v231, v143, v179\n"
      "v_pk_mul_f16 v100, v244, v164\n"
      "v_mfma_f32_16x16x32_f16 v[84:87], a[16:19], v[112:115], v[84:87]\n"
      "v_fma_f32 v197, |v80|, v48, 4.0\n"
      "v_fma_f32 v197, |v81|, v49, v197\n"
      "v_mfma_f32_16x16x32_f16 v[88:91], a[32:35], v[112:115], v[24:27]\n"
      "v_fma_f32 v197, |v82|, v50, v197\n"
      "v_fma_f32 v197, |v83|, v51, v197\n"
      "v_mfma_f32_16x16x32_f16 v[88:91], a[36:39], v[116:119], v[88:91]\n"
      "v_pk_mul_f16 v101, v245, v165\n"
      "v_pk_mul_f16 v102, v246, v166\n"
      "v_mfma_f32_16x16x32_f16 v[88:91], a[40:43], v[120:123], v[88:91]\n"
      "v_pk_mul_f16 v103, v247, v167\n"
      "v_pk_mul_f16 v232, v156, v176\n"
      "v_mfma_f32_16x16x32_f16 v[88:91], a[44:47], v[124:127], v[88:91]\n"
      "v_fma_f32 v197, |v84|, v52, v197\n"
      "v_fma_f32 v197, |v85|, v53, v197\n"
      "v_mfma_f32_16x16x32_f16 v[92:95], a[60:63], v[124:127], v[28:31]\n"
      "v_fma_f32 v197, |v86|, v54, v197\n"
      "v_fma_f32 v197, |v87|, v55, v197\n"
      "v_mfma_f32_16x16x32_f16 v[92:95], a[56:59], v[120:123], v[92:95]\n"
      "s_waitcnt lgkmcnt(0)\n"
      "v_pk_mul_f16 v104, v248, v168\n"
      "v_pk_mul_f16 v233, v157, v177\n"
      "v_mfma_f32_16x16x32_f16 v[92:95], a[52:55], v[116:119], v[92:95]\n"
      "v_pk_mul_f16 v105, v249, v169\n"
      "v_pk_mul_f16 v234, v158, v178\n"
      "v_mfma_f32_16x16x32_f16 v[92:95], a[48:51], v[112:115], v[92:95]\n"
      "v_pk_mul_f16 v106, v250, v170\n"
      "v_pk_mul_f16 v235, v159, v179\n"
      "v_mfma_f32_16x16x32_f16 a[128:131], v[228:231], a[164:167], a[128:131]\n"
      "v_fma_f32 v197, |v88|, v56, v197\n"
      "v_fma_f32 v197, |v89|, v57, v197\n"
      "v_mfma_f32_16x16x32_f16 v[80:83], a[64:67], v[112:115], v[32:35]\n"
      "v_pk_mul_f16 v107, v251, v171\n"
      "v_pk_mul_f16 v236, v252, v176\n"
      "v_mfma_f32_16x16x32_f16 v[80:83], a[68:71], v[116:119], v[80:83]\n"
      "v_fma_f32 v197, |v90|, v58, v197\n"
      "v_fma_f32 v197, |v91|, v59, v197\n"
      "v_mfma_f32_16x16x32_f16 v[80:83], a[72:75], v[120:123], v[80:83]\n"
      "ds_write_b64 v218, a[128:129] offset:0\n"
      "v_mfma_f32_16x16x32_f16 v[80:83], a[76:79], v[124:127], v[80:83]\n"
      "v_fma_f32 v197, |v92|, v60, v197\n"
      "v_fma_f32 v197, |v93|, v61, v197\n"
      "v_mfma_f32_16x16x32_f16 v[84:87], a[92:95], v[124:127], v[36:39]\n"
      "ds_read_b128 v[172:175], v202 offset:4800\n"
      "v_fma_f32 v197, |v94|, v62, v197\n"
      "v_mfma_f32_16x16x32_f16 v[84:87], a[88:91], v[120:123], v[84:87]\n"
      "ds_write_b64 v218, a[130:131] offset:8\n"
      "v_mfma_f32_16x16x32_f16 v[84:87], a[84:87], v[116:119], v[84:87]\n"
      "v_fma_f32 v197, |v95|, v63, v197\n"
      "v_fma_f32 v197, |v80|, v64, v197\n"
      "v_mfma_f32_16x16x32_f16 v[84:87], a[80:83], v[112:115], v[84:87]\n"
      "v_pk_mul_f16 v237, v253, v177\n"
      "v_pk_mul_f16 v238, v254, v178\n"
      "v_mfma_f32_16x16x32_f16 a[132:135], v[232:235], a[180:183], a[132:135]\n"
      "v_pk_mul_f16 v239, v255, v179\n"
      "v_fma_f32 v197, |v81|, v65, v197\n"
      "v_mfma_f32_16x16x32_f16 v[88:91], a[96:99], v[112:115], v[40:43]\n"
      "v_fma_f32 v197, |v82|, v66, v197\n"
      "v_fma_f32 v197, |v83|, v67, v197\n"
      "v_mfma_f32_16x16x32_f16 v[88:91], a[100:103], v[116:119], v[88:91]\n"
      "ds_read_b128 v[160:163], v203 offset:6912\n"
      "v_fma_f32 v197, |v84|, v68, v197\n"
      "v_mfma_f32_16x16x32_f16 v[88:91], a[104:107], v[120:123], v[88:91]\n"
      "ds_write_b64 v218, a[132:133] offset:1280\n"
      "v_mfma_f32_16x16x32_f16 v[88:91], a[108:111], v[124:127], v[88:91]\n"
      "s_waitcnt lgkmcnt(3)\n"
      "v_pk_mul_f16 v108, v252, v172\n"
      "v_pk_mul_f16 v109, v253, v173\n"
      "v_mfma_f32_16x16x32_f16 v[92:95], a[124:127], v[124:127], v[44:47]\n"
      "ds_write_b64 v218, a[134:135] offset:1288\n"
      "v_mfma_f32_16x16x32_f16 v[92:95], a[120:123], v[120:123], v[92:95]\n"
      "v_pk_mul_f16 v110, v254, v174\n"
      "v_pk_mul_f16 v111, v255, v175\n"
      "v_mfma_f32_16x16x32_f16 v[92:95], a[116:119], v[116:119], v[92:95]\n"
      "v_fma_f32 v197, |v85|, v69, v197\n"
      "v_fma_f32 v197, |v86|, v70, v197\n"
      "v_mfma_f32_16x16x32_f16 v[92:95], a[112:115], v[112:115], v[92:95]\n"
      "v_fma_f32 v197, |v87|, v71, v197\n"
      "v_fma_f32 v197, |v88|, v72, v197\n"
      "v_mfma_f32_16x16x32_f16 a[136:139], v[236:239], a[196:199], a[136:139]\n"
      "v_fma_f32 v197, |v89|, v73, v197\n"
      "v_fma_f32 v197, |v90|, v74, v197\n"
      "v_mfma_f32_16x16x32_f16 v[80:83], a[0:3], v[96:99], v[16:19]\n"
      "ds_read_b128 v[164:167], v203 offset:6976\n"
      "v_fma_f32 v197, |v91|, v75, v197\n"
      "v_mfma_f32_16x16x32_f16 v[80:83], a[4:7], v[100:103], v[80:83]\n"
      "s_waitcnt lgkmcnt(3)\n"
      "v_pk_mul_f16 v112, v128, v160\n"
      "v_pk_mul_f16 v113, v129, v161\n"
      "v_mfma_f32_16x16x32_f16 v[80:83], a[8:11], v[104:107], v[80:83]\n"
      "ds_write_b128 v218, a[136:139] offset:2560\n"
      "v_mfma_f32_16x16x32_f16 v[80:83], a[12:15], v[108:111], v[80:83]\n"
      "v_fma_f32 v197, |v92|, v76, v197\n"
      "v_fma_f32 v197, |v93|, v77, v197\n"
      "v_mfma_f32_16x16x32_f16 v[84:87], a[28:31], v[108:111], v[20:23]\n"
      "v_fma_f32 v197, |v94|, v78, v197\n"
      "v_fma_f32 v197, |v95|, v79, v197\n"
      "v_mfma_f32_16x16x32_f16 v[84:87], a[24:27], v[104:107], v[84:87]\n"
      "v_pk_mul_f16 v114, v130, v162\n"
      "v_pk_mul_f16 v115, v131, v163\n"
      "v_mfma_f32_16x16x32_f16 v[84:87], a[20:23], v[100:103], v[84:87]\n"
      "v_permlane32_swap_b32 v191, v197\n"
      "v_mfma_f32_16x16x32_f16 v[84:87], a[16:19], v[96:99], v[84:87]\n"
      "ds_read_b128 v[168:171], v203 offset:7040\n"
      "v_add_f32 v190, v190, v196\n"
      "v_mfma_f32_16x16x32_f16 v[88:91], a[32:35], v[96:99], v[24:27]\n"
      "v_fma_f32 v198, |v80|, v48, 4.0\n"
      "v_fma_f32 v198, |v81|, v49, v198\n"
      "v_mfma_f32_16x16x32_f16 v[88:91], a[36:39], v[100:103], v[88:91]\n"
      "v_permlane16_swap_b32 v194, v190\n"
      "v_mfma_f32_16x16x32_f16 v[88:91], a[40:43], v[104:107], v[88:91]\n"
      "v_fma_f32 v198, |v82|, v50, v198\n"
      "v_fma_f32 v198, |v83|, v51, v198\n"
      "v_mfma_f32_16x16x32_f16 v[88:91], a[44:47], v[108:111], v[88:91]\n"
      "s_waitcnt lgkmcnt(2)\n"
      "v_pk_mul_f16 v116, v132, v164\n"
      "v_pk_mul_f16 v117, v133, v165\n"
      "v_mfma_f32_16x16x32_f16 v[92:95], a[60:63], v[108:111], v[28:31]\n"
      "v_add_f32 v194, v194, v190\n"
      "v_add_f32 v191, v191, v197\n"
      "v_mfma_f32_16x16x32_f16 v[92:95], a[56:59], v[104:107], v[92:95]\n"
      "v_pk_mul_f16 v118, v134, v166\n"
      "v_pk_mul_f16 v119, v135, v167\n"
      "v_mfma_f32_16x16x32_f16 v[92:95], a[52:55], v[100:103], v[92:95]\n"
      "v_fma_f32 v198, |v84|, v52, v198\n"
      "v_fma_f32 v198, |v85|, v53, v198\n"
      "v_mfma_f32_16x16x32_f16 v[92:95], a[48:51], v[96:99], v[92:95]\n"
      "v_fma_f32 v198, |v86|, v54, v198\n"
      "v_fma_f32 v198, |v87|, v55, v198\n"
      "v_mfma_f32_16x16x32_f16 v[80:83], a[64:67], v[96:99], v[32:35]\n"
      "v_permlane16_swap_b32 v195, v191\n"
      "v_mfma_f32_16x16x32_f16 v[80:83], a[68:71], v[100:103], v[80:83]\n"
      "s_waitcnt lgkmcnt(0)\n"
      "v_pk_mul_f16 v120, v136, v168\n"
      "v_pk_mul_f16 v121, v137, v169\n"
      "v_mfma_f32_16x16x32_f16 v[80:83], a[72:75], v[104:107], v[80:83]\n"
      "v_fma_f32 v198, |v88|, v56, v198\n"
      "v_fma_f32 v198, |v89|, v57, v198\n"
      "v_mfma_f32_16x16x32_f16 v[80:83], a[76:79], v[108:111], v[80:83]\n"
      "ds_read_b128 v[172:175], v203 offset:7104\n"
      "v_fma_f32 v198, |v90|, v58, v198\n"
      "v_mfma_f32_16x16x32_f16 v[84:87], a[92:95], v[108:111], v[36:39]\n"
      "v_fma_f32 v198, |v91|, v59, v198\n"
      "v_fma_f32 v198, |v92|, v60, v198\n"
      "v_mfma_f32_16x16x32_f16 v[84:87], a[88:91], v[104:107], v[84:87]\n"
      "v_pk_mul_f16 v122, v138, v170\n"
      "v_pk_mul_f16 v123, v139, v171\n"
      "v_mfma_f32_16x16x32_f16 v[84:87], a[84:87], v[100:103], v[84:87]\n"
      "v_fma_f32 v198, |v93|, v61, v198\n"
      "v_fma_f32 v198, |v94|, v62, v198\n"
      "v_mfma_f32_16x16x32_f16 v[84:87], a[80:83], v[96:99], v[84:87]\n"
      "v_fma_f32 v198, |v95|, v63, v198\n"
      "v_fma_f32 v198, |v80|, v64, v198\n"
      "v_mfma_f32_16x16x32_f16 v[88:91], a[96:99], v[96:99], v[40:43]\n"
      "v_fma_f32 v198, |v81|, v65, v198\n"
      "v_fma_f32 v198, |v82|, v66, v198\n"
      "v_mfma_f32_16x16x32_f16 v[88:91], a[100:103], v[100:103], v[88:91]\n"
      "v_fma_f32 v198, |v83|, v67, v198\n"
      "v_add_f32 v195, v195, v191\n"
      "v_mfma_f32_16x16x32_f16 v[88:91], a[104:107], v[104:107], v[88:91]\n"
      "v_fma_f32 v198, |v84|, v68, v198\n"
      "v_fma_f32 v198, |v85|, v69, v198\n"
      "v_mfma_f32_16x16x32_f16 v[88:91], a[108:111], v[108:111], v[88:91]\n"
      "s_waitcnt lgkmcnt(0)\n"
      "v_pk_mul_f16 v124, v140, v172\n"
      "v_pk_mul_f16 v125, v141, v173\n"
      "v_mfma_f32_16x16x32_f16 v[92:95], a[124:127], v[108:111], v[44:47]\n"
      "v_pk_mul_f16 v126, v142, v174\n"
      "v_pk_mul_f16 v127, v143, v175\n"
      "v_mfma_f32_16x16x32_f16 v[92:95], a[120:123], v[104:107], v[92:95]\n"
      "v_fma_f32 v198, |v86|, v70, v198\n"
      "v_fma_f32 v198, |v87|, v71, v198\n"
      "v_mfma_f32_16x16x32_f16 v[92:95], a[116:119], v[100:103], v[92:95]\n"
      "v_fma_f32 v198, |v88|, v72, v198\n"
      "v_fma_f32 v198, |v89|, v73, v198\n"
      "v_mfma_f32_16x16x32_f16 v[92:95], a[112:115], v[96:99], v[92:95]\n"
      "v_fma_f32 v198, |v90|, v74, v198\n"
      "v_fma_f32 v198, |v91|, v75, v198\n"
      "v_mfma_f32_16x16x32_f16 v[80:83], a[0:3], v[112:115], v[16:19]\n"
      "v_pk_mul_f16 v96, v144, v160\n"
      "v_pk_mul_f16 v97, v145, v161\n"
      "v_mfma_f32_16x16x32_f16 v[80:83], a[4:7], v[116:119], v[80:83]\n"
      "v_pk_mul_f16 v98, v146, v162\n"
      "v_pk_mul_f16 v99, v147, v163\n"
      "v_mfma_f32_16x16x32_f16 v[80:83], a[8:11], v[120:123], v[80:83]\n"
      "v_fma_f32 v198, |v92|, v76, v198\n"
      "v_fma_f32 v198, |v93|, v77, v198\n"
      "v_mfma_f32_16x16x32_f16 v[80:83], a[12:15], v[124:127], v[80:83]\n"
      "v_fma_f32 v198, |v94|, v78, v198\n"
      "v_fma_f32 v198, |v95|, v79, v198\n"
      "v_mfma_f32_16x16x32_f16 v[84:87], a[28:31], v[124:127], v[20:23]\n"
      "v_pk_mul_f16 v100, v148, v164\n"
      "v_pk_mul_f16 v101, v149, v165\n"
      "v_mfma_f32_16x16x32_f16 v[84:87], a[24:27], v[120:123], v[84:87]\n"
      "v_pk_mul_f16 v102, v150, v166\n"
      "v_pk_mul_f16 v103, v151, v167\n"
      "v_mfma_f32_16x16x32_f16 v[84:87], a[20:23], v[116:119], v[84:87]\n"
      "v_fma_f32 v192, |v80|, v48, 4.0\n"
      "v_fma_f32 v192, |v81|, v49, v192\n"
      "v_mfma_f32_16x16x32_f16 v[84:87], a[16:19], v[112:115], v[84:87]\n"
      "v_fma_f32 v192, |v82|, v50, v192\n"
      "v_fma_f32 v192, |v83|, v51, v192\n"
      "v_mfma_f32_16x16x32_f16 v[88:91], a[32:35], v[112:115], v[24:27]\n"
      "v_pk_mul_f16 v104, v152, v168\n"
      "v_pk_mul_f16 v105, v153, v169\n"
      "v_mfma_f32_16x16x32_f16 v[88:91], a[36:39], v[116:119], v[88:91]\n"
      "v_pk_mul_f16 v106, v154, v170\n"
      "v_pk_mul_f16 v107, v155, v171\n"
      "v_mfma_f32_16x16x32_f16 v[88:91], a[40:43], v[120:123], v[88:91]\n"
      "v_fma_f32 v192, |v84|, v52, v192\n"
      "v_fma_f32 v192, |v85|, v53, v192\n"
      "v_mfma_f32_16x16x32_f16 v[88:91], a[44:47], v[124:127], v[88:91]\n"
      "v_fma_f32 v192, |v86|, v54, v192\n"
      "v_fma_f32 v192, |v87|, v55, v192\n"
      "v_mfma_f32_16x16x32_f16 v[92:95], a[60:63], v[124:127], v[28:31]\n"
      "ds_read2_b32 v[228:229], v204 offset0:0 offset1:8\n"
      "ds_read2_b32 v[230:231], v205 offset0:0 offset1:8\n"
      "v_mfma_f32_16x16x32_f16 v[92:95], a[56:59], v[120:123], v[92:95]\n"
      "ds_read_b32 v224, v227 offset:37640\n"
      "ds_read2_b32 v[232:233], v206 offset0:0 offset1:8\n"
      "v_mfma_f32_16x16x32_f16 v[92:95], a[52:55], v[116:119], v[92:95]\n"
      "v_fma_f32 v192, |v88|, v56, v192\n"
      "v_fma_f32 v192, |v89|, v57, v192\n"
      "v_mfma_f32_16x16x32_f16 v[92:95], a[48:51], v[112:115], v[92:95]\n"
      "v_fma_f32 v192, |v90|, v58, v192\n"
      "v_fma_f32 v192, |v91|, v59, v192\n"
      "v_mfma_f32_16x16x32_f16 v[80:83], a[64:67], v[112:115], v[32:35]\n"
      "v_pk_mul_f16 v108, v156, v172\n"
      "v_pk_mul_f16 v109, v157, v173\n"
      "v_mfma_f32_16x16x32_f16 v[80:83], a[68:71], v[116:119], v[80:83]\n"
      "ds_read2_b32 v[234:235], v207 offset0:0 offset1:8\n"
      "ds_read2_b32 v[236:237], v208 offset0:0 offset1:8\n"
      "v_mfma_f32_16x16x32_f16 v[80:83], a[72:75], v[120:123], v[80:83]\n"
      "v_fma_f32 v192, |v92|, v60, v192\n"
      "v_fma_f32 v192, |v93|, v61, v192\n"
      "v_mfma_f32_16x16x32_f16 v[80:83], a[76:79], v[124:127], v[80:83]\n"
      "v_fma_f32 v192, |v94|, v62, v192\n"
      "v_fma_f32 v192, |v95|, v63, v192\n"
      "v_mfma_f32_16x16x32_f16 v[84:87], a[92:95], v[124:127], v[36:39]\n"
      "v_pk_mul_f16 v110, v158, v174\n"
      "v_pk_mul_f16 v111, v159, v175\n"
      "v_mfma_f32_16x16x32_f16 v[84:87], a[88:91], v[120:123], v[84:87]\n"
      "s_waitcnt lgkmcnt(3)\n"
      "ds_read2_b32 v[238:239], v209 offset0:0 offset1:8\n"
      "v_cmp_eq_u32 vcc, 1, v224\n"
      "v_mfma_f32_16x16x32_f16 v[84:87], a[84:87], v[116:119], v[84:87]\n"
      "v_fma_f32 v192, |v80|, v64, v192\n"
      "v_fma_f32 v192, |v81|, v65, v192\n"
      "v_mfma_f32_16x16x32_f16 v[84:87], a[80:83], v[112:115], v[84:87]\n"
      "s_and_b32 s44, vcc_lo, s54\n"
      "v_fma_f32 v192, |v82|, v66, v192\n"
      "v_fma_f32 v192, |v83|, v67, v192\n"
      "v_mfma_f32_16x16x32_f16 v[88:91], a[96:99], v[112:115], v[40:43]\n"
      "s_cmp_lg_u32 s44, s54\n"
      "ds_read_b128 v[128:131], v210 offset:0\n"
      "ds_read_b128 v[144:147], v211 offset:0\n"
      "v_mfma_f32_16x16x32_f16 v[88:91], a[100:103], v[116:119], v[88:91]\n"
      "s_cbranch_scc1 80f\n"
      "81:\n"
      "v_mfma_f32_16x16x32_f16 v[88:91], a[104:107], v[120:123], v[88:91]\n"
      "v_fma_f32 v192, |v84|, v68, v192\n"
      "v_fma_f32 v192, |v85|, v69, v192\n"
      "v_mfma_f32_16x16x32_f16 v[88:91], a[108:111], v[124:127], v[88:91]\n"
      "v_fma_f32 v192, |v86|, v70, v192\n"
      "v_fma_f32 v192, |v87|, v71, v192\n"
      "v_mfma_f32_16x16x32_f16 v[92:95], a[124:127], v[124:127], v[44:47]\n"
      "v_pk_mul_f16 v124, v252, v172\n"
      "v_pk_mul_f16 v125, v253, v173\n"
      "v_mfma_f32_16x16x32_f16 v[92:95], a[120:123], v[120:123], v[92:95]\n"
      "v_pk_mul_f16 v120, v248, v168\n"
      "v_pk_mul_f16 v121, v249, v169\n"
      "v_mfma_f32_16x16x32_f16 v[92:95], a[116:119], v[116:119], v[92:95]\n"
      "v_fma_f32 v192, |v88|, v72, v192\n"
      "v_fma_f32 v192, |v89|, v73, v192\n"
      "v_mfma_f32_16x16x32_f16 v[92:95], a[112:115], v[112:115], v[92:95]\n"
      "v_fma_f32 v192, |v90|, v74, v192\n"
      "v_fma_f32 v192, |v91|, v75, v192\n"
      "v_mfma_f32_16x16x32_f16 v[80:83], a[0:3], v[96:99], v[16:19]\n"
      "v_pk_mul_f16 v112, v240, v160\n"
      "v_pk_mul_f16 v113, v241, v161\n"
      "v_mfma_f32_16x16x32_f16 v[80:83], a[4:7], v[100:103], v[80:83]\n"
      "v_pk_mul_f16 v114, v242, v162\n"
      "v_pk_mul_f16 v115, v243, v163\n"
      "v_mfma_f32_16x16x32_f16 v[80:83], a[8:11], v[104:107], v[80:83]\n"
      "v_fma_f32 v192, |v92|, v76, v192\n"
      "v_fma_f32 v192, |v93|, v77, v192\n"
      "v_mfma_f32_16x16x32_f16 v[80:83], a[12:15], v[108:111], v[80:83]\n"
      "v_fma_f32 v192, |v94|, v78, v192\n"
      "v_fma_f32 v192, |v95|, v79, v192\n"
      "v_mfma_f32_16x16x32_f16 v[84:87], a[28:31], v[108:111], v[20:23]\n"
      "v_pk_mul_f16 v116, v244, v164\n"
      "v_pk_mul_f16 v117, v245, v165\n"
      "v_mfma_f32_16x16x32_f16 v[84:87], a[24:27], v[104:107], v[84:87]\n"
      "v_pk_mul_f16 v118, v246, v166\n"
      "v_pk_mul_f16 v119, v247, v167\n"
      "v_mfma_f32_16x16x32_f16 v[84:87], a[20:23], v[100:103], v[84:87]\n"
      "v_fma_f32 v193, |v80|, v48, 4.0\n"
      "v_fma_f32 v193, |v81|, v49, v193\n"
      "v_mfma_f32_16x16x32_f16 v[84:87], a[16:19], v[96:99], v[84:87]\n"
      "v_fma_f32 v193, |v82|, v50, v193\n"
      "v_fma_f32 v193, |v83|, v51, v193\n"
      "v_mfma_f32_16x16x32_f16 v[88:91], a[32:35], v[96:99], v[24:27]\n"
      "v_pk_mul_f16 v122, v250, v170\n"
      "v_pk_mul_f16 v123, v251, v171\n"
      "v_mfma_f32_16x16x32_f16 v[88:91], a[36:39], v[100:103], v[88:91]\n"
      "v_pk_mul_f16 v126, v254, v174\n"
      "v_pk_mul_f16 v127, v255, v175\n"
      "v_mfma_f32_16x16x32_f16 v[88:91], a[40:43], v[104:107], v[88:91]\n"
      "v_fma_f32 v193, |v84|, v52, v193\n"
      "v_fma_f32 v193, |v85|, v53, v193\n"
      "v_mfma_f32_16x16x32_f16 v[88:91], a[44:47], v[108:111], v[88:91]\n"
      "v_fma_f32 v193, |v86|, v54, v193\n"
      "v_fma_f32 v193, |v87|, v55, v193\n"
      "v_mfma_f32_16x16x32_f16 v[92:95], a[60:63], v[108:111], v[28:31]\n"
      "ds_read_b128 v[160:163], v10 offset:0\n"
      "ds_read_b128 v[240:243], v11 offset:0\n"
      "v_mfma_f32_16x16x32_f16 v[92:95], a[56:59], v[104:107], v[92:95]\n"
      "ds_read_b128 v[164:167], v10 offset:64\n"
      "ds_read_b128 v[244:247], v11 offset:64\n"
      "v_mfma_f32_16x16x32_f16 v[92:95], a[52:55], v[100:103], v[92:95]\n"
      "v_fma_f32 v193, |v88|, v56, v193\n"
      "v_fma_f32 v193, |v89|, v57, v193\n"
      "v_mfma_f32_16x16x32_f16 v[92:95], a[48:51], v[96:99], v[92:95]\n"
      "v_fma_f32 v193, |v90|, v58, v193\n"
      "v_fma_f32 v193, |v91|, v59, v193\n"
      "v_mfma_f32_16x16x32_f16 v[80:83], a[64:67], v[96:99], v[32:35]\n"
      "ds_read_b128 v[168:171], v10 offset:128\n"
      "ds_read_b128 v[248:251], v11 offset:128\n"
      "v_mfma_f32_16x16x32_f16 v[80:83], a[68:71], v[100:103], v[80:83]\n"
      "ds_read_b128 v[172:175], v10 offset:192\n"
      "ds_read_b128 v[252:255], v11 offset:192\n"
      "v_mfma_f32_16x16x32_f16 v[80:83], a[72:75], v[104:107], v[80:83]\n"
      "v_fma_f32 v193, |v92|, v60, v193\n"
      "v_fma_f32 v193, |v93|, v61, v193\n"
      "v_mfma_f32_16x16x32_f16 v[80:83], a[76:79], v[108:111], v[80:83]\n"
      "v_fma_f32 v193, |v94|, v62, v193\n"
      "v_fma_f32 v193, |v95|, v63, v193\n"
      "v_mfma_f32_16x16x32_f16 v[84:87], a[92:95], v[108:111], v[36:39]\n"
      "s_waitcnt lgkmcnt(6)\n"
      "v_pk_mul_f16 v160, v160, v240\n"
      "v_pk_mul_f16 v161, v161, v241\n"
      "v_mfma_f32_16x16x32_f16 v[84:87], a[88:91], v[104:107], v[84:87]\n"
      "v_pk_mul_f16 v162, v162, v242\n"
      "v_pk_mul_f16 v163, v163, v243\n"
      "v_mfma_f32_16x16x32_f16 v[84:87], a[84:87], v[100:103], v[84:87]\n"
      "v_fma_f32 v193, |v80|, v64, v193\n"
      "v_fma_f32 v193, |v81|, v65, v193\n"
      "v_mfma_f32_16x16x32_f16 v[84:87], a[80:83], v[96:99], v[84:87]\n"
      "v_fma_f32 v193, |v82|, v66, v193\n"
      "v_fma_f32 v193, |v83|, v67, v193\n"
      "v_mfma_f32_16x16x32_f16 v[88:91], a[96:99], v[96:99], v[40:43]\n"
      "s_waitcnt lgkmcnt(4)\n"
      "v_pk_mul_f16 v164, v164, v244\n"
      "v_pk_mul_f16 v165, v165, v245\n"
      "v_mfma_f32_16x16x32_f16 v[88:91], a[100:103], v[100:103], v[88:91]\n"
      "43:\n"
      "s_cbranch_execz 44b\n"
      "v_pk_mul_f16 v166, v166, v246\n"
      "v_mfma_f32_16x16x32_f16 v[88:91], a[104:107], v[104:107], v[88:91]\n"
      "v_fma_f32 v193, |v84|, v68, v193\n"
      "v_fma_f32 v193, |v85|, v69, v193\n"
      "v_mfma_f32_16x16x32_f16 v[88:91], a[108:111], v[108:111], v[88:91]\n"
      "v_fma_f32 v193, |v86|, v70, v193\n"
      "v_fma_f32 v193, |v87|, v71, v193\n"
      "v_mfma_f32_16x16x32_f16 v[92:95], a[124:127], v[108:111], v[44:47]\n"
      "s_waitcnt lgkmcnt(2)\n"
      "v_pk_mul_f16 v167, v167, v247\n"
      "v_pk_mul_f16 v168, v168, v248\n"
      "v_mfma_f32_16x16x32_f16 v[92:95], a[120:123], v[104:107], v[92:95]\n"
      "v_pk_mul_f16 v169, v169, v249\n"
      "v_pk_mul_f16 v170, v170, v250\n"
      "v_mfma_f32_16x16x32_f16 v[92:95], a[116:119], v[100:103], v[92:95]\n"
      "v_fma_f32 v193, |v88|, v72, v193\n"
      "v_fma_f32 v193, |v89|, v73, v193\n"
      "v_mfma_f32_16x16x32_f16 v[92:95], a[112:115], v[96:99], v[92:95]\n"
      "v_fma_f32 v193, |v90|, v74, v193\n"
      "v_fma_f32 v193, |v91|, v75, v193\n"
      "v_mfma_f32_16x16x32_f16 v[80:83], a[0:3], v[112:115], v[16:19]\n"
      "ds_read_b128 v[132:135], v210 offset:64\n"
      "ds_read_b128 v[148:151], v211 offset:64\n"
      "v_mfma_f32_16x16x32_f16 v[80:83], a[4:7], v[116:119], v[80:83]\n"
      "v_pk_mul_f16 v128, v128, v144\n"
      "v_pk_mul_f16 v171, v171, v251\n"
      "v_mfma_f32_16x16x32_f16 v[80:83], a[8:11], v[120:123], v[80:83]\n"
      "v_fma_f32 v193, |v92|, v76, v193\n"
      "v_fma_f32 v193, |v93|, v77, v193\n"
      "v_mfma_f32_16x16x32_f16 v[80:83], a[12:15], v[124:127], v[80:83]\n"
      "v_fma_f32 v193, |v94|, v78, v193\n"
      "v_fma_f32 v193, |v95|, v79, v193\n"
      "v_mfma_f32_16x16x32_f16 v[84:87], a[28:31], v[124:127], v[20:23]\n"
      "v_pk_mul_f16 v129, v129, v145\n"
      "v_pk_mul_f16 v130, v130, v146\n"
      "v_mfma_f32_16x16x32_f16 v[84:87], a[24:27], v[120:123], v[84:87]\n"
      "s_waitcnt lgkmcnt(2)\n"
      "v_pk_mul_f16 v131, v131, v147\n"
      "v_pk_mul_f16 v172, v172, v252\n"
      "v_mfma_f32_16x16x32_f16 v[84:87], a[20:23], v[116:119], v[84:87]\n"
      "v_fma_f32 v190, |v80|, v48, 4.0\n"
      "v_fma_f32 v190, |v81|, v49, v190\n"
      "v_mfma_f32_16x16x32_f16 v[84:87], a[16:19], v[112:115], v[84:87]\n"
      "v_fma_f32 v190, |v82|, v50, v190\n"
      "v_fma_f32 v190, |v83|, v51, v190\n"
      "v_mfma_f32_16x16x32_f16 v[88:91], a[32:35], v[112:115], v[24:27]\n"
      "v_pk_mul_f16 v173, v173, v253\n"
      "v_pk_mul_f16 v174, v174, v254\n"
      "v_mfma_f32_16x16x32_f16 v[88:91], a[36:39], v[116:119], v[88:91]\n"
      "ds_read_b128 v[136:139], v210 offset:128\n"
      "ds_read_b128 v[152:155], v211 offset:128\n"
      "v_mfma_f32_16x16x32_f16 v[88:91], a[40:43], v[120:123], v[88:91]\n"
      "v_fma_f32 v190, |v84|, v52, v190\n"
      "v_fma_f32 v190, |v85|, v53, v190\n"
      "v_mfma_f32_16x16x32_f16 v[88:91], a[44:47], v[124:127], v[88:91]\n"
      "v_fma_f32 v190, |v86|, v54, v190\n"
      "v_fma_f32 v190, |v87|, v55, v190\n"
      "v_mfma_f32_16x16x32_f16 v[92:95], a[60:63], v[124:127], v[28:31]\n"
      "s_waitcnt lgkmcnt(2)\n"
      "v_pk_mul_f16 v132, v132, v148\n"
      "v_pk_mul_f16 v175, v175, v255\n"
      "v_mfma_f32_16x16x32_f16 v[92:95], a[56:59], v[120:123], v[92:95]\n"
      "v_pk_mul_f16 v133, v133, v149\n"
      "v_pk_mul_f16 v134, v134, v150\n"
      "v_mfma_f32_16x16x32_f16 v[92:95], a[52:55], v[116:119], v[92:95]\n"
      "v_fma_f32 v190, |v88|, v56, v190\n"
      "v_fma_f32 v190, |v89|, v57, v190\n"
      "v_mfma_f32_16x16x32_f16 v[92:95], a[48:51], v[112:115], v[92:95]\n"
      "v_fma_f32 v190, |v90|, v58, v190\n"
      "v_fma_f32 v190, |v91|, v59, v190\n"
      "v_mfma_f32_16x16x32_f16 v[80:83], a[64:67], v[112:115], v[32:35]\n"
      "v_fmac_f32 v189, v200, v228\n"
      "v_fmac_f32 v194, v200, v230\n"
      "v_mfma_f32_16x16x32_f16 v[80:83], a[68:71], v[116:119], v[80:83]\n"
      "v_permlane32_swap_b32 v198, v192\n"
      "v_mfma_f32_16x16x32_f16 v[80:83], a[72:75], v[120:123], v[80:83]\n"
      "v_fma_f32 v190, |v92|, v60, v190\n"
      "v_fma_f32 v190, |v93|, v61, v190\n"
      "v_mfma_f32_16x16x32_f16 v[80:83], a[76:79], v[124:127], v[80:83]\n"
      "v_fma_f32 v190, |v94|, v62, v190\n"
      "v_fma_f32 v190, |v95|, v63, v190\n"
      "v_mfma_f32_16x16x32_f16 v[84:87], a[92:95], v[124:127], v[36:39]\n"
      "s_waitcnt lgkmcnt(0)\n"
      "v_pk_mul_f16 v135, v135, v151\n"
      "v_pk_mul_f16 v136, v136, v152\n"
      "v_mfma_f32_16x16x32_f16 v[84:87], a[88:91], v[120:123], v[84:87]\n"
      "v_fmac_f32 v195, v200, v232\n"
      "v_max3_f32 v219, v189, v194, v195\n"
      "v_mfma_f32_16x16x32_f16 v[84:87], a[84:87], v[116:119], v[84:87]\n"
      "v_fma_f32 v190, |v80|, v64, v190\n"
      "v_fma_f32 v190, |v81|, v65, v190\n"
      "v_mfma_f32_16x16x32_f16 v[84:87], a[80:83], v[112:115], v[84:87]\n"
      "v_fma_f32 v190, |v82|, v66, v190\n"
      "v_fma_f32 v190, |v83|, v67, v190\n"
      "v_mfma_f32_16x16x32_f16 v[88:91], a[96:99], v[112:115], v[40:43]\n"
      "ds_read_b128 v[140:143], v210 offset:192\n"
      "v_min3_f32 v226, v189, v194, v195\n"
      "v_mfma_f32_16x16x32_f16 v[88:91], a[100:103], v[116:119], v[88:91]\n"
      "ds_read_b128 v[156:159], v211 offset:192\n"
      "ds_read_b128 a[204:207], v215 offset:11968\n"
      "v_mfma_f32_16x16x32_f16 v[88:91], a[104:107], v[120:123], v[88:91]\n"
      "v_fma_f32 v190, |v84|, v68, v190\n"
      "v_fma_f32 v190, |v85|, v69, v190\n"
      "v_mfma_f32_16x16x32_f16 v[88:91], a[108:111], v[124:127], v[88:91]\n"
      "v_fma_f32 v190, |v86|, v70, v190\n"
      "v_fma_f32 v190, |v87|, v71, v190\n"
      "v_mfma_f32_16x16x32_f16 v[92:95], a[124:127], v[124:127], v[44:47]\n"
      "v_sub_f32 v189, v189, v219\n"
      "v_sub_f32 v194, v194, v219\n"
      "v_mfma_f32_16x16x32_f16 v[92:95], a[120:123], v[120:123], v[92:95]\n"
      "ds_read_b128 a[208:211], v215 offset:12032\n"
      "v_pk_mul_f16 v137, v137, v153\n"
      "v_mfma_f32_16x16x32_f16 v[92:95], a[116:119], v[116:119], v[92:95]\n"
      "v_fma_f32 v190, |v88|, v72, v190\n"
      "v_fma_f32 v190, |v89|, v73, v190\n"
      "v_mfma_f32_16x16x32_f16 v[92:95], a[112:115], v[112:115], v[92:95]\n"
      "v_fma_f32 v190, |v90|, v74, v190\n"
      "v_fma_f32 v190, |v91|, v75, v190\n"
      "v_mfma_f32_16x16x32_f16 v[80:83], a[0:3], v[160:163], v[16:19]\n"
      "ds_read_b128 a[212:215], v215 offset:12096\n"
      "v_sub_f32 v195, v195, v219\n"
      "v_mfma_f32_16x16x32_f16 v[80:83], a[4:7], v[164:167], v[80:83]\n"
      "v_exp_f32 v189, v189\n"
      "v_mfma_f32_16x16x32_f16 v[80:83], a[8:11], v[168:171], v[80:83]\n"
      "v_exp_f32 v194, v194\n"
      "v_mfma_f32_16x16x32_f16 v[80:83], a[12:15], v[172:175], v[80:83]\n"
      "v_exp_f32 v195, v195\n"
      "v_mfma_f32_16x16x32_f16 v[84:87], a[28:31], v[172:175], v[20:23]\n"
      "v_pk_mul_f16 v138, v138, v154\n"
      "v_pk_mul_f16 v139, v139, v155\n"
      "v_mfma_f32_16x16x32_f16 v[84:87], a[24:27], v[168:171], v[84:87]\n"
      "v_fma_f32 v190, |v92|, v76, v190\n"
      "v_fma_f32 v190, |v93|, v77, v190\n"
      "v_mfma_f32_16x16x32_f16 v[84:87], a[20:23], v[164:167], v[84:87]\n"
      "v_fma_f32 v190, |v94|, v78, v190\n"
      "v_fma_f32 v190, |v95|, v79, v190\n"
      "v_mfma_f32_16x16x32_f16 v[84:87], a[16:19], v[160:163], v[84:87]\n"
      "v_mul_f32 v221, v189, v229\n"
      "v_add_f32 v198, v198, v192\n"
      "v_mfma_f32_16x16x32_f16 v[88:91], a[32:35], v[160:163], v[24:27]\n"
      "v_permlane32_swap_b32 v193, v190\n"
      "v_mfma_f32_16x16x32_f16 v[88:91], a[36:39], v[164:167], v[88:91]\n"
      "v_add_f32 v220, v189, v194\n"
      "v_add_f32 v193, v193, v190\n"
      "v_mfma_f32_16x16x32_f16 v[88:91], a[40:43], v[168:171], v[88:91]\n"
      "v_permlane16_swap_b32 v188, v198\n"
      "v_mfma_f32_16x16x32_f16 v[88:91], a[44:47], v[172:175], v[88:91]\n"
      "v_fma_f32 v199, |v80|, v48, 2.0\n"
      "v_fma_f32 v199, |v81|, v49, v199\n"
      "v_mfma_f32_16x16x32_f16 v[92:95], a[60:63], v[172:175], v[28:31]\n"
      "s_waitcnt lgkmcnt(3)\n"
      "v_pk_mul_f16 v140, v140, v156\n"
      "v_fmac_f32 v221, v194, v231\n"
      "v_mfma_f32_16x16x32_f16 v[92:95], a[56:59], v[168:171], v[92:95]\n"
      "v_fma_f32 v199, |v82|, v50, v199\n"
      "v_fma_f32 v199, |v83|, v51, v199\n"
      "v_mfma_f32_16x16x32_f16 v[92:95], a[52:55], v[164:167], v[92:95]\n"
      "v_pk_mul_f16 v141, v141, v157\n"
      "v_add_f32 v220, v220, v195\n"
      "v_mfma_f32_16x16x32_f16 v[92:95], a[48:51], v[160:163], v[92:95]\n"
      "v_add_f32 v188, v188, v198\n"
      "v_pk_mul_f16 v142, v142, v158\n"
      "v_mfma_f32_16x16x32_f16 v[80:83], a[0:3], v[128:131], v[16:19]\n"
      "v_fmac_f32 v221, v195, v233\n"
      "v_fmac_f32 v188, v200, v234\n"
      "v_mfma_f32_16x16x32_f16 v[80:83], a[4:7], v[132:135], v[80:83]\n"
      "v_pk_mul_f16 v143, v143, v159\n"
      "v_fma_f32 v199, |v84|, v52, v199\n"
      "v_mfma_f32_16x16x32_f16 v[80:83], a[8:11], v[136:139], v[80:83]\n"
      "v_fma_f32 v199, |v85|, v53, v199\n"
      "v_fma_f32 v199, |v86|, v54, v199\n"
      "v_mfma_f32_16x16x32_f16 v[80:83], a[12:15], v[140:143], v[80:83]\n"
      "v_fma_f32 v199, |v87|, v55, v199\n"
      "v_fma_f32 v199, |v88|, v56, v199\n"
      "v_mfma_f32_16x16x32_f16 v[84:87], a[28:31], v[140:143], v[20:23]\n"
      "v_fma_f32 v199, |v89|, v57, v199\n"
      "v_fma_f32 v199, |v90|, v58, v199\n"
      "v_mfma_f32_16x16x32_f16 v[84:87], a[24:27], v[136:139], v[84:87]\n"
      "v_fma_f32 v199, |v91|, v59, v199\n"
      "v_fma_f32 v199, |v92|, v60, v199\n"
      "v_mfma_f32_16x16x32_f16 v[84:87], a[20:23], v[132:135], v[84:87]\n"
      "ds_read_b128 v[112:115], v216 offset:0\n"
      "ds_read_b128 v[144:147], v217 offset:0\n"
      "v_mfma_f32_16x16x32_f16 v[84:87], a[16:19], v[128:131], v[84:87]\n"
      "v_fma_f32 v199, |v93|, v61, v199\n"
      "v_fma_f32 v199, |v94|, v62, v199\n"
      "v_mfma_f32_16x16x32_f16 v[180:183], a[200:203], v[160:163], 0\n"
      "v_fma_f32 v199, |v95|, v63, v199\n"
      "v_fma_f32 v191, |v80|, v48, 4.0\n"
      "v_mfma_f32_16x16x32_f16 v[88:91], a[32:35], v[128:131], v[24:27]\n"
      "ds_write_b32 v1, v199 offset:12992\n"
      "v_mfma_f32_16x16x32_f16 v[88:91], a[36:39], v[132:135], v[88:91]\n"
      "ds_read_b128 v[116:119], v216 offset:64\n"
      "ds_read_b128 v[148:151], v217 offset:64\n"
      "v_mfma_f32_16x16x32_f16 v[88:91], a[40:43], v[136:139], v[88:91]\n"
      "v_permlane16_swap_b32 v193, v7\n"
      "v_mfma_f32_16x16x32_f16 v[88:91], a[44:47], v[140:143], v[88:91]\n"
      "v_fma_f32 v191, |v81|, v49, v191\n"
      "v_fma_f32 v191, |v82|, v50, v191\n"
      "v_mfma_f32_16x16x32_f16 v[92:95], a[60:63], v[140:143], v[28:31]\n"
      "v_fma_f32 v191, |v83|, v51, v191\n"
      "v_fma_f32 v191, |v84|, v52, v191\n"
      "v_mfma_f32_16x16x32_f16 v[92:95], a[56:59], v[136:139], v[92:95]\n"
      "s_waitcnt lgkmcnt(3)\n"
      "v_pk_mul_f16 v112, v112, v144\n"
      "v_pk_mul_f16 v113, v113, v145\n"
      "v_mfma_f32_16x16x32_f16 v[92:95], a[52:55], v[132:135], v[92:95]\n"
      "v_pk_mul_f16 v114, v114, v146\n"
      "v_pk_mul_f16 v115, v115, v147\n"
      "v_mfma_f32_16x16x32_f16 v[92:95], a[48:51], v[128:131], v[92:95]\n"
      "ds_read_b128 v[120:123], v216 offset:128\n"
      "ds_read_b128 v[152:155], v217 offset:128\n"
      "v_mfma_f32_16x16x32_f16 v[180:183], a[204:207], v[164:167], v[180:183]\n"
      "v_fma_f32 v191, |v85|, v53, v191\n"
      "v_fma_f32 v191, |v86|, v54, v191\n"
      "v_mfma_f32_16x16x32_f16 v[80:83], a[64:67], v[128:131], v[32:35]\n"
      "v_fma_f32 v191, |v87|, v55, v191\n"
      "v_fma_f32 v191, |v88|, v56, v191\n"
      "v_mfma_f32_16x16x32_f16 v[80:83], a[68:71], v[132:135], v[80:83]\n"
      "s_waitcnt lgkmcnt(2)\n"
      "v_pk_mul_f16 v116, v116, v148\n"
      "v_pk_mul_f16 v117, v117, v149\n"
      "v_mfma_f32_16x16x32_f16 v[80:83], a[72:75], v[136:139], v[80:83]\n"
      "v_fma_f32 v191, |v89|, v57, v191\n"
      "v_fma_f32 v191, |v90|, v58, v191\n"
      "v_mfma_f32_16x16x32_f16 v[80:83], a[76:79], v[140:143], v[80:83]\n"
      "v_pk_mul_f16 v118, v118, v150\n"
      "v_pk_mul_f16 v119, v119, v151\n"
      "v_mfma_f32_16x16x32_f16 v[84:87], a[92:95], v[140:143], v[36:39]\n"
      "ds_read_b128 v[124:127], v216 offset:192\n"
      "ds_read_b128 v[156:159], v217 offset:192\n"
      "v_mfma_f32_16x16x32_f16 v[84:87], a[88:91], v[136:139], v[84:87]\n"
      "v_fma_f32 v191, |v91|, v59, v191\n"
      "v_fma_f32 v191, |v92|, v60, v191\n"
      "v_mfma_f32_16x16x32_f16 v[84:87], a[84:87], v[132:135], v[84:87]\n"
      "s_waitcnt lgkmcnt(2)\n"
      "v_pk_mul_f16 v120, v120, v152\n"
      "v_pk_mul_f16 v121, v121, v153\n"
      "v_mfma_f32_16x16x32_f16 v[84:87], a[80:83], v[128:131], v[84:87]\n"
      "v_fma_f32 v191, |v93|, v61, v191\n"
      "v_fma_f32 v191, |v94|, v62, v191\n"
      "v_mfma_f32_16x16x32_f16 v[88:91], a[96:99], v[128:131], v[40:43]\n"
      "v_fma_f32 v191, |v95|, v63, v191\n"
      "v_fma_f32 v191, |v80|, v64, v191\n"
      "v_mfma_f32_16x16x32_f16 v[88:91], a[100:103], v[132:135], v[88:91]\n"
      "v_pk_mul_f16 v122, v122, v154\n"
      "v_pk_mul_f16 v123, v123, v155\n"
      "v_mfma_f32_16x16x32_f16 v[88:91], a[104:107], v[136:139], v[88:91]\n"
      "v_fma_f32 v191, |v81|, v65, v191\n"
      "v_fma_f32 v191, |v82|, v66, v191\n"
      "v_mfma_f32_16x16x32_f16 v[88:91], a[108:111], v[140:143], v[88:91]\n"
      "v_fma_f32 v191, |v83|, v67, v191\n"
      "v_fma_f32 v191, |v84|, v68, v191\n"
      "v_mfma_f32_16x16x32_f16 v[92:95], a[124:127], v[140:143], v[44:47]\n"
      "s_waitcnt lgkmcnt(0)\n"
      "v_pk_mul_f16 v124, v124, v156\n"
      "v_pk_mul_f16 v125, v125, v157\n"
      "v_mfma_f32_16x16x32_f16 v[92:95], a[120:123], v[136:139], v[92:95]\n"
      "v_pk_mul_f16 v126, v126, v158\n"
      "v_pk_mul_f16 v127, v127, v159\n"
      "v_mfma_f32_16x16x32_f16 v[92:95], a[116:119], v[132:135], v[92:95]\n"
      "v_fma_f32 v191, |v85|, v69, v191\n"
      "v_fma_f32 v191, |v86|, v70, v191\n"
      "v_mfma_f32_16x16x32_f16 v[92:95], a[112:115], v[128:131], v[92:95]\n"
      "v_fma_f32 v191, |v87|, v71, v191\n"
      "v_fma_f32 v191, |v88|, v72, v191\n"
      "v_mfma_f32_16x16x32_f16 v[80:83], a[0:3], v[112:115], v[16:19]\n"
      "v_fma_f32 v191, |v89|, v73, v191\n"
      "v_fma_f32 v191, |v90|, v74, v191\n"
      "v_mfma_f32_16x16x32_f16 v[80:83], a[4:7], v[116:119], v[80:83]\n"
      "v_fma_f32 v191, |v91|, v75, v191\n"
      "v_add_f32 v193, v193, v7\n"
      "v_mfma_f32_16x16x32_f16 v[80:83], a[8:11], v[120:123], v[80:83]\n"
      "v_fma_f32 v191, |v92|, v76, v191\n"
      "v_fma_f32 v191, |v93|, v77, v191\n"
      "v_mfma_f32_16x16x32_f16 v[80:83], a[12:15], v[124:127], v[80:83]\n"
      "v_fma_f32 v191, |v94|, v78, v191\n"
      "v_fma_f32 v191, |v95|, v79, v191\n"
      "v_mfma_f32_16x16x32_f16 v[84:87], a[28:31], v[124:127], v[20:23]\n"
      "ds_read_b32 v224, v12 offset:12992\n"
      "v_fmac_f32 v193, v200, v236\n"
      "v_mfma_f32_16x16x32_f16 v[84:87], a[24:27], v[120:123], v[84:87]\n"
      "v_max3_f32 v222, v219, v188, v193\n"
      "v_min3_f32 v226, v226, v188, v193\n"
      "v_mfma_f32_16x16x32_f16 v[84:87], a[20:23], v[116:119], v[84:87]\n"
      "v_fma_f32 v196, |v80|, v48, 4.0\n"
      "v_fma_f32 v196, |v81|, v49, v196\n"
      "v_mfma_f32_16x16x32_f16 v[84:87], a[16:19], v[112:115], v[84:87]\n"
      "v_fma_f32 v196, |v82|, v50, v196\n"
      "v_fma_f32 v196, |v83|, v51, v196\n"
      "v_mfma_f32_16x16x32_f16 v[88:91], a[32:35], v[112:115], v[24:27]\n"
      "v_sub_f32 v219, v219, v222\n"
      "v_sub_f32 v188, v188, v222\n"
      "v_mfma_f32_16x16x32_f16 v[88:91], a[36:39], v[116:119], v[88:91]\n"
      "v_sub_f32 v193, v193, v222\n"
      "v_mfma_f32_16x16x32_f16 v[88:91], a[40:43], v[120:123], v[88:91]\n"
      "v_exp_f32 v219, v219\n"
      "v_mfma_f32_16x16x32_f16 v[88:91], a[44:47], v[124:127], v[88:91]\n"
      "v_exp_f32 v188, v188\n"
      "v_mfma_f32_16x16x32_f16 v[92:95], a[60:63], v[124:127], v[28:31]\n"
      "v_exp_f32 v193, v193\n"
      "v_mfma_f32_16x16x32_f16 v[92:95], a[56:59], v[120:123], v[92:95]\n"
      "v_fma_f32 v196, |v84|, v52, v196\n"
      "v_fma_f32 v196, |v85|, v53, v196\n"
      "v_mfma_f32_16x16x32_f16 v[92:95], a[52:55], v[116:119], v[92:95]\n"
      "v_fma_f32 v196, |v86|, v54, v196\n"
      "v_fma_f32 v196, |v87|, v55, v196\n"
      "v_mfma_f32_16x16x32_f16 v[92:95], a[48:51], v[112:115], v[92:95]\n"
      "v_fma_f32 v196, |v88|, v56, v196\n"
      "v_fma_f32 v196, |v89|, v57, v196\n"
      "v_mfma_f32_16x16x32_f16 v[80:83], a[64:67], v[112:115], v[32:35]\n"
      "v_fma_f32 v196, |v90|, v58, v196\n"
      "v_fma_f32 v196, |v91|, v59, v196\n"
      "v_mfma_f32_16x16x32_f16 v[80:83], a[68:71], v[116:119], v[80:83]\n"
      "s_waitcnt lgkmcnt(0)\n"
      "v_cmp_eq_u32 vcc, s37, v224\n"
      "v_mul_f32 v221, v221, v219\n"
      "v_mfma_f32_16x16x32_f16 v[80:83], a[72:75], v[120:123], v[80:83]\n"
      "v_fma_f32 v196, |v92|, v60, v196\n"
      "v_fma_f32 v196, |v93|, v61, v196\n"
      "v_mfma_f32_16x16x32_f16 v[80:83], a[76:79], v[124:127], v[80:83]\n"
      "s_or_b32 vcc_lo, vcc_lo, s36\n"
      "v_fma_f32 v220, v220, v219, v188\n"
      "v_fma_f32 v196, |v94|, v62, v196\n"
      "v_mfma_f32_16x16x32_f16 v[84:87], a[92:95], v[124:127], v[36:39]\n"
      "s_cmp_lg_u64 vcc, 0\n"
      "v_fma_f32 v196, |v95|, v63, v196\n"
      "v_fmac_f32 v221, v188, v235\n"
      "v_mfma_f32_16x16x32_f16 v[84:87], a[88:91], v[120:123], v[84:87]\n"
      "s_cbranch_scc1 82f\n"
      "83:\n"
      "v_mfma_f32_16x16x32_f16 v[84:87], a[84:87], v[116:119], v[84:87]\n"
      "v_fma_f32 v196, |v80|, v64, v196\n"
      "v_fma_f32 v196, |v81|, v65, v196\n"
      "v_mfma_f32_16x16x32_f16 v[84:87], a[80:83], v[112:115], v[84:87]\n"
      "v_fma_f32 v196, |v82|, v66, v196\n"
      "v_fma_f32 v196, |v83|, v67, v196\n"
      "v_mfma_f32_16x16x32_f16 v[88:91], a[96:99], v[112:115], v[40:43]\n"
      "v_add_f32 v199, v199, v224\n"
      "v_add_f32 v220, v220, v193\n"
      "v_mfma_f32_16x16x32_f16 v[88:91], a[100:103], v[116:119], v[88:91]\n"
      "v_cndmask_b32 v199, v8, v199, s[100:101]\n"
      "v_fmac_f32 v221, v193, v237\n"
      "v_mfma_f32_16x16x32_f16 v[88:91], a[104:107], v[120:123], v[88:91]\n"
      "v_fma_f32 v196, |v84|, v68, v196\n"
      "v_fma_f32 v196, |v85|, v69, v196\n"
      "v_mfma_f32_16x16x32_f16 v[88:91], a[108:111], v[124:127], v[88:91]\n"
      "v_fma_f32 v196, |v86|, v70, v196\n"
      "v_fma_f32 v196, |v87|, v71, v196\n"
      "v_mfma_f32_16x16x32_f16 v[92:95], a[124:127], v[124:127], v[44:47]\n"
      "v_permlane32_swap_b32 v199, v191\n"
      "v_mfma_f32_16x16x32_f16 v[92:95], a[120:123], v[120:123], v[92:95]\n"
      "v_fma_f32 v220, v226, 0, v220\n"
      "v_add_f32 v199, v199, v191\n"
      "v_mfma_f32_16x16x32_f16 v[92:95], a[116:119], v[116:119], v[92:95]\n"
      "v_fma_f32 v196, |v88|, v72, v196\n"
      "v_fma_f32 v196, |v89|, v73, v196\n"
      "v_mfma_f32_16x16x32_f16 v[92:95], a[112:115], v[112:115], v[92:95]\n"
      "v_fma_f32 v196, |v90|, v74, v196\n"
      "v_fma_f32 v196, |v91|, v75, v196\n"
      "v_mfma_f32_16x16x32_f16 v[180:183], a[208:211], v[168:171], v[180:183]\n"
      "v_mfma_f32_16x16x32_f16 v[180:183], a[212:215], v[172:175], v[180:183]\n"
      "s_nop 3\n"
      "v_fma_f32 v196, |v92|, v76, v196\n"
      "v_fma_f32 v196, |v93|, v77, v196\n"
      "v_fma_f32 v196, |v94|, v78, v196\n"
      "v_fma_f32 v196, |v95|, v79, v196\n"
      "v_mov_b32_dpp v238, v180 quad_perm:[0,1,2,3] row_mask:0x1 bank_mask:0xf\n"
      "v_mov_b32_dpp v239, v181 quad_perm:[0,1,2,3] row_mask:0x1 bank_mask:0xf\n"
      "v_permlane32_swap_b32 v196, v8\n"
      "v_add_f32 v196, v196, v8\n"
      "s_movk_i32 s45, 0x207\n"
      "s_nop 0\n"
      "v_permlane16_swap_b32 v199, v196\n"
      "v_add_f32 v199, v199, v196\n"
      "v_fmac_f32 v199, v200, v238\n"
      "v_max_f32 v225, v222, v199\n"
      "v_fma_f32 v220, v199, 0, v220\n"
      "s_nop 0\n"
      "v_max_f32_dpp v223, v225, v225 row_ror:4 row_mask:0xf bank_mask:0xf\n"
      "v_max_f32_dpp v223, v225, v223 row_ror:8 row_mask:0xf bank_mask:0xf\n"
      "v_max_f32_dpp v223, v225, v223 row_ror:12 row_mask:0xf bank_mask:0xf\n"
      "s_nop 1\n"
      "v_max_f32_dpp v225, v223, v223 row_ror:1 row_mask:0xf bank_mask:0xf\n"
      "v_max_f32_dpp v225, v223, v225 row_ror:2 row_mask:0xf bank_mask:0xf\n"
      "v_max_f32_dpp v225, v223, v225 row_ror:3 row_mask:0xf bank_mask:0xf\n"
      "s_nop 1\n"
      "v_max_f32_dpp v225, v225, v225 row_bcast:15 row_mask:0xa bank_mask:0xf\n"
      "s_nop 1\n"
      "v_max_f32_dpp v225, v225, v225 row_bcast:31 row_mask:0xc bank_mask:0xf\n"
      "s_nop 0\n"
      "v_readlane_b32 s44, v225, 63\n"
      "s_nop 1\n"
      "v_subrev_f32 v223, s44, v222\n"
      "v_subrev_f32 v199, s44, v199\n"
      "v_exp_f32 v223, v223\n"
      "v_exp_f32 v199, v199\n"
      "v_mul_f32 v221, v221, v223\n"
      "v_fma_f32 v220, v220, v223, v199\n"
      "v_fmac_f32 v221, v199, v239\n"
      "s_nop 0\n"
      "v_add_f32_dpp v220, v220, v220 row_ror:8 row_mask:0xf bank_mask:0xf\n"
      "v_add_f32_dpp v221, v221, v221 row_ror:8 row_mask:0xf bank_mask:0xf\n"
      "s_nop 0\n"
      "v_add_f32_dpp v220, v220, v220 row_ror:4 row_mask:0xf bank_mask:0xf\n"
      "v_add_f32_dpp v221, v221, v221 row_ror:4 row_mask:0xf bank_mask:0xf\n"
      "s_nop 0\n"
      "v_add_f32_dpp v220, v220, v220 row_ror:2 row_mask:0xf bank_mask:0xf\n"
      "v_add_f32_dpp v221, v221, v221 row_ror:2 row_mask:0xf bank_mask:0xf\n"
      "s_nop 0\n"
      "v_add_f32_dpp v220, v220, v220 row_ror:1 row_mask:0xf bank_mask:0xf\n"
      "v_add_f32_dpp v221, v221, v221 row_ror:1 row_mask:0xf bank_mask:0xf\n"
      "s_nop 0\n"
      "v_add_f32_dpp v220, v220, v220 row_bcast:15 row_mask:0xa bank_mask:0xf\n"
      "v_add_f32_dpp v221, v221, v221 row_bcast:15 row_mask:0xa bank_mask:0xf\n"
      "s_nop 0\n"
      "v_add_f32_dpp v220, v220, v220 row_bcast:31 row_mask:0xc bank_mask:0xf\n"
      "v_add_f32_dpp v221, v221, v221 row_bcast:31 row_mask:0xc bank_mask:0xf\n"
      "s_mov_b32 exec_lo, 0\n"
      "v_rcp_f32 v220, v220\n"
      "v_ldexp_f32 v221, v221, s39\n"
      "s_mov_b32 exec_hi, 0x80000000\n"
      "v_mul_f32 v221, v221, v220\n"
      "v_cvt_pk_bf16_f32 v220, v221, v221\n"
      "global_store_short v3, v220, s[24:25]\n"
      "v_cmp_class_f32 vcc, v221, s45\n"
      "s_nop 0\n"
      "s_cmp_lt_i32 vcc_hi, 0\n"
      "s_cbranch_scc1 7f\n"
      "s_endpgm\n"
      "s_mov_b64 exec, -1\n"
      "s_branch 9f\n"
      "80:\n"
      "s_waitcnt vmcnt(0) lgkmcnt(0)\n"
      "ds_read_b32 v224, v227 offset:37640\n"
      "s_waitcnt lgkmcnt(0)\n"
      "v_cmp_eq_u32 vcc, 0, v224\n"
      "s_nop 4\n"
      "s_and_b32 s44, vcc_lo, s54\n"
      "s_cbranch_scc1 80b\n"
      "v_cmp_eq_u32 vcc, 1, v224\n"
      "s_nop 4\n"
      "s_and_b32 s44, vcc_lo, s54\n"
      "s_cmp_eq_u32 s44, s54\n"
      "s_cbranch_scc1 81b\n"
      "s_mov_b32 s36, 1\n"
      "s_add_u32 s44, s38, 1152\n"
      "v_cndmask_b32 v10, v201, v10, s[100:101]\n"
      "v_cndmask_b32 v11, v201, v11, s[100:101]\n"
      "v_mov_b32 v1, s44\n"
      "s_nop 1\n"
      "s_branch 81b\n"
      "82:\n"
      "s_cmp_lg_u32 s36, 0\n"
      "s_cbranch_scc1 84f\n"
      "85:\n"
      "ds_read_b32 v224, v12 offset:12992\n"
      "s_waitcnt lgkmcnt(0)\n"
      "v_cmp_eq_u32 vcc, s37, v224\n"
      "s_nop 4\n"
      "s_cmp_lg_u64 vcc, 0\n"
      "s_cbranch_scc1 85b\n"
      "s_branch 83b\n"
      "84:\n"
      "s_waitcnt lgkmcnt(0)\n"
      "v_mfma_f32_16x16x32_f16 v[240:243], a[64:67], v[160:163], v[32:35]\n"
      "v_mfma_f32_16x16x32_f16 v[240:243], a[68:71], v[164:167], v[240:243]\n"
      "v_mfma_f32_16x16x32_f16 v[240:243], a[72:75], v[168:171], v[240:243]\n"
      "v_mfma_f32_16x16x32_f16 v[240:243], a[76:79], v[172:175], v[240:243]\n"
      "v_mfma_f32_16x16x32_f16 v[244:247], a[80:83], v[160:163], v[36:39]\n"
      "v_mfma_f32_16x16x32_f16 v[244:247], a[84:87], v[164:167], v[244:247]\n"
      "v_mfma_f32_16x16x32_f16 v[244:247], a[88:91], v[168:171], v[244:247]\n"
      "v_mfma_f32_16x16x32_f16 v[244:247], a[92:95], v[172:175], v[244:247]\n"
      "v_mfma_f32_16x16x32_f16 v[248:251], a[96:99], v[160:163], v[40:43]\n"
      "v_mfma_f32_16x16x32_f16 v[248:251], a[100:103], v[164:167], v[248:251]\n"
      "v_mfma_f32_16x16x32_f16 v[248:251], a[104:107], v[168:171], v[248:251]\n"
      "v_mfma_f32_16x16x32_f16 v[248:251], a[108:111], v[172:175], v[248:251]\n"
      "v_mfma_f32_16x16x32_f16 v[252:255], a[112:115], v[160:163], v[44:47]\n"
      "v_mfma_f32_16x16x32_f16 v[252:255], a[116:119], v[164:167], v[252:255]\n"
      "v_mfma_f32_16x16x32_f16 v[252:255], a[120:123], v[168:171], v[252:255]\n"
      "v_mfma_f32_16x16x32_f16 v[252:255], a[124:127], v[172:175], v[252:255]\n"
      "s_nop 7\n"
      "v_fma_f32 v224, |v240|, v64, 2.0\n"
      "v_fma_f32 v224, |v241|, v65, v224\n"
      "v_fma_f32 v224, |v242|, v66, v224\n"
      "v_fma_f32 v224, |v243|, v67, v224\n"
      "v_fma_f32 v224, |v244|, v68, v224\n"
      "v_fma_f32 v224, |v245|, v69, v224\n"
      "v_fma_f32 v224, |v246|, v70, v224\n"
      "v_fma_f32 v224, |v247|, v71, v224\n"
      "v_fma_f32 v224, |v248|, v72, v224\n"
      "v_fma_f32 v224, |v249|, v73, v224\n"
      "v_fma_f32 v224, |v250|, v74, v224\n"
      "v_fma_f32 v224, |v251|, v75, v224\n"
      "v_fma_f32 v224, |v252|, v76, v224\n"
      "v_fma_f32 v224, |v253|, v77, v224\n"
      "v_fma_f32 v224, |v254|, v78, v224\n"
      "v_fma_f32 v224, |v255|, v79, v224\n"
      "s_nop 1\n"
      "s_branch 83b\n"
      "5:\n"
      "s_waitcnt lgkmcnt(0)\n"
      "ds_read_b32 v224, v227 offset:37632\n"
      "s_waitcnt lgkmcnt(0)\n"
      "v_cmp_eq_u32 vcc, 0, v224\n"
      "s_nop 4\n"
      "s_cmp_lg_u32 vcc_lo, 0\n"
      "s_cbranch_scc1 5b\n"
      "v_cmp_gt_u32 vcc, 4, v224\n"
      "s_nop 4\n"
      "s_cmp_lg_u32 vcc_lo, 0\n"
      "s_cbranch_scc1 8f\n"
      "s_cmp_lg_u32 s58, 0\n"
      "s_cbranch_scc1 52f\n"
      "ds_read_b32 v226, v213 offset:12480\n"
      "v_cmp_eq_u32 s[44:45], 5, v224\n"
      "s_waitcnt lgkmcnt(0)\n"
      "v_max_f16_sdwa v226, |v226|, |v226| dst_sel:DWORD dst_unused:UNUSED_PAD src0_sel:WORD_0 src1_sel:WORD_1\n"
      "s_nop 1\n"
      "v_cmp_le_f16 vcc, 0x6400, v226\n"
      "s_nop 4\n"
      "s_or_b64 vcc, vcc, s[44:45]\n"
      "s_cbranch_scc0 52f\n"
      "v_and_b32 v224, 63, %3\n"
      "v_lshlrev_b32 v224, 9, v224\n"
      "global_load_dwordx4 v[176:179], v224, s[16:17]\n"
      "global_load_dwordx4 v[180:183], v224, s[16:17] offset:336\n"
      "s_waitcnt vmcnt(0)\n"
      "v_max3_f32 v224, |v176|, |v177|, |v178|\n"
      "v_max3_f32 v224, |v179|, |v180|, v224\n"
      "v_max3_f32 v224, |v181|, |v182|, v224\n"
      "v_max_f32 v224, |v183|, v224\n"
      "s_nop 1\n"
      "v_max_f32_dpp v224, v224, v224 row_ror:8 row_mask:0xf bank_mask:0xf\n"
      "s_nop 1\n"
      "v_max_f32_dpp v224, v224, v224 row_ror:4 row_mask:0xf bank_mask:0xf\n"
      "s_nop 1\n"
      "v_max_f32_dpp v224, v224, v224 row_ror:2 row_mask:0xf bank_mask:0xf\n"
      "s_nop 1\n"
      "v_max_f32_dpp v224, v224, v224 row_ror:1 row_mask:0xf bank_mask:0xf\n"
      "s_nop 0\n"
      "v_readlane_b32 s44, v224, 0\n"
      "v_readlane_b32 s45, v224, 16\n"
      "v_readlane_b32 s46, v224, 32\n"
      "v_readlane_b32 s47, v224, 48\n"
      "s_max_u32 s44, s44, s45\n"
      "s_max_u32 s46, s46, s47\n"
      "s_max_u32 s44, s44, s46\n"
      "s_lshr_b32 s44, s44, 23\n"
      "s_sub_i32 s58, 135, s44\n"
      "s_cmp_eq_u32 s44, 0\n"
      "s_cselect_b32 s58, 0, s58\n"
      "s_max_i32 s58, s58, 11\n"
      "s_cmp_gt_u32 s58, 51\n"
      "s_cbranch_scc1 8f\n"
      "s_mov_b32 s41, 1\n"
      "s_mov_b32 s35, 0\n"
      "s_waitcnt vmcnt(0) lgkmcnt(0)\n"
      "s_barrier\n"
      "s_branch 0b\n"
      "52:\n"
      "ds_read_b64 v[176:177], v5 offset:37120\n"
      "v_cmp_eq_u32 vcc, 9, v224\n"
      "v_cmp_eq_u32 s[50:51], 5, v224\n"
      "s_waitcnt lgkmcnt(0)\n"
      "v_max_f32 v225, |v176|, |v177|\n"
      "s_nop 1\n"
      "v_max_f32_dpp v225, v225, v225 row_ror:8 row_mask:0xf bank_mask:0xf\n"
      "s_nop 1\n"
      "v_max_f32_dpp v225, v225, v225 row_ror:4 row_mask:0xf bank_mask:0xf\n"
      "s_nop 1\n"
      "v_max_f32_dpp v225, v225, v225 row_ror:2 row_mask:0xf bank_mask:0xf\n"
      "s_nop 1\n"
      "v_max_f32_dpp v225, v225, v225 row_ror:1 row_mask:0xf bank_mask:0xf\n"
      "s_nop 0\n"
      "v_readlane_b32 s44, v225, 0\n"
      "v_readlane_b32 s45, v225, 16\n"
      "v_readlane_b32 s46, v225, 32\n"
      "v_readlane_b32 s47, v225, 48\n"
      "s_max_u32 s44, s44, s45\n"
      "s_max_u32 s46, s46, s47\n"
      "s_max_u32 s44, s44, s46\n"
      "v_frexp_exp_i32_f32 v224, s44\n"
      "v_sub_u32 v224, 0, v224\n"
      "s_or_b32 s30, s30, vcc_lo\n"
      "s_cmp_lg_u32 s55, 0\n"
      "s_cselect_b32 s30, 0, s30\n"
      "v_readfirstlane_b32 s51, v224\n"
      "s_cmp_lg_u32 s30, 0\n"
      "s_cbranch_scc1 6f\n"
      "v_ldexp_f32 v176, v176, v224\n"
      "v_ldexp_f32 v177, v177, v224\n"
      "v_readfirstlane_b32 s56, v224\n"
      "s_nop 1\n"
      "s_branch 4b\n"
      "22:\n"
      "s_nop 7\n"
      "v_lshlrev_b32 v176, 16, v192\n"
      "v_and_b32 v177, s43, v192\n"
      "v_lshlrev_b32 v178, 16, v193\n"
      "v_and_b32 v179, s43, v193\n"
      "v_lshlrev_b32 v180, 16, v194\n"
      "v_and_b32 v181, s43, v194\n"
      "v_lshlrev_b32 v182, 16, v195\n"
      "v_and_b32 v183, s43, v195\n"
      "v_max3_f32 v222, |v176|, |v177|, |v178|\n"
      "v_max3_f32 v222, |v179|, |v180|, v222\n"
      "v_max3_f32 v222, |v181|, |v182|, v222\n"
      "v_max_f32 v222, |v183|, v222\n"
      "s_nop 1\n"
      "v_max_f32_dpp v222, v222, v222 row_ror:8 row_mask:0xf bank_mask:0xf\n"
      "s_nop 1\n"
      "v_max_f32_dpp v222, v222, v222 row_ror:4 row_mask:0xf bank_mask:0xf\n"
      "s_nop 1\n"
      "v_max_f32_dpp v222, v222, v222 row_ror:2 row_mask:0xf bank_mask:0xf\n"
      "s_nop 1\n"
      "v_max_f32_dpp v222, v222, v222 row_ror:1 row_mask:0xf bank_mask:0xf\n"
      "v_frexp_exp_i32_f32 v90, v222\n"
      "v_sub_u32 v90, 0, v90\n"
      "v_ldexp_f32 v176, v176, v90\n"
      "v_ldexp_f32 v177, v177, v90\n"
      "v_ldexp_f32 v178, v178, v90\n"
      "v_ldexp_f32 v179, v179, v90\n"
      "v_ldexp_f32 v180, v180, v90\n"
      "v_ldexp_f32 v181, v181, v90\n"
      "v_ldexp_f32 v182, v182, v90\n"
      "v_ldexp_f32 v183, v183, v90\n"
      "v_ldexp_f32 v176, v176, 9\n"
      "v_ldexp_f32 v177, v177, 9\n"
      "v_ldexp_f32 v178, v178, 9\n"
      "v_ldexp_f32 v179, v179, 9\n"
      "v_ldexp_f32 v180, v180, 9\n"
      "v_ldexp_f32 v181, v181, 9\n"
      "v_ldexp_f32 v182, v182, 9\n"
      "v_ldexp_f32 v183, v183, 9\n"
      "v_readlane_b32 s59, v90, 0\n"
      "v_readlane_b32 s57, v90, 32\n"
      "s_lshl_b64 exec, -1, 48\n"
      "v_xor_b32 v176, 0x80000000, v176\n"
      "v_xor_b32 v177, 0x80000000, v177\n"
      "v_xor_b32 v178, 0x80000000, v178\n"
      "v_xor_b32 v179, 0x80000000, v179\n"
      "v_xor_b32 v180, 0x80000000, v180\n"
      "v_xor_b32 v181, 0x80000000, v181\n"
      "v_xor_b32 v182, 0x80000000, v182\n"
      "v_xor_b32 v183, 0x80000000, v183\n"
      "s_mov_b64 exec, -1\n"
      "s_cmp_lt_i32 s57, 0\n"
      "s_cselect_b32 s41, 1, s41\n"
      "s_branch 23b\n"
      "70:\n"
      "s_mov_b32 s49, 0\n"
      "s_add_u32 s44, s58, 127\n"
      "s_lshl_b32 s44, s44, 23\n"
      "s_waitcnt vmcnt(0) lgkmcnt(0)\n"
      "s_nop 7\n"
      "v_lshlrev_b32 v128, 16, v96\n"
      "v_and_b32 v129, s43, v96\n"
      "v_mul_f32 v128, s44, v128\n"
      "v_mul_f32 v129, s44, v129\n"
      "v_cvt_pk_bf16_f32 v96, v128, v129\n"
      "v_lshlrev_b32 v128, 16, v97\n"
      "v_and_b32 v129, s43, v97\n"
      "v_mul_f32 v128, s44, v128\n"
      "v_mul_f32 v129, s44, v129\n"
      "v_cvt_pk_bf16_f32 v97, v128, v129\n"
      "v_lshlrev_b32 v128, 16, v98\n"
      "v_and_b32 v129, s43, v98\n"
      "v_mul_f32 v128, s44, v128\n"
      "v_mul_f32 v129, s44, v129\n"
      "v_cvt_pk_bf16_f32 v98, v128, v129\n"
      "v_lshlrev_b32 v128, 16, v99\n"
      "v_and_b32 v129, s43, v99\n"
      "v_mul_f32 v128, s44, v128\n"
      "v_mul_f32 v129, s44, v129\n"
      "v_cvt_pk_bf16_f32 v99, v128, v129\n"
      "v_lshlrev_b32 v128, 16, v100\n"
      "v_and_b32 v129, s43, v100\n"
      "v_mul_f32 v128, s44, v128\n"
      "v_mul_f32 v129, s44, v129\n"
      "v_cvt_pk_bf16_f32 v100, v128, v129\n"
      "v_lshlrev_b32 v128, 16, v101\n"
      "v_and_b32 v129, s43, v101\n"
      "v_mul_f32 v128, s44, v128\n"
      "v_mul_f32 v129, s44, v129\n"
      "v_cvt_pk_bf16_f32 v101, v128, v129\n"
      "v_lshlrev_b32 v128, 16, v102\n"
      "v_and_b32 v129, s43, v102\n"
      "v_mul_f32 v128, s44, v128\n"
      "v_mul_f32 v129, s44, v129\n"
      "v_cvt_pk_bf16_f32 v102, v128, v129\n"
      "v_lshlrev_b32 v128, 16, v103\n"
      "v_and_b32 v129, s43, v103\n"
      "v_mul_f32 v128, s44, v128\n"
      "v_mul_f32 v129, s44, v129\n"
      "v_cvt_pk_bf16_f32 v103, v128, v129\n"
      "v_lshlrev_b32 v128, 16, v104\n"
      "v_and_b32 v129, s43, v104\n"
      "v_mul_f32 v128, s44, v128\n"
      "v_mul_f32 v129, s44, v129\n"
      "v_cvt_pk_bf16_f32 v104, v128, v129\n"
      "v_lshlrev_b32 v128, 16, v105\n"
      "v_and_b32 v129, s43, v105\n"
      "v_mul_f32 v128, s44, v128\n"
      "v_mul_f32 v129, s44, v129\n"
      "v_cvt_pk_bf16_f32 v105, v128, v129\n"
      "v_lshlrev_b32 v128, 16, v106\n"
      "v_and_b32 v129, s43, v106\n"
      "v_mul_f32 v128, s44, v128\n"
      "v_mul_f32 v129, s44, v129\n"
      "v_cvt_pk_bf16_f32 v106, v128, v129\n"
      "v_lshlrev_b32 v128, 16, v107\n"
      "v_and_b32 v129, s43, v107\n"
      "v_mul_f32 v128, s44, v128\n"
      "v_mul_f32 v129, s44, v129\n"
      "v_cvt_pk_bf16_f32 v107, v128, v129\n"
      "v_lshlrev_b32 v128, 16, v108\n"
      "v_and_b32 v129, s43, v108\n"
      "v_mul_f32 v128, s44, v128\n"
      "v_mul_f32 v129, s44, v129\n"
      "v_cvt_pk_bf16_f32 v108, v128, v129\n"
      "v_lshlrev_b32 v128, 16, v109\n"
      "v_and_b32 v129, s43, v109\n"
      "v_mul_f32 v128, s44, v128\n"
      "v_mul_f32 v129, s44, v129\n"
      "v_cvt_pk_bf16_f32 v109, v128, v129\n"
      "v_lshlrev_b32 v128, 16, v110\n"
      "v_and_b32 v129, s43, v110\n"
      "v_mul_f32 v128, s44, v128\n"
      "v_mul_f32 v129, s44, v129\n"
      "v_cvt_pk_bf16_f32 v110, v128, v129\n"
      "v_lshlrev_b32 v128, 16, v111\n"
      "v_and_b32 v129, s43, v111\n"
      "v_mul_f32 v128, s44, v128\n"
      "v_mul_f32 v129, s44, v129\n"
      "v_cvt_pk_bf16_f32 v111, v128, v129\n"
      "v_lshlrev_b32 v128, 16, v112\n"
      "v_and_b32 v129, s43, v112\n"
      "v_mul_f32 v128, s44, v128\n"
      "v_mul_f32 v129, s44, v129\n"
      "v_cvt_pk_bf16_f32 v112, v128, v129\n"
      "v_lshlrev_b32 v128, 16, v113\n"
      "v_and_b32 v129, s43, v113\n"
      "v_mul_f32 v128, s44, v128\n"
      "v_mul_f32 v129, s44, v129\n"
      "v_cvt_pk_bf16_f32 v113, v128, v129\n"
      "v_lshlrev_b32 v128, 16, v114\n"
      "v_and_b32 v129, s43, v114\n"
      "v_mul_f32 v128, s44, v128\n"
      "v_mul_f32 v129, s44, v129\n"
      "v_cvt_pk_bf16_f32 v114, v128, v129\n"
      "v_lshlrev_b32 v128, 16, v115\n"
      "v_and_b32 v129, s43, v115\n"
      "v_mul_f32 v128, s44, v128\n"
      "v_mul_f32 v129, s44, v129\n"
      "v_cvt_pk_bf16_f32 v115, v128, v129\n"
      "v_lshlrev_b32 v128, 16, v116\n"
      "v_and_b32 v129, s43, v116\n"
      "v_mul_f32 v128, s44, v128\n"
      "v_mul_f32 v129, s44, v129\n"
      "v_cvt_pk_bf16_f32 v116, v128, v129\n"
      "v_lshlrev_b32 v128, 16, v117\n"
      "v_and_b32 v129, s43, v117\n"
      "v_mul_f32 v128, s44, v128\n"
      "v_mul_f32 v129, s44, v129\n"
      "v_cvt_pk_bf16_f32 v117, v128, v129\n"
      "v_lshlrev_b32 v128, 16, v118\n"
      "v_and_b32 v129, s43, v118\n"
      "v_mul_f32 v128, s44, v128\n"
      "v_mul_f32 v129, s44, v129\n"
      "v_cvt_pk_bf16_f32 v118, v128, v129\n"
      "v_lshlrev_b32 v128, 16, v119\n"
      "v_and_b32 v129, s43, v119\n"
      "v_mul_f32 v128, s44, v128\n"
      "v_mul_f32 v129, s44, v129\n"
      "v_cvt_pk_bf16_f32 v119, v128, v129\n"
      "v_lshlrev_b32 v128, 16, v120\n"
      "v_and_b32 v129, s43, v120\n"
      "v_mul_f32 v128, s44, v128\n"
      "v_mul_f32 v129, s44, v129\n"
      "v_cvt_pk_bf16_f32 v120, v128, v129\n"
      "v_lshlrev_b32 v128, 16, v121\n"
      "v_and_b32 v129, s43, v121\n"
      "v_mul_f32 v128, s44, v128\n"
      "v_mul_f32 v129, s44, v129\n"
      "v_cvt_pk_bf16_f32 v121, v128, v129\n"
      "v_lshlrev_b32 v128, 16, v122\n"
      "v_and_b32 v129, s43, v122\n"
      "v_mul_f32 v128, s44, v128\n"
      "v_mul_f32 v129, s44, v129\n"
      "v_cvt_pk_bf16_f32 v122, v128, v129\n"
      "v_lshlrev_b32 v128, 16, v123\n"
      "v_and_b32 v129, s43, v123\n"
      "v_mul_f32 v128, s44, v128\n"
      "v_mul_f32 v129, s44, v129\n"
      "v_cvt_pk_bf16_f32 v123, v128, v129\n"
      "v_lshlrev_b32 v128, 16, v124\n"
      "v_and_b32 v129, s43, v124\n"
      "v_mul_f32 v128, s44, v128\n"
      "v_mul_f32 v129, s44, v129\n"
      "v_cvt_pk_bf16_f32 v124, v128, v129\n"
      "v_lshlrev_b32 v128, 16, v125\n"
      "v_and_b32 v129, s43, v125\n"
      "v_mul_f32 v128, s44, v128\n"
      "v_mul_f32 v129, s44, v129\n"
      "v_cvt_pk_bf16_f32 v125, v128, v129\n"
      "v_lshlrev_b32 v128, 16, v126\n"
      "v_and_b32 v129, s43, v126\n"
      "v_mul_f32 v128, s44, v128\n"
      "v_mul_f32 v129, s44, v129\n"
      "v_cvt_pk_bf16_f32 v126, v128, v129\n"
      "v_lshlrev_b32 v128, 16, v127\n"
      "v_and_b32 v129, s43, v127\n"
      "v_mul_f32 v128, s44, v128\n"
      "v_mul_f32 v129, s44, v129\n"
      "v_cvt_pk_bf16_f32 v127, v128, v129\n"
      "s_nop 1\n"
      "s_branch 71b\n"
      "72:\n"
      "s_nop 7\n"
      "s_sub_u32 s44, 0, s58\n"
      "v_ldexp_f32 v16, v16, s58\n"
      "v_ldexp_f32 v48, v48, s44\n"
      "v_ldexp_f32 v17, v17, s58\n"
      "v_ldexp_f32 v49, v49, s44\n"
      "v_ldexp_f32 v18, v18, s58\n"
      "v_ldexp_f32 v50, v50, s44\n"
      "v_ldexp_f32 v19, v19, s58\n"
      "v_ldexp_f32 v51, v51, s44\n"
      "v_ldexp_f32 v20, v20, s58\n"
      "v_ldexp_f32 v52, v52, s44\n"
      "v_ldexp_f32 v21, v21, s58\n"
      "v_ldexp_f32 v53, v53, s44\n"
      "v_ldexp_f32 v22, v22, s58\n"
      "v_ldexp_f32 v54, v54, s44\n"
      "v_ldexp_f32 v23, v23, s58\n"
      "v_ldexp_f32 v55, v55, s44\n"
      "v_ldexp_f32 v24, v24, s58\n"
      "v_ldexp_f32 v56, v56, s44\n"
      "v_ldexp_f32 v25, v25, s58\n"
      "v_ldexp_f32 v57, v57, s44\n"
      "v_ldexp_f32 v26, v26, s58\n"
      "v_ldexp_f32 v58, v58, s44\n"
      "v_ldexp_f32 v27, v27, s58\n"
      "v_ldexp_f32 v59, v59, s44\n"
      "v_ldexp_f32 v28, v28, s58\n"
      "v_ldexp_f32 v60, v60, s44\n"
      "v_ldexp_f32 v29, v29, s58\n"
      "v_ldexp_f32 v61, v61, s44\n"
      "v_ldexp_f32 v30, v30, s58\n"
      "v_ldexp_f32 v62, v62, s44\n"
      "v_ldexp_f32 v31, v31, s58\n"
      "v_ldexp_f32 v63, v63, s44\n"
      "v_ldexp_f32 v32, v32, s58\n"
      "v_ldexp_f32 v64, v64, s44\n"
      "v_ldexp_f32 v33, v33, s58\n"
      "v_ldexp_f32 v65, v65, s44\n"
      "v_ldexp_f32 v34, v34, s58\n"
      "v_ldexp_f32 v66, v66, s44\n"
      "v_ldexp_f32 v35, v35, s58\n"
      "v_ldexp_f32 v67, v67, s44\n"
      "v_ldexp_f32 v36, v36, s58\n"
      "v_ldexp_f32 v68, v68, s44\n"
      "v_ldexp_f32 v37, v37, s58\n"
      "v_ldexp_f32 v69, v69, s44\n"
      "v_ldexp_f32 v38, v38, s58\n"
      "v_ldexp_f32 v70, v70, s44\n"
      "v_ldexp_f32 v39, v39, s58\n"
      "v_ldexp_f32 v71, v71, s44\n"
      "v_ldexp_f32 v40, v40, s58\n"
      "v_ldexp_f32 v72, v72, s44\n"
      "v_ldexp_f32 v41, v41, s58\n"
      "v_ldexp_f32 v73, v73, s44\n"
      "v_ldexp_f32 v42, v42, s58\n"
      "v_ldexp_f32 v74, v74, s44\n"
      "v_ldexp_f32 v43, v43, s58\n"
      "v_ldexp_f32 v75, v75, s44\n"
      "v_ldexp_f32 v44, v44, s58\n"
      "v_ldexp_f32 v76, v76, s44\n"
      "v_ldexp_f32 v45, v45, s58\n"
      "v_ldexp_f32 v77, v77, s44\n"
      "v_ldexp_f32 v46, v46, s58\n"
      "v_ldexp_f32 v78, v78, s44\n"
      "v_ldexp_f32 v47, v47, s58\n"
      "v_ldexp_f32 v79, v79, s44\n"
      "s_branch 73b\n"
      "6:\n"
      "s_waitcnt vmcnt(0) lgkmcnt(0)\n"
      "s_nop 7\n"
      "s_mov_b32 s55, 1\n"
      "s_mov_b32 s41, 1\n"
      "s_mov_b32 s45, 0x7fff7fff\n"
      "v_mov_b32 v226, 0\n"
      "v_and_b32 v176, s45, v228\n"
      "v_pk_max_u16 v226, v226, v176\n"
      "v_and_b32 v176, s45, v229\n"
      "v_pk_max_u16 v226, v226, v176\n"
      "v_and_b32 v176, s45, v230\n"
      "v_pk_max_u16 v226, v226, v176\n"
      "v_and_b32 v176, s45, v231\n"
      "v_pk_max_u16 v226, v226, v176\n"
      "v_and_b32 v176, s45, v232\n"
      "v_pk_max_u16 v226, v226, v176\n"
      "v_and_b32 v176, s45, v233\n"
      "v_pk_max_u16 v226, v226, v176\n"
      "v_and_b32 v176, s45, v234\n"
      "v_pk_max_u16 v226, v226, v176\n"
      "v_and_b32 v176, s45, v235\n"
      "v_pk_max_u16 v226, v226, v176\n"
      "v_and_b32 v176, s45, v236\n"
      "v_pk_max_u16 v226, v226, v176\n"
      "v_and_b32 v176, s45, v237\n"
      "v_pk_max_u16 v226, v226, v176\n"
      "v_and_b32 v176, s45, v238\n"
      "v_pk_max_u16 v226, v226, v176\n"
      "v_and_b32 v176, s45, v239\n"
      "v_pk_max_u16 v226, v226, v176\n"
      "v_and_b32 v176, s45, v240\n"
      "v_pk_max_u16 v226, v226, v176\n"
      "v_and_b32 v176, s45, v241\n"
      "v_pk_max_u16 v226, v226, v176\n"
      "v_and_b32 v176, s45, v242\n"
      "v_pk_max_u16 v226, v226, v176\n"
      "v_and_b32 v176, s45, v243\n"
      "v_pk_max_u16 v226, v226, v176\n"
      "v_and_b32 v176, s45, v244\n"
      "v_pk_max_u16 v226, v226, v176\n"
      "v_and_b32 v176, s45, v245\n"
      "v_pk_max_u16 v226, v226, v176\n"
      "v_and_b32 v176, s45, v246\n"
      "v_pk_max_u16 v226, v226, v176\n"
      "v_and_b32 v176, s45, v247\n"
      "v_pk_max_u16 v226, v226, v176\n"
      "v_and_b32 v176, s45, v248\n"
      "v_pk_max_u16 v226, v226, v176\n"
      "v_and_b32 v176, s45, v249\n"
      "v_pk_max_u16 v226, v226, v176\n"
      "v_and_b32 v176, s45, v250\n"
      "v_pk_max_u16 v226, v226, v176\n"
      "v_and_b32 v176, s45, v251\n"
      "v_pk_max_u16 v226, v226, v176\n"
      "v_and_b32 v176, s45, v252\n"
      "v_pk_max_u16 v226, v226, v176\n"
      "v_and_b32 v176, s45, v253\n"
      "v_pk_max_u16 v226, v226, v176\n"
      "v_and_b32 v176, s45, v254\n"
      "v_pk_max_u16 v226, v226, v176\n"
      "v_and_b32 v176, s45, v255\n"
      "v_pk_max_u16 v226, v226, v176\n"
      "v_lshlrev_b32 v176, 16, v226\n"
      "v_max_u32 v226, v226, v176\n"
      "s_nop 1\n"
      "v_max_u32_dpp v226, v226, v226 row_ror:8 row_mask:0xf bank_mask:0xf\n"
      "s_nop 1\n"
      "v_max_u32_dpp v226, v226, v226 row_ror:4 row_mask:0xf bank_mask:0xf\n"
      "s_nop 1\n"
      "v_max_u32_dpp v226, v226, v226 row_ror:2 row_mask:0xf bank_mask:0xf\n"
      "s_nop 1\n"
      "v_max_u32_dpp v226, v226, v226 row_ror:1 row_mask:0xf bank_mask:0xf\n"
      "s_nop 0\n"
      "v_readlane_b32 s44, v226, 0\n"
      "v_readlane_b32 s45, v226, 16\n"
      "v_readlane_b32 s46, v226, 32\n"
      "v_readlane_b32 s47, v226, 48\n"
      "s_max_u32 s44, s44, s45\n"
      "s_max_u32 s46, s46, s47\n"
      "s_max_u32 s44, s44, s46\n"
      "s_lshr_b32 s44, s44, 23\n"
      "s_sub_i32 s40, 137, s44\n"
      "s_cmp_eq_u32 s44, 0\n"
      "s_cselect_b32 s40, 0, s40\n"
      "s_add_i32 s45, s40, 33\n"
      "s_cmp_gt_u32 s45, 80\n"
      "s_cbranch_scc1 8f\n"
      "s_lshl1_add_u32 s44, s40, s58\n"
      "s_add_i32 s44, s44, s57\n"
      "s_add_i32 s45, s44, s51\n"
      "s_cmp_lt_i32 s45, 1\n"
      "s_cbranch_scc1 8f\n"
      "s_cmp_lg_u32 s50, 0\n"
      "s_cselect_b32 s44, s44, 64\n"
      "s_cmp_lt_i32 s44, 24\n"
      "s_cbranch_scc1 8f\n"
      "s_add_u32 s39, s40, 120\n"
      "s_lshl_b32 s39, s39, 23\n"
      "s_lshl_b32 s44, s40, 1\n"
      "s_sub_u32 s45, 127, s44\n"
      "s_add_u32 s44, s44, 127\n"
      "s_lshl_b32 s44, s44, 23\n"
      "s_lshl_b32 s45, s45, 23\n"
      "v_mul_f32 v16, s44, v16\n"
      "v_mul_f32 v48, s45, v48\n"
      "v_mul_f32 v17, s44, v17\n"
      "v_mul_f32 v49, s45, v49\n"
      "v_mul_f32 v18, s44, v18\n"
      "v_mul_f32 v50, s45, v50\n"
      "v_mul_f32 v19, s44, v19\n"
      "v_mul_f32 v51, s45, v51\n"
      "v_mul_f32 v20, s44, v20\n"
      "v_mul_f32 v52, s45, v52\n"
      "v_mul_f32 v21, s44, v21\n"
      "v_mul_f32 v53, s45, v53\n"
      "v_mul_f32 v22, s44, v22\n"
      "v_mul_f32 v54, s45, v54\n"
      "v_mul_f32 v23, s44, v23\n"
      "v_mul_f32 v55, s45, v55\n"
      "v_mul_f32 v24, s44, v24\n"
      "v_mul_f32 v56, s45, v56\n"
      "v_mul_f32 v25, s44, v25\n"
      "v_mul_f32 v57, s45, v57\n"
      "v_mul_f32 v26, s44, v26\n"
      "v_mul_f32 v58, s45, v58\n"
      "v_mul_f32 v27, s44, v27\n"
      "v_mul_f32 v59, s45, v59\n"
      "v_mul_f32 v28, s44, v28\n"
      "v_mul_f32 v60, s45, v60\n"
      "v_mul_f32 v29, s44, v29\n"
      "v_mul_f32 v61, s45, v61\n"
      "v_mul_f32 v30, s44, v30\n"
      "v_mul_f32 v62, s45, v62\n"
      "v_mul_f32 v31, s44, v31\n"
      "v_mul_f32 v63, s45, v63\n"
      "v_mul_f32 v32, s44, v32\n"
      "v_mul_f32 v64, s45, v64\n"
      "v_mul_f32 v33, s44, v33\n"
      "v_mul_f32 v65, s45, v65\n"
      "v_mul_f32 v34, s44, v34\n"
      "v_mul_f32 v66, s45, v66\n"
      "v_mul_f32 v35, s44, v35\n"
      "v_mul_f32 v67, s45, v67\n"
      "v_mul_f32 v36, s44, v36\n"
      "v_mul_f32 v68, s45, v68\n"
      "v_mul_f32 v37, s44, v37\n"
      "v_mul_f32 v69, s45, v69\n"
      "v_mul_f32 v38, s44, v38\n"
      "v_mul_f32 v70, s45, v70\n"
      "v_mul_f32 v39, s44, v39\n"
      "v_mul_f32 v71, s45, v71\n"
      "v_mul_f32 v40, s44, v40\n"
      "v_mul_f32 v72, s45, v72\n"
      "v_mul_f32 v41, s44, v41\n"
      "v_mul_f32 v73, s45, v73\n"
      "v_mul_f32 v42, s44, v42\n"
      "v_mul_f32 v74, s45, v74\n"
      "v_mul_f32 v43, s44, v43\n"
      "v_mul_f32 v75, s45, v75\n"
      "v_mul_f32 v44, s44, v44\n"
      "v_mul_f32 v76, s45, v76\n"
      "v_mul_f32 v45, s44, v45\n"
      "v_mul_f32 v77, s45, v77\n"
      "v_mul_f32 v46, s44, v46\n"
      "v_mul_f32 v78, s45, v78\n"
      "v_mul_f32 v47, s44, v47\n"
      "v_mul_f32 v79, s45, v79\n"
      "v_lshlrev_b32 v176, 16, v228\n"
      "v_and_b32 v177, s43, v228\n"
      "v_lshlrev_b32 v178, 16, v229\n"
      "v_and_b32 v179, s43, v229\n"
      "v_lshlrev_b32 v180, 16, v230\n"
      "v_and_b32 v181, s43, v230\n"
      "v_lshlrev_b32 v182, 16, v231\n"
      "v_and_b32 v183, s43, v231\n"
      "v_mul_f32 v176, s39, v176\n"
      "v_mul_f32 v177, s39, v177\n"
      "v_mul_f32 v178, s39, v178\n"
      "v_mul_f32 v179, s39, v179\n"
      "v_mul_f32 v180, s39, v180\n"
      "v_mul_f32 v181, s39, v181\n"
      "v_mul_f32 v182, s39, v182\n"
      "v_mul_f32 v183, s39, v183\n"
      "v_cvt_pk_f16_f32 v184, v176, v177\n"
      "v_cvt_pk_f16_f32 v185, v178, v179\n"
      "v_cvt_pk_f16_f32 v186, v180, v181\n"
      "v_cvt_pk_f16_f32 v187, v182, v183\n"
      "ds_write_b128 v1, v[184:187] offset:0\n"
      "v_lshlrev_b32 v176, 16, v232\n"
      "v_and_b32 v177, s43, v232\n"
      "v_lshlrev_b32 v178, 16, v233\n"
      "v_and_b32 v179, s43, v233\n"
      "v_lshlrev_b32 v180, 16, v234\n"
      "v_and_b32 v181, s43, v234\n"
      "v_lshlrev_b32 v182, 16, v235\n"
      "v_and_b32 v183, s43, v235\n"
      "v_mul_f32 v176, s39, v176\n"
      "v_mul_f32 v177, s39, v177\n"
      "v_mul_f32 v178, s39, v178\n"
      "v_mul_f32 v179, s39, v179\n"
      "v_mul_f32 v180, s39, v180\n"
      "v_mul_f32 v181, s39, v181\n"
      "v_mul_f32 v182, s39, v182\n"
      "v_mul_f32 v183, s39, v183\n"
      "v_cvt_pk_f16_f32 v184, v176, v177\n"
      "v_cvt_pk_f16_f32 v185, v178, v179\n"
      "v_cvt_pk_f16_f32 v186, v180, v181\n"
      "v_cvt_pk_f16_f32 v187, v182, v183\n"
      "ds_write_b128 v1, v[184:187] offset:1152\n"
      "v_lshlrev_b32 v176, 16, v236\n"
      "v_and_b32 v177, s43, v236\n"
      "v_lshlrev_b32 v178, 16, v237\n"
      "v_and_b32 v179, s43, v237\n"
      "v_lshlrev_b32 v180, 16, v238\n"
      "v_and_b32 v181, s43, v238\n"
      "v_lshlrev_b32 v182, 16, v239\n"
      "v_and_b32 v183, s43, v239\n"
      "v_mul_f32 v176, s39, v176\n"
      "v_mul_f32 v177, s39, v177\n"
      "v_mul_f32 v178, s39, v178\n"
      "v_mul_f32 v179, s39, v179\n"
      "v_mul_f32 v180, s39, v180\n"
      "v_mul_f32 v181, s39, v181\n"
      "v_mul_f32 v182, s39, v182\n"
      "v_mul_f32 v183, s39, v183\n"
      "v_cvt_pk_f16_f32 v184, v176, v177\n"
      "v_cvt_pk_f16_f32 v185, v178, v179\n"
      "v_cvt_pk_f16_f32 v186, v180, v181\n"
      "v_cvt_pk_f16_f32 v187, v182, v183\n"
      "ds_write_b128 v1, v[184:187] offset:2304\n"
      "v_lshlrev_b32 v176, 16, v240\n"
      "v_and_b32 v177, s43, v240\n"
      "v_lshlrev_b32 v178, 16, v241\n"
      "v_and_b32 v179, s43, v241\n"
      "v_lshlrev_b32 v180, 16, v242\n"
      "v_and_b32 v181, s43, v242\n"
      "v_lshlrev_b32 v182, 16, v243\n"
      "v_and_b32 v183, s43, v243\n"
      "v_mul_f32 v176, s39, v176\n"
      "v_mul_f32 v177, s39, v177\n"
      "v_mul_f32 v178, s39, v178\n"
      "v_mul_f32 v179, s39, v179\n"
      "v_mul_f32 v180, s39, v180\n"
      "v_mul_f32 v181, s39, v181\n"
      "v_mul_f32 v182, s39, v182\n"
      "v_mul_f32 v183, s39, v183\n"
      "v_cvt_pk_f16_f32 v184, v176, v177\n"
      "v_cvt_pk_f16_f32 v185, v178, v179\n"
      "v_cvt_pk_f16_f32 v186, v180, v181\n"
      "v_cvt_pk_f16_f32 v187, v182, v183\n"
      "ds_write_b128 v1, v[184:187] offset:3456\n"
      "v_lshlrev_b32 v176, 16, v244\n"
      "v_and_b32 v177, s43, v244\n"
      "v_lshlrev_b32 v178, 16, v245\n"
      "v_and_b32 v179, s43, v245\n"
      "v_lshlrev_b32 v180, 16, v246\n"
      "v_and_b32 v181, s43, v246\n"
      "v_lshlrev_b32 v182, 16, v247\n"
      "v_and_b32 v183, s43, v247\n"
      "v_mul_f32 v176, s39, v176\n"
      "v_mul_f32 v177, s39, v177\n"
      "v_mul_f32 v178, s39, v178\n"
      "v_mul_f32 v179, s39, v179\n"
      "v_mul_f32 v180, s39, v180\n"
      "v_mul_f32 v181, s39, v181\n"
      "v_mul_f32 v182, s39, v182\n"
      "v_mul_f32 v183, s39, v183\n"
      "v_cvt_pk_f16_f32 v184, v176, v177\n"
      "v_cvt_pk_f16_f32 v185, v178, v179\n"
      "v_cvt_pk_f16_f32 v186, v180, v181\n"
      "v_cvt_pk_f16_f32 v187, v182, v183\n"
      "ds_write_b128 v1, v[184:187] offset:4608\n"
      "v_lshlrev_b32 v176, 16, v248\n"
      "v_and_b32 v177, s43, v248\n"
      "v_lshlrev_b32 v178, 16, v249\n"
      "v_and_b32 v179, s43, v249\n"
      "v_lshlrev_b32 v180, 16, v250\n"
      "v_and_b32 v181, s43, v250\n"
      "v_lshlrev_b32 v182, 16, v251\n"
      "v_and_b32 v183, s43, v251\n"
      "v_mul_f32 v176, s39, v176\n"
      "v_mul_f32 v177, s39, v177\n"
      "v_mul_f32 v178, s39, v178\n"
      "v_mul_f32 v179, s39, v179\n"
      "v_mul_f32 v180, s39, v180\n"
      "v_mul_f32 v181, s39, v181\n"
      "v_mul_f32 v182, s39, v182\n"
      "v_mul_f32 v183, s39, v183\n"
      "v_cvt_pk_f16_f32 v184, v176, v177\n"
      "v_cvt_pk_f16_f32 v185, v178, v179\n"
      "v_cvt_pk_f16_f32 v186, v180, v181\n"
      "v_cvt_pk_f16_f32 v187, v182, v183\n"
      "ds_write_b128 v1, v[184:187] offset:5760\n"
      "v_lshlrev_b32 v176, 16, v252\n"
      "v_and_b32 v177, s43, v252\n"
      "v_lshlrev_b32 v178, 16, v253\n"
      "v_and_b32 v179, s43, v253\n"
      "v_lshlrev_b32 v180, 16, v254\n"
      "v_and_b32 v181, s43, v254\n"
      "v_lshlrev_b32 v182, 16, v255\n"
      "v_and_b32 v183, s43, v255\n"
      "v_mul_f32 v176, s39, v176\n"
      "v_mul_f32 v177, s39, v177\n"
      "v_mul_f32 v178, s39, v178\n"
      "v_mul_f32 v179, s39, v179\n"
      "v_mul_f32 v180, s39, v180\n"
      "v_mul_f32 v181, s39, v181\n"
      "v_mul_f32 v182, s39, v182\n"
      "v_mul_f32 v183, s39, v183\n"
      "v_cvt_pk_f16_f32 v184, v176, v177\n"
      "v_cvt_pk_f16_f32 v185, v178, v179\n"
      "v_cvt_pk_f16_f32 v186, v180, v181\n"
      "v_cvt_pk_f16_f32 v187, v182, v183\n"
      "ds_write_b128 v1, v[184:187] offset:6912\n"
      "v_mfma_f32_16x16x32_f16 v[192:195], a[184:187], a[152:155], 0\n"
      "v_mfma_f32_16x16x32_f16 v[192:195], a[188:191], a[156:159], v[192:195]\n"
      "v_mfma_f32_16x16x32_f16 v[192:195], a[192:195], a[160:163], v[192:195]\n"
      "v_mfma_f32_16x16x32_f16 v[192:195], a[196:199], a[164:167], v[192:195]\n"
      "v_mfma_f32_16x16x32_f16 v[252:255], a[184:187], a[168:171], 0\n"
      "v_mfma_f32_16x16x32_f16 v[252:255], a[188:191], a[172:175], v[252:255]\n"
      "v_mfma_f32_16x16x32_f16 v[252:255], a[192:195], a[176:179], v[252:255]\n"
      "v_mfma_f32_16x16x32_f16 v[252:255], a[196:199], a[180:183], v[252:255]\n"
      "s_sub_u32 s44, 96512, s38\n"
      "s_mov_b32 s98, 0\n"
      "v_add_u32 v1, s44, v1\n"
      "s_waitcnt lgkmcnt(0)\n"
      "ds_read_b128 v[128:131], v201 offset:0\n"
      "ds_read_b128 v[160:163], v202 offset:0\n"
      "ds_read_b128 v[132:135], v201 offset:64\n"
      "ds_read_b128 v[164:167], v202 offset:64\n"
      "ds_read_b128 v[136:139], v201 offset:128\n"
      "ds_read_b128 v[168:171], v202 offset:128\n"
      "ds_read_b128 v[140:143], v201 offset:192\n"
      "ds_read_b128 v[172:175], v202 offset:192\n"
      "s_waitcnt lgkmcnt(0)\n"
      "v_pk_mul_f16 v96, v128, v160\n"
      "v_pk_mul_f16 v97, v129, v161\n"
      "v_pk_mul_f16 v98, v130, v162\n"
      "v_pk_mul_f16 v99, v131, v163\n"
      "v_pk_mul_f16 v100, v132, v164\n"
      "v_pk_mul_f16 v101, v133, v165\n"
      "v_pk_mul_f16 v102, v134, v166\n"
      "v_pk_mul_f16 v103, v135, v167\n"
      "v_pk_mul_f16 v104, v136, v168\n"
      "v_pk_mul_f16 v105, v137, v169\n"
      "v_pk_mul_f16 v106, v138, v170\n"
      "v_pk_mul_f16 v107, v139, v171\n"
      "s_nop 1\n"
      "s_branch 3b\n"
      "7:\n"
      "s_mov_b64 exec, -1\n"
      "s_cmp_lg_u32 s41, 0\n"
      "s_cbranch_scc1 8f\n"
      "s_mov_b32 s41, 1\n"
      "s_waitcnt vmcnt(0)\n"
      "s_branch 0b\n"
      "8:\n"
      "v_mov_b32 v222, 2\n"
      "s_mov_b32 s42, 1\n"
      "ds_write_b32 v2, v222 offset:8\n"
      "9:\n"
      "s_nop 7\n"
      "s_waitcnt vmcnt(0) lgkmcnt(0)\n"
      "s_mov_b32 %0, s42\n"
      : "=s"(flag)
      : "s"(__builtin_amdgcn_kernarg_segment_ptr()), "s"(blockIdx.x), "v"(threadIdx.x), "s"(lbase)
      :
      "v1", "v2", "v3", "v4", "v5", "v6", "v7", "v8", "v9", "v10", "v11", "v12", "v13", "v14", "v15", "v16", "v17", "v18", "v19", "v20", "v21", "v22", "v23", "v24",
      "v25", "v26", "v27", "v28", "v29", "v30", "v31", "v32", "v33", "v34", "v35", "v36", "v37", "v38", "v39", "v40", "v41", "v42", "v43", "v44", "v45", "v46", "v47", "v48",
      "v49", "v50", "v51", "v52", "v53", "v54", "v55", "v56", "v57", "v58", "v59", "v60", "v61", "v62", "v63", "v64", "v65", "v66", "v67", "v68", "v69", "v70", "v71", "v72",
      "v73", "v74", "v75", "v76", "v77", "v78", "v79", "v80", "v81", "v82", "v83", "v84", "v85", "v86", "v87", "v88", "v89", "v90", "v91", "v92", "v93", "v94", "v95", "v96",
      "v97", "v98", "v99", "v100", "v101", "v102", "v103", "v104", "v105", "v106", "v107", "v108", "v109", "v110", "v111", "v112", "v113", "v114", "v115", "v116", "v117", "v118", "v119", "v120",
      "v121", "v122", "v123", "v124", "v125", "v126", "v127", "v128", "v129", "v130", "v131", "v132", "v133", "v134", "v135", "v136", "v137", "v138", "v139", "v140", "v141", "v142", "v143", "v144",
      "v145", "v146", "v147", "v148", "v149", "v150", "v151", "v152", "v153", "v154", "v155", "v156", "v157", "v158", "v159", "v160", "v161", "v162", "v163", "v164", "v165", "v166", "v167", "v168",
      "v169", "v170", "v171", "v172", "v173", "v174", "v175", "v176", "v177", "v178", "v179", "v180", "v181", "v182", "v183", "v184", "v185", "v186", "v187", "v188", "v189", "v190", "v191", "v192",
      "v193", "v194", "v195", "v196", "v197", "v198", "v199", "v200", "v201", "v202", "v203", "v204", "v205", "v206", "v207", "v208", "v209", "v210", "v211", "v212", "v213", "v214", "v215", "v216",
      "v217", "v218", "v219", "v220", "v221", "v222", "v223", "v224", "v225", "v226", "v227", "v228", "v229", "v230", "v231", "v232", "v233", "v234", "v235", "v236", "v237", "v238", "v239", "v240",
      "v241", "v242", "v243", "v244", "v245", "v246", "v247", "v248", "v249", "v250", "v251", "v252", "v253", "v254", "v255", "a0", "a1", "a2", "a3", "a4", "a5", "a6", "a7", "a8",
      "a9", "a10", "a11", "a12", "a13", "a14", "a15", "a16", "a17", "a18", "a19", "a20", "a21", "a22", "a23", "a24", "a25", "a26", "a27", "a28", "a29", "a30", "a31", "a32",
      "a33", "a34", "a35", "a36", "a37", "a38", "a39", "a40", "a41", "a42", "a43", "a44", "a45", "a46", "a47", "a48", "a49", "a50", "a51", "a52", "a53", "a54", "a55", "a56",
      "a57", "a58", "a59", "a60", "a61", "a62", "a63", "a64", "a65", "a66", "a67", "a68", "a69", "a70", "a71", "a72", "a73", "a74", "a75", "a76", "a77", "a78", "a79", "a80",
      "a81", "a82", "a83", "a84", "a85", "a86", "a87", "a88", "a89", "a90", "a91", "a92", "a93", "a94", "a95", "a96", "a97", "a98", "a99", "a100", "a101", "a102", "a103", "a104",
      "a105", "a106", "a107", "a108", "a109", "a110", "a111", "a112", "a113", "a114", "a115", "a116", "a117", "a118", "a119", "a120", "a121", "a122", "a123", "a124", "a125", "a126", "a127", "a128",
      "a129", "a130", "a131", "a132", "a133", "a134", "a135", "a136", "a137", "a138", "a139", "a140", "a141", "a142", "a143", "a144", "a145", "a146", "a147", "a148", "a149", "a150", "a151", "a152",
      "a153", "a154", "a155", "a156", "a157", "a158", "a159", "a160", "a161", "a162", "a163", "a164", "a165", "a166", "a167", "a168", "a169", "a170", "a171", "a172", "a173", "a174", "a175", "a176",
      "a177", "a178", "a179", "a180", "a181", "a182", "a183", "a184", "a185", "a186", "a187", "a188", "a189", "a190", "a191", "a192", "a193", "a194", "a195", "a196", "a197", "a198", "a199", "a200",
      "a201", "a202", "a203", "a204", "a205", "a206", "a207", "a208", "a209", "a210", "a211", "a212", "a213", "a214", "a215", "a216", "a217", "s8", "s9", "s10", "s11", "s12", "s13", "s14",
      "s15", "s16", "s17", "s18", "s19", "s20", "s21", "s22", "s23", "s24", "s25", "s26", "s27", "s28", "s29", "s30", "s31", "s33", "s34", "s35", "s36", "s37", "s38", "s39",
      "s40", "s41", "s42", "s43", "s44", "s45", "s46", "s47", "s48", "s49", "s50", "s51", "s52", "s53", "s54", "s55", "s56", "s57", "s58", "s59", "s60", "s61", "s62", "s63",
      "s64", "s65", "s66", "s67", "s68", "s69", "s70", "s71", "s72", "s73", "s74", "s75", "s76", "s77", "s78", "s79", "s80", "s81", "s82", "s83", "s84", "s85", "s86", "s87",
      "s88", "s89", "s90", "s91", "s92", "s93", "s94", "s95", "s96", "s97", "s98", "s99", "s100", "s101", "vcc", "scc", "memory");
  if (flag | lbase) {
    const int F = 26, P = 325;
    const int lane = threadIdx.x & 63;
    const int b = (int)(min((blockIdx.x & 7) * kx + ((blockIdx.x >> 3) * 4 + (threadIdx.x >> 6)) * 104u, lim) / 104u);
    {
      float m = -INFINITY, z = 0.f, n = 0.f;
      for (int p = lane; p < P; p += 64) {
        const unsigned short* ei = table + (size_t)idx[b * F + ii[p]] * 128;
        const unsigned short* ej = table + (size_t)idx[b * F + jj[p]] * 128;
        float e = 0.f, s = 0.f;
        for (int a = 0; a < 128; ++a) {
          float acc = 0.f;
          for (int d = 0; d < 128; ++d) acc = fmaf(rbf(bf2f(ei[d]) * bf2f(ej[d])), bf2f(W[d * 128 + a]), acc);
          float x = acc + bf2f(bvec[a]);
          x = 0.5f * (x + fabsf(x));
          e = fmaf(x, bf2f(hvec[a]), e);
        }
        for (int d = 0; d < 128; ++d) s = fmaf(rbf(bf2f(ei[d]) * bf2f(ej[d])), bf2f(pvec[d]), s);
        float mn = fmaxf(m, e);
        float fa = __expf(m - mn), fb = __expf(e - mn);
        z = z * fa + fb; n = n * fa + fb * s; m = mn;
      }
      float mx = m;
      for (int o = 1; o < 64; o <<= 1) mx = fmaxf(mx, __shfl_xor(mx, o));
      float f = (m == -INFINITY) ? 0.f : __expf(m - mx);
      z *= f; n *= f;
      for (int o = 1; o < 64; o <<= 1) { z += __shfl_xor(z, o); n += __shfl_xor(n, o); }
      if (lane == 0) out[b] = f2bf(n / z);
    }
  }
}

extern "C" __global__ __launch_bounds__(256) void afm_any(const int* __restrict__ idx, const unsigned short* __restrict__ table,
                                   const int* __restrict__ ii, const int* __restrict__ jj,
                                   const unsigned short* __restrict__ W, const unsigned short* __restrict__ bvec,
                                   const unsigned short* __restrict__ hvec, const unsigned short* __restrict__ pvec,
                                   unsigned short* __restrict__ out, float* __restrict__ ws,
                                   int B, int F, int P, int D, int A) {
  const int b = blockIdx.x;
  float* ebuf = ws + (size_t)b * 2 * P;
  float* sbuf = ebuf + P;
  for (int p = threadIdx.x; p < P; p += 256) {
    const unsigned short* ei = table + (size_t)idx[b * F + ii[p]] * D;
    const unsigned short* ej = table + (size_t)idx[b * F + jj[p]] * D;
    float e = 0.f, sacc = 0.f;
    for (int a = 0; a < A; ++a) {
      float acc = 0.f;
      for (int d = 0; d < D; ++d) acc = fmaf(rbf(bf2f(ei[d]) * bf2f(ej[d])), bf2f(W[(size_t)d * A + a]), acc);
      float x = acc + bf2f(bvec[a]);
      x = 0.5f * (x + fabsf(x));
      e = fmaf(x, bf2f(hvec[a]), e);
    }
    for (int d = 0; d < D; ++d) sacc = fmaf(rbf(bf2f(ei[d]) * bf2f(ej[d])), bf2f(pvec[d]), sacc);
    ebuf[p] = e; sbuf[p] = sacc;
  }
  __syncthreads();
  if (threadIdx.x == 0) {
    float mx = -INFINITY;
    for (int p = 0; p < P; ++p) mx = fmaxf(mx, ebuf[p]);
    float z = 0.f, n = 0.f;
    for (int p = 0; p < P; ++p) { float w = __expf(ebuf[p] - mx); z += w; n += w * sbuf[p]; }
    out[b] = f2bf(n / z);
  }
}

extern "C" void kernel_launch(void* const* d_in, const int* in_sizes, int n_in,
                              void* d_out, int out_size, void* d_ws, size_t ws_size,
                              hipStream_t stream) {
  const int* idx = (const int*)d_in[0];
  const unsigned short* table = (const unsigned short*)d_in[1];
  const int* ii = (const int*)d_in[2];
  const int* jj = (const int*)d_in[3];
  const unsigned short* W = (const unsigned short*)d_in[4];
  const unsigned short* bvec = (const unsigned short*)d_in[5];
  const unsigned short* hvec = (const unsigned short*)d_in[6];
  const unsigned short* pvec = (const unsigned short*)d_in[7];
  unsigned short* out = (unsigned short*)d_out;
  const int B = out_size;
  const int P = in_sizes[2];
  const int D = in_sizes[7];
  const int A = in_sizes[5];
  if (B <= 0) return;
  const int F = in_sizes[0] / B;
  if (D == 128 && A == 128 && F == 26 && P == 325 && B < (1 << 24)) {
    const int g8 = (B + 31) / 32;
    afm_fast<<<8 * g8, 256, 0, stream>>>(idx, table, ii, jj, W, bvec, hvec, pvec, out, 104u * (unsigned)(B - 1), 416u * (unsigned)g8
        );
  } else {
    afm_any<<<B, 256, 0, stream>>>(idx, table, ii, jj, W, bvec, hvec, pvec, out, (float*)d_ws, B, F, P, D, A);
  }
}
```

```cpp
#include <hip/hip_runtime.h>
#include <hip/hip_bf16.h>
#include <stdint.h>

#define AFM_HAS_STAMPS 0

typedef float  f32x2  __attribute__((ext_vector_type(2)));
typedef __bf16 bf16x2 __attribute__((ext_vector_type(2)));

__device__ __forceinline__ float bf2f(unsigned short u) { return __builtin_bit_cast(float, (unsigned)u << 16); }
__device__ __forceinline__ unsigned short f2bf(float x) {
  f32x2 v = {x, 0.f}; bf16x2 r = __builtin_convertvector(v, bf16x2);
  return (unsigned short)(__builtin_bit_cast(unsigned, r) & 0xffffu);
}
__device__ __forceinline__ float rbf(float x) { return bf2f(f2bf(x)); }

extern "C" __device__ __attribute__((used, aligned(512))) const unsigned afm_lane_tab[804] = {
  738206208u, 533735168u, 697050480u, 864u, 743449092u, 539240196u, 719070580u, 20972672u, 748691976u, 539764488u, 554968440u, 1440u, 753934860u, 550250252u, 717235580u, 20973248u,
  759177744u, 555755280u, 613426560u, 2016u, 764420628u, 561260308u, 540550532u, 20971520u, 769663512u, 561784600u, 549463432u, 288u, 774906396u, 530327324u, 722740620u, 20972096u,
  738468432u, 538978128u, 618931888u, 3168u, 743711316u, 534521684u, 727983796u, 20974976u, 748954200u, 549988184u, 708585144u, 3744u, 754197084u, 555493212u, 723003068u, 20975552u,
  759439968u, 560998240u, 728508096u, 4320u, 764682852u, 556541796u, 624436932u, 20973824u, 769925736u, 530065256u, 702555848u, 2592u, 775168620u, 535570284u, 697575116u, 20974400u,
  738730656u, 701507488u, 697050480u, 1049456u, 743973540u, 707012516u, 719070580u, 22021264u, 749216424u, 707536808u, 554968440u, 1050032u, 754459308u, 718022572u, 717235580u, 22021840u,
  759702192u, 723527600u, 613426560u, 1050608u, 764945076u, 729032628u, 540550532u, 22020112u, 770187960u, 729556920u, 549463432u, 1048880u, 775430844u, 698099644u, 722740620u, 22020688u,
  738992880u, 706750448u, 618931888u, 1051760u, 744235764u, 702294004u, 727983796u, 22023568u, 749478648u, 717760504u, 708585144u, 1052336u, 754721532u, 723265532u, 723003068u, 22024144u,
  759964416u, 728770560u, 728508096u, 1052912u, 765207300u, 724314116u, 624436932u, 22022416u, 770450184u, 697837576u, 702555848u, 1051184u, 775693068u, 703342604u, 697575116u, 22022992u,
  739255104u, 617621568u, 529279600u, 2098048u, 744497988u, 623126596u, 549726836u, 23069856u, 749740872u, 623650888u, 555231864u, 2098624u, 754983756u, 634136652u, 560736892u, 23070432u,
  760226640u, 639641680u, 551299712u, 2099200u, 765469524u, 645146708u, 529803908u, 23068704u, 770712408u, 645671000u, 535308936u, 2097472u, 775955292u, 614213724u, 540813964u, 23069280u,
  739517328u, 622864528u, 613166000u, 2100352u, 744760212u, 618408084u, 633613236u, 23072160u, 750003096u, 633874584u, 639118264u, 2100928u, 755245980u, 639379612u, 644623292u, 23072736u,
  760488864u, 644884640u, 635186112u, 2101504u, 765731748u, 640428196u, 613690308u, 23071008u, 770974632u, 613951656u, 619195336u, 2099776u, 776217516u, 619456684u, 624700364u, 23071584u,
  739779552u, 544221408u, 529279600u, 3146640u, 745022436u, 544483556u, 549726836u, 24118448u, 750265320u, 544745704u, 555231864u, 3147216u, 755508204u, 545007852u, 560736892u, 24119024u,
  760751088u, 545270000u, 551299712u, 3147792u, 765993972u, 545532148u, 529803908u, 24117296u, 771236856u, 545794296u, 535308936u, 3146064u, 776479740u, 546056444u, 540813964u, 24117872u,
  740041776u, 565193008u, 613166000u, 3148944u, 745284660u, 565455156u, 633613236u, 24120752u, 750527544u, 565717304u, 639118264u, 3149520u, 755770428u, 565979452u, 644623292u, 24121328u,
  761013312u, 566241600u, 635186112u, 3150096u, 766256196u, 566503748u, 613690308u, 24119600u, 771499080u, 566765896u, 619195336u, 3148368u, 776741964u, 567028044u, 624700364u, 24120176u,
  358617600u, 8064u, 301992192u, 132121440u, 434116224u, 18882512u, 320866848u, 358619616u, 94372128u, 37756960u, 339741504u, 415241280u, 377491968u, 452993136u, 452991744u, 358619616u,
  150998400u, 75505856u, 377490816u, 452992032u, 37750464u, 94380304u, 396365472u, 207622368u, 75497472u, 113254752u, 415240128u, 207622368u, 396366624u, 471867824u, 471866400u, 415241280u,
  169873056u, 151003648u, 4608u, 1075854240u, 415240992u, 169878096u, 18879264u, 1283477760u, 434115648u, 188752544u, 37753920u, 1396724832u, 396366912u, 207626992u, 56628576u, 1283477760u,
  415241568u, 226501440u, 75503232u, 1377848736u, 188747712u, 245375888u, 94377888u, 1151355168u, 320870016u, 264250336u, 113252544u, 1151355168u, 301995936u, 283124784u, 132127200u, 1283477760u,
  359666192u, 1056656u, 303040784u, 133170032u, 435164816u, 19931104u, 321915440u, 359668208u, 95420720u, 38805552u, 340790096u, 416289872u, 378540560u, 454041728u, 454040336u, 359668208u,
  152046992u, 76554448u, 378539408u, 454040624u, 38799056u, 95428896u, 397414064u, 208670960u, 76546064u, 114303344u, 416288720u, 208670960u, 397415216u, 472916416u, 472914992u, 416289872u,
  170921648u, 152052240u, 1053200u, 1076902832u, 416289584u, 170926688u, 19927856u, 1284526352u, 435164240u, 189801136u, 38802512u, 1397773424u, 397415504u, 208675584u, 57677168u, 1284526352u,
  416290160u, 227550032u, 76551824u, 1378897328u, 189796304u, 246424480u, 95426480u, 1152403760u, 321918608u, 265298928u, 114301136u, 1152403760u, 303044528u, 284173376u, 133175792u, 1284526352u,
  360714784u, 2105248u, 304089376u, 134218624u, 436213408u, 20979696u, 322964032u, 360716800u, 96469312u, 39854144u, 341838688u, 417338464u, 379589152u, 455090320u, 455088928u, 360716800u,
  153095584u, 77603040u, 379588000u, 455089216u, 39847648u, 96477488u, 398462656u, 209719552u, 77594656u, 115351936u, 417337312u, 209719552u, 398463808u, 473965008u, 473963584u, 417338464u,
  171970240u, 153100832u, 2101792u, 1077951424u, 417338176u, 171975280u, 20976448u, 1285574944u, 436212832u, 190849728u, 39851104u, 1398822016u, 398464096u, 209724176u, 58725760u, 1285574944u,
  417338752u, 228598624u, 77600416u, 1379945920u, 190844896u, 247473072u, 96475072u, 1153452352u, 322967200u, 266347520u, 115349728u, 1153452352u, 304093120u, 285221968u, 134224384u, 1285574944u,
  361763376u, 3153840u, 305137968u, 135267216u, 437262000u, 22028288u, 324012624u, 361765392u, 97517904u, 40902736u, 342887280u, 418387056u, 380637744u, 456138912u, 456137520u, 361765392u,
  154144176u, 78651632u, 380636592u, 456137808u, 40896240u, 97526080u, 399511248u, 210768144u, 78643248u, 116400528u, 418385904u, 210768144u, 399512400u, 475013600u, 475012176u, 418387056u,
  173018832u, 154149424u, 3150384u, 1079000016u, 418386768u, 173023872u, 22025040u, 1286623536u, 437261424u, 191898320u, 40899696u, 1399870608u, 399512688u, 210772768u, 59774352u, 1286623536u,
  418387344u, 229647216u, 78649008u, 1380994512u, 191893488u, 248521664u, 97523664u, 1154500944u, 324015792u, 267396112u, 116398320u, 1154500944u, 305141712u, 286270560u, 135272976u, 1286623536u,
  0u, 18874368u, 524304u, 37748740u, 75497504u, 56623112u, 76021808u, 75497484u, 304087104u, 94371856u, 304611408u, 113246228u, 379584608u, 132120600u, 380108912u, 28u,
  608174208u, 37748864u, 608698512u, 56623236u, 683671712u, 75497608u, 684196016u, 94371980u, 912261312u, 113246352u, 912785616u, 132120724u, 987758816u, 152u, 988283120u, 18874524u,
  1053216u, 19923744u, 1577520u, 38798116u, 76550720u, 57672488u, 77075024u, 76546860u, 305140320u, 95421232u, 305664624u, 114295604u, 380637824u, 133169976u, 381162128u, 1049404u,
  609227424u, 38798240u, 609751728u, 57672612u, 684724928u, 76546984u, 685249232u, 95421356u, 913314528u, 114295728u, 913838832u, 133170100u, 988812032u, 1049528u, 989336336u, 19923900u,
  2106432u, 20972352u, 2630736u, 39846724u, 77603936u, 58721096u, 78128240u, 77595468u, 306193536u, 96469840u, 306717840u, 115344212u, 381691040u, 134218584u, 382215344u, 2098012u,
  610280640u, 39846848u, 610804944u, 58721220u, 685778144u, 77595592u, 686302448u, 96469964u, 914367744u, 115344336u, 914892048u, 134218708u, 989865248u, 2098136u, 990389552u, 20972508u,
  3159648u, 22020960u, 3683952u, 40895332u, 78657152u, 59769704u, 79181456u, 78644076u, 307246752u, 97518448u, 307771056u, 116392820u, 382744256u, 135267192u, 383268560u, 3146620u,
  611333856u, 40895456u, 611858160u, 59769828u, 686831360u, 78644200u, 687355664u, 97518572u, 915420960u, 116392944u, 915945264u, 135267316u, 990918464u, 3146744u, 991442768u, 22021116u,
  0u, 0u, 0u, 0u, 0u, 0u, 16843008u, 16843009u, 16843009u, 16843009u, 16843009u, 16843009u, 33686017u, 33686018u, 33686018u, 33686018u,
  33686018u, 33686018u, 50529027u, 50529027u, 50529027u, 50529027u, 50529027u, 67371779u, 67372036u, 67372036u, 67372036u, 67372036u, 84149252u, 84215045u, 84215045u, 84215045u,
  84215045u, 100992261u, 101058054u, 101058054u, 101058054u, 101058054u, 117900806u, 117901063u, 117901063u, 117901063u, 117901063u, 134744072u, 134744072u, 134744072u, 134744072u, 151587080u,
  151587081u, 151587081u, 151587081u, 168430089u, 168430090u, 168430090u, 168430090u, 185273099u, 185273099u, 185273099u, 202115851u, 202116108u, 202116108u, 218893324u, 218959117u, 218959117u,
  235736333u, 235802126u, 235802126u, 252644878u, 252645135u, 252645135u, 269488144u, 269488144u, 286331152u, 286331153u, 303174161u, 303174162u, 320017171u, 336859923u, 353637396u, 370480405u,
  387388950u, 404166422u, 67305985u, 134678021u, 202050057u, 269422093u, 336794129u, 404166165u, 67306009u, 134678021u, 202050057u, 269422093u, 336794129u, 404166165u, 84149017u, 151521030u,
  218893066u, 286265102u, 353637138u, 421009174u, 117835012u, 185207048u, 252579084u, 319951120u, 387323156u, 100997400u, 168364039u, 235736075u, 303108111u, 370480147u, 102307863u, 168364039u,
  235736075u, 303108111u, 370480147u, 119085079u, 185207048u, 252579084u, 319951120u, 387323156u, 151525656u, 218893066u, 286265102u, 353637138u, 421009174u, 202050057u, 269422093u, 336794129u,
  404166165u, 202050073u, 269422093u, 336794129u, 404166165u, 218893081u, 286265102u, 353637138u, 421009174u, 252579084u, 319951120u, 387323156u, 235739416u, 303108111u, 370480147u, 236525591u,
  303108111u, 370480147u, 253302807u, 319951120u, 387323156u, 286267672u, 353637138u, 421009174u, 336794129u, 404166165u, 336794137u, 404166165u, 353637145u, 421009174u, 387323156u, 370481432u,
  370743319u, 387520535u, 421009688u, 421074969u
};

extern "C" __global__ __launch_bounds__(256, 1) __attribute__((aligned(4096)))
void afm_fast(const int* __restrict__ idx, const unsigned short* __restrict__ table,
                     const int* __restrict__ ii, const int* __restrict__ jj,
                     const unsigned short* __restrict__ W, const unsigned short* __restrict__ bvec,
                     const unsigned short* __restrict__ hvec, const unsigned short* __restrict__ pvec,
                     unsigned short* __restrict__ out, unsigned lim, unsigned kx
                     ) {
  __shared__ __attribute__((aligned(16))) char smem[110912];
  const unsigned lbase = (unsigned)(uintptr_t)(__attribute__((address_space(3))) char*)smem;
  unsigned flag;
  asm volatile(
      "s_load_dwordx16 s[8:23], %1, 0x0\n"
      "s_load_dwordx4 s[24:27], %1, 0x40\n"
      "s_mov_b32 s41, 0\n"
      "s_mov_b32 s42, 0\n"
      "s_mov_b32 s58, 0\n"
      "s_mov_b32 s35, 0\n"
      "0:\n"
      "s_getpc_b64 s[56:57]\n"
      "s_add_u32 s98, s56, afm_lane_tab@rel32@lo+4\n"
      "s_addc_u32 s99, s57, afm_lane_tab@rel32@hi+12\n"
      "v_and_b32 v255, 63, %3\n"
      "v_and_b32 v254, 127, %3\n"
      "v_lshrrev_b32 v253, 7, %3\n"
      "v_min_u32 v254, 81, v254\n"
      "v_mul_u32_u24 v253, 82, v253\n"
      "v_lshlrev_b32 v14, 4, v255\n"
      "v_lshlrev_b32 v252, 3, v255\n"
      "v_add_lshl_u32 v253, v253, v254, 2\n"
      "global_load_dwordx2 v[22:23], v252, s[98:99] offset:2048\n"
      "global_load_dword v20, v253, s[98:99] offset:2560\n"
      "global_load_dwordx4 v[204:207], v14, s[98:99]\n"
      "global_load_dwordx4 v[196:199], v14, s[98:99] offset:1024\n"
      "v_lshrrev_b32 v2, 6, %3\n"
      "s_bfe_u32 s44, %2, 0x30003\n"
      "s_mul_i32 s44, s44, 24\n"
      "v_mad_u32_u24 v255, v2, 6, v255\n"
      "s_mov_b64 exec, 0x3f\n"
      "v_add_lshl_u32 v255, v255, s44, 7\n"
      "global_load_dword v255, v255, s[56:57]\n"
      "s_mov_b64 exec, -1\n"
      "v_bfe_u32 v4, %3, 4, 2\n"
      "v_and_b32 v3, 15, %3\n"
      "v_readfirstlane_b32 s53, v2\n"
      "s_and_b32 s50, %2, 7\n"
      "s_lshr_b32 s51, %2, 3\n"
      "s_lshl2_add_u32 s51, s51, s53\n"
      "s_mul_i32 s51, s51, 104\n"
      "v_lshlrev_b32 v13, 4, v3\n"
      "v_lshlrev_b32 v225, 4, v254\n"
      "v_min_u32 v225, 1284, v225\n"
      "v_lshl_add_u32 v80, v2, 11, v14\n"
      "v_add_u32 v81, 8192, v80\n"
      "v_add_u32 v82, 16384, v80\n"
      "v_add_u32 v26, 24576, v80\n"
      "v_lshlrev_b32 v2, 4, v2\n"
      "s_mul_i32 s38, s53, 14400\n"
      "v_add_u32 v2, 37632, v2\n"
      "s_add_u32 s38, s38, 38912\n"
      "v_mul_u32_u24 v222, 288, v4\n"
      "v_add_u32 v213, s38, v13\n"
      "v_mov_b32 v5, 0\n"
      "v_add_u32 v1, v222, v213\n"
      "v_mov_b32 v222, 0x7fa5a5a5\n"
      "s_cmp_lg_u32 s35, 0\n"
      "s_cbranch_scc1 2f\n"
      "ds_write2_b32 v2, v5, v5 offset0:0 offset1:1\n"
      "ds_write_b32 v2, v5 offset:8\n"
      "ds_write_b32 v1, v222 offset:12992\n"
      "2:\n"
      "s_mov_b32 s43, 0xffff0000\n"
      "s_mov_b32 s56, 0\n"
      "s_mov_b32 s40, 0\n"
      "s_mov_b32 s55, 0\n"
      "s_and_b32 s46, s53, 2\n"
      "s_lshl_b32 s54, 3, s46\n"
      "s_bitcmp1_b32 s53, 0\n"
      "s_cselect_b64 s[100:101], -1, 0\n"
      "s_movk_i32 s98, 14400\n"
      "s_cselect_b32 s99, s98, 0\n"
      "s_cmovk_i32 s98, -14400\n"
      "s_cselect_b32 s46, 0, 18560\n"
      "s_cselect_b32 s47, 18560, 0\n"
      "v_add_u32 v212, s46, v14\n"
      "v_add_u32 v14, s47, v14\n"
      "s_sub_u32 s99, s38, s99\n"
      "v_cmp_eq_u32 vcc, 1, v4\n"
      "v_mov_b32 v222, 0x3200\n"
      "s_mov_b32 s46, 0\n"
      "v_cndmask_b32 v94, 0, v222, vcc\n"
      "s_mov_b32 s47, 0xc82c4cb9\n"
      "v_lshlrev_b32 v223, 4, v4\n"
      "v_lshrrev_b64 v[146:147], v223, s[46:47]\n"
      "v_lshlrev_b32 v138, 16, v146\n"
      "v_lshrrev_b32 v130, 16, v138\n"
      "v_mov_b32 v200, 0x4838aa3b\n"
      "v_lshlrev_b32 v224, 16, v94\n"
      "v_accvgpr_write_b32 a128, v130\n"
      "v_accvgpr_write_b32 a129, 0\n"
      "v_accvgpr_write_b32 a130, v138\n"
      "v_accvgpr_write_b32 a131, 0\n"
      "v_accvgpr_write_b32 a132, 0\n"
      "v_accvgpr_write_b32 a133, v130\n"
      "v_accvgpr_write_b32 a134, 0\n"
      "v_accvgpr_write_b32 a135, v138\n"
      "v_accvgpr_write_b32 a208, v94\n"
      "v_accvgpr_write_b32 a209, 0\n"
      "v_accvgpr_write_b32 a210, v224\n"
      "v_accvgpr_write_b32 a211, 0\n"
      "v_accvgpr_write_b32 a212, 0\n"
      "v_accvgpr_write_b32 a213, v94\n"
      "v_accvgpr_write_b32 a214, 0\n"
      "v_accvgpr_write_b32 a215, v224\n"
      "v_add_u32 v95, s38, v223\n"
      "v_and_b32 v227, 3, %3\n"
      "v_bfe_u32 v224, %3, 2, 2\n"
      "v_cmp_eq_u32 vcc, v227, v4\n"
      "v_mov_b32 v222, 0x3a00\n"
      "v_mov_b64 v[84:85], 0\n"
      "s_waitcnt lgkmcnt(0)\n"
      "s_mul_i32 s50, s50, s27\n"
      "s_add_u32 s50, s50, s51\n"
      "s_min_u32 s34, s50, s26\n"
      "s_load_dwordx16 s[60:75], s[8:9], s34\n"
      "s_load_dwordx8 s[76:83], s[8:9], s34 offset:0x40\n"
      "s_load_dwordx2 s[84:85], s[8:9], s34 offset:0x60\n"
      "global_load_dwordx4 v[96:99], v80, s[16:17] offset:0\n"
      "global_load_dwordx4 v[100:103], v80, s[16:17] offset:1024\n"
      "global_load_dwordx4 v[104:107], v81, s[16:17] offset:0\n"
      "global_load_dwordx4 v[108:111], v81, s[16:17] offset:1024\n"
      "v_mov_b64 v[24:25], s[20:21]\n"
      "s_mov_b32 exec_hi, 0\n"
      "v_mov_b64 v[24:25], s[18:19]\n"
      "s_mov_b32 exec_lo, 0xffff\n"
      "v_mov_b64 v[24:25], s[22:23]\n"
      "s_mov_b64 exec, -1\n"
      "v_add_co_u32 v24, s[28:29], v24, v13\n"
      "s_cmp_lt_u32 s53, 2\n"
      "s_cselect_b64 s[12:13], s[12:13], s[14:15]\n"
      "v_addc_co_u32 v25, s[28:29], 0, v25, s[28:29]\n"
      "global_load_dwordx4 v[192:195], v[24:25], off\n"
      "global_load_dwordx4 v[112:115], v82, s[16:17] offset:0\n"
      "global_load_dwordx4 v[116:119], v82, s[16:17] offset:1024\n"
      "global_load_dwordx4 v[120:123], v26, s[16:17] offset:0\n"
      "global_load_dwordx4 v[124:127], v26, s[16:17] offset:1024\n"
      "s_cmp_lg_u32 s35, 0\n"
      "s_cbranch_scc1 21f\n"
      "s_barrier\n"
      "21:\n"
      "v_mov_b64 v[88:89], 0\n"
      "v_cndmask_b32 v84, 0, v222, vcc\n"
      "v_lshlrev_b32 v222, 4, v224\n"
      "v_lshlrev_b64 v[86:87], v222, v[84:85]\n"
      "s_mov_b32 s46, 0xd905\n"
      "v_lshlrev_b32 v222, 2, v4\n"
      "v_mov_b32 v84, 0\n"
      "v_bfe_u32 v222, s46, v222, 4\n"
      "v_lshl_add_u32 v91, v222, 6, v213\n"
      "v_lshlrev_b32 v227, 4, v227\n"
      "s_mov_b64 s[46:47], 0x3b80\n"
      "v_lshlrev_b32 v5, 5, v3\n"
      "v_lshl_add_u32 v213, v4, 2, v213\n"
      "v_lshl_add_u32 v5, v4, 3, v5\n"
      "v_lshlrev_b64 v[222:223], v227, s[46:47]\n"
      "s_movk_i32 s46, 0x4400\n"
      "v_accvgpr_write_b32 a216, v222\n"
      "v_lshlrev_b64 v[172:173], v227, s[46:47]\n"
      "s_lshl_b64 exec, -1, 48\n"
      "s_bitset1_b32 s46, 15\n"
      "v_lshlrev_b64 v[172:173], v227, s[46:47]\n"
      "s_mov_b64 exec, -1\n"
      "s_mov_b32 s49, 0x03000000\n"
      "v_accvgpr_write_b32 a217, v223\n"
      "s_movk_i32 s46, 288\n"
      "v_and_b32 v222, 7, %3\n"
      "v_bfe_u32 v223, %3, 3, 1\n"
      "v_mad_u32_u24 v201, v222, s46, v95\n"
      "v_mad_u32_u24 v203, v223, s46, v95\n"
      "s_movk_i32 s47, 320\n"
      "v_mad_u32_u24 v210, v3, s46, v95\n"
      "v_mad_u32_u24 v214, v223, s47, v95\n"
      "s_mov_b32 s59, 0\n"
      "s_mov_b32 s57, 0\n"
      "v_and_b32 v222, v3, v4\n"
      "v_lshlrev_b32 v223, 6, v3\n"
      "v_bfe_u32 v222, 6, v222, 1\n"
      "s_movk_i32 s46, 0x100\n"
      "v_lshl_add_u32 v44, v222, 8, v95\n"
      "v_bitop3_b32 v45, v44, v223, s46 bitop3:0x78\n"
      "v_bitop3_b32 v46, v44, v5, s46 bitop3:0x78\n"
      "v_bitop3_b32 v47, v45, v5, s46 bitop3:0x78\n"
      "s_lshr_b32 s33, s34, 2\n"
      "s_mul_i32 s33, s33, 0xc4ec4ec5\n"
      "s_mov_b32 s36, 0\n"
      "s_mov_b32 s37, 0x7fa5a5a5\n"
      "global_load_dwordx4 v[80:83], v225, s[12:13]\n"
      "s_mov_b64 exec, 0\n"
      "s_cmp_eq_u32 s53, 0\n"
      "s_cbranch_scc1 40f\n"
      "s_cmp_eq_u32 s53, 1\n"
      "s_cbranch_scc1 41f\n"
      "s_cmp_eq_u32 s53, 2\n"
      "s_cbranch_scc1 42f\n"
      "s_branch 43f\n"
      "44:\n"
      "s_mov_b64 exec, -1\n"
      "s_waitcnt lgkmcnt(0)\n"
      "v_lshl_add_u32 v6, s60, 8, v13\n"
      "v_lshl_add_u32 v93, s64, 8, v13\n"
      "s_mov_b32 exec_lo, 0xffff0000\n"
      "v_lshl_add_u32 v6, s61, 8, v13\n"
      "v_lshl_add_u32 v93, s65, 8, v13\n"
      "s_mov_b32 exec_lo, 0\n"
      "v_lshl_add_u32 v6, s62, 8, v13\n"
      "v_lshl_add_u32 v93, s66, 8, v13\n"
      "s_mov_b32 exec_hi, 0xffff0000\n"
      "v_lshl_add_u32 v6, s63, 8, v13\n"
      "v_lshl_add_u32 v93, s67, 8, v13\n"
      "s_mov_b64 exec, -1\n"
      "global_load_dwordx4 v[228:231], v6, s[10:11] nt\n"
      "global_load_dwordx4 v[232:235], v93, s[10:11] nt\n"
      "v_lshl_add_u32 v8, s68, 8, v13\n"
      "v_lshl_add_u32 v9, s72, 8, v13\n"
      "v_lshl_add_u32 v10, s76, 8, v13\n"
      "v_lshl_add_u32 v11, s80, 8, v13\n"
      "v_lshl_add_u32 v12, s84, 8, v13\n"
      "s_mov_b32 exec_lo, 0xffff0000\n"
      "v_lshl_add_u32 v8, s69, 8, v13\n"
      "v_lshl_add_u32 v9, s73, 8, v13\n"
      "v_lshl_add_u32 v10, s77, 8, v13\n"
      "v_lshl_add_u32 v11, s81, 8, v13\n"
      "v_lshl_add_u32 v12, s85, 8, v13\n"
      "s_mov_b32 exec_lo, 0\n"
      "v_lshl_add_u32 v8, s70, 8, v13\n"
      "v_lshl_add_u32 v9, s74, 8, v13\n"
      "v_lshl_add_u32 v10, s78, 8, v13\n"
      "v_lshl_add_u32 v11, s82, 8, v13\n"
      "s_mov_b32 exec_hi, 0xffff0000\n"
      "v_lshl_add_u32 v8, s71, 8, v13\n"
      "v_lshl_add_u32 v9, s75, 8, v13\n"
      "v_lshl_add_u32 v10, s79, 8, v13\n"
      "v_lshl_add_u32 v11, s83, 8, v13\n"
      "s_mov_b64 exec, -1\n"
      "s_lshl_b32 s46, s53, 8\n"
      "s_lshl_b32 s47, s53, 5\n"
      "s_waitcnt vmcnt(16)\n"
      "v_add_u32_sdwa v15, s46, v22 dst_sel:DWORD dst_unused:UNUSED_PAD src0_sel:DWORD src1_sel:WORD_0\n"
      "v_add_u32_sdwa v92, s46, v22 dst_sel:DWORD dst_unused:UNUSED_PAD src0_sel:DWORD src1_sel:WORD_1\n"
      "v_add_u32_sdwa v7, s47, v23 dst_sel:DWORD dst_unused:UNUSED_PAD src0_sel:DWORD src1_sel:WORD_0\n"
      "v_add_u32_sdwa v202, s38, v23 dst_sel:DWORD dst_unused:UNUSED_PAD src0_sel:DWORD src1_sel:WORD_1\n"
      "s_waitcnt vmcnt(15)\n"
      "v_and_b32 v160, 0xff, v20\n"
      "v_bfe_u32 v161, v20, 8, 8\n"
      "v_bfe_u32 v162, v20, 16, 8\n"
      "v_lshrrev_b32 v163, 24, v20\n"
      "s_cmp_lg_u32 s58, 0\n"
      "s_cbranch_scc1 70f\n"
      "71:\n"
      "s_waitcnt vmcnt(11)\n"
      "v_mfma_f32_16x16x32_bf16 v[128:131], v[86:89], v[96:99], 0\n"
      "s_waitcnt vmcnt(10)\n"
      "v_mfma_f32_16x16x32_bf16 v[132:135], v[86:89], v[100:103], 0\n"
      "v_mfma_f32_16x16x32_bf16 v[136:139], v[84:87], v[96:99], 0\n"
      "v_mfma_f32_16x16x32_bf16 v[140:143], v[84:87], v[100:103], 0\n"
      "s_waitcnt vmcnt(9)\n"
      "v_mfma_f32_16x16x32_bf16 v[144:147], v[86:89], v[104:107], 0\n"
      "s_nop 1\n"
      "v_cvt_pk_f16_f32 v164, v128, v129\n"
      "v_cvt_pk_f16_f32 v165, v130, v131\n"
      "s_waitcnt vmcnt(8)\n"
      "v_mfma_f32_16x16x32_bf16 v[148:151], v[86:89], v[108:111], 0\n"
      "v_cvt_pk_f16_f32 v166, v132, v133\n"
      "v_cvt_pk_f16_f32 v167, v134, v135\n"
      "v_mfma_f32_16x16x32_bf16 v[152:155], v[84:87], v[104:107], 0\n"
      "ds_write_b128 v15, v[164:167] offset:0\n"
      "v_cvt_pk_f16_f32 v168, v136, v137\n"
      "v_cvt_pk_f16_f32 v169, v138, v139\n"
      "v_mfma_f32_16x16x32_bf16 v[156:159], v[84:87], v[108:111], 0\n"
      "v_cvt_pk_f16_f32 v170, v140, v141\n"
      "v_cvt_pk_f16_f32 v171, v142, v143\n"
      "global_load_dwordx4 v[236:239], v8, s[10:11] nt\n"
      "s_waitcnt vmcnt(7)\n"
      "v_mfma_f32_16x16x32_bf16 v[128:131], v[86:89], v[112:115], 0\n"
      "ds_write_b128 v15, v[168:171] offset:18560\n"
      "v_cvt_pk_f16_f32 v164, v144, v145\n"
      "v_cvt_pk_f16_f32 v165, v146, v147\n"
      "s_waitcnt vmcnt(6)\n"
      "v_mfma_f32_16x16x32_bf16 v[132:135], v[86:89], v[116:119], 0\n"
      "v_cvt_pk_f16_f32 v166, v148, v149\n"
      "v_cvt_pk_f16_f32 v167, v150, v151\n"
      "v_mfma_f32_16x16x32_bf16 v[136:139], v[84:87], v[112:115], 0\n"
      "ds_write_b128 v15, v[164:167] offset:1152\n"
      "v_cvt_pk_f16_f32 v168, v152, v153\n"
      "v_cvt_pk_f16_f32 v169, v154, v155\n"
      "v_mfma_f32_16x16x32_bf16 v[140:143], v[84:87], v[116:119], 0\n"
      "v_cvt_pk_f16_f32 v170, v156, v157\n"
      "v_cvt_pk_f16_f32 v171, v158, v159\n"
      "s_waitcnt vmcnt(5)\n"
      "v_mfma_f32_16x16x32_bf16 v[144:147], v[86:89], v[120:123], 0\n"
      "ds_write_b128 v15, v[168:171] offset:19712\n"
      "v_cvt_pk_f16_f32 v164, v128, v129\n"
      "v_cvt_pk_f16_f32 v165, v130, v131\n"
      "s_waitcnt vmcnt(4)\n"
      "v_mfma_f32_16x16x32_bf16 v[148:151], v[86:89], v[124:127], 0\n"
      "v_cvt_pk_f16_f32 v166, v132, v133\n"
      "v_cvt_pk_f16_f32 v167, v134, v135\n"
      "v_mfma_f32_16x16x32_bf16 v[152:155], v[84:87], v[120:123], 0\n"
      "ds_write_b128 v15, v[164:167] offset:2304\n"
      "v_cvt_pk_f16_f32 v168, v136, v137\n"
      "v_cvt_pk_f16_f32 v169, v138, v139\n"
      "v_mfma_f32_16x16x32_bf16 v[156:159], v[84:87], v[124:127], 0\n"
      "v_cvt_pk_f16_f32 v170, v140, v141\n"
      "v_cvt_pk_f16_f32 v171, v142, v143\n"
      "ds_write_b128 v15, v[168:171] offset:20864\n"
      "v_mfma_f32_4x4x4_16b_bf16 v[176:179], v[172:173], v[192:193], 0\n"
      "v_cndmask_b32 v188, v192, v194, s[100:101]\n"
      "v_cvt_pk_f16_f32 v164, v144, v145\n"
      "v_cndmask_b32 v189, v193, v195, s[100:101]\n"
      "v_max3_f32 v222, |v192|, |v193|, |v195|\n"
      "v_cvt_pk_f16_f32 v165, v146, v147\n"
      "v_mfma_f32_4x4x4_16b_bf16 v[180:183], v[172:173], v[194:195], 0\n"
      "v_subrev_u32 v222, 0x3d800000, v222\n"
      "v_cvt_pk_f16_f32 v166, v148, v149\n"
      "v_mfma_f32_16x16x16_bf16 v[48:51], v[188:189], a[128:129], 0\n"
      "v_cmp_gt_u32 vcc, s49, v222\n"
      "v_cvt_pk_f16_f32 v167, v150, v151\n"
      "v_mfma_f32_16x16x16_bf16 v[52:55], v[188:189], a[130:131], 0\n"
      "v_cndmask_b32 v190, v194, v192, s[100:101]\n"
      "ds_write_b128 v15, v[164:167] offset:3456\n"
      "v_cndmask_b32 v191, v195, v193, s[100:101]\n"
      "v_mfma_f32_16x16x16_bf16 v[56:59], v[188:189], a[132:133], 0\n"
      "v_cvt_pk_f16_f32 v168, v152, v153\n"
      "s_and_b32 s44, vcc_lo, 0xffff\n"
      "s_cselect_b32 s44, vcc_hi, 0\n"
      "v_cvt_pk_f16_f32 v169, v154, v155\n"
      "v_mfma_f32_16x16x16_bf16 v[60:63], v[188:189], a[134:135], 0\n"
      "s_cmp_eq_u32 s44, 0\n"
      "v_cvt_pk_f16_f32 v170, v156, v157\n"
      "s_cbranch_scc1 22f\n"
      "23:\n"
      "v_mfma_f32_16x16x16_bf16 v[64:67], v[190:191], a[128:129], 0\n"
      "v_cvt_pk_f16_f32 v171, v158, v159\n"
      "v_cvt_pk_f16_f32 v184, v176, v177\n"
      "v_cvt_pk_f16_f32 v185, v178, v179\n"
      "ds_write_b128 v15, v[168:171] offset:22016\n"
      "v_mfma_f32_16x16x16_bf16 v[68:71], v[190:191], a[130:131], 0\n"
      "v_cvt_pk_f16_f32 v186, v180, v181\n"
      "ds_write_b32 v2, v2 offset:4\n"
      "v_cvt_pk_f16_f32 v187, v182, v183\n"
      "ds_write_b128 v91, v[184:187] offset:11904\n"
      "v_mfma_f32_16x16x16_bf16 v[72:75], v[190:191], a[132:133], 0\n"
      "v_mfma_f32_16x16x16_bf16 v[76:79], v[190:191], a[134:135], 0\n"
      "v_mov_b32 v3, s33\n"
      "global_load_ushort v250, v3, s[24:25]\n"
      "v_mfma_f32_16x16x16_bf16 v[16:19], v[188:189], a[208:209], 0\n"
      "ds_read_b128 a[184:187], v44 offset:12480\n"
      "ds_read_b128 a[188:191], v45 offset:12544\n"
      "v_mfma_f32_16x16x16_bf16 v[20:23], v[188:189], a[210:211], 0\n"
      "ds_read_b128 a[192:195], v46 offset:12608\n"
      "ds_read_b128 a[196:199], v47 offset:12672\n"
      "v_mfma_f32_16x16x16_bf16 v[24:27], v[188:189], a[212:213], 0\n"
      "ds_read_b64_tr_b16 a[152:153], v92 offset:0\n"
      "ds_read_b64_tr_b16 a[154:155], v92 offset:18560\n"
      "ds_read_b64_tr_b16 a[156:157], v92 offset:64\n"
      "v_mfma_f32_16x16x16_bf16 v[28:31], v[188:189], a[214:215], 0\n"
      "ds_read_b64_tr_b16 a[158:159], v92 offset:18624\n"
      "ds_read_b64_tr_b16 a[160:161], v92 offset:128\n"
      "ds_read_b64_tr_b16 a[162:163], v92 offset:18688\n"
      "v_mfma_f32_16x16x16_bf16 v[32:35], v[190:191], a[208:209], 0\n"
      "ds_read_b64_tr_b16 a[164:165], v92 offset:192\n"
      "ds_read_b64_tr_b16 a[166:167], v92 offset:18752\n"
      "ds_read_b64_tr_b16 a[168:169], v92 offset:2304\n"
      "v_mfma_f32_16x16x16_bf16 v[36:39], v[190:191], a[210:211], 0\n"
      "ds_read_b64_tr_b16 a[170:171], v92 offset:20864\n"
      "ds_read_b64_tr_b16 a[172:173], v92 offset:2368\n"
      "ds_read_b64_tr_b16 a[174:175], v92 offset:20928\n"
      "v_mfma_f32_16x16x16_bf16 v[40:43], v[190:191], a[212:213], 0\n"
      "ds_read_b64_tr_b16 a[176:177], v92 offset:2432\n"
      "ds_read_b64_tr_b16 a[178:179], v92 offset:20992\n"
      "ds_read_b64_tr_b16 a[180:181], v92 offset:2496\n"
      "v_mfma_f32_16x16x16_bf16 v[44:47], v[190:191], a[214:215], 0\n"
      "ds_read_b64_tr_b16 a[182:183], v92 offset:21056\n"
      "ds_read_b32 v224, v227 offset:37636\n"
      "s_waitcnt lgkmcnt(15)\n"
      "v_mfma_f32_16x16x32_f16 v[192:195], a[184:187], a[152:155], 0\n"
      "s_waitcnt vmcnt(4)\n"
      "v_cmp_eq_u64 vcc, v[80:81], v[160:161]\n"
      "s_waitcnt lgkmcnt(13)\n"
      "v_mfma_f32_16x16x32_f16 v[192:195], a[188:191], a[156:159], v[192:195]\n"
      "v_cmp_eq_u64 s[28:29], v[82:83], v[162:163]\n"
      "s_waitcnt lgkmcnt(11)\n"
      "v_mfma_f32_16x16x32_f16 v[192:195], a[192:195], a[160:163], v[192:195]\n"
      "s_and_b64 vcc, vcc, s[28:29]\n"
      "s_cmp_eq_u64 vcc, -1\n"
      "s_waitcnt lgkmcnt(9)\n"
      "v_mfma_f32_16x16x32_f16 v[192:195], a[196:199], a[164:167], v[192:195]\n"
      "s_cselect_b32 s52, 13, 1\n"
      "s_waitcnt lgkmcnt(0)\n"
      "v_cmp_eq_u32 vcc, 0, v224\n"
      "s_cbranch_vccz 13f\n"
      "11:\n"
      "ds_read_b32 v224, v227 offset:37636\n"
      "s_waitcnt lgkmcnt(0)\n"
      "v_cmp_eq_u32 vcc, 0, v224\n"
      "s_cbranch_vccnz 11b\n"
      "13:\n"
      "v_mfma_f32_16x16x32_f16 v[252:255], a[184:187], a[168:171], 0\n"
      "ds_read_b128 a[0:3], v14 offset:0\n"
      "ds_read_b128 a[4:7], v14 offset:1152\n"
      "v_mfma_f32_16x16x32_f16 v[252:255], a[188:191], a[172:175], v[252:255]\n"
      "ds_read_b128 a[8:11], v14 offset:2304\n"
      "ds_read_b128 a[12:15], v14 offset:3456\n"
      "v_mfma_f32_16x16x32_f16 v[252:255], a[192:195], a[176:179], v[252:255]\n"
      "ds_read_b128 a[16:19], v14 offset:4640\n"
      "ds_read_b128 a[20:23], v14 offset:5792\n"
      "v_mfma_f32_16x16x32_f16 v[252:255], a[196:199], a[180:183], v[252:255]\n"
      "ds_read_b128 a[24:27], v14 offset:6944\n"
      "ds_read_b128 a[28:31], v14 offset:8096\n"
      "s_cmp_lg_u32 s58, 0\n"
      "s_cbranch_scc1 72f\n"
      "73:\n"
      "global_load_dwordx4 v[240:243], v9, s[10:11] nt\n"
      "s_waitcnt vmcnt(4)\n"
      "v_mfma_f32_4x4x4_16b_bf16 v[176:179], a[216:217], v[228:229], 0\n"
      "v_mfma_f32_4x4x4_16b_bf16 v[180:183], a[216:217], v[230:231], 0\n"
      "ds_read_b128 a[32:35], v14 offset:9280\n"
      "s_waitcnt vmcnt(3)\n"
      "v_mfma_f32_4x4x4_16b_bf16 v[144:147], a[216:217], v[232:233], 0\n"
      "ds_read_b128 a[36:39], v14 offset:10432\n"
      "v_mfma_f32_4x4x4_16b_bf16 v[148:151], a[216:217], v[234:235], 0\n"
      "ds_read_b128 a[40:43], v14 offset:11584\n"
      "ds_read_b128 a[44:47], v14 offset:12736\n"
      "v_cvt_pk_f16_f32 v184, v176, v177\n"
      "v_cvt_pk_f16_f32 v185, v178, v179\n"
      "v_cvt_pk_f16_f32 v186, v180, v181\n"
      "v_cvt_pk_f16_f32 v187, v182, v183\n"
      "ds_write_b128 v1, v[184:187] offset:0\n"
      "v_cvt_pk_f16_f32 v152, v144, v145\n"
      "v_cvt_pk_f16_f32 v153, v146, v147\n"
      "v_cvt_pk_f16_f32 v154, v148, v149\n"
      "v_cvt_pk_f16_f32 v155, v150, v151\n"
      "ds_write_b128 v1, v[152:155] offset:1152\n"
      "ds_read_b128 v[128:131], v201 offset:0\n"
      "ds_read_b128 v[160:163], v202 offset:0\n"
      "ds_read_b128 v[132:135], v201 offset:64\n"
      "ds_read_b128 v[164:167], v202 offset:64\n"
      "ds_read_b128 v[136:139], v201 offset:128\n"
      "ds_read_b128 v[168:171], v202 offset:128\n"
      "ds_read_b128 v[140:143], v201 offset:192\n"
      "ds_read_b128 v[172:175], v202 offset:192\n"
      "s_waitcnt vmcnt(2)\n"
      "v_mfma_f32_4x4x4_16b_bf16 v[112:115], a[216:217], v[236:237], 0\n"
      "v_mfma_f32_4x4x4_16b_bf16 v[116:119], a[216:217], v[238:239], 0\n"
      "s_nop 3\n"
      "v_cvt_pk_f16_f32 v120, v112, v113\n"
      "v_cvt_pk_f16_f32 v121, v114, v115\n"
      "v_cvt_pk_f16_f32 v122, v116, v117\n"
      "v_cvt_pk_f16_f32 v123, v118, v119\n"
      "ds_write_b128 v1, v[120:123] offset:2304\n"
      "s_waitcnt lgkmcnt(7)\n"
      "v_pk_mul_f16 v96, v128, v160\n"
      "v_pk_mul_f16 v97, v129, v161\n"
      "v_pk_mul_f16 v98, v130, v162\n"
      "v_pk_mul_f16 v99, v131, v163\n"
      "s_waitcnt lgkmcnt(5)\n"
      "v_pk_mul_f16 v100, v132, v164\n"
      "v_pk_mul_f16 v101, v133, v165\n"
      "v_pk_mul_f16 v102, v134, v166\n"
      "v_pk_mul_f16 v103, v135, v167\n"
      "s_waitcnt lgkmcnt(3)\n"
      "v_pk_mul_f16 v104, v136, v168\n"
      "v_pk_mul_f16 v105, v137, v169\n"
      "v_pk_mul_f16 v106, v138, v170\n"
      "v_pk_mul_f16 v107, v139, v171\n"
      "3:\n"
      "v_mfma_f32_16x16x32_f16 v[80:83], a[0:3], v[96:99], v[16:19]\n"
      "s_waitcnt lgkmcnt(1)\n"
      "v_pk_mul_f16 v108, v140, v172\n"
      "v_pk_mul_f16 v109, v141, v173\n"
      "v_mfma_f32_16x16x32_f16 v[80:83], a[4:7], v[100:103], v[80:83]\n"
      "v_pk_mul_f16 v110, v142, v174\n"
      "v_pk_mul_f16 v111, v143, v175\n"
      "v_mfma_f32_16x16x32_f16 v[80:83], a[8:11], v[104:107], v[80:83]\n"
      "ds_read_b128 a[60:63], v14 offset:17376\n"
      "ds_read_b128 a[56:59], v14 offset:16224\n"
      "v_mfma_f32_16x16x32_f16 v[80:83], a[12:15], v[108:111], v[80:83]\n"
      "ds_read_b128 a[52:55], v14 offset:15072\n"
      "ds_read_b128 a[48:51], v14 offset:13920\n"
      "v_mfma_f32_16x16x32_f16 v[84:87], a[28:31], v[108:111], v[20:23]\n"
      "ds_read_b128 a[64:67], v212 offset:0\n"
      "ds_read_b128 v[144:147], v203 offset:2304\n"
      "v_mfma_f32_16x16x32_f16 v[84:87], a[24:27], v[104:107], v[84:87]\n"
      "ds_read_b128 a[68:71], v212 offset:1152\n"
      "ds_read_b128 a[72:75], v212 offset:2304\n"
      "v_mfma_f32_16x16x32_f16 v[84:87], a[20:23], v[100:103], v[84:87]\n"
      "ds_read_b128 a[76:79], v212 offset:3456\n"
      "ds_read_b128 a[92:95], v212 offset:8096\n"
      "v_mfma_f32_16x16x32_f16 v[84:87], a[16:19], v[96:99], v[84:87]\n"
      "ds_read_b128 a[88:91], v212 offset:6944\n"
      "ds_read_b128 v[148:151], v203 offset:2368\n"
      "v_mfma_f32_16x16x32_f16 v[88:91], a[32:35], v[96:99], v[24:27]\n"
      "ds_read_b128 a[84:87], v212 offset:5792\n"
      "ds_read_b128 a[80:83], v212 offset:4640\n"
      "v_mfma_f32_16x16x32_f16 v[88:91], a[36:39], v[100:103], v[88:91]\n"
      "v_fma_f32 v188, |v80|, v48, 4.0\n"
      "v_fma_f32 v188, |v81|, v49, v188\n"
      "v_mfma_f32_16x16x32_f16 v[88:91], a[40:43], v[104:107], v[88:91]\n"
      "ds_read_b128 a[96:99], v212 offset:9280\n"
      "v_fma_f32 v188, |v82|, v50, v188\n"
      "v_mfma_f32_16x16x32_f16 v[88:91], a[44:47], v[108:111], v[88:91]\n"
      "s_waitcnt lgkmcnt(13)\n"
      "v_fma_f32 v188, |v83|, v51, v188\n"
      "v_fma_f32 v188, |v84|, v52, v188\n"
      "v_mfma_f32_16x16x32_f16 v[92:95], a[60:63], v[108:111], v[28:31]\n"
      "s_waitcnt lgkmcnt(9)\n"
      "v_pk_mul_f16 v112, v128, v144\n"
      "v_pk_mul_f16 v113, v129, v145\n"
      "v_mfma_f32_16x16x32_f16 v[92:95], a[56:59], v[104:107], v[92:95]\n"
      "ds_read_b128 a[100:103], v212 offset:10432\n"
      "ds_read_b128 a[104:107], v212 offset:11584\n"
      "v_mfma_f32_16x16x32_f16 v[92:95], a[52:55], v[100:103], v[92:95]\n"
      "v_pk_mul_f16 v114, v130, v146\n"
      "v_pk_mul_f16 v115, v131, v147\n"
      "v_mfma_f32_16x16x32_f16 v[92:95], a[48:51], v[96:99], v[92:95]\n"
      "s_waitcnt lgkmcnt(10)\n"
      "ds_read_b128 v[152:155], v203 offset:2432\n"
      "ds_read_b128 a[108:111], v212 offset:12736\n"
      "v_mfma_f32_16x16x32_f16 v[80:83], a[64:67], v[96:99], v[32:35]\n"
      "ds_read_b128 a[124:127], v212 offset:17376\n"
      "v_fma_f32 v188, |v85|, v53, v188\n"
      "v_mfma_f32_16x16x32_f16 v[80:83], a[68:71], v[100:103], v[80:83]\n"
      "s_waitcnt lgkmcnt(11)\n"
      "v_fma_f32 v188, |v86|, v54, v188\n"
      "v_fma_f32 v188, |v87|, v55, v188\n"
      "v_mfma_f32_16x16x32_f16 v[80:83], a[72:75], v[104:107], v[80:83]\n"
      "s_waitcnt lgkmcnt(8)\n"
      "ds_read_b128 a[120:123], v212 offset:16224\n"
      "v_pk_mul_f16 v116, v132, v148\n"
      "v_mfma_f32_16x16x32_f16 v[80:83], a[76:79], v[108:111], v[80:83]\n"
      "ds_read_b128 a[116:119], v212 offset:15072\n"
      "v_pk_mul_f16 v117, v133, v149\n"
      "v_mfma_f32_16x16x32_f16 v[84:87], a[92:95], v[108:111], v[36:39]\n"
      "ds_read_b128 a[112:115], v212 offset:13920\n"
      "v_fma_f32 v188, |v88|, v56, v188\n"
      "v_mfma_f32_16x16x32_f16 v[84:87], a[88:91], v[104:107], v[84:87]\n"
      "s_waitcnt lgkmcnt(9)\n"
      "v_fma_f32 v188, |v89|, v57, v188\n"
      "v_pk_mul_f16 v118, v134, v150\n"
      "v_mfma_f32_16x16x32_f16 v[84:87], a[84:87], v[100:103], v[84:87]\n"
      "ds_read_b128 v[156:159], v203 offset:2496\n"
      "v_fma_f32 v188, |v90|, v58, v188\n"
      "v_mfma_f32_16x16x32_f16 v[84:87], a[80:83], v[96:99], v[84:87]\n"
      "s_waitcnt lgkmcnt(8)\n"
      "v_fma_f32 v188, |v91|, v59, v188\n"
      "v_pk_mul_f16 v119, v135, v151\n"
      "v_mfma_f32_16x16x32_f16 v[88:91], a[96:99], v[96:99], v[40:43]\n"
      "s_waitcnt lgkmcnt(6)\n"
      "v_pk_mul_f16 v120, v136, v152\n"
      "v_fma_f32 v188, |v92|, v60, v188\n"
      "v_mfma_f32_16x16x32_f16 v[88:91], a[100:103], v[100:103], v[88:91]\n"
      "s_waitcnt lgkmcnt(5)\n"
      "v_fma_f32 v188, |v93|, v61, v188\n"
      "v_pk_mul_f16 v121, v137, v153\n"
      "v_mfma_f32_16x16x32_f16 v[88:91], a[104:107], v[104:107], v[88:91]\n"
      "v_fma_f32 v188, |v94|, v62, v188\n"
      "v_fma_f32 v188, |v95|, v63, v188\n"
      "v_mfma_f32_16x16x32_f16 v[88:91], a[108:111], v[108:111], v[88:91]\n"
      "s_waitcnt lgkmcnt(3)\n"
      "v_pk_mul_f16 v122, v138, v154\n"
      "v_pk_mul_f16 v123, v139, v155\n"
      "v_mfma_f32_16x16x32_f16 v[92:95], a[124:127], v[108:111], v[44:47]\n"
      "global_load_dwordx4 v[244:247], v10, s[10:11] nt\n"
      "v_fma_f32 v188, |v80|, v64, v188\n"
      "v_mfma_f32_16x16x32_f16 v[92:95], a[120:123], v[104:107], v[92:95]\n"
      "s_waitcnt lgkmcnt(1)\n"
      "v_fma_f32 v188, |v81|, v65, v188\n"
      "v_fma_f32 v188, |v82|, v66, v188\n"
      "v_mfma_f32_16x16x32_f16 v[92:95], a[116:119], v[100:103], v[92:95]\n"
      "s_waitcnt lgkmcnt(0)\n"
      "v_pk_mul_f16 v124, v140, v156\n"
      "v_fma_f32 v188, |v83|, v67, v188\n"
      "v_mfma_f32_16x16x32_f16 v[92:95], a[112:115], v[96:99], v[92:95]\n"
      "v_pk_mul_f16 v125, v141, v157\n"
      "v_pk_mul_f16 v126, v142, v158\n"
      "v_mfma_f32_16x16x32_f16 v[80:83], a[0:3], v[112:115], v[16:19]\n"
      "ds_read_b128 v[160:163], v203 offset:2880\n"
      "v_pk_mul_f16 v127, v143, v159\n"
      "v_mfma_f32_16x16x32_f16 v[80:83], a[4:7], v[116:119], v[80:83]\n"
      "v_fma_f32 v188, |v84|, v68, v188\n"
      "v_fma_f32 v188, |v85|, v69, v188\n"
      "v_mfma_f32_16x16x32_f16 v[80:83], a[8:11], v[120:123], v[80:83]\n"
      "v_fma_f32 v188, |v86|, v70, v188\n"
      "v_fma_f32 v188, |v87|, v71, v188\n"
      "v_mfma_f32_16x16x32_f16 v[80:83], a[12:15], v[124:127], v[80:83]\n"
      "v_fma_f32 v188, |v88|, v72, v188\n"
      "v_fma_f32 v188, |v89|, v73, v188\n"
      "v_mfma_f32_16x16x32_f16 v[84:87], a[28:31], v[124:127], v[20:23]\n"
      "v_fma_f32 v188, |v90|, v74, v188\n"
      "v_fma_f32 v188, |v91|, v75, v188\n"
      "v_mfma_f32_16x16x32_f16 v[84:87], a[24:27], v[120:123], v[84:87]\n"
      "ds_read_b128 v[164:167], v203 offset:2944\n"
      "v_fma_f32 v188, |v92|, v76, v188\n"
      "v_mfma_f32_16x16x32_f16 v[84:87], a[20:23], v[116:119], v[84:87]\n"
      "v_fma_f32 v188, |v93|, v77, v188\n"
      "v_fma_f32 v188, |v94|, v78, v188\n"
      "v_mfma_f32_16x16x32_f16 v[84:87], a[16:19], v[112:115], v[84:87]\n"
      "v_fma_f32 v188, |v95|, v79, v188\n"
      "v_fma_f32 v189, |v80|, v48, 4.0\n"
      "v_mfma_f32_16x16x32_f16 v[88:91], a[32:35], v[112:115], v[24:27]\n"
      "s_waitcnt lgkmcnt(1)\n"
      "v_pk_mul_f16 v96, v128, v160\n"
      "v_pk_mul_f16 v97, v129, v161\n"
      "v_mfma_f32_16x16x32_f16 v[88:91], a[36:39], v[116:119], v[88:91]\n"
      "40:\n"
      "s_cbranch_execz 44b\n"
      "v_pk_mul_f16 v98, v130, v162\n"
      "v_mfma_f32_16x16x32_f16 v[88:91], a[40:43], v[120:123], v[88:91]\n"
      "ds_read_b128 v[168:171], v203 offset:3008\n"
      "v_pk_mul_f16 v99, v131, v163\n"
      "v_mfma_f32_16x16x32_f16 v[88:91], a[44:47], v[124:127], v[88:91]\n"
      "s_waitcnt vmcnt(1)\n"
      "v_fma_f32 v189, |v81|, v49, v189\n"
      "v_fma_f32 v189, |v82|, v50, v189\n"
      "v_mfma_f32_4x4x4_16b_bf16 v[176:179], a[216:217], v[240:241], 0\n"
      "v_fma_f32 v189, |v83|, v51, v189\n"
      "v_mfma_f32_4x4x4_16b_bf16 v[180:183], a[216:217], v[242:243], 0\n"
      "s_waitcnt lgkmcnt(1)\n"
      "v_pk_mul_f16 v100, v132, v164\n"
      "v_pk_mul_f16 v101, v133, v165\n"
      "v_mfma_f32_16x16x32_f16 v[92:95], a[60:63], v[124:127], v[28:31]\n"
      "v_pk_mul_f16 v102, v134, v166\n"
      "v_pk_mul_f16 v103, v135, v167\n"
      "v_mfma_f32_16x16x32_f16 v[92:95], a[56:59], v[120:123], v[92:95]\n"
      "v_fma_f32 v189, |v84|, v52, v189\n"
      "v_fma_f32 v189, |v85|, v53, v189\n"
      "v_mfma_f32_16x16x32_f16 v[92:95], a[52:55], v[116:119], v[92:95]\n"
      "v_fma_f32 v189, |v86|, v54, v189\n"
      "v_fma_f32 v189, |v87|, v55, v189\n"
      "v_mfma_f32_16x16x32_f16 v[92:95], a[48:51], v[112:115], v[92:95]\n"
      "v_fma_f32 v189, |v88|, v56, v189\n"
      "v_fma_f32 v189, |v89|, v57, v189\n"
      "v_mfma_f32_16x16x32_f16 v[80:83], a[64:67], v[112:115], v[32:35]\n"
      "s_waitcnt lgkmcnt(0)\n"
      "v_pk_mul_f16 v104, v136, v168\n"
      "v_pk_mul_f16 v105, v137, v169\n"
      "v_mfma_f32_16x16x32_f16 v[80:83], a[68:71], v[116:119], v[80:83]\n"
      "v_pk_mul_f16 v106, v138, v170\n"
      "v_pk_mul_f16 v107, v139, v171\n"
      "v_mfma_f32_16x16x32_f16 v[80:83], a[72:75], v[120:123], v[80:83]\n"
      "ds_read_b128 v[172:175], v203 offset:3072\n"
      "v_fma_f32 v189, |v90|, v58, v189\n"
      "v_mfma_f32_16x16x32_f16 v[80:83], a[76:79], v[124:127], v[80:83]\n"
      "v_fma_f32 v189, |v91|, v59, v189\n"
      "v_fma_f32 v189, |v92|, v60, v189\n"
      "v_mfma_f32_16x16x32_f16 v[84:87], a[92:95], v[124:127], v[36:39]\n"
      "v_cvt_pk_f16_f32 v184, v176, v177\n"
      "v_cvt_pk_f16_f32 v185, v178, v179\n"
      "v_mfma_f32_16x16x32_f16 v[84:87], a[88:91], v[120:123], v[84:87]\n"
      "v_fma_f32 v189, |v93|, v61, v189\n"
      "v_fma_f32 v189, |v94|, v62, v189\n"
      "v_mfma_f32_16x16x32_f16 v[84:87], a[84:87], v[116:119], v[84:87]\n"
      "v_fma_f32 v189, |v95|, v63, v189\n"
      "v_fma_f32 v189, |v80|, v64, v189\n"
      "v_mfma_f32_16x16x32_f16 v[84:87], a[80:83], v[112:115], v[84:87]\n"
      "v_cvt_pk_f16_f32 v186, v180, v181\n"
      "v_cvt_pk_f16_f32 v187, v182, v183\n"
      "v_mfma_f32_16x16x32_f16 v[88:91], a[96:99], v[112:115], v[40:43]\n"
      "ds_write_b128 v1, v[184:187] offset:3456\n"
      "v_mfma_f32_16x16x32_f16 v[88:91], a[100:103], v[116:119], v[88:91]\n"
      "v_fma_f32 v189, |v81|, v65, v189\n"
      "v_fma_f32 v189, |v82|, v66, v189\n"
      "v_mfma_f32_16x16x32_f16 v[88:91], a[104:107], v[120:123], v[88:91]\n"
      "s_waitcnt lgkmcnt(1)\n"
      "v_pk_mul_f16 v108, v140, v172\n"
      "v_pk_mul_f16 v109, v141, v173\n"
      "v_mfma_f32_16x16x32_f16 v[88:91], a[108:111], v[124:127], v[88:91]\n"
      "v_fma_f32 v189, |v83|, v67, v189\n"
      "v_fma_f32 v189, |v84|, v68, v189\n"
      "v_mfma_f32_16x16x32_f16 v[92:95], a[124:127], v[124:127], v[44:47]\n"
      "v_pk_mul_f16 v110, v142, v174\n"
      "v_pk_mul_f16 v111, v143, v175\n"
      "v_mfma_f32_16x16x32_f16 v[92:95], a[120:123], v[120:123], v[92:95]\n"
      "ds_read_b128 v[160:163], v203 offset:3456\n"
      "v_fma_f32 v189, |v85|, v69, v189\n"
      "v_mfma_f32_16x16x32_f16 v[92:95], a[116:119], v[116:119], v[92:95]\n"
      "v_fma_f32 v189, |v86|, v70, v189\n"
      "v_fma_f32 v189, |v87|, v71, v189\n"
      "v_mfma_f32_16x16x32_f16 v[92:95], a[112:115], v[112:115], v[92:95]\n"
      "v_fma_f32 v189, |v88|, v72, v189\n"
      "v_fma_f32 v189, |v89|, v73, v189\n"
      "v_mfma_f32_16x16x32_f16 v[80:83], a[0:3], v[96:99], v[16:19]\n"
      "v_fma_f32 v189, |v90|, v74, v189\n"
      "v_fma_f32 v189, |v91|, v75, v189\n"
      "v_mfma_f32_16x16x32_f16 v[80:83], a[4:7], v[100:103], v[80:83]\n"
      "ds_read_b128 v[164:167], v203 offset:3520\n"
      "ds_read_b128 v[168:171], v203 offset:3584\n"
      "v_mfma_f32_16x16x32_f16 v[80:83], a[8:11], v[104:107], v[80:83]\n"
      "v_fma_f32 v189, |v92|, v76, v189\n"
      "v_fma_f32 v189, |v93|, v77, v189\n"
      "v_mfma_f32_16x16x32_f16 v[80:83], a[12:15], v[108:111], v[80:83]\n"
      "v_fma_f32 v189, |v94|, v78, v189\n"
      "v_fma_f32 v189, |v95|, v79, v189\n"
      "v_mfma_f32_16x16x32_f16 v[84:87], a[28:31], v[108:111], v[20:23]\n"
      "ds_write_b32 v7, v252 offset:37376\n"
      "v_mfma_f32_16x16x32_f16 v[84:87], a[24:27], v[104:107], v[84:87]\n"
      "s_waitcnt lgkmcnt(3)\n"
      "v_pk_mul_f16 v112, v128, v160\n"
      "v_pk_mul_f16 v113, v129, v161\n"
      "v_mfma_f32_16x16x32_f16 v[84:87], a[20:23], v[100:103], v[84:87]\n"
      "ds_write_b32 v7, v192 offset:37120\n"
      "v_mfma_f32_16x16x32_f16 v[84:87], a[16:19], v[96:99], v[84:87]\n"
      "v_pk_mul_f16 v114, v130, v162\n"
      "v_pk_mul_f16 v115, v131, v163\n"
      "v_mfma_f32_16x16x32_f16 v[88:91], a[32:35], v[96:99], v[24:27]\n"
      "v_fma_f32 v190, |v80|, v48, 4.0\n"
      "v_fma_f32 v190, |v81|, v49, v190\n"
      "v_mfma_f32_16x16x32_f16 v[88:91], a[36:39], v[100:103], v[88:91]\n"
      "v_fma_f32 v190, |v82|, v50, v190\n"
      "v_fma_f32 v190, |v83|, v51, v190\n"
      "v_mfma_f32_16x16x32_f16 v[88:91], a[40:43], v[104:107], v[88:91]\n"
      "s_waitcnt lgkmcnt(3)\n"
      "v_pk_mul_f16 v116, v132, v164\n"
      "v_pk_mul_f16 v117, v133, v165\n"
      "v_mfma_f32_16x16x32_f16 v[88:91], a[44:47], v[108:111], v[88:91]\n"
      "v_pk_mul_f16 v118, v134, v166\n"
      "v_pk_mul_f16 v119, v135, v167\n"
      "v_mfma_f32_16x16x32_f16 v[92:95], a[60:63], v[108:111], v[28:31]\n"
      "v_fma_f32 v190, |v84|, v52, v190\n"
      "v_fma_f32 v190, |v85|, v53, v190\n"
      "v_mfma_f32_16x16x32_f16 v[92:95], a[56:59], v[104:107], v[92:95]\n"
      "v_fma_f32 v190, |v86|, v54, v190\n"
      "v_fma_f32 v190, |v87|, v55, v190\n"
      "v_mfma_f32_16x16x32_f16 v[92:95], a[52:55], v[100:103], v[92:95]\n"
      "s_waitcnt lgkmcnt(2)\n"
      "v_pk_mul_f16 v120, v136, v168\n"
      "v_pk_mul_f16 v121, v137, v169\n"
      "v_mfma_f32_16x16x32_f16 v[92:95], a[48:51], v[96:99], v[92:95]\n"
      "v_fma_f32 v190, |v88|, v56, v190\n"
      "v_fma_f32 v190, |v89|, v57, v190\n"
      "v_mfma_f32_16x16x32_f16 v[80:83], a[64:67], v[96:99], v[32:35]\n"
      "ds_read_b128 v[172:175], v203 offset:3648\n"
      "v_fma_f32 v190, |v90|, v58, v190\n"
      "v_mfma_f32_16x16x32_f16 v[80:83], a[68:71], v[100:103], v[80:83]\n"
      "v_fma_f32 v190, |v91|, v59, v190\n"
      "v_pk_mul_f16 v122, v138, v170\n"
      "v_mfma_f32_16x16x32_f16 v[80:83], a[72:75], v[104:107], v[80:83]\n"
      "global_load_dwordx4 v[248:251], v11, s[10:11] nt\n"
      "v_pk_mul_f16 v123, v139, v171\n"
      "v_mfma_f32_16x16x32_f16 v[80:83], a[76:79], v[108:111], v[80:83]\n"
      "v_fma_f32 v190, |v92|, v60, v190\n"
      "v_fma_f32 v190, |v93|, v61, v190\n"
      "v_mfma_f32_16x16x32_f16 v[84:87], a[92:95], v[108:111], v[36:39]\n"
      "v_fma_f32 v190, |v94|, v62, v190\n"
      "v_fma_f32 v190, |v95|, v63, v190\n"
      "v_mfma_f32_16x16x32_f16 v[84:87], a[88:91], v[104:107], v[84:87]\n"
      "v_max3_f32 v195, |v193|, |v194|, |v195|\n"
      "v_max3_f32 v194, |v253|, |v254|, |v255|\n"
      "v_mfma_f32_16x16x32_f16 v[84:87], a[84:87], v[100:103], v[84:87]\n"
      "v_fma_f32 v190, |v80|, v64, v190\n"
      "v_fma_f32 v190, |v81|, v65, v190\n"
      "v_mfma_f32_16x16x32_f16 v[84:87], a[80:83], v[96:99], v[84:87]\n"
      "v_fma_f32 v190, |v82|, v66, v190\n"
      "v_fma_f32 v190, |v83|, v67, v190\n"
      "v_mfma_f32_16x16x32_f16 v[88:91], a[96:99], v[96:99], v[40:43]\n"
      "s_waitcnt lgkmcnt(0)\n"
      "v_pk_mul_f16 v124, v140, v172\n"
      "v_pk_mul_f16 v125, v141, v173\n"
      "v_mfma_f32_16x16x32_f16 v[88:91], a[100:103], v[100:103], v[88:91]\n"
      "v_max_f32 v194, |v252|, v194\n"
      "v_max_f32 v195, |v192|, v195\n"
      "v_mfma_f32_16x16x32_f16 v[88:91], a[104:107], v[104:107], v[88:91]\n"
      "v_pk_mul_f16 v126, v142, v174\n"
      "v_pk_mul_f16 v127, v143, v175\n"
      "v_mfma_f32_16x16x32_f16 v[88:91], a[108:111], v[108:111], v[88:91]\n"
      "ds_read_b128 v[160:163], v203 offset:4032\n"
      "v_fma_f32 v190, |v84|, v68, v190\n"
      "v_mfma_f32_16x16x32_f16 v[92:95], a[124:127], v[108:111], v[44:47]\n"
      "v_fma_f32 v190, |v85|, v69, v190\n"
      "v_fma_f32 v190, |v86|, v70, v190\n"
      "v_mfma_f32_16x16x32_f16 v[92:95], a[120:123], v[104:107], v[92:95]\n"
      "global_load_dwordx4 v[252:255], v12, s[10:11] nt\n"
      "v_cmp_le_f32 vcc, 0x3ca3d70a, v194\n"
      "v_mfma_f32_16x16x32_f16 v[92:95], a[116:119], v[100:103], v[92:95]\n"
      "v_fma_f32 v190, |v87|, v71, v190\n"
      "v_fma_f32 v190, |v88|, v72, v190\n"
      "v_mfma_f32_16x16x32_f16 v[92:95], a[112:115], v[96:99], v[92:95]\n"
      "s_or_b32 s44, vcc_lo, vcc_hi\n"
      "v_fma_f32 v190, |v89|, v73, v190\n"
      "v_fma_f32 v190, |v90|, v74, v190\n"
      "v_mfma_f32_16x16x32_f16 v[80:83], a[0:3], v[112:115], v[16:19]\n"
      "v_cmp_le_f32 vcc, 0x3ca3d70a, v195\n"
      "v_max_f32 v195, v195, v194\n"
      "v_mfma_f32_16x16x32_f16 v[80:83], a[4:7], v[116:119], v[80:83]\n"
      "ds_read_b128 v[164:167], v203 offset:4096\n"
      "v_fma_f32 v190, |v91|, v75, v190\n"
      "v_mfma_f32_16x16x32_f16 v[80:83], a[8:11], v[120:123], v[80:83]\n"
      "s_or_b32 s45, vcc_lo, vcc_hi\n"
      "v_fma_f32 v190, |v92|, v76, v190\n"
      "v_fma_f32 v190, |v93|, v77, v190\n"
      "v_mfma_f32_16x16x32_f16 v[80:83], a[12:15], v[124:127], v[80:83]\n"
      "s_waitcnt lgkmcnt(1)\n"
      "v_cmp_le_f32 vcc, 0x3e800000, v195\n"
      "v_pk_mul_f16 v96, v128, v160\n"
      "v_mfma_f32_16x16x32_f16 v[84:87], a[28:31], v[124:127], v[20:23]\n"
      "s_pack_ll_b32_b16 s46, s44, s45\n"
      "v_pk_mul_f16 v97, v129, v161\n"
      "v_pk_mul_f16 v98, v130, v162\n"
      "v_mfma_f32_16x16x32_f16 v[84:87], a[24:27], v[120:123], v[84:87]\n"
      "s_pack_hh_b32_b16 s44, s44, s45\n"
      "v_fma_f32 v190, |v94|, v78, v190\n"
      "v_fma_f32 v190, |v95|, v79, v190\n"
      "v_mfma_f32_16x16x32_f16 v[84:87], a[20:23], v[116:119], v[84:87]\n"
      "s_or_b32 s44, s44, s46\n"
      "ds_read_b128 v[168:171], v203 offset:4160\n"
      "v_pk_mul_f16 v99, v131, v163\n"
      "v_mfma_f32_16x16x32_f16 v[84:87], a[16:19], v[112:115], v[84:87]\n"
      "s_cmp_eq_u64 vcc, 0\n"
      "v_fma_f32 v191, |v80|, v48, 4.0\n"
      "v_fma_f32 v191, |v81|, v49, v191\n"
      "v_mfma_f32_16x16x32_f16 v[88:91], a[32:35], v[112:115], v[24:27]\n"
      "s_cselect_b32 s45, 13, 9\n"
      "v_fma_f32 v191, |v82|, v50, v191\n"
      "v_fma_f32 v191, |v83|, v51, v191\n"
      "v_mfma_f32_16x16x32_f16 v[88:91], a[36:39], v[116:119], v[88:91]\n"
      "s_waitcnt lgkmcnt(1)\n"
      "v_pk_mul_f16 v100, v132, v164\n"
      "v_pk_mul_f16 v101, v133, v165\n"
      "v_mfma_f32_16x16x32_f16 v[88:91], a[40:43], v[120:123], v[88:91]\n"
      "s_cmp_eq_u32 s44, -1\n"
      "v_pk_mul_f16 v102, v134, v166\n"
      "v_pk_mul_f16 v103, v135, v167\n"
      "v_mfma_f32_16x16x32_f16 v[88:91], a[44:47], v[124:127], v[88:91]\n"
      "s_cselect_b32 s44, s45, 5\n"
      "v_fma_f32 v191, |v84|, v52, v191\n"
      "v_fma_f32 v191, |v85|, v53, v191\n"
      "v_mfma_f32_16x16x32_f16 v[92:95], a[60:63], v[124:127], v[28:31]\n"
      "s_min_u32 s44, s44, s52\n"
      "v_fma_f32 v191, |v86|, v54, v191\n"
      "v_fma_f32 v191, |v87|, v55, v191\n"
      "v_mfma_f32_16x16x32_f16 v[92:95], a[56:59], v[120:123], v[92:95]\n"
      "s_movk_i32 s31, 0x1c00\n"
      "ds_read_b128 v[172:175], v203 offset:4224\n"
      "v_mov_b32 v223, s44\n"
      "v_mfma_f32_16x16x32_f16 v[92:95], a[52:55], v[116:119], v[92:95]\n"
      "s_mov_b32 s35, 1\n"
      "ds_write_b32 v2, v223\n"
      "v_mfma_f32_16x16x32_f16 v[92:95], a[48:51], v[112:115], v[92:95]\n"
      "s_waitcnt lgkmcnt(2)\n"
      "v_pk_mul_f16 v104, v136, v168\n"
      "v_pk_mul_f16 v105, v137, v169\n"
      "v_mfma_f32_16x16x32_f16 v[80:83], a[64:67], v[112:115], v[32:35]\n"
      "v_fma_f32 v191, |v88|, v56, v191\n"
      "v_fma_f32 v191, |v89|, v57, v191\n"
      "v_mfma_f32_16x16x32_f16 v[80:83], a[68:71], v[116:119], v[80:83]\n"
      "v_fma_f32 v191, |v90|, v58, v191\n"
      "v_fma_f32 v191, |v91|, v59, v191\n"
      "v_mfma_f32_16x16x32_f16 v[80:83], a[72:75], v[120:123], v[80:83]\n"
      "v_pk_mul_f16 v106, v138, v170\n"
      "v_pk_mul_f16 v107, v139, v171\n"
      "v_mfma_f32_16x16x32_f16 v[80:83], a[76:79], v[124:127], v[80:83]\n"
      "v_fma_f32 v191, |v92|, v60, v191\n"
      "v_fma_f32 v191, |v93|, v61, v191\n"
      "v_mfma_f32_16x16x32_f16 v[84:87], a[92:95], v[124:127], v[36:39]\n"
      "v_fma_f32 v191, |v94|, v62, v191\n"
      "v_fma_f32 v191, |v95|, v63, v191\n"
      "v_mfma_f32_16x16x32_f16 v[84:87], a[88:91], v[120:123], v[84:87]\n"
      "s_waitcnt lgkmcnt(1)\n"
      "v_pk_mul_f16 v108, v140, v172\n"
      "v_pk_mul_f16 v109, v141, v173\n"
      "v_mfma_f32_16x16x32_f16 v[84:87], a[84:87], v[116:119], v[84:87]\n"
      "v_fma_f32 v191, |v80|, v64, v191\n"
      "v_fma_f32 v191, |v81|, v65, v191\n"
      "v_mfma_f32_16x16x32_f16 v[84:87], a[80:83], v[112:115], v[84:87]\n"
      "v_fma_f32 v191, |v82|, v66, v191\n"
      "v_fma_f32 v191, |v83|, v67, v191\n"
      "v_mfma_f32_16x16x32_f16 v[88:91], a[96:99], v[112:115], v[40:43]\n"
      "ds_read_b128 v[144:147], v201 offset:2304\n"
      "v_pk_mul_f16 v110, v142, v174\n"
      "v_mfma_f32_16x16x32_f16 v[88:91], a[100:103], v[116:119], v[88:91]\n"
      "ds_read_b128 v[160:163], v202 offset:2304\n"
      "v_pk_mul_f16 v111, v143, v175\n"
      "v_mfma_f32_16x16x32_f16 v[88:91], a[104:107], v[120:123], v[88:91]\n"
      "v_fma_f32 v191, |v84|, v68, v191\n"
      "v_fma_f32 v191, |v85|, v69, v191\n"
      "v_mfma_f32_16x16x32_f16 v[88:91], a[108:111], v[124:127], v[88:91]\n"
      "v_fma_f32 v191, |v86|, v70, v191\n"
      "v_fma_f32 v191, |v87|, v71, v191\n"
      "v_mfma_f32_16x16x32_f16 v[92:95], a[124:127], v[124:127], v[44:47]\n"
      "ds_read_b128 v[148:151], v201 offset:2368\n"
      "ds_read_b128 v[164:167], v202 offset:2368\n"
      "v_mfma_f32_16x16x32_f16 v[92:95], a[120:123], v[120:123], v[92:95]\n"
      "s_waitcnt vmcnt(2)\n"
      "ds_read_b128 v[152:155], v201 offset:2432\n"
      "v_lshlrev_b32 v176, 16, v244\n"
      "v_mfma_f32_16x16x32_f16 v[92:95], a[116:119], v[116:119], v[92:95]\n"
      "v_fma_f32 v191, |v88|, v72, v191\n"
      "v_fma_f32 v191, |v89|, v73, v191\n"
      "v_mfma_f32_16x16x32_f16 v[92:95], a[112:115], v[112:115], v[92:95]\n"
      "v_fma_f32 v191, |v90|, v74, v191\n"
      "v_fma_f32 v191, |v91|, v75, v191\n"
      "v_mfma_f32_16x16x32_f16 v[80:83], a[0:3], v[96:99], v[16:19]\n"
      "ds_read_b128 v[168:171], v202 offset:2432\n"
      "v_and_b32 v177, s43, v244\n"
      "v_mfma_f32_16x16x32_f16 v[80:83], a[4:7], v[100:103], v[80:83]\n"
      "s_waitcnt lgkmcnt(4)\n"
      "v_pk_mul_f16 v112, v144, v160\n"
      "v_pk_mul_f16 v113, v145, v161\n"
      "v_mfma_f32_16x16x32_f16 v[80:83], a[8:11], v[104:107], v[80:83]\n"
      "v_fma_f32 v191, |v92|, v76, v191\n"
      "v_fma_f32 v191, |v93|, v77, v191\n"
      "v_mfma_f32_16x16x32_f16 v[80:83], a[12:15], v[108:111], v[80:83]\n"
      "v_fma_f32 v191, |v94|, v78, v191\n"
      "v_fma_f32 v191, |v95|, v79, v191\n"
      "v_mfma_f32_16x16x32_f16 v[84:87], a[28:31], v[108:111], v[20:23]\n"
      "v_pk_mul_f16 v114, v146, v162\n"
      "v_pk_mul_f16 v115, v147, v163\n"
      "v_mfma_f32_16x16x32_f16 v[84:87], a[24:27], v[104:107], v[84:87]\n"
      "v_permlane32_swap_b32 v189, v191\n"
      "v_mfma_f32_16x16x32_f16 v[84:87], a[20:23], v[100:103], v[84:87]\n"
      "v_fma_f32 v192, |v80|, v48, 4.0\n"
      "v_fma_f32 v192, |v81|, v49, v192\n"
      "v_mfma_f32_16x16x32_f16 v[84:87], a[16:19], v[96:99], v[84:87]\n"
      "v_fma_f32 v192, |v82|, v50, v192\n"
      "v_fma_f32 v192, |v83|, v51, v192\n"
      "v_mfma_f32_16x16x32_f16 v[88:91], a[32:35], v[96:99], v[24:27]\n"
      "s_waitcnt lgkmcnt(2)\n"
      "v_lshlrev_b32 v178, 16, v245\n"
      "v_pk_mul_f16 v116, v148, v164\n"
      "v_mfma_f32_16x16x32_f16 v[88:91], a[36:39], v[100:103], v[88:91]\n"
      "v_and_b32 v179, s43, v245\n"
      "v_pk_mul_f16 v117, v149, v165\n"
      "v_mfma_f32_16x16x32_f16 v[88:91], a[40:43], v[104:107], v[88:91]\n"
      "ds_read_b128 v[156:159], v201 offset:2496\n"
      "v_lshlrev_b32 v180, 16, v246\n"
      "v_mfma_f32_16x16x32_f16 v[88:91], a[44:47], v[108:111], v[88:91]\n"
      "v_pk_mul_f16 v118, v150, v166\n"
      "v_pk_mul_f16 v119, v151, v167\n"
      "v_mfma_f32_16x16x32_f16 v[92:95], a[60:63], v[108:111], v[28:31]\n"
      "v_fma_f32 v192, |v84|, v52, v192\n"
      "v_fma_f32 v192, |v85|, v53, v192\n"
      "v_mfma_f32_16x16x32_f16 v[92:95], a[56:59], v[104:107], v[92:95]\n"
      "v_and_b32 v181, s43, v246\n"
      "v_fma_f32 v192, |v86|, v54, v192\n"
      "v_mfma_f32_16x16x32_f16 v[92:95], a[52:55], v[100:103], v[92:95]\n"
      "v_fma_f32 v192, |v87|, v55, v192\n"
      "v_fma_f32 v192, |v88|, v56, v192\n"
      "v_mfma_f32_16x16x32_f16 v[92:95], a[48:51], v[96:99], v[92:95]\n"
      "v_lshlrev_b32 v182, 16, v247\n"
      "v_and_b32 v183, s43, v247\n"
      "v_mfma_f32_16x16x32_f16 v[80:83], a[64:67], v[96:99], v[32:35]\n"
      "v_cvt_pk_f16_f32 v184, v176, v177\n"
      "v_cvt_pk_f16_f32 v185, v178, v179\n"
      "v_mfma_f32_16x16x32_f16 v[80:83], a[68:71], v[100:103], v[80:83]\n"
      "s_waitcnt lgkmcnt(1)\n"
      "v_pk_mul_f16 v120, v152, v168\n"
      "v_pk_mul_f16 v121, v153, v169\n"
      "v_mfma_f32_16x16x32_f16 v[80:83], a[72:75], v[104:107], v[80:83]\n"
      "v_fma_f32 v192, |v89|, v57, v192\n"
      "v_fma_f32 v192, |v90|, v58, v192\n"
      "v_mfma_f32_16x16x32_f16 v[80:83], a[76:79], v[108:111], v[80:83]\n"
      "ds_read_b128 v[172:175], v202 offset:2496\n"
      "v_fma_f32 v192, |v91|, v59, v192\n"
      "v_mfma_f32_16x16x32_f16 v[84:87], a[92:95], v[108:111], v[36:39]\n"
      "v_cvt_pk_f16_f32 v186, v180, v181\n"
      "v_cvt_pk_f16_f32 v187, v182, v183\n"
      "v_mfma_f32_16x16x32_f16 v[84:87], a[88:91], v[104:107], v[84:87]\n"
      "v_pk_mul_f16 v122, v154, v170\n"
      "v_pk_mul_f16 v123, v155, v171\n"
      "v_mfma_f32_16x16x32_f16 v[84:87], a[84:87], v[100:103], v[84:87]\n"
      "v_pk_mul_f16 v184, v184, s31 op_sel_hi:[1,0]\n"
      "v_pk_mul_f16 v185, v185, s31 op_sel_hi:[1,0]\n"
      "v_mfma_f32_16x16x32_f16 v[84:87], a[80:83], v[96:99], v[84:87]\n"
      "v_fma_f32 v192, |v92|, v60, v192\n"
      "v_fma_f32 v192, |v93|, v61, v192\n"
      "v_mfma_f32_16x16x32_f16 v[88:91], a[96:99], v[96:99], v[40:43]\n"
      "v_fma_f32 v192, |v94|, v62, v192\n"
      "v_fma_f32 v192, |v95|, v63, v192\n"
      "v_mfma_f32_16x16x32_f16 v[88:91], a[100:103], v[100:103], v[88:91]\n"
      "v_pk_mul_f16 v186, v186, s31 op_sel_hi:[1,0]\n"
      "v_pk_mul_f16 v187, v187, s31 op_sel_hi:[1,0]\n"
      "v_mfma_f32_16x16x32_f16 v[88:91], a[104:107], v[104:107], v[88:91]\n"
      "ds_write_b128 v1, v[184:187] offset:4608\n"
      "v_mfma_f32_16x16x32_f16 v[88:91], a[108:111], v[108:111], v[88:91]\n"
      "v_fma_f32 v192, |v80|, v64, v192\n"
      "v_fma_f32 v192, |v81|, v65, v192\n"
      "v_mfma_f32_16x16x32_f16 v[92:95], a[124:127], v[108:111], v[44:47]\n"
      "s_waitcnt lgkmcnt(1)\n"
      "v_pk_mul_f16 v124, v156, v172\n"
      "v_pk_mul_f16 v125, v157, v173\n"
      "v_mfma_f32_16x16x32_f16 v[92:95], a[120:123], v[104:107], v[92:95]\n"
      "v_fma_f32 v192, |v82|, v66, v192\n"
      "v_fma_f32 v192, |v83|, v67, v192\n"
      "v_mfma_f32_16x16x32_f16 v[92:95], a[116:119], v[100:103], v[92:95]\n"
      "v_pk_mul_f16 v126, v158, v174\n"
      "v_pk_mul_f16 v127, v159, v175\n"
      "v_mfma_f32_16x16x32_f16 v[92:95], a[112:115], v[96:99], v[92:95]\n"
      "ds_read_b128 v[160:163], v203 offset:4608\n"
      "v_fma_f32 v192, |v84|, v68, v192\n"
      "v_mfma_f32_16x16x32_f16 v[80:83], a[0:3], v[112:115], v[16:19]\n"
      "v_fma_f32 v192, |v85|, v69, v192\n"
      "v_fma_f32 v192, |v86|, v70, v192\n"
      "v_mfma_f32_16x16x32_f16 v[80:83], a[4:7], v[116:119], v[80:83]\n"
      "v_fma_f32 v192, |v87|, v71, v192\n"
      "v_fma_f32 v192, |v88|, v72, v192\n"
      "v_mfma_f32_16x16x32_f16 v[80:83], a[8:11], v[120:123], v[80:83]\n"
      "v_fma_f32 v192, |v89|, v73, v192\n"
      "v_fma_f32 v192, |v90|, v74, v192\n"
      "v_mfma_f32_16x16x32_f16 v[80:83], a[12:15], v[124:127], v[80:83]\n"
      "v_fma_f32 v192, |v91|, v75, v192\n"
      "v_fma_f32 v192, |v92|, v76, v192\n"
      "v_mfma_f32_16x16x32_f16 v[84:87], a[28:31], v[124:127], v[20:23]\n"
      "ds_read_b128 v[164:167], v203 offset:4672\n"
      "v_fma_f32 v192, |v93|, v77, v192\n"
      "v_mfma_f32_16x16x32_f16 v[84:87], a[24:27], v[120:123], v[84:87]\n"
      "v_fma_f32 v192, |v94|, v78, v192\n"
      "v_fma_f32 v192, |v95|, v79, v192\n"
      "v_mfma_f32_16x16x32_f16 v[84:87], a[20:23], v[116:119], v[84:87]\n"
      "ds_read_b128 v[168:171], v203 offset:4736\n"
      "v_min3_f32 v226, |v228|, |v233|, |v238|\n"
      "v_mfma_f32_16x16x32_f16 v[84:87], a[16:19], v[112:115], v[84:87]\n"
      "s_waitcnt lgkmcnt(2)\n"
      "v_pk_mul_f16 v96, v128, v160\n"
      "v_pk_mul_f16 v97, v129, v161\n"
      "v_mfma_f32_16x16x32_f16 v[88:91], a[32:35], v[112:115], v[24:27]\n"
      "v_pk_mul_f16 v98, v130, v162\n"
      "v_pk_mul_f16 v99, v131, v163\n"
      "v_mfma_f32_16x16x32_f16 v[88:91], a[36:39], v[116:119], v[88:91]\n"
      "v_fma_f32 v193, |v80|, v48, 4.0\n"
      "v_fma_f32 v193, |v81|, v49, v193\n"
      "v_mfma_f32_16x16x32_f16 v[88:91], a[40:43], v[120:123], v[88:91]\n"
      "s_waitcnt vmcnt(0)\n"
      "v_min3_f32 v226, |v243|, |v244|, v226\n"
      "v_min3_f32 v226, |v249|, |v254|, v226\n"
      "v_mfma_f32_16x16x32_f16 v[88:91], a[44:47], v[124:127], v[88:91]\n"
      "v_fma_f32 v193, |v82|, v50, v193\n"
      "v_fma_f32 v193, |v83|, v51, v193\n"
      "v_mfma_f32_16x16x32_f16 v[92:95], a[60:63], v[124:127], v[28:31]\n"
      "s_waitcnt lgkmcnt(1)\n"
      "v_pk_mul_f16 v100, v132, v164\n"
      "v_pk_mul_f16 v101, v133, v165\n"
      "v_mfma_f32_16x16x32_f16 v[92:95], a[56:59], v[120:123], v[92:95]\n"
      "v_cmp_le_f32 vcc, 0x3da00000, v226\n"
      "v_pk_mul_f16 v102, v134, v166\n"
      "v_mfma_f32_16x16x32_f16 v[92:95], a[52:55], v[116:119], v[92:95]\n"
      "v_fma_f32 v193, |v84|, v52, v193\n"
      "v_fma_f32 v193, |v85|, v53, v193\n"
      "v_mfma_f32_16x16x32_f16 v[92:95], a[48:51], v[112:115], v[92:95]\n"
      "s_waitcnt lgkmcnt(0)\n"
      "v_pk_mul_f16 v103, v135, v167\n"
      "v_pk_mul_f16 v104, v136, v168\n"
      "v_mfma_f32_16x16x32_f16 v[80:83], a[64:67], v[112:115], v[32:35]\n"
      "s_quadmask_b64 s[44:45], vcc\n"
      "v_fma_f32 v193, |v86|, v54, v193\n"
      "v_fma_f32 v193, |v87|, v55, v193\n"
      "v_mfma_f32_16x16x32_f16 v[80:83], a[68:71], v[116:119], v[80:83]\n"
      "s_quadmask_b32 s30, s44\n"
      "v_fma_f32 v193, |v88|, v56, v193\n"
      "v_fma_f32 v193, |v89|, v57, v193\n"
      "v_mfma_f32_16x16x32_f16 v[80:83], a[72:75], v[120:123], v[80:83]\n"
      "s_xor_b32 s30, s30, 15\n"
      "v_pk_mul_f16 v105, v137, v169\n"
      "v_pk_mul_f16 v106, v138, v170\n"
      "v_mfma_f32_16x16x32_f16 v[80:83], a[76:79], v[124:127], v[80:83]\n"
      "s_or_b32 s30, s30, s41\n"
      "ds_read_b128 v[172:175], v203 offset:4800\n"
      "v_fma_f32 v193, |v90|, v58, v193\n"
      "v_mfma_f32_16x16x32_f16 v[84:87], a[92:95], v[124:127], v[36:39]\n"
      "s_cmp_lg_u32 s55, 0\n"
      "v_fma_f32 v193, |v91|, v59, v193\n"
      "v_fma_f32 v193, |v92|, v60, v193\n"
      "v_mfma_f32_16x16x32_f16 v[84:87], a[88:91], v[120:123], v[84:87]\n"
      "s_cselect_b32 s30, 0, s30\n"
      "v_pk_mul_f16 v107, v139, v171\n"
      "v_fma_f32 v193, |v93|, v61, v193\n"
      "v_mfma_f32_16x16x32_f16 v[84:87], a[84:87], v[116:119], v[84:87]\n"
      "v_fma_f32 v193, |v94|, v62, v193\n"
      "v_fma_f32 v193, |v95|, v63, v193\n"
      "v_mfma_f32_16x16x32_f16 v[84:87], a[80:83], v[112:115], v[84:87]\n"
      "v_fma_f32 v193, |v80|, v64, v193\n"
      "v_fma_f32 v193, |v81|, v65, v193\n"
      "v_mfma_f32_16x16x32_f16 v[88:91], a[96:99], v[112:115], v[40:43]\n"
      "v_fma_f32 v193, |v82|, v66, v193\n"
      "v_fma_f32 v193, |v83|, v67, v193\n"
      "v_mfma_f32_16x16x32_f16 v[88:91], a[100:103], v[116:119], v[88:91]\n"
      "ds_read_b32 v224, v227 offset:37632\n"
      "v_add_f32 v189, v189, v191\n"
      "v_mfma_f32_16x16x32_f16 v[88:91], a[104:107], v[120:123], v[88:91]\n"
      "v_permlane32_swap_b32 v190, v192\n"
      "v_mfma_f32_16x16x32_f16 v[88:91], a[108:111], v[124:127], v[88:91]\n"
      "s_waitcnt lgkmcnt(1)\n"
      "v_pk_mul_f16 v108, v140, v172\n"
      "v_pk_mul_f16 v109, v141, v173\n"
      "v_mfma_f32_16x16x32_f16 v[92:95], a[124:127], v[124:127], v[44:47]\n"
      "v_pk_mul_f16 v110, v142, v174\n"
      "v_pk_mul_f16 v111, v143, v175\n"
      "v_mfma_f32_16x16x32_f16 v[92:95], a[120:123], v[120:123], v[92:95]\n"
      "v_fma_f32 v193, |v84|, v68, v193\n"
      "v_fma_f32 v193, |v85|, v69, v193\n"
      "v_mfma_f32_16x16x32_f16 v[92:95], a[116:119], v[116:119], v[92:95]\n"
      "v_fma_f32 v193, |v86|, v70, v193\n"
      "v_fma_f32 v193, |v87|, v71, v193\n"
      "v_mfma_f32_16x16x32_f16 v[92:95], a[112:115], v[112:115], v[92:95]\n"
      "v_fma_f32 v193, |v88|, v72, v193\n"
      "v_fma_f32 v193, |v89|, v73, v193\n"
      "v_mfma_f32_16x16x32_f16 v[80:83], a[0:3], v[96:99], v[16:19]\n"
      "v_fma_f32 v193, |v90|, v74, v193\n"
      "v_fma_f32 v193, |v91|, v75, v193\n"
      "v_mfma_f32_16x16x32_f16 v[80:83], a[4:7], v[100:103], v[80:83]\n"
      "ds_read_b64 v[176:177], v5 offset:37120\n"
      "v_pk_mul_f16 v112, v144, v160\n"
      "v_mfma_f32_16x16x32_f16 v[80:83], a[8:11], v[104:107], v[80:83]\n"
      "ds_read_b32 v226, v213 offset:12480\n"
      "v_fma_f32 v193, |v92|, v76, v193\n"
      "v_mfma_f32_16x16x32_f16 v[80:83], a[12:15], v[108:111], v[80:83]\n"
      "v_fma_f32 v193, |v93|, v77, v193\n"
      "v_fma_f32 v193, |v94|, v78, v193\n"
      "v_mfma_f32_16x16x32_f16 v[84:87], a[28:31], v[108:111], v[20:23]\n"
      "v_pk_mul_f16 v113, v145, v161\n"
      "v_pk_mul_f16 v114, v146, v162\n"
      "v_mfma_f32_16x16x32_f16 v[84:87], a[24:27], v[104:107], v[84:87]\n"
      "s_waitcnt lgkmcnt(2)\n"
      "v_cmp_ne_u32 vcc, 13, v224\n"
      "v_fma_f32 v193, |v95|, v79, v193\n"
      "v_mfma_f32_16x16x32_f16 v[84:87], a[20:23], v[100:103], v[84:87]\n"
      "v_pk_mul_f16 v115, v147, v163\n"
      "v_pk_mul_f16 v116, v148, v164\n"
      "v_mfma_f32_16x16x32_f16 v[84:87], a[16:19], v[96:99], v[84:87]\n"
      "s_or_b32 s45, vcc_lo, s30\n"
      "v_permlane32_swap_b32 v188, v193\n"
      "v_mfma_f32_16x16x32_f16 v[88:91], a[32:35], v[96:99], v[24:27]\n"
      "v_add_f32 v190, v190, v192\n"
      "v_add_f32 v188, v188, v193\n"
      "v_mfma_f32_16x16x32_f16 v[88:91], a[36:39], v[100:103], v[88:91]\n"
      "v_fma_f32 v194, |v80|, v48, 4.0\n"
      "v_fma_f32 v194, |v81|, v49, v194\n"
      "v_mfma_f32_16x16x32_f16 v[88:91], a[40:43], v[104:107], v[88:91]\n"
      "s_waitcnt lgkmcnt(0)\n"
      "v_max_f16_sdwa v226, |v226|, |v226| dst_sel:DWORD dst_unused:UNUSED_PAD src0_sel:WORD_0 src1_sel:WORD_1\n"
      "v_cmp_le_f16 vcc, 0x6400, v226\n"
      "v_mfma_f32_16x16x32_f16 v[88:91], a[44:47], v[108:111], v[88:91]\n"
      "v_permlane16_swap_b32 v189, v190\n"
      "v_mfma_f32_16x16x32_f16 v[92:95], a[60:63], v[108:111], v[28:31]\n"
      "s_or_b32 vcc_lo, vcc_lo, s45\n"
      "v_fma_f32 v194, |v82|, v50, v194\n"
      "v_fma_f32 v194, |v83|, v51, v194\n"
      "v_mfma_f32_16x16x32_f16 v[92:95], a[56:59], v[104:107], v[92:95]\n"
      "s_cmp_lg_u64 vcc, 0\n"
      "v_pk_mul_f16 v117, v149, v165\n"
      "v_pk_mul_f16 v118, v150, v166\n"
      "v_mfma_f32_16x16x32_f16 v[92:95], a[52:55], v[100:103], v[92:95]\n"
      "v_add_f32 v189, v189, v190\n"
      "v_fma_f32 v194, |v84|, v52, v194\n"
      "v_mfma_f32_16x16x32_f16 v[92:95], a[48:51], v[96:99], v[92:95]\n"
      "s_cbranch_scc1 5f\n"
      "4:\n"
      "v_mfma_f32_16x16x32_f16 v[80:83], a[64:67], v[96:99], v[32:35]\n"
      "v_fma_f32 v194, |v85|, v53, v194\n"
      "v_pk_mul_f16 v119, v151, v167\n"
      "v_mfma_f32_16x16x32_f16 v[80:83], a[68:71], v[100:103], v[80:83]\n"
      "41:\n"
      "s_cbranch_execz 44b\n"
      "v_cvt_pk_f16_f32 v176, v176, v177\n"
      "v_mfma_f32_16x16x32_f16 v[80:83], a[72:75], v[104:107], v[80:83]\n"
      "s_lshl_b32 s49, s40, 1\n"
      "ds_write_b32 v213, v176 offset:11904\n"
      "v_mfma_f32_16x16x32_f16 v[80:83], a[76:79], v[108:111], v[80:83]\n"
      "s_add_u32 s51, s57, s49\n"
      "v_fma_f32 v194, |v86|, v54, v194\n"
      "v_fma_f32 v194, |v87|, v55, v194\n"
      "v_mfma_f32_16x16x32_f16 v[84:87], a[92:95], v[108:111], v[36:39]\n"
      "s_add_u32 s51, s51, s58\n"
      "v_pk_mul_f16 v120, v152, v168\n"
      "v_fma_f32 v194, |v88|, v56, v194\n"
      "v_mfma_f32_16x16x32_f16 v[84:87], a[88:91], v[104:107], v[84:87]\n"
      "s_add_u32 s51, s51, s56\n"
      "v_fma_f32 v194, |v89|, v57, v194\n"
      "v_pk_mul_f16 v121, v153, v169\n"
      "v_mfma_f32_16x16x32_f16 v[84:87], a[84:87], v[100:103], v[84:87]\n"
      "s_sub_u32 s51, 0, s51\n"
      "v_fma_f32 v194, |v90|, v58, v194\n"
      "v_fma_f32 v194, |v91|, v59, v194\n"
      "v_mfma_f32_16x16x32_f16 v[84:87], a[80:83], v[96:99], v[84:87]\n"
      "s_add_u32 s49, s59, s49\n"
      "v_pk_mul_f16 v122, v154, v170\n"
      "v_pk_mul_f16 v123, v155, v171\n"
      "v_mfma_f32_16x16x32_f16 v[88:91], a[96:99], v[96:99], v[40:43]\n"
      "s_sub_u32 s39, 7, s49\n"
      "v_fma_f32 v194, |v92|, v60, v194\n"
      "v_fma_f32 v194, |v93|, v61, v194\n"
      "v_mfma_f32_16x16x32_f16 v[88:91], a[100:103], v[100:103], v[88:91]\n"
      "v_fma_f32 v194, |v94|, v62, v194\n"
      "v_fma_f32 v194, |v95|, v63, v194\n"
      "v_mfma_f32_16x16x32_f16 v[88:91], a[104:107], v[104:107], v[88:91]\n"
      "v_fma_f32 v194, |v80|, v64, v194\n"
      "v_fma_f32 v194, |v81|, v65, v194\n"
      "v_mfma_f32_16x16x32_f16 v[88:91], a[108:111], v[108:111], v[88:91]\n"
      "v_pk_mul_f16 v124, v156, v172\n"
      "v_pk_mul_f16 v125, v157, v173\n"
      "v_mfma_f32_16x16x32_f16 v[92:95], a[124:127], v[108:111], v[44:47]\n"
      "v_fma_f32 v194, |v82|, v66, v194\n"
      "v_fma_f32 v194, |v83|, v67, v194\n"
      "v_mfma_f32_16x16x32_f16 v[92:95], a[120:123], v[104:107], v[92:95]\n"
      "v_pk_mul_f16 v126, v158, v174\n"
      "v_pk_mul_f16 v127, v159, v175\n"
      "v_mfma_f32_16x16x32_f16 v[92:95], a[116:119], v[100:103], v[92:95]\n"
      "ds_read_b128 v[160:163], v203 offset:5184\n"
      "v_ldexp_f32 v200, v200, s51\n"
      "v_mfma_f32_16x16x32_f16 v[92:95], a[112:115], v[96:99], v[92:95]\n"
      "v_fma_f32 v194, |v84|, v68, v194\n"
      "v_fma_f32 v194, |v85|, v69, v194\n"
      "v_mfma_f32_16x16x32_f16 v[80:83], a[0:3], v[112:115], v[16:19]\n"
      "v_fma_f32 v194, |v86|, v70, v194\n"
      "v_fma_f32 v194, |v87|, v71, v194\n"
      "v_mfma_f32_16x16x32_f16 v[80:83], a[4:7], v[116:119], v[80:83]\n"
      "ds_read_b128 v[164:167], v203 offset:5248\n"
      "v_fma_f32 v194, |v88|, v72, v194\n"
      "v_mfma_f32_16x16x32_f16 v[80:83], a[8:11], v[120:123], v[80:83]\n"
      "v_fma_f32 v194, |v89|, v73, v194\n"
      "v_fma_f32 v194, |v90|, v74, v194\n"
      "v_mfma_f32_16x16x32_f16 v[80:83], a[12:15], v[124:127], v[80:83]\n"
      "v_fma_f32 v194, |v91|, v75, v194\n"
      "v_fma_f32 v194, |v92|, v76, v194\n"
      "v_mfma_f32_4x4x4_16b_bf16 v[176:179], a[216:217], v[248:249], 0\n"
      "v_fma_f32 v194, |v93|, v77, v194\n"
      "v_mfma_f32_4x4x4_16b_bf16 v[180:183], a[216:217], v[250:251], 0\n"
      "v_fma_f32 v194, |v94|, v78, v194\n"
      "v_mfma_f32_4x4x4_16b_bf16 v[228:231], a[216:217], v[252:253], 0\n"
      "v_fma_f32 v194, |v95|, v79, v194\n"
      "v_mfma_f32_4x4x4_16b_bf16 v[232:235], a[216:217], v[254:255], 0\n"
      "s_waitcnt lgkmcnt(1)\n"
      "v_pk_mul_f16 v96, v128, v160\n"
      "v_pk_mul_f16 v97, v129, v161\n"
      "v_mfma_f32_16x16x32_f16 v[84:87], a[28:31], v[124:127], v[20:23]\n"
      "v_pk_mul_f16 v98, v130, v162\n"
      "v_pk_mul_f16 v99, v131, v163\n"
      "v_mfma_f32_16x16x32_f16 v[84:87], a[24:27], v[120:123], v[84:87]\n"
      "s_waitcnt lgkmcnt(0)\n"
      "ds_read_b128 v[168:171], v203 offset:5312\n"
      "v_pk_mul_f16 v100, v132, v164\n"
      "v_mfma_f32_16x16x32_f16 v[84:87], a[20:23], v[116:119], v[84:87]\n"
      "v_pk_mul_f16 v101, v133, v165\n"
      "v_pk_mul_f16 v102, v134, v166\n"
      "v_mfma_f32_16x16x32_f16 v[84:87], a[16:19], v[112:115], v[84:87]\n"
      "v_fma_f32 v195, |v80|, v48, 4.0\n"
      "v_fma_f32 v195, |v81|, v49, v195\n"
      "v_mfma_f32_16x16x32_f16 v[88:91], a[32:35], v[112:115], v[24:27]\n"
      "v_pk_mul_f16 v103, v135, v167\n"
      "v_fma_f32 v195, |v82|, v50, v195\n"
      "v_mfma_f32_16x16x32_f16 v[88:91], a[36:39], v[116:119], v[88:91]\n"
      "ds_read_b128 v[172:175], v203 offset:5376\n"
      "v_fma_f32 v195, |v83|, v51, v195\n"
      "v_mfma_f32_16x16x32_f16 v[88:91], a[40:43], v[120:123], v[88:91]\n"
      "v_fma_f32 v195, |v84|, v52, v195\n"
      "v_fma_f32 v195, |v85|, v53, v195\n"
      "v_mfma_f32_16x16x32_f16 v[88:91], a[44:47], v[124:127], v[88:91]\n"
      "v_fma_f32 v195, |v86|, v54, v195\n"
      "v_fma_f32 v195, |v87|, v55, v195\n"
      "v_mfma_f32_16x16x32_f16 v[92:95], a[60:63], v[124:127], v[28:31]\n"
      "v_add_u32_sdwa v219, s38, v197 dst_sel:DWORD dst_unused:UNUSED_PAD src0_sel:DWORD src1_sel:WORD_1\n"
      "v_add_u32_sdwa v220, s38, v198 dst_sel:DWORD dst_unused:UNUSED_PAD src0_sel:DWORD src1_sel:WORD_0\n"
      "v_mfma_f32_16x16x32_f16 v[92:95], a[56:59], v[120:123], v[92:95]\n"
      "s_waitcnt lgkmcnt(1)\n"
      "v_pk_mul_f16 v104, v136, v168\n"
      "v_pk_mul_f16 v105, v137, v169\n"
      "v_mfma_f32_16x16x32_f16 v[92:95], a[52:55], v[116:119], v[92:95]\n"
      "v_pk_mul_f16 v106, v138, v170\n"
      "v_pk_mul_f16 v107, v139, v171\n"
      "v_mfma_f32_16x16x32_f16 v[92:95], a[48:51], v[112:115], v[92:95]\n"
      "v_fma_f32 v195, |v88|, v56, v195\n"
      "v_fma_f32 v195, |v89|, v57, v195\n"
      "v_mfma_f32_16x16x32_f16 v[80:83], a[64:67], v[112:115], v[32:35]\n"
      "v_fma_f32 v195, |v90|, v58, v195\n"
      "v_fma_f32 v195, |v91|, v59, v195\n"
      "v_mfma_f32_16x16x32_f16 v[80:83], a[68:71], v[116:119], v[80:83]\n"
      "s_waitcnt lgkmcnt(0)\n"
      "v_pk_mul_f16 v108, v140, v172\n"
      "v_pk_mul_f16 v109, v141, v173\n"
      "v_mfma_f32_16x16x32_f16 v[80:83], a[72:75], v[120:123], v[80:83]\n"
      "v_fma_f32 v195, |v92|, v60, v195\n"
      "v_fma_f32 v195, |v93|, v61, v195\n"
      "v_mfma_f32_16x16x32_f16 v[80:83], a[76:79], v[124:127], v[80:83]\n"
      "v_fma_f32 v195, |v94|, v62, v195\n"
      "v_fma_f32 v195, |v95|, v63, v195\n"
      "v_mfma_f32_16x16x32_f16 v[84:87], a[92:95], v[124:127], v[36:39]\n"
      "v_pk_mul_f16 v110, v142, v174\n"
      "v_pk_mul_f16 v111, v143, v175\n"
      "v_mfma_f32_16x16x32_f16 v[84:87], a[88:91], v[120:123], v[84:87]\n"
      "v_add_u32_sdwa v221, s38, v198 dst_sel:DWORD dst_unused:UNUSED_PAD src0_sel:DWORD src1_sel:WORD_1\n"
      "v_add_u32_sdwa v216, s38, v196 dst_sel:DWORD dst_unused:UNUSED_PAD src0_sel:DWORD src1_sel:WORD_0\n"
      "v_mfma_f32_16x16x32_f16 v[84:87], a[84:87], v[116:119], v[84:87]\n"
      "v_fma_f32 v195, |v80|, v64, v195\n"
      "v_fma_f32 v195, |v81|, v65, v195\n"
      "v_mfma_f32_16x16x32_f16 v[84:87], a[80:83], v[112:115], v[84:87]\n"
      "v_fma_f32 v195, |v82|, v66, v195\n"
      "v_fma_f32 v195, |v83|, v67, v195\n"
      "v_mfma_f32_16x16x32_f16 v[88:91], a[96:99], v[112:115], v[40:43]\n"
      "v_cvt_pk_f16_f32 v184, v176, v177\n"
      "v_cvt_pk_f16_f32 v185, v178, v179\n"
      "v_mfma_f32_16x16x32_f16 v[88:91], a[100:103], v[116:119], v[88:91]\n"
      "v_cvt_pk_f16_f32 v186, v180, v181\n"
      "v_cvt_pk_f16_f32 v187, v182, v183\n"
      "v_mfma_f32_16x16x32_f16 v[88:91], a[104:107], v[120:123], v[88:91]\n"
      "ds_write_b128 v1, v[184:187] offset:5760\n"
      "v_mfma_f32_16x16x32_f16 v[88:91], a[108:111], v[124:127], v[88:91]\n"
      "v_fma_f32 v195, |v84|, v68, v195\n"
      "v_fma_f32 v195, |v85|, v69, v195\n"
      "v_mfma_f32_16x16x32_f16 v[92:95], a[124:127], v[124:127], v[44:47]\n"
      "v_fma_f32 v195, |v86|, v70, v195\n"
      "v_fma_f32 v195, |v87|, v71, v195\n"
      "v_mfma_f32_16x16x32_f16 v[92:95], a[120:123], v[120:123], v[92:95]\n"
      "v_pk_mul_f16 v120, v152, v168\n"
      "v_pk_mul_f16 v121, v153, v169\n"
      "v_mfma_f32_16x16x32_f16 v[92:95], a[116:119], v[116:119], v[92:95]\n"
      "v_fma_f32 v195, |v88|, v72, v195\n"
      "v_fma_f32 v195, |v89|, v73, v195\n"
      "v_mfma_f32_16x16x32_f16 v[92:95], a[112:115], v[112:115], v[92:95]\n"
      "v_fma_f32 v195, |v90|, v74, v195\n"
      "v_fma_f32 v195, |v91|, v75, v195\n"
      "v_mfma_f32_16x16x32_f16 v[80:83], a[0:3], v[96:99], v[16:19]\n"
      "v_pk_mul_f16 v112, v144, v160\n"
      "v_pk_mul_f16 v113, v145, v161\n"
      "v_mfma_f32_16x16x32_f16 v[80:83], a[4:7], v[100:103], v[80:83]\n"
      "v_pk_mul_f16 v114, v146, v162\n"
      "v_pk_mul_f16 v115, v147, v163\n"
      "v_mfma_f32_16x16x32_f16 v[80:83], a[8:11], v[104:107], v[80:83]\n"
      "v_fma_f32 v195, |v92|, v76, v195\n"
      "v_fma_f32 v195, |v93|, v77, v195\n"
      "v_mfma_f32_16x16x32_f16 v[80:83], a[12:15], v[108:111], v[80:83]\n"
      "v_fma_f32 v195, |v94|, v78, v195\n"
      "v_fma_f32 v195, |v95|, v79, v195\n"
      "v_mfma_f32_16x16x32_f16 v[84:87], a[28:31], v[108:111], v[20:23]\n"
      "v_pk_mul_f16 v116, v148, v164\n"
      "v_pk_mul_f16 v117, v149, v165\n"
      "v_mfma_f32_16x16x32_f16 v[84:87], a[24:27], v[104:107], v[84:87]\n"
      "v_pk_mul_f16 v118, v150, v166\n"
      "v_pk_mul_f16 v119, v151, v167\n"
      "v_mfma_f32_16x16x32_f16 v[84:87], a[20:23], v[100:103], v[84:87]\n"
      "v_fma_f32 v190, |v80|, v48, 4.0\n"
      "v_fma_f32 v190, |v81|, v49, v190\n"
      "v_mfma_f32_16x16x32_f16 v[84:87], a[16:19], v[96:99], v[84:87]\n"
      "v_fma_f32 v190, |v82|, v50, v190\n"
      "v_fma_f32 v190, |v83|, v51, v190\n"
      "v_mfma_f32_16x16x32_f16 v[88:91], a[32:35], v[96:99], v[24:27]\n"
      "v_pk_mul_f16 v122, v154, v170\n"
      "v_pk_mul_f16 v123, v155, v171\n"
      "v_mfma_f32_16x16x32_f16 v[88:91], a[36:39], v[100:103], v[88:91]\n"
      "v_pk_mul_f16 v124, v156, v172\n"
      "v_pk_mul_f16 v125, v157, v173\n"
      "v_mfma_f32_16x16x32_f16 v[88:91], a[40:43], v[104:107], v[88:91]\n"
      "v_fma_f32 v190, |v84|, v52, v190\n"
      "v_fma_f32 v190, |v85|, v53, v190\n"
      "v_mfma_f32_16x16x32_f16 v[88:91], a[44:47], v[108:111], v[88:91]\n"
      "v_fma_f32 v190, |v86|, v54, v190\n"
      "v_fma_f32 v190, |v87|, v55, v190\n"
      "v_mfma_f32_16x16x32_f16 v[92:95], a[60:63], v[108:111], v[28:31]\n"
      "v_pk_mul_f16 v126, v158, v174\n"
      "v_pk_mul_f16 v127, v159, v175\n"
      "v_mfma_f32_16x16x32_f16 v[92:95], a[56:59], v[104:107], v[92:95]\n"
      "v_cvt_pk_f16_f32 v184, v228, v229\n"
      "v_cvt_pk_f16_f32 v185, v230, v231\n"
      "v_mfma_f32_16x16x32_f16 v[92:95], a[52:55], v[100:103], v[92:95]\n"
      "v_fma_f32 v190, |v88|, v56, v190\n"
      "v_fma_f32 v190, |v89|, v57, v190\n"
      "v_mfma_f32_16x16x32_f16 v[92:95], a[48:51], v[96:99], v[92:95]\n"
      "v_fma_f32 v190, |v90|, v58, v190\n"
      "v_fma_f32 v190, |v91|, v59, v190\n"
      "v_mfma_f32_16x16x32_f16 v[80:83], a[64:67], v[96:99], v[32:35]\n"
      "v_cvt_pk_f16_f32 v186, v232, v233\n"
      "v_cvt_pk_f16_f32 v187, v234, v235\n"
      "v_mfma_f32_16x16x32_f16 v[80:83], a[68:71], v[100:103], v[80:83]\n"
      "ds_write_b128 v1, v[184:187] offset:6912\n"
      "v_mfma_f32_16x16x32_f16 v[80:83], a[72:75], v[104:107], v[80:83]\n"
      "v_fma_f32 v190, |v92|, v60, v190\n"
      "v_fma_f32 v190, |v93|, v61, v190\n"
      "v_mfma_f32_16x16x32_f16 v[80:83], a[76:79], v[108:111], v[80:83]\n"
      "v_fma_f32 v190, |v94|, v62, v190\n"
      "v_fma_f32 v190, |v95|, v63, v190\n"
      "v_mfma_f32_16x16x32_f16 v[84:87], a[92:95], v[108:111], v[36:39]\n"
      "ds_read_b128 v[160:163], v203 offset:5760\n"
      "ds_read_b128 v[176:179], v214 offset:11904\n"
      "v_mfma_f32_16x16x32_f16 v[84:87], a[88:91], v[104:107], v[84:87]\n"
      "ds_read_b128 a[152:155], v219 offset:0\n"
      "ds_read_b128 a[168:171], v220 offset:0\n"
      "v_mfma_f32_16x16x32_f16 v[84:87], a[84:87], v[100:103], v[84:87]\n"
      "v_fma_f32 v190, |v80|, v64, v190\n"
      "v_fma_f32 v190, |v81|, v65, v190\n"
      "v_mfma_f32_16x16x32_f16 v[84:87], a[80:83], v[96:99], v[84:87]\n"
      "v_fma_f32 v190, |v82|, v66, v190\n"
      "v_fma_f32 v190, |v83|, v67, v190\n"
      "v_mfma_f32_16x16x32_f16 v[88:91], a[96:99], v[96:99], v[40:43]\n"
      "ds_read_b128 a[184:187], v221 offset:0\n"
      "ds_read_b128 a[156:159], v219 offset:64\n"
      "v_mfma_f32_16x16x32_f16 v[88:91], a[100:103], v[100:103], v[88:91]\n"
      "v_add_u32_sdwa v217, s38, v196 dst_sel:DWORD dst_unused:UNUSED_PAD src0_sel:DWORD src1_sel:WORD_1\n"
      "v_add_u32_sdwa v218, s38, v197 dst_sel:DWORD dst_unused:UNUSED_PAD src0_sel:DWORD src1_sel:WORD_0\n"
      "v_mfma_f32_16x16x32_f16 v[88:91], a[104:107], v[104:107], v[88:91]\n"
      "v_fma_f32 v190, |v84|, v68, v190\n"
      "v_fma_f32 v190, |v85|, v69, v190\n"
      "v_mfma_f32_16x16x32_f16 v[88:91], a[108:111], v[108:111], v[88:91]\n"
      "v_fma_f32 v190, |v86|, v70, v190\n"
      "v_fma_f32 v190, |v87|, v71, v190\n"
      "v_mfma_f32_16x16x32_f16 v[92:95], a[124:127], v[108:111], v[44:47]\n"
      "s_waitcnt lgkmcnt(4)\n"
      "ds_read_b128 v[164:167], v203 offset:5824\n"
      "v_pk_mul_f16 v228, v128, v176\n"
      "v_mfma_f32_16x16x32_f16 v[92:95], a[120:123], v[104:107], v[92:95]\n"
      "v_pk_mul_f16 v229, v129, v177\n"
      "v_pk_mul_f16 v230, v130, v178\n"
      "v_mfma_f32_16x16x32_f16 v[92:95], a[116:119], v[100:103], v[92:95]\n"
      "v_fma_f32 v190, |v88|, v72, v190\n"
      "v_fma_f32 v190, |v89|, v73, v190\n"
      "v_mfma_f32_16x16x32_f16 v[92:95], a[112:115], v[96:99], v[92:95]\n"
      "v_fma_f32 v190, |v90|, v74, v190\n"
      "v_fma_f32 v190, |v91|, v75, v190\n"
      "v_mfma_f32_16x16x32_f16 v[80:83], a[0:3], v[112:115], v[16:19]\n"
      "v_pk_mul_f16 v96, v128, v160\n"
      "v_pk_mul_f16 v97, v129, v161\n"
      "v_mfma_f32_16x16x32_f16 v[80:83], a[4:7], v[116:119], v[80:83]\n"
      "v_pk_mul_f16 v98, v130, v162\n"
      "v_pk_mul_f16 v99, v131, v163\n"
      "v_mfma_f32_16x16x32_f16 v[80:83], a[8:11], v[120:123], v[80:83]\n"
      "v_fma_f32 v190, |v92|, v76, v190\n"
      "v_fma_f32 v190, |v93|, v77, v190\n"
      "v_mfma_f32_16x16x32_f16 v[80:83], a[12:15], v[124:127], v[80:83]\n"
      "v_fma_f32 v190, |v94|, v78, v190\n"
      "v_fma_f32 v190, |v95|, v79, v190\n"
      "v_mfma_f32_16x16x32_f16 v[84:87], a[28:31], v[124:127], v[20:23]\n"
      "s_waitcnt lgkmcnt(0)\n"
      "ds_read_b128 v[168:171], v203 offset:5888\n"
      "v_pk_mul_f16 v100, v132, v164\n"
      "v_mfma_f32_16x16x32_f16 v[84:87], a[24:27], v[120:123], v[84:87]\n"
      "v_pk_mul_f16 v101, v133, v165\n"
      "v_pk_mul_f16 v102, v134, v166\n"
      "v_mfma_f32_16x16x32_f16 v[84:87], a[20:23], v[116:119], v[84:87]\n"
      "v_fma_f32 v191, |v80|, v48, 4.0\n"
      "v_fma_f32 v191, |v81|, v49, v191\n"
      "v_mfma_f32_16x16x32_f16 v[84:87], a[16:19], v[112:115], v[84:87]\n"
      "v_fma_f32 v191, |v82|, v50, v191\n"
      "v_fma_f32 v191, |v83|, v51, v191\n"
      "v_mfma_f32_16x16x32_f16 v[88:91], a[32:35], v[112:115], v[24:27]\n"
      "v_pk_mul_f16 v103, v135, v167\n"
      "v_pk_mul_f16 v231, v131, v179\n"
      "v_mfma_f32_16x16x32_f16 v[88:91], a[36:39], v[116:119], v[88:91]\n"
      "ds_read_b128 v[172:175], v203 offset:5952\n"
      "ds_read_b128 v[240:243], v201 offset:4608\n"
      "v_mfma_f32_16x16x32_f16 v[88:91], a[40:43], v[120:123], v[88:91]\n"
      "v_fma_f32 v191, |v84|, v52, v191\n"
      "v_fma_f32 v191, |v85|, v53, v191\n"
      "v_mfma_f32_16x16x32_f16 v[88:91], a[44:47], v[124:127], v[88:91]\n"
      "v_fma_f32 v191, |v86|, v54, v191\n"
      "v_fma_f32 v191, |v87|, v55, v191\n"
      "v_mfma_f32_16x16x32_f16 v[92:95], a[60:63], v[124:127], v[28:31]\n"
      "s_waitcnt lgkmcnt(2)\n"
      "v_pk_mul_f16 v104, v136, v168\n"
      "v_pk_mul_f16 v105, v137, v169\n"
      "v_mfma_f32_16x16x32_f16 v[92:95], a[56:59], v[120:123], v[92:95]\n"
      "v_pk_mul_f16 v106, v138, v170\n"
      "v_pk_mul_f16 v107, v139, v171\n"
      "v_mfma_f32_16x16x32_f16 v[92:95], a[52:55], v[116:119], v[92:95]\n"
      "v_fma_f32 v191, |v88|, v56, v191\n"
      "v_fma_f32 v191, |v89|, v57, v191\n"
      "v_mfma_f32_16x16x32_f16 v[92:95], a[48:51], v[112:115], v[92:95]\n"
      "v_fma_f32 v191, |v90|, v58, v191\n"
      "v_fma_f32 v191, |v91|, v59, v191\n"
      "v_mfma_f32_16x16x32_f16 v[80:83], a[64:67], v[112:115], v[32:35]\n"
      "ds_read_b128 v[244:247], v201 offset:4672\n"
      "ds_read_b128 v[248:251], v201 offset:4736\n"
      "v_mfma_f32_16x16x32_f16 v[80:83], a[68:71], v[116:119], v[80:83]\n"
      "s_waitcnt lgkmcnt(3)\n"
      "ds_read_b128 v[252:255], v201 offset:4800\n"
      "v_pk_mul_f16 v108, v140, v172\n"
      "v_mfma_f32_16x16x32_f16 v[80:83], a[72:75], v[120:123], v[80:83]\n"
      "v_fma_f32 v191, |v92|, v60, v191\n"
      "v_fma_f32 v191, |v93|, v61, v191\n"
      "v_mfma_f32_16x16x32_f16 v[80:83], a[76:79], v[124:127], v[80:83]\n"
      "v_fma_f32 v191, |v94|, v62, v191\n"
      "v_fma_f32 v191, |v95|, v63, v191\n"
      "v_mfma_f32_16x16x32_f16 v[84:87], a[92:95], v[124:127], v[36:39]\n"
      "v_pk_mul_f16 v109, v141, v173\n"
      "v_pk_mul_f16 v110, v142, v174\n"
      "v_mfma_f32_16x16x32_f16 v[84:87], a[88:91], v[120:123], v[84:87]\n"
      "v_pk_mul_f16 v111, v143, v175\n"
      "v_pk_mul_f16 v232, v144, v176\n"
      "v_mfma_f32_16x16x32_f16 v[84:87], a[84:87], v[116:119], v[84:87]\n"
      "v_fma_f32 v191, |v80|, v64, v191\n"
      "v_fma_f32 v191, |v81|, v65, v191\n"
      "v_mfma_f32_16x16x32_f16 v[84:87], a[80:83], v[112:115], v[84:87]\n"
      "v_fma_f32 v191, |v82|, v66, v191\n"
      "v_fma_f32 v191, |v83|, v67, v191\n"
      "v_mfma_f32_16x16x32_f16 v[88:91], a[96:99], v[112:115], v[40:43]\n"
      "v_pk_mul_f16 v233, v145, v177\n"
      "v_pk_mul_f16 v234, v146, v178\n"
      "v_mfma_f32_16x16x32_f16 v[88:91], a[100:103], v[116:119], v[88:91]\n"
      "s_waitcnt lgkmcnt(3)\n"
      "v_pk_mul_f16 v235, v147, v179\n"
      "v_pk_mul_f16 v236, v240, v176\n"
      "v_mfma_f32_16x16x32_f16 v[88:91], a[104:107], v[120:123], v[88:91]\n"
      "v_fma_f32 v191, |v84|, v68, v191\n"
      "v_fma_f32 v191, |v85|, v69, v191\n"
      "v_mfma_f32_16x16x32_f16 v[88:91], a[108:111], v[124:127], v[88:91]\n"
      "v_fma_f32 v191, |v86|, v70, v191\n"
      "v_fma_f32 v191, |v87|, v71, v191\n"
      "v_mfma_f32_16x16x32_f16 v[92:95], a[124:127], v[124:127], v[44:47]\n"
      "v_pk_mul_f16 v237, v241, v177\n"
      "v_pk_mul_f16 v238, v242, v178\n"
      "v_mfma_f32_16x16x32_f16 v[92:95], a[120:123], v[120:123], v[92:95]\n"
      "v_pk_mul_f16 v120, v152, v168\n"
      "v_pk_mul_f16 v121, v153, v169\n"
      "v_mfma_f32_16x16x32_f16 v[92:95], a[116:119], v[116:119], v[92:95]\n"
      "v_fma_f32 v191, |v88|, v72, v191\n"
      "v_fma_f32 v191, |v89|, v73, v191\n"
      "v_mfma_f32_16x16x32_f16 v[92:95], a[112:115], v[112:115], v[92:95]\n"
      "v_fma_f32 v191, |v90|, v74, v191\n"
      "v_fma_f32 v191, |v91|, v75, v191\n"
      "v_mfma_f32_16x16x32_f16 v[80:83], a[0:3], v[96:99], v[16:19]\n"
      "v_pk_mul_f16 v112, v144, v160\n"
      "v_pk_mul_f16 v113, v145, v161\n"
      "v_mfma_f32_16x16x32_f16 v[80:83], a[4:7], v[100:103], v[80:83]\n"
      "v_pk_mul_f16 v114, v146, v162\n"
      "v_pk_mul_f16 v115, v147, v163\n"
      "v_mfma_f32_16x16x32_f16 v[80:83], a[8:11], v[104:107], v[80:83]\n"
      "v_fma_f32 v191, |v92|, v76, v191\n"
      "v_fma_f32 v191, |v93|, v77, v191\n"
      "v_mfma_f32_16x16x32_f16 v[80:83], a[12:15], v[108:111], v[80:83]\n"
      "v_fma_f32 v191, |v94|, v78, v191\n"
      "v_fma_f32 v191, |v95|, v79, v191\n"
      "v_mfma_f32_16x16x32_f16 v[84:87], a[28:31], v[108:111], v[20:23]\n"
      "v_pk_mul_f16 v116, v148, v164\n"
      "v_pk_mul_f16 v117, v149, v165\n"
      "v_mfma_f32_16x16x32_f16 v[84:87], a[24:27], v[104:107], v[84:87]\n"
      "v_pk_mul_f16 v118, v150, v166\n"
      "v_pk_mul_f16 v119, v151, v167\n"
      "v_mfma_f32_16x16x32_f16 v[84:87], a[20:23], v[100:103], v[84:87]\n"
      "v_fma_f32 v192, |v80|, v48, 4.0\n"
      "v_fma_f32 v192, |v81|, v49, v192\n"
      "v_mfma_f32_16x16x32_f16 v[84:87], a[16:19], v[96:99], v[84:87]\n"
      "v_fma_f32 v192, |v82|, v50, v192\n"
      "v_fma_f32 v192, |v83|, v51, v192\n"
      "v_mfma_f32_16x16x32_f16 v[88:91], a[32:35], v[96:99], v[24:27]\n"
      "v_pk_mul_f16 v122, v154, v170\n"
      "v_pk_mul_f16 v123, v155, v171\n"
      "v_mfma_f32_16x16x32_f16 v[88:91], a[36:39], v[100:103], v[88:91]\n"
      "v_pk_mul_f16 v239, v243, v179\n"
      "v_pk_mul_f16 v124, v156, v172\n"
      "v_mfma_f32_16x16x32_f16 v[88:91], a[40:43], v[104:107], v[88:91]\n"
      "v_fma_f32 v192, |v84|, v52, v192\n"
      "v_fma_f32 v192, |v85|, v53, v192\n"
      "v_mfma_f32_16x16x32_f16 v[88:91], a[44:47], v[108:111], v[88:91]\n"
      "v_fma_f32 v192, |v86|, v54, v192\n"
      "v_fma_f32 v192, |v87|, v55, v192\n"
      "v_mfma_f32_16x16x32_f16 v[92:95], a[60:63], v[108:111], v[28:31]\n"
      "v_pk_mul_f16 v125, v157, v173\n"
      "v_pk_mul_f16 v126, v158, v174\n"
      "v_mfma_f32_16x16x32_f16 v[92:95], a[56:59], v[104:107], v[92:95]\n"
      "ds_read_b128 v[176:179], v214 offset:11968\n"
      "v_pk_mul_f16 v127, v159, v175\n"
      "v_mfma_f32_16x16x32_f16 v[92:95], a[52:55], v[100:103], v[92:95]\n"
      "v_fma_f32 v192, |v88|, v56, v192\n"
      "v_fma_f32 v192, |v89|, v57, v192\n"
      "v_mfma_f32_16x16x32_f16 v[92:95], a[48:51], v[96:99], v[92:95]\n"
      "v_fma_f32 v192, |v90|, v58, v192\n"
      "v_fma_f32 v192, |v91|, v59, v192\n"
      "v_mfma_f32_16x16x32_f16 a[128:131], v[228:231], a[152:155], 0\n"
      "v_add_u32_sdwa v215, s38, v207 dst_sel:DWORD dst_unused:UNUSED_PAD src0_sel:DWORD src1_sel:WORD_1\n"
      "v_add_u32_sdwa v211, s38, v207 dst_sel:DWORD dst_unused:UNUSED_PAD src0_sel:DWORD src1_sel:WORD_0\n"
      "v_mfma_f32_16x16x32_f16 v[80:83], a[64:67], v[96:99], v[32:35]\n"
      "v_add_u32_sdwa v209, s38, v206 dst_sel:DWORD dst_unused:UNUSED_PAD src0_sel:DWORD src1_sel:WORD_1\n"
      "v_add_u32_sdwa v208, s38, v206 dst_sel:DWORD dst_unused:UNUSED_PAD src0_sel:DWORD src1_sel:WORD_0\n"
      "v_mfma_f32_16x16x32_f16 v[80:83], a[68:71], v[100:103], v[80:83]\n"
      "v_fma_f32 v192, |v92|, v60, v192\n"
      "v_fma_f32 v192, |v93|, v61, v192\n"
      "v_mfma_f32_16x16x32_f16 v[80:83], a[72:75], v[104:107], v[80:83]\n"
      "v_fma_f32 v192, |v94|, v62, v192\n"
      "v_fma_f32 v192, |v95|, v63, v192\n"
      "v_mfma_f32_16x16x32_f16 v[80:83], a[76:79], v[108:111], v[80:83]\n"
      "v_add_u32_sdwa v207, s38, v205 dst_sel:DWORD dst_unused:UNUSED_PAD src0_sel:DWORD src1_sel:WORD_1\n"
      "v_add_u32_sdwa v206, s38, v205 dst_sel:DWORD dst_unused:UNUSED_PAD src0_sel:DWORD src1_sel:WORD_0\n"
      "v_mfma_f32_16x16x32_f16 v[84:87], a[92:95], v[108:111], v[36:39]\n"
      "s_waitcnt lgkmcnt(0)\n"
      "v_pk_mul_f16 v228, v132, v176\n"
      "v_pk_mul_f16 v229, v133, v177\n"
      "v_mfma_f32_16x16x32_f16 v[84:87], a[88:91], v[104:107], v[84:87]\n"
      "v_pk_mul_f16 v230, v134, v178\n"
      "v_pk_mul_f16 v231, v135, v179\n"
      "v_mfma_f32_16x16x32_f16 v[84:87], a[84:87], v[100:103], v[84:87]\n"
      "v_fma_f32 v192, |v80|, v64, v192\n"
      "v_fma_f32 v192, |v81|, v65, v192\n"
      "v_mfma_f32_16x16x32_f16 v[84:87], a[80:83], v[96:99], v[84:87]\n"
      "v_fma_f32 v192, |v82|, v66, v192\n"
      "v_fma_f32 v192, |v83|, v67, v192\n"
      "v_mfma_f32_16x16x32_f16 a[132:135], v[232:235], a[168:171], 0\n"
      "ds_read_b128 v[160:163], v203 offset:6336\n"
      "v_add_u32_sdwa v205, s38, v204 dst_sel:DWORD dst_unused:UNUSED_PAD src0_sel:DWORD src1_sel:WORD_1\n"
      "v_mfma_f32_16x16x32_f16 v[88:91], a[96:99], v[96:99], v[40:43]\n"
      "ds_read_b128 a[172:175], v220 offset:64\n"
      "v_add_u32_sdwa v204, s38, v204 dst_sel:DWORD dst_unused:UNUSED_PAD src0_sel:DWORD src1_sel:WORD_0\n"
      "v_mfma_f32_16x16x32_f16 v[88:91], a[100:103], v[100:103], v[88:91]\n"
      "v_fma_f32 v192, |v84|, v68, v192\n"
      "v_fma_f32 v192, |v85|, v69, v192\n"
      "v_mfma_f32_16x16x32_f16 v[88:91], a[104:107], v[104:107], v[88:91]\n"
      "v_fma_f32 v192, |v86|, v70, v192\n"
      "v_fma_f32 v192, |v87|, v71, v192\n"
      "v_mfma_f32_16x16x32_f16 v[88:91], a[108:111], v[108:111], v[88:91]\n"
      "ds_read_b128 v[164:167], v203 offset:6400\n"
      "v_pk_mul_f16 v232, v148, v176\n"
      "v_mfma_f32_16x16x32_f16 v[92:95], a[124:127], v[108:111], v[44:47]\n"
      "v_pk_mul_f16 v233, v149, v177\n"
      "v_pk_mul_f16 v234, v150, v178\n"
      "v_mfma_f32_16x16x32_f16 v[92:95], a[120:123], v[104:107], v[92:95]\n"
      "ds_read_b128 a[188:191], v221 offset:64\n"
      "v_pk_mul_f16 v235, v151, v179\n"
      "v_mfma_f32_16x16x32_f16 v[92:95], a[116:119], v[100:103], v[92:95]\n"
      "v_fma_f32 v192, |v88|, v72, v192\n"
      "v_fma_f32 v192, |v89|, v73, v192\n"
      "v_mfma_f32_16x16x32_f16 v[92:95], a[112:115], v[96:99], v[92:95]\n"
      "v_fma_f32 v192, |v90|, v74, v192\n"
      "v_fma_f32 v192, |v91|, v75, v192\n"
      "v_mfma_f32_16x16x32_f16 a[136:139], v[236:239], a[184:187], 0\n"
      "s_waitcnt lgkmcnt(3)\n"
      "v_pk_mul_f16 v96, v128, v160\n"
      "v_pk_mul_f16 v97, v129, v161\n"
      "v_mfma_f32_16x16x32_f16 v[80:83], a[0:3], v[112:115], v[16:19]\n"
      "v_pk_mul_f16 v98, v130, v162\n"
      "v_pk_mul_f16 v99, v131, v163\n"
      "v_mfma_f32_16x16x32_f16 v[80:83], a[4:7], v[116:119], v[80:83]\n"
      "v_fma_f32 v192, |v92|, v76, v192\n"
      "v_fma_f32 v192, |v93|, v77, v192\n"
      "v_mfma_f32_16x16x32_f16 v[80:83], a[8:11], v[120:123], v[80:83]\n"
      "v_fma_f32 v192, |v94|, v78, v192\n"
      "v_fma_f32 v192, |v95|, v79, v192\n"
      "v_mfma_f32_16x16x32_f16 v[80:83], a[12:15], v[124:127], v[80:83]\n"
      "s_waitcnt lgkmcnt(1)\n"
      "ds_read_b128 v[168:171], v203 offset:6464\n"
      "v_pk_mul_f16 v100, v132, v164\n"
      "v_mfma_f32_16x16x32_f16 v[84:87], a[28:31], v[124:127], v[20:23]\n"
      "v_permlane32_swap_b32 v194, v192\n"
      "v_mfma_f32_16x16x32_f16 v[84:87], a[24:27], v[120:123], v[84:87]\n"
      "v_pk_mul_f16 v101, v133, v165\n"
      "v_pk_mul_f16 v102, v134, v166\n"
      "v_mfma_f32_16x16x32_f16 v[84:87], a[20:23], v[116:119], v[84:87]\n"
      "v_fma_f32 v193, |v80|, v48, 4.0\n"
      "v_fma_f32 v193, |v81|, v49, v193\n"
      "v_mfma_f32_16x16x32_f16 v[84:87], a[16:19], v[112:115], v[84:87]\n"
      "v_fma_f32 v193, |v82|, v50, v193\n"
      "v_fma_f32 v193, |v83|, v51, v193\n"
      "v_mfma_f32_16x16x32_f16 v[88:91], a[32:35], v[112:115], v[24:27]\n"
      "v_pk_mul_f16 v103, v135, v167\n"
      "v_pk_mul_f16 v236, v244, v176\n"
      "v_mfma_f32_16x16x32_f16 v[88:91], a[36:39], v[116:119], v[88:91]\n"
      "v_pk_mul_f16 v237, v245, v177\n"
      "v_pk_mul_f16 v238, v246, v178\n"
      "v_mfma_f32_16x16x32_f16 v[88:91], a[40:43], v[120:123], v[88:91]\n"
      "v_fma_f32 v193, |v84|, v52, v193\n"
      "v_fma_f32 v193, |v85|, v53, v193\n"
      "v_mfma_f32_16x16x32_f16 v[88:91], a[44:47], v[124:127], v[88:91]\n"
      "v_fma_f32 v193, |v86|, v54, v193\n"
      "v_fma_f32 v193, |v87|, v55, v193\n"
      "v_mfma_f32_16x16x32_f16 v[92:95], a[60:63], v[124:127], v[28:31]\n"
      "s_waitcnt lgkmcnt(0)\n"
      "v_pk_mul_f16 v104, v136, v168\n"
      "v_pk_mul_f16 v105, v137, v169\n"
      "v_mfma_f32_16x16x32_f16 v[92:95], a[56:59], v[120:123], v[92:95]\n"
      "v_pk_mul_f16 v106, v138, v170\n"
      "v_pk_mul_f16 v107, v139, v171\n"
      "v_mfma_f32_16x16x32_f16 v[92:95], a[52:55], v[116:119], v[92:95]\n"
      "v_fma_f32 v193, |v88|, v56, v193\n"
      "v_fma_f32 v193, |v89|, v57, v193\n"
      "v_mfma_f32_16x16x32_f16 v[92:95], a[48:51], v[112:115], v[92:95]\n"
      "v_fma_f32 v193, |v90|, v58, v193\n"
      "v_fma_f32 v193, |v91|, v59, v193\n"
      "v_mfma_f32_16x16x32_f16 a[128:131], v[228:231], a[156:159], a[128:131]\n"
      "ds_read_b128 v[172:175], v203 offset:6528\n"
      "v_add_u32_sdwa v10, s99, v199 dst_sel:DWORD dst_unused:UNUSED_PAD src0_sel:DWORD src1_sel:WORD_0\n"
      "v_mfma_f32_16x16x32_f16 v[80:83], a[64:67], v[112:115], v[32:35]\n"
      "v_pk_mul_f16 v239, v247, v179\n"
      "v_add_u32_sdwa v11, s99, v199 dst_sel:DWORD dst_unused:UNUSED_PAD src0_sel:DWORD src1_sel:WORD_1\n"
      "v_mfma_f32_16x16x32_f16 v[80:83], a[68:71], v[116:119], v[80:83]\n"
      "v_fma_f32 v193, |v92|, v60, v193\n"
      "v_fma_f32 v193, |v93|, v61, v193\n"
      "v_mfma_f32_16x16x32_f16 v[80:83], a[72:75], v[120:123], v[80:83]\n"
      "v_fma_f32 v193, |v94|, v62, v193\n"
      "v_fma_f32 v193, |v95|, v63, v193\n"
      "v_mfma_f32_16x16x32_f16 v[80:83], a[76:79], v[124:127], v[80:83]\n"
      "42:\n"
      "s_cbranch_execz 44b\n"
      "v_mov_b32 v7, 0xf149f2ca\n"
      "v_mfma_f32_16x16x32_f16 v[84:87], a[92:95], v[124:127], v[36:39]\n"
      "v_add_u32 v12, s98, v1\n"
      "v_mov_b32 v8, 0xf149f2ca\n"
      "v_mfma_f32_16x16x32_f16 v[84:87], a[88:91], v[120:123], v[84:87]\n"
      "s_and_b32 s100, s100, 0x37373737\n"
      "ds_read_b128 v[176:179], v214 offset:12032\n"
      "ds_read_b128 a[160:163], v219 offset:128\n"
      "v_mfma_f32_16x16x32_f16 v[84:87], a[84:87], v[116:119], v[84:87]\n"
      "s_xor_b32 s100, s100, 0x370037\n"
      "v_fma_f32 v193, |v80|, v64, v193\n"
      "v_fma_f32 v193, |v81|, v65, v193\n"
      "v_mfma_f32_16x16x32_f16 v[84:87], a[80:83], v[112:115], v[84:87]\n"
      "s_waitcnt lgkmcnt(2)\n"
      "v_pk_mul_f16 v108, v140, v172\n"
      "v_pk_mul_f16 v109, v141, v173\n"
      "v_mfma_f32_16x16x32_f16 a[132:135], v[232:235], a[172:175], a[132:135]\n"
      "s_mov_b32 s101, s100\n"
      "v_fma_f32 v193, |v82|, v66, v193\n"
      "v_fma_f32 v193, |v83|, v67, v193\n"
      "v_mfma_f32_16x16x32_f16 v[88:91], a[96:99], v[112:115], v[40:43]\n"
      "s_or_b32 s44, s55, s41\n"
      "v_pk_mul_f16 v110, v142, v174\n"
      "v_pk_mul_f16 v111, v143, v175\n"
      "v_mfma_f32_16x16x32_f16 v[88:91], a[100:103], v[116:119], v[88:91]\n"
      "s_cselect_b32 s44, 2, 1\n"
      "v_fma_f32 v193, |v84|, v68, v193\n"
      "v_fma_f32 v193, |v85|, v69, v193\n"
      "v_mfma_f32_16x16x32_f16 v[88:91], a[104:107], v[120:123], v[88:91]\n"
      "v_fma_f32 v193, |v86|, v70, v193\n"
      "v_fma_f32 v193, |v87|, v71, v193\n"
      "v_mfma_f32_16x16x32_f16 v[88:91], a[108:111], v[124:127], v[88:91]\n"
      "ds_read_b128 a[176:179], v220 offset:128\n"
      "ds_read_b128 a[192:195], v221 offset:128\n"
      "v_mfma_f32_16x16x32_f16 v[92:95], a[124:127], v[124:127], v[44:47]\n"
      "s_waitcnt lgkmcnt(3)\n"
      "v_pk_mul_f16 v228, v136, v176\n"
      "v_pk_mul_f16 v229, v137, v177\n"
      "v_mfma_f32_16x16x32_f16 v[92:95], a[120:123], v[120:123], v[92:95]\n"
      "v_pk_mul_f16 v230, v138, v178\n"
      "v_pk_mul_f16 v231, v139, v179\n"
      "v_mfma_f32_16x16x32_f16 v[92:95], a[116:119], v[116:119], v[92:95]\n"
      "v_fma_f32 v193, |v88|, v72, v193\n"
      "v_fma_f32 v193, |v89|, v73, v193\n"
      "v_mfma_f32_16x16x32_f16 v[92:95], a[112:115], v[112:115], v[92:95]\n"
      "v_fma_f32 v193, |v90|, v74, v193\n"
      "v_fma_f32 v193, |v91|, v75, v193\n"
      "v_mfma_f32_16x16x32_f16 a[136:139], v[236:239], a[188:191], a[136:139]\n"
      "v_pk_mul_f16 v112, v144, v160\n"
      "v_pk_mul_f16 v113, v145, v161\n"
      "v_mfma_f32_16x16x32_f16 v[80:83], a[0:3], v[96:99], v[16:19]\n"
      "v_pk_mul_f16 v114, v146, v162\n"
      "v_pk_mul_f16 v115, v147, v163\n"
      "v_mfma_f32_16x16x32_f16 v[80:83], a[4:7], v[100:103], v[80:83]\n"
      "v_fma_f32 v193, |v92|, v76, v193\n"
      "v_fma_f32 v193, |v93|, v77, v193\n"
      "v_mfma_f32_16x16x32_f16 v[80:83], a[8:11], v[104:107], v[80:83]\n"
      "v_fma_f32 v193, |v94|, v78, v193\n"
      "v_fma_f32 v193, |v95|, v79, v193\n"
      "v_mfma_f32_16x16x32_f16 v[80:83], a[12:15], v[108:111], v[80:83]\n"
      "v_pk_mul_f16 v116, v148, v164\n"
      "v_pk_mul_f16 v117, v149, v165\n"
      "v_mfma_f32_16x16x32_f16 v[84:87], a[28:31], v[108:111], v[20:23]\n"
      "v_permlane32_swap_b32 v195, v193\n"
      "v_mfma_f32_16x16x32_f16 v[84:87], a[24:27], v[104:107], v[84:87]\n"
      "v_pk_mul_f16 v118, v150, v166\n"
      "v_pk_mul_f16 v119, v151, v167\n"
      "v_mfma_f32_16x16x32_f16 v[84:87], a[20:23], v[100:103], v[84:87]\n"
      "v_fma_f32 v196, |v80|, v48, 4.0\n"
      "v_fma_f32 v196, |v81|, v49, v196\n"
      "v_mfma_f32_16x16x32_f16 v[84:87], a[16:19], v[96:99], v[84:87]\n"
      "v_fma_f32 v196, |v82|, v50, v196\n"
      "v_fma_f32 v196, |v83|, v51, v196\n"
      "v_mfma_f32_16x16x32_f16 v[88:91], a[32:35], v[96:99], v[24:27]\n"
      "v_pk_mul_f16 v232, v152, v176\n"
      "v_pk_mul_f16 v120, v152, v168\n"
      "v_mfma_f32_16x16x32_f16 v[88:91], a[36:39], v[100:103], v[88:91]\n"
      "v_pk_mul_f16 v233, v153, v177\n"
      "v_pk_mul_f16 v121, v153, v169\n"
      "v_mfma_f32_16x16x32_f16 v[88:91], a[40:43], v[104:107], v[88:91]\n"
      "v_fma_f32 v196, |v84|, v52, v196\n"
      "v_fma_f32 v196, |v85|, v53, v196\n"
      "v_mfma_f32_16x16x32_f16 v[88:91], a[44:47], v[108:111], v[88:91]\n"
      "v_fma_f32 v196, |v86|, v54, v196\n"
      "v_fma_f32 v196, |v87|, v55, v196\n"
      "v_mfma_f32_16x16x32_f16 v[92:95], a[60:63], v[108:111], v[28:31]\n"
      "v_pk_mul_f16 v234, v154, v178\n"
      "v_pk_mul_f16 v122, v154, v170\n"
      "v_mfma_f32_16x16x32_f16 v[92:95], a[56:59], v[104:107], v[92:95]\n"
      "v_pk_mul_f16 v235, v155, v179\n"
      "v_pk_mul_f16 v123, v155, v171\n"
      "v_mfma_f32_16x16x32_f16 v[92:95], a[52:55], v[100:103], v[92:95]\n"
      "v_fma_f32 v196, |v88|, v56, v196\n"
      "v_fma_f32 v196, |v89|, v57, v196\n"
      "v_mfma_f32_16x16x32_f16 v[92:95], a[48:51], v[96:99], v[92:95]\n"
      "s_waitcnt lgkmcnt(2)\n"
      "v_fma_f32 v196, |v90|, v58, v196\n"
      "v_fma_f32 v196, |v91|, v59, v196\n"
      "v_mfma_f32_16x16x32_f16 a[128:131], v[228:231], a[160:163], a[128:131]\n"
      "v_pk_mul_f16 v236, v248, v176\n"
      "v_pk_mul_f16 v237, v249, v177\n"
      "v_mfma_f32_16x16x32_f16 v[80:83], a[64:67], v[96:99], v[32:35]\n"
      "v_pk_mul_f16 v238, v250, v178\n"
      "v_pk_mul_f16 v239, v251, v179\n"
      "v_mfma_f32_16x16x32_f16 v[80:83], a[68:71], v[100:103], v[80:83]\n"
      "v_fma_f32 v196, |v92|, v60, v196\n"
      "v_fma_f32 v196, |v93|, v61, v196\n"
      "v_mfma_f32_16x16x32_f16 v[80:83], a[72:75], v[104:107], v[80:83]\n"
      "v_fma_f32 v196, |v94|, v62, v196\n"
      "v_fma_f32 v196, |v95|, v63, v196\n"
      "v_mfma_f32_16x16x32_f16 v[80:83], a[76:79], v[108:111], v[80:83]\n"
      "v_pk_mul_f16 v124, v156, v172\n"
      "v_pk_mul_f16 v125, v157, v173\n"
      "v_mfma_f32_16x16x32_f16 v[84:87], a[92:95], v[108:111], v[36:39]\n"
      "v_mov_b32 v223, s44\n"
      "v_pk_mul_f16 v126, v158, v174\n"
      "v_mfma_f32_16x16x32_f16 v[84:87], a[88:91], v[104:107], v[84:87]\n"
      "ds_write_b32 v2, v223 offset:8\n"
      "v_mfma_f32_16x16x32_f16 v[84:87], a[84:87], v[100:103], v[84:87]\n"
      "v_fma_f32 v196, |v80|, v64, v196\n"
      "v_fma_f32 v196, |v81|, v65, v196\n"
      "v_mfma_f32_16x16x32_f16 v[84:87], a[80:83], v[96:99], v[84:87]\n"
      "s_waitcnt lgkmcnt(2)\n"
      "v_fma_f32 v196, |v82|, v66, v196\n"
      "v_fma_f32 v196, |v83|, v67, v196\n"
      "v_mfma_f32_16x16x32_f16 a[132:135], v[232:235], a[176:179], a[132:135]\n"
      "ds_read_b128 v[176:179], v214 offset:12096\n"
      "v_add_f32 v194, v194, v192\n"
      "v_mfma_f32_16x16x32_f16 v[88:91], a[96:99], v[96:99], v[40:43]\n"
      "ds_read_b128 v[160:163], v202 offset:4608\n"
      "v_pk_mul_f16 v127, v159, v175\n"
      "v_mfma_f32_16x16x32_f16 v[88:91], a[100:103], v[100:103], v[88:91]\n"
      "v_fma_f32 v196, |v84|, v68, v196\n"
      "v_fma_f32 v196, |v85|, v69, v196\n"
      "v_mfma_f32_16x16x32_f16 v[88:91], a[104:107], v[104:107], v[88:91]\n"
      "v_fma_f32 v196, |v86|, v70, v196\n"
      "v_fma_f32 v196, |v87|, v71, v196\n"
      "v_mfma_f32_16x16x32_f16 v[88:91], a[108:111], v[108:111], v[88:91]\n"
      "ds_read_b128 a[164:167], v219 offset:192\n"
      "ds_read_b128 v[164:167], v202 offset:4672\n"
      "v_mfma_f32_16x16x32_f16 v[92:95], a[124:127], v[108:111], v[44:47]\n"
      "ds_read_b128 a[180:183], v220 offset:192\n"
      "ds_read_b128 a[196:199], v221 offset:192\n"
      "v_mfma_f32_16x16x32_f16 v[92:95], a[120:123], v[104:107], v[92:95]\n"
      "ds_read_b128 a[200:203], v215 offset:11904\n"
      "v_add_f32 v195, v195, v193\n"
      "v_mfma_f32_16x16x32_f16 v[92:95], a[116:119], v[100:103], v[92:95]\n"
      "v_fma_f32 v196, |v88|, v72, v196\n"
      "v_fma_f32 v196, |v89|, v73, v196\n"
      "v_mfma_f32_16x16x32_f16 v[92:95], a[112:115], v[96:99], v[92:95]\n"
      "s_waitcnt lgkmcnt(8)\n"
      "v_fma_f32 v196, |v90|, v74, v196\n"
      "v_fma_f32 v196, |v91|, v75, v196\n"
      "v_mfma_f32_16x16x32_f16 a[136:139], v[236:239], a[192:195], a[136:139]\n"
      "s_waitcnt lgkmcnt(5)\n"
      "v_pk_mul_f16 v96, v240, v160\n"
      "v_pk_mul_f16 v97, v241, v161\n"
      "v_mfma_f32_16x16x32_f16 v[80:83], a[0:3], v[112:115], v[16:19]\n"
      "v_pk_mul_f16 v98, v242, v162\n"
      "v_pk_mul_f16 v228, v140, v176\n"
      "v_mfma_f32_16x16x32_f16 v[80:83], a[4:7], v[116:119], v[80:83]\n"
      "v_fma_f32 v196, |v92|, v76, v196\n"
      "v_fma_f32 v196, |v93|, v77, v196\n"
      "v_mfma_f32_16x16x32_f16 v[80:83], a[8:11], v[120:123], v[80:83]\n"
      "v_fma_f32 v196, |v94|, v78, v196\n"
      "v_fma_f32 v196, |v95|, v79, v196\n"
      "v_mfma_f32_16x16x32_f16 v[80:83], a[12:15], v[124:127], v[80:83]\n"
      "v_pk_mul_f16 v99, v243, v163\n"
      "v_pk_mul_f16 v229, v141, v177\n"
      "v_mfma_f32_16x16x32_f16 v[84:87], a[28:31], v[124:127], v[20:23]\n"
      "v_permlane32_swap_b32 v190, v196\n"
      "v_mfma_f32_16x16x32_f16 v[84:87], a[24:27], v[120:123], v[84:87]\n"
      "ds_read_b128 v[168:171], v202 offset:4736\n"
      "v_pk_mul_f16 v230, v142, v178\n"
      "v_mfma_f32_16x16x32_f16 v[84:87], a[20:23], v[116:119], v[84:87]\n"
      "s_waitcnt lgkmcnt(4)\n"
      "v_pk_mul_f16 v231, v143, v179\n"
      "v_pk_mul_f16 v100, v244, v164\n"
      "v_mfma_f32_16x16x32_f16 v[84:87], a[16:19], v[112:115], v[84:87]\n"
      "v_fma_f32 v197, |v80|, v48, 4.0\n"
      "v_fma_f32 v197, |v81|, v49, v197\n"
      "v_mfma_f32_16x16x32_f16 v[88:91], a[32:35], v[112:115], v[24:27]\n"
      "v_fma_f32 v197, |v82|, v50, v197\n"
      "v_fma_f32 v197, |v83|, v51, v197\n"
      "v_mfma_f32_16x16x32_f16 v[88:91], a[36:39], v[116:119], v[88:91]\n"
      "v_pk_mul_f16 v101, v245, v165\n"
      "v_pk_mul_f16 v102, v246, v166\n"
      "v_mfma_f32_16x16x32_f16 v[88:91], a[40:43], v[120:123], v[88:91]\n"
      "v_pk_mul_f16 v103, v247, v167\n"
      "v_pk_mul_f16 v232, v156, v176\n"
      "v_mfma_f32_16x16x32_f16 v[88:91], a[44:47], v[124:127], v[88:91]\n"
      "v_fma_f32 v197, |v84|, v52, v197\n"
      "v_fma_f32 v197, |v85|, v53, v197\n"
      "v_mfma_f32_16x16x32_f16 v[92:95], a[60:63], v[124:127], v[28:31]\n"
      "v_fma_f32 v197, |v86|, v54, v197\n"
      "v_fma_f32 v197, |v87|, v55, v197\n"
      "v_mfma_f32_16x16x32_f16 v[92:95], a[56:59], v[120:123], v[92:95]\n"
      "s_waitcnt lgkmcnt(0)\n"
      "v_pk_mul_f16 v104, v248, v168\n"
      "v_pk_mul_f16 v233, v157, v177\n"
      "v_mfma_f32_16x16x32_f16 v[92:95], a[52:55], v[116:119], v[92:95]\n"
      "v_pk_mul_f16 v105, v249, v169\n"
      "v_pk_mul_f16 v234, v158, v178\n"
      "v_mfma_f32_16x16x32_f16 v[92:95], a[48:51], v[112:115], v[92:95]\n"
      "v_pk_mul_f16 v106, v250, v170\n"
      "v_pk_mul_f16 v235, v159, v179\n"
      "v_mfma_f32_16x16x32_f16 a[128:131], v[228:231], a[164:167], a[128:131]\n"
      "v_fma_f32 v197, |v88|, v56, v197\n"
      "v_fma_f32 v197, |v89|, v57, v197\n"
      "v_mfma_f32_16x16x32_f16 v[80:83], a[64:67], v[112:115], v[32:35]\n"
      "v_pk_mul_f16 v107, v251, v171\n"
      "v_pk_mul_f16 v236, v252, v176\n"
      "v_mfma_f32_16x16x32_f16 v[80:83], a[68:71], v[116:119], v[80:83]\n"
      "v_fma_f32 v197, |v90|, v58, v197\n"
      "v_fma_f32 v197, |v91|, v59, v197\n"
      "v_mfma_f32_16x16x32_f16 v[80:83], a[72:75], v[120:123], v[80:83]\n"
      "ds_write_b64 v218, a[128:129] offset:0\n"
      "v_mfma_f32_16x16x32_f16 v[80:83], a[76:79], v[124:127], v[80:83]\n"
      "v_fma_f32 v197, |v92|, v60, v197\n"
      "v_fma_f32 v197, |v93|, v61, v197\n"
      "v_mfma_f32_16x16x32_f16 v[84:87], a[92:95], v[124:127], v[36:39]\n"
      "ds_read_b128 v[172:175], v202 offset:4800\n"
      "v_fma_f32 v197, |v94|, v62, v197\n"
      "v_mfma_f32_16x16x32_f16 v[84:87], a[88:91], v[120:123], v[84:87]\n"
      "ds_write_b64 v218, a[130:131] offset:8\n"
      "v_mfma_f32_16x16x32_f16 v[84:87], a[84:87], v[116:119], v[84:87]\n"
      "v_fma_f32 v197, |v95|, v63, v197\n"
      "v_fma_f32 v197, |v80|, v64, v197\n"
      "v_mfma_f32_16x16x32_f16 v[84:87], a[80:83], v[112:115], v[84:87]\n"
      "v_pk_mul_f16 v237, v253, v177\n"
      "v_pk_mul_f16 v238, v254, v178\n"
      "v_mfma_f32_16x16x32_f16 a[132:135], v[232:235], a[180:183], a[132:135]\n"
      "v_pk_mul_f16 v239, v255, v179\n"
      "v_fma_f32 v197, |v81|, v65, v197\n"
      "v_mfma_f32_16x16x32_f16 v[88:91], a[96:99], v[112:115], v[40:43]\n"
      "v_fma_f32 v197, |v82|, v66, v197\n"
      "v_fma_f32 v197, |v83|, v67, v197\n"
      "v_mfma_f32_16x16x32_f16 v[88:91], a[100:103], v[116:119], v[88:91]\n"
      "ds_read_b128 v[160:163], v203 offset:6912\n"
      "v_fma_f32 v197, |v84|, v68, v197\n"
      "v_mfma_f32_16x16x32_f16 v[88:91], a[104:107], v[120:123], v[88:91]\n"
      "ds_write_b64 v218, a[132:133] offset:1280\n"
      "v_mfma_f32_16x16x32_f16 v[88:91], a[108:111], v[124:127], v[88:91]\n"
      "s_waitcnt lgkmcnt(3)\n"
      "v_pk_mul_f16 v108, v252, v172\n"
      "v_pk_mul_f16 v109, v253, v173\n"
      "v_mfma_f32_16x16x32_f16 v[92:95], a[124:127], v[124:127], v[44:47]\n"
      "ds_write_b64 v218, a[134:135] offset:1288\n"
      "v_mfma_f32_16x16x32_f16 v[92:95], a[120:123], v[120:123], v[92:95]\n"
      "v_pk_mul_f16 v110, v254, v174\n"
      "v_pk_mul_f16 v111, v255, v175\n"
      "v_mfma_f32_16x16x32_f16 v[92:95], a[116:119], v[116:119], v[92:95]\n"
      "v_fma_f32 v197, |v85|, v69, v197\n"
      "v_fma_f32 v197, |v86|, v70, v197\n"
      "v_mfma_f32_16x16x32_f16 v[92:95], a[112:115], v[112:115], v[92:95]\n"
      "v_fma_f32 v197, |v87|, v71, v197\n"
      "v_fma_f32 v197, |v88|, v72, v197\n"
      "v_mfma_f32_16x16x32_f16 a[136:139], v[236:239], a[196:199], a[136:139]\n"
      "v_fma_f32 v197, |v89|, v73, v197\n"
      "v_fma_f32 v197, |v90|, v74, v197\n"
      "v_mfma_f32_16x16x32_f16 v[80:83], a[0:3], v[96:99], v[16:19]\n"
      "ds_read_b128 v[164:167], v203 offset:6976\n"
      "v_fma_f32 v197, |v91|, v75, v197\n"
      "v_mfma_f32_16x16x32_f16 v[80:83], a[4:7], v[100:103], v[80:83]\n"
      "s_waitcnt lgkmcnt(3)\n"
      "v_pk_mul_f16 v112, v128, v160\n"
      "v_pk_mul_f16 v113, v129, v161\n"
      "v_mfma_f32_16x16x32_f16 v[80:83], a[8:11], v[104:107], v[80:83]\n"
      "ds_write_b128 v218, a[136:139] offset:2560\n"
      "v_mfma_f32_16x16x32_f16 v[80:83], a[12:15], v[108:111], v[80:83]\n"
      "v_fma_f32 v197, |v92|, v76, v197\n"
      "v_fma_f32 v197, |v93|, v77, v197\n"
      "v_mfma_f32_16x16x32_f16 v[84:87], a[28:31], v[108:111], v[20:23]\n"
      "v_fma_f32 v197, |v94|, v78, v197\n"
      "v_fma_f32 v197, |v95|, v79, v197\n"
      "v_mfma_f32_16x16x32_f16 v[84:87], a[24:27], v[104:107], v[84:87]\n"
      "v_pk_mul_f16 v114, v130, v162\n"
      "v_pk_mul_f16 v115, v131, v163\n"
      "v_mfma_f32_16x16x32_f16 v[84:87], a[20:23], v[100:103], v[84:87]\n"
      "v_permlane32_swap_b32 v191, v197\n"
      "v_mfma_f32_16x16x32_f16 v[84:87], a[16:19], v[96:99], v[84:87]\n"
      "ds_read_b128 v[168:171], v203 offset:7040\n"
      "v_add_f32 v190, v190, v196\n"
      "v_mfma_f32_16x16x32_f16 v[88:91], a[32:35], v[96:99], v[24:27]\n"
      "v_fma_f32 v198, |v80|, v48, 4.0\n"
      "v_fma_f32 v198, |v81|, v49, v198\n"
      "v_mfma_f32_16x16x32_f16 v[88:91], a[36:39], v[100:103], v[88:91]\n"
      "v_permlane16_swap_b32 v194, v190\n"
      "v_mfma_f32_16x16x32_f16 v[88:91], a[40:43], v[104:107], v[88:91]\n"
      "v_fma_f32 v198, |v82|, v50, v198\n"
      "v_fma_f32 v198, |v83|, v51, v198\n"
      "v_mfma_f32_16x16x32_f16 v[88:91], a[44:47], v[108:111], v[88:91]\n"
      "s_waitcnt lgkmcnt(2)\n"
      "v_pk_mul_f16 v116, v132, v164\n"
      "v_pk_mul_f16 v117, v133, v165\n"
      "v_mfma_f32_16x16x32_f16 v[92:95], a[60:63], v[108:111], v[28:31]\n"
      "v_add_f32 v194, v194, v190\n"
      "v_add_f32 v191, v191, v197\n"
      "v_mfma_f32_16x16x32_f16 v[92:95], a[56:59], v[104:107], v[92:95]\n"
      "v_pk_mul_f16 v118, v134, v166\n"
      "v_pk_mul_f16 v119, v135, v167\n"
      "v_mfma_f32_16x16x32_f16 v[92:95], a[52:55], v[100:103], v[92:95]\n"
      "v_fma_f32 v198, |v84|, v52, v198\n"
      "v_fma_f32 v198, |v85|, v53, v198\n"
      "v_mfma_f32_16x16x32_f16 v[92:95], a[48:51], v[96:99], v[92:95]\n"
      "v_fma_f32 v198, |v86|, v54, v198\n"
      "v_fma_f32 v198, |v87|, v55, v198\n"
      "v_mfma_f32_16x16x32_f16 v[80:83], a[64:67], v[96:99], v[32:35]\n"
      "v_permlane16_swap_b32 v195, v191\n"
      "v_mfma_f32_16x16x32_f16 v[80:83], a[68:71], v[100:103], v[80:83]\n"
      "s_waitcnt lgkmcnt(0)\n"
      "v_pk_mul_f16 v120, v136, v168\n"
      "v_pk_mul_f16 v121, v137, v169\n"
      "v_mfma_f32_16x16x32_f16 v[80:83], a[72:75], v[104:107], v[80:83]\n"
      "v_fma_f32 v198, |v88|, v56, v198\n"
      "v_fma_f32 v198, |v89|, v57, v198\n"
      "v_mfma_f32_16x16x32_f16 v[80:83], a[76:79], v[108:111], v[80:83]\n"
      "ds_read_b128 v[172:175], v203 offset:7104\n"
      "v_fma_f32 v198, |v90|, v58, v198\n"
      "v_mfma_f32_16x16x32_f16 v[84:87], a[92:95], v[108:111], v[36:39]\n"
      "v_fma_f32 v198, |v91|, v59, v198\n"
      "v_fma_f32 v198, |v92|, v60, v198\n"
      "v_mfma_f32_16x16x32_f16 v[84:87], a[88:91], v[104:107], v[84:87]\n"
      "v_pk_mul_f16 v122, v138, v170\n"
      "v_pk_mul_f16 v123, v139, v171\n"
      "v_mfma_f32_16x16x32_f16 v[84:87], a[84:87], v[100:103], v[84:87]\n"
      "v_fma_f32 v198, |v93|, v61, v198\n"
      "v_fma_f32 v198, |v94|, v62, v198\n"
      "v_mfma_f32_16x16x32_f16 v[84:87], a[80:83], v[96:99], v[84:87]\n"
      "v_fma_f32 v198, |v95|, v63, v198\n"
      "v_fma_f32 v198, |v80|, v64, v198\n"
      "v_mfma_f32_16x16x32_f16 v[88:91], a[96:99], v[96:99], v[40:43]\n"
      "v_fma_f32 v198, |v81|, v65, v198\n"
      "v_fma_f32 v198, |v82|, v66, v198\n"
      "v_mfma_f32_16x16x32_f16 v[88:91], a[100:103], v[100:103], v[88:91]\n"
      "v_fma_f32 v198, |v83|, v67, v198\n"
      "v_add_f32 v195, v195, v191\n"
      "v_mfma_f32_16x16x32_f16 v[88:91], a[104:107], v[104:107], v[88:91]\n"
      "v_fma_f32 v198, |v84|, v68, v198\n"
      "v_fma_f32 v198, |v85|, v69, v198\n"
      "v_mfma_f32_16x16x32_f16 v[88:91], a[108:111], v[108:111], v[88:91]\n"
      "s_waitcnt lgkmcnt(0)\n"
      "v_pk_mul_f16 v124, v140, v172\n"
      "v_pk_mul_f16 v125, v141, v173\n"
      "v_mfma_f32_16x16x32_f16 v[92:95], a[124:127], v[108:111], v[44:47]\n"
      "v_pk_mul_f16 v126, v142, v174\n"
      "v_pk_mul_f16 v127, v143, v175\n"
      "v_mfma_f32_16x16x32_f16 v[92:95], a[120:123], v[104:107], v[92:95]\n"
      "v_fma_f32 v198, |v86|, v70, v198\n"
      "v_fma_f32 v198, |v87|, v71, v198\n"
      "v_mfma_f32_16x16x32_f16 v[92:95], a[116:119], v[100:103], v[92:95]\n"
      "v_fma_f32 v198, |v88|, v72, v198\n"
      "v_fma_f32 v198, |v89|, v73, v198\n"
      "v_mfma_f32_16x16x32_f16 v[92:95], a[112:115], v[96:99], v[92:95]\n"
      "v_fma_f32 v198, |v90|, v74, v198\n"
      "v_fma_f32 v198, |v91|, v75, v198\n"
      "v_mfma_f32_16x16x32_f16 v[80:83], a[0:3], v[112:115], v[16:19]\n"
      "v_pk_mul_f16 v96, v144, v160\n"
      "v_pk_mul_f16 v97, v145, v161\n"
      "v_mfma_f32_16x16x32_f16 v[80:83], a[4:7], v[116:119], v[80:83]\n"
      "v_pk_mul_f16 v98, v146, v162\n"
      "v_pk_mul_f16 v99, v147, v163\n"
      "v_mfma_f32_16x16x32_f16 v[80:83], a[8:11], v[120:123], v[80:83]\n"
      "v_fma_f32 v198, |v92|, v76, v198\n"
      "v_fma_f32 v198, |v93|, v77, v198\n"
      "v_mfma_f32_16x16x32_f16 v[80:83], a[12:15], v[124:127], v[80:83]\n"
      "v_fma_f32 v198, |v94|, v78, v198\n"
      "v_fma_f32 v198, |v95|, v79, v198\n"
      "v_mfma_f32_16x16x32_f16 v[84:87], a[28:31], v[124:127], v[20:23]\n"
      "v_pk_mul_f16 v100, v148, v164\n"
      "v_pk_mul_f16 v101, v149, v165\n"
      "v_mfma_f32_16x16x32_f16 v[84:87], a[24:27], v[120:123], v[84:87]\n"
      "v_pk_mul_f16 v102, v150, v166\n"
      "v_pk_mul_f16 v103, v151, v167\n"
      "v_mfma_f32_16x16x32_f16 v[84:87], a[20:23], v[116:119], v[84:87]\n"
      "v_fma_f32 v192, |v80|, v48, 4.0\n"
      "v_fma_f32 v192, |v81|, v49, v192\n"
      "v_mfma_f32_16x16x32_f16 v[84:87], a[16:19], v[112:115], v[84:87]\n"
      "v_fma_f32 v192, |v82|, v50, v192\n"
      "v_fma_f32 v192, |v83|, v51, v192\n"
      "v_mfma_f32_16x16x32_f16 v[88:91], a[32:35], v[112:115], v[24:27]\n"
      "v_pk_mul_f16 v104, v152, v168\n"
      "v_pk_mul_f16 v105, v153, v169\n"
      "v_mfma_f32_16x16x32_f16 v[88:91], a[36:39], v[116:119], v[88:91]\n"
      "v_pk_mul_f16 v106, v154, v170\n"
      "v_pk_mul_f16 v107, v155, v171\n"
      "v_mfma_f32_16x16x32_f16 v[88:91], a[40:43], v[120:123], v[88:91]\n"
      "v_fma_f32 v192, |v84|, v52, v192\n"
      "v_fma_f32 v192, |v85|, v53, v192\n"
      "v_mfma_f32_16x16x32_f16 v[88:91], a[44:47], v[124:127], v[88:91]\n"
      "v_fma_f32 v192, |v86|, v54, v192\n"
      "v_fma_f32 v192, |v87|, v55, v192\n"
      "v_mfma_f32_16x16x32_f16 v[92:95], a[60:63], v[124:127], v[28:31]\n"
      "ds_read2_b32 v[228:229], v204 offset0:0 offset1:8\n"
      "ds_read2_b32 v[230:231], v205 offset0:0 offset1:8\n"
      "v_mfma_f32_16x16x32_f16 v[92:95], a[56:59], v[120:123], v[92:95]\n"
      "ds_read_b32 v224, v227 offset:37640\n"
      "ds_read2_b32 v[232:233], v206 offset0:0 offset1:8\n"
      "v_mfma_f32_16x16x32_f16 v[92:95], a[52:55], v[116:119], v[92:95]\n"
      "v_fma_f32 v192, |v88|, v56, v192\n"
      "v_fma_f32 v192, |v89|, v57, v192\n"
      "v_mfma_f32_16x16x32_f16 v[92:95], a[48:51], v[112:115], v[92:95]\n"
      "v_fma_f32 v192, |v90|, v58, v192\n"
      "v_fma_f32 v192, |v91|, v59, v192\n"
      "v_mfma_f32_16x16x32_f16 v[80:83], a[64:67], v[112:115], v[32:35]\n"
      "v_pk_mul_f16 v108, v156, v172\n"
      "v_pk_mul_f16 v109, v157, v173\n"
      "v_mfma_f32_16x16x32_f16 v[80:83], a[68:71], v[116:119], v[80:83]\n"
      "ds_read2_b32 v[234:235], v207 offset0:0 offset1:8\n"
      "ds_read2_b32 v[236:237], v208 offset0:0 offset1:8\n"
      "v_mfma_f32_16x16x32_f16 v[80:83], a[72:75], v[120:123], v[80:83]\n"
      "v_fma_f32 v192, |v92|, v60, v192\n"
      "v_fma_f32 v192, |v93|, v61, v192\n"
      "v_mfma_f32_16x16x32_f16 v[80:83], a[76:79], v[124:127], v[80:83]\n"
      "v_fma_f32 v192, |v94|, v62, v192\n"
      "v_fma_f32 v192, |v95|, v63, v192\n"
      "v_mfma_f32_16x16x32_f16 v[84:87], a[92:95], v[124:127], v[36:39]\n"
      "v_pk_mul_f16 v110, v158, v174\n"
      "v_pk_mul_f16 v111, v159, v175\n"
      "v_mfma_f32_16x16x32_f16 v[84:87], a[88:91], v[120:123], v[84:87]\n"
      "s_waitcnt lgkmcnt(3)\n"
      "ds_read2_b32 v[238:239], v209 offset0:0 offset1:8\n"
      "v_cmp_eq_u32 vcc, 1, v224\n"
      "v_mfma_f32_16x16x32_f16 v[84:87], a[84:87], v[116:119], v[84:87]\n"
      "v_fma_f32 v192, |v80|, v64, v192\n"
      "v_fma_f32 v192, |v81|, v65, v192\n"
      "v_mfma_f32_16x16x32_f16 v[84:87], a[80:83], v[112:115], v[84:87]\n"
      "s_and_b32 s44, vcc_lo, s54\n"
      "v_fma_f32 v192, |v82|, v66, v192\n"
      "v_fma_f32 v192, |v83|, v67, v192\n"
      "v_mfma_f32_16x16x32_f16 v[88:91], a[96:99], v[112:115], v[40:43]\n"
      "s_cmp_lg_u32 s44, s54\n"
      "ds_read_b128 v[128:131], v210 offset:0\n"
      "ds_read_b128 v[144:147], v211 offset:0\n"
      "v_mfma_f32_16x16x32_f16 v[88:91], a[100:103], v[116:119], v[88:91]\n"
      "s_cbranch_scc1 80f\n"
      "81:\n"
      "v_mfma_f32_16x16x32_f16 v[88:91], a[104:107], v[120:123], v[88:91]\n"
      "v_fma_f32 v192, |v84|, v68, v192\n"
      "v_fma_f32 v192, |v85|, v69, v192\n"
      "v_mfma_f32_16x16x32_f16 v[88:91], a[108:111], v[124:127], v[88:91]\n"
      "v_fma_f32 v192, |v86|, v70, v192\n"
      "v_fma_f32 v192, |v87|, v71, v192\n"
      "v_mfma_f32_16x16x32_f16 v[92:95], a[124:127], v[124:127], v[44:47]\n"
      "v_pk_mul_f16 v124, v252, v172\n"
      "v_pk_mul_f16 v125, v253, v173\n"
      "v_mfma_f32_16x16x32_f16 v[92:95], a[120:123], v[120:123], v[92:95]\n"
      "v_pk_mul_f16 v120, v248, v168\n"
      "v_pk_mul_f16 v121, v249, v169\n"
      "v_mfma_f32_16x16x32_f16 v[92:95], a[116:119], v[116:119], v[92:95]\n"
      "v_fma_f32 v192, |v88|, v72, v192\n"
      "v_fma_f32 v192, |v89|, v73, v192\n"
      "v_mfma_f32_16x16x32_f16 v[92:95], a[112:115], v[112:115], v[92:95]\n"
      "v_fma_f32 v192, |v90|, v74, v192\n"
      "v_fma_f32 v192, |v91|, v75, v192\n"
      "v_mfma_f32_16x16x32_f16 v[80:83], a[0:3], v[96:99], v[16:19]\n"
      "v_pk_mul_f16 v112, v240, v160\n"
      "v_pk_mul_f16 v113, v241, v161\n"
      "v_mfma_f32_16x16x32_f16 v[80:83], a[4:7], v[100:103], v[80:83]\n"
      "v_pk_mul_f16 v114, v242, v162\n"
      "v_pk_mul_f16 v115, v243, v163\n"
      "v_mfma_f32_16x16x32_f16 v[80:83], a[8:11], v[104:107], v[80:83]\n"
      "v_fma_f32 v192, |v92|, v76, v192\n"
      "v_fma_f32 v192, |v93|, v77, v192\n"
      "v_mfma_f32_16x16x32_f16 v[80:83], a[12:15], v[108:111], v[80:83]\n"
      "v_fma_f32 v192, |v94|, v78, v192\n"
      "v_fma_f32 v192, |v95|, v79, v192\n"
      "v_mfma_f32_16x16x32_f16 v[84:87], a[28:31], v[108:111], v[20:23]\n"
      "v_pk_mul_f16 v116, v244, v164\n"
      "v_pk_mul_f16 v117, v245, v165\n"
      "v_mfma_f32_16x16x32_f16 v[84:87], a[24:27], v[104:107], v[84:87]\n"
      "v_pk_mul_f16 v118, v246, v166\n"
      "v_pk_mul_f16 v119, v247, v167\n"
      "v_mfma_f32_16x16x32_f16 v[84:87], a[20:23], v[100:103], v[84:87]\n"
      "v_fma_f32 v193, |v80|, v48, 4.0\n"
      "v_fma_f32 v193, |v81|, v49, v193\n"
      "v_mfma_f32_16x16x32_f16 v[84:87], a[16:19], v[96:99], v[84:87]\n"
      "v_fma_f32 v193, |v82|, v50, v193\n"
      "v_fma_f32 v193, |v83|, v51, v193\n"
      "v_mfma_f32_16x16x32_f16 v[88:91], a[32:35], v[96:99], v[24:27]\n"
      "v_pk_mul_f16 v122, v250, v170\n"
      "v_pk_mul_f16 v123, v251, v171\n"
      "v_mfma_f32_16x16x32_f16 v[88:91], a[36:39], v[100:103], v[88:91]\n"
      "v_pk_mul_f16 v126, v254, v174\n"
      "v_pk_mul_f16 v127, v255, v175\n"
      "v_mfma_f32_16x16x32_f16 v[88:91], a[40:43], v[104:107], v[88:91]\n"
      "v_fma_f32 v193, |v84|, v52, v193\n"
      "v_fma_f32 v193, |v85|, v53, v193\n"
      "v_mfma_f32_16x16x32_f16 v[88:91], a[44:47], v[108:111], v[88:91]\n"
      "v_fma_f32 v193, |v86|, v54, v193\n"
      "v_fma_f32 v193, |v87|, v55, v193\n"
      "v_mfma_f32_16x16x32_f16 v[92:95], a[60:63], v[108:111], v[28:31]\n"
      "ds_read_b128 v[160:163], v10 offset:0\n"
      "ds_read_b128 v[240:243], v11 offset:0\n"
      "v_mfma_f32_16x16x32_f16 v[92:95], a[56:59], v[104:107], v[92:95]\n"
      "ds_read_b128 v[164:167], v10 offset:64\n"
      "ds_read_b128 v[244:247], v11 offset:64\n"
      "v_mfma_f32_16x16x32_f16 v[92:95], a[52:55], v[100:103], v[92:95]\n"
      "v_fma_f32 v193, |v88|, v56, v193\n"
      "v_fma_f32 v193, |v89|, v57, v193\n"
      "v_mfma_f32_16x16x32_f16 v[92:95], a[48:51], v[96:99], v[92:95]\n"
      "v_fma_f32 v193, |v90|, v58, v193\n"
      "v_fma_f32 v193, |v91|, v59, v193\n"
      "v_mfma_f32_16x16x32_f16 v[80:83], a[64:67], v[96:99], v[32:35]\n"
      "ds_read_b128 v[168:171], v10 offset:128\n"
      "ds_read_b128 v[248:251], v11 offset:128\n"
      "v_mfma_f32_16x16x32_f16 v[80:83], a[68:71], v[100:103], v[80:83]\n"
      "ds_read_b128 v[172:175], v10 offset:192\n"
      "ds_read_b128 v[252:255], v11 offset:192\n"
      "v_mfma_f32_16x16x32_f16 v[80:83], a[72:75], v[104:107], v[80:83]\n"
      "v_fma_f32 v193, |v92|, v60, v193\n"
      "v_fma_f32 v193, |v93|, v61, v193\n"
      "v_mfma_f32_16x16x32_f16 v[80:83], a[76:79], v[108:111], v[80:83]\n"
      "v_fma_f32 v193, |v94|, v62, v193\n"
      "v_fma_f32 v193, |v95|, v63, v193\n"
      "v_mfma_f32_16x16x32_f16 v[84:87], a[92:95], v[108:111], v[36:39]\n"
      "s_waitcnt lgkmcnt(6)\n"
      "v_pk_mul_f16 v160, v160, v240\n"
      "v_pk_mul_f16 v161, v161, v241\n"
      "v_mfma_f32_16x16x32_f16 v[84:87], a[88:91], v[104:107], v[84:87]\n"
      "v_pk_mul_f16 v162, v162, v242\n"
      "v_pk_mul_f16 v163, v163, v243\n"
      "v_mfma_f32_16x16x32_f16 v[84:87], a[84:87], v[100:103], v[84:87]\n"
      "v_fma_f32 v193, |v80|, v64, v193\n"
      "v_fma_f32 v193, |v81|, v65, v193\n"
      "v_mfma_f32_16x16x32_f16 v[84:87], a[80:83], v[96:99], v[84:87]\n"
      "v_fma_f32 v193, |v82|, v66, v193\n"
      "v_fma_f32 v193, |v83|, v67, v193\n"
      "v_mfma_f32_16x16x32_f16 v[88:91], a[96:99], v[96:99], v[40:43]\n"
      "s_waitcnt lgkmcnt(4)\n"
      "v_pk_mul_f16 v164, v164, v244\n"
      "v_pk_mul_f16 v165, v165, v245\n"
      "v_mfma_f32_16x16x32_f16 v[88:91], a[100:103], v[100:103], v[88:91]\n"
      "43:\n"
      "s_cbranch_execz 44b\n"
      "v_pk_mul_f16 v166, v166, v246\n"
      "v_mfma_f32_16x16x32_f16 v[88:91], a[104:107], v[104:107], v[88:91]\n"
      "v_fma_f32 v193, |v84|, v68, v193\n"
      "v_fma_f32 v193, |v85|, v69, v193\n"
      "v_mfma_f32_16x16x32_f16 v[88:91], a[108:111], v[108:111], v[88:91]\n"
      "v_fma_f32 v193, |v86|, v70, v193\n"
      "v_fma_f32 v193, |v87|, v71, v193\n"
      "v_mfma_f32_16x16x32_f16 v[92:95], a[124:127], v[108:111], v[44:47]\n"
      "s_waitcnt lgkmcnt(2)\n"
      "v_pk_mul_f16 v167, v167, v247\n"
      "v_pk_mul_f16 v168, v168, v248\n"
      "v_mfma_f32_16x16x32_f16 v[92:95], a[120:123], v[104:107], v[92:95]\n"
      "v_pk_mul_f16 v169, v169, v249\n"
      "v_pk_mul_f16 v170, v170, v250\n"
      "v_mfma_f32_16x16x32_f16 v[92:95], a[116:119], v[100:103], v[92:95]\n"
      "v_fma_f32 v193, |v88|, v72, v193\n"
      "v_fma_f32 v193, |v89|, v73, v193\n"
      "v_mfma_f32_16x16x32_f16 v[92:95], a[112:115], v[96:99], v[92:95]\n"
      "v_fma_f32 v193, |v90|, v74, v193\n"
      "v_fma_f32 v193, |v91|, v75, v193\n"
      "v_mfma_f32_16x16x32_f16 v[80:83], a[0:3], v[112:115], v[16:19]\n"
      "ds_read_b128 v[132:135], v210 offset:64\n"
      "ds_read_b128 v[148:151], v211 offset:64\n"
      "v_mfma_f32_16x16x32_f16 v[80:83], a[4:7], v[116:119], v[80:83]\n"
      "v_pk_mul_f16 v128, v128, v144\n"
      "v_pk_mul_f16 v171, v171, v251\n"
      "v_mfma_f32_16x16x32_f16 v[80:83], a[8:11], v[120:123], v[80:83]\n"
      "v_fma_f32 v193, |v92|, v76, v193\n"
      "v_fma_f32 v193, |v93|, v77, v193\n"
      "v_mfma_f32_16x16x32_f16 v[80:83], a[12:15], v[124:127], v[80:83]\n"
      "v_fma_f32 v193, |v94|, v78, v193\n"
      "v_fma_f32 v193, |v95|, v79, v193\n"
      "v_mfma_f32_16x16x32_f16 v[84:87], a[28:31], v[124:127], v[20:23]\n"
      "v_pk_mul_f16 v129, v129, v145\n"
      "v_pk_mul_f16 v130, v130, v146\n"
      "v_mfma_f32_16x16x32_f16 v[84:87], a[24:27], v[120:123], v[84:87]\n"
      "s_waitcnt lgkmcnt(2)\n"
      "v_pk_mul_f16 v131, v131, v147\n"
      "v_pk_mul_f16 v172, v172, v252\n"
      "v_mfma_f32_16x16x32_f16 v[84:87], a[20:23], v[116:119], v[84:87]\n"
      "v_fma_f32 v190, |v80|, v48, 4.0\n"
      "v_fma_f32 v190, |v81|, v49, v190\n"
      "v_mfma_f32_16x16x32_f16 v[84:87], a[16:19], v[112:115], v[84:87]\n"
      "v_fma_f32 v190, |v82|, v50, v190\n"
      "v_fma_f32 v190, |v83|, v51, v190\n"
      "v_mfma_f32_16x16x32_f16 v[88:91], a[32:35], v[112:115], v[24:27]\n"
      "v_pk_mul_f16 v173, v173, v253\n"
      "v_pk_mul_f16 v174, v174, v254\n"
      "v_mfma_f32_16x16x32_f16 v[88:91], a[36:39], v[116:119], v[88:91]\n"
      "ds_read_b128 v[136:139], v210 offset:128\n"
      "ds_read_b128 v[152:155], v211 offset:128\n"
      "v_mfma_f32_16x16x32_f16 v[88:91], a[40:43], v[120:123], v[88:91]\n"
      "v_fma_f32 v190, |v84|, v52, v190\n"
      "v_fma_f32 v190, |v85|, v53, v190\n"
      "v_mfma_f32_16x16x32_f16 v[88:91], a[44:47], v[124:127], v[88:91]\n"
      "v_fma_f32 v190, |v86|, v54, v190\n"
      "v_fma_f32 v190, |v87|, v55, v190\n"
      "v_mfma_f32_16x16x32_f16 v[92:95], a[60:63], v[124:127], v[28:31]\n"
      "s_waitcnt lgkmcnt(2)\n"
      "v_pk_mul_f16 v132, v132, v148\n"
      "v_pk_mul_f16 v175, v175, v255\n"
      "v_mfma_f32_16x16x32_f16 v[92:95], a[56:59], v[120:123], v[92:95]\n"
      "v_pk_mul_f16 v133, v133, v149\n"
      "v_pk_mul_f16 v134, v134, v150\n"
      "v_mfma_f32_16x16x32_f16 v[92:95], a[52:55], v[116:119], v[92:95]\n"
      "v_fma_f32 v190, |v88|, v56, v190\n"
      "v_fma_f32 v190, |v89|, v57, v190\n"
      "v_mfma_f32_16x16x32_f16 v[92:95], a[48:51], v[112:115], v[92:95]\n"
      "v_fma_f32 v190, |v90|, v58, v190\n"
      "v_fma_f32 v190, |v91|, v59, v190\n"
      "v_mfma_f32_16x16x32_f16 v[80:83], a[64:67], v[112:115], v[32:35]\n"
      "v_fmac_f32 v189, v200, v228\n"
      "v_fmac_f32 v194, v200, v230\n"
      "v_mfma_f32_16x16x32_f16 v[80:83], a[68:71], v[116:119], v[80:83]\n"
      "v_permlane32_swap_b32 v198, v192\n"
      "v_mfma_f32_16x16x32_f16 v[80:83], a[72:75], v[120:123], v[80:83]\n"
      "v_fma_f32 v190, |v92|, v60, v190\n"
      "v_fma_f32 v190, |v93|, v61, v190\n"
      "v_mfma_f32_16x16x32_f16 v[80:83], a[76:79], v[124:127], v[80:83]\n"
      "v_fma_f32 v190, |v94|, v62, v190\n"
      "v_fma_f32 v190, |v95|, v63, v190\n"
      "v_mfma_f32_16x16x32_f16 v[84:87], a[92:95], v[124:127], v[36:39]\n"
      "s_waitcnt lgkmcnt(0)\n"
      "v_pk_mul_f16 v135, v135, v151\n"
      "v_pk_mul_f16 v136, v136, v152\n"
      "v_mfma_f32_16x16x32_f16 v[84:87], a[88:91], v[120:123], v[84:87]\n"
      "v_fmac_f32 v195, v200, v232\n"
      "v_max3_f32 v219, v189, v194, v195\n"
      "v_mfma_f32_16x16x32_f16 v[84:87], a[84:87], v[116:119], v[84:87]\n"
      "v_fma_f32 v190, |v80|, v64, v190\n"
      "v_fma_f32 v190, |v81|, v65, v190\n"
      "v_mfma_f32_16x16x32_f16 v[84:87], a[80:83], v[112:115], v[84:87]\n"
      "v_fma_f32 v190, |v82|, v66, v190\n"
      "v_fma_f32 v190, |v83|, v67, v190\n"
      "v_mfma_f32_16x16x32_f16 v[88:91], a[96:99], v[112:115], v[40:43]\n"
      "ds_read_b128 v[140:143], v210 offset:192\n"
      "v_min3_f32 v226, v189, v194, v195\n"
      "v_mfma_f32_16x16x32_f16 v[88:91], a[100:103], v[116:119], v[88:91]\n"
      "ds_read_b128 v[156:159], v211 offset:192\n"
      "ds_read_b128 a[204:207], v215 offset:11968\n"
      "v_mfma_f32_16x16x32_f16 v[88:91], a[104:107], v[120:123], v[88:91]\n"
      "v_fma_f32 v190, |v84|, v68, v190\n"
      "v_fma_f32 v190, |v85|, v69, v190\n"
      "v_mfma_f32_16x16x32_f16 v[88:91], a[108:111], v[124:127], v[88:91]\n"
      "v_fma_f32 v190, |v86|, v70, v190\n"
      "v_fma_f32 v190, |v87|, v71, v190\n"
      "v_mfma_f32_16x16x32_f16 v[92:95], a[124:127], v[124:127], v[44:47]\n"
      "v_sub_f32 v189, v189, v219\n"
      "v_sub_f32 v194, v194, v219\n"
      "v_mfma_f32_16x16x32_f16 v[92:95], a[120:123], v[120:123], v[92:95]\n"
      "ds_read_b128 a[208:211], v215 offset:12032\n"
      "v_pk_mul_f16 v137, v137, v153\n"
      "v_mfma_f32_16x16x32_f16 v[92:95], a[116:119], v[116:119], v[92:95]\n"
      "v_fma_f32 v190, |v88|, v72, v190\n"
      "v_fma_f32 v190, |v89|, v73, v190\n"
      "v_mfma_f32_16x16x32_f16 v[92:95], a[112:115], v[112:115], v[92:95]\n"
      "v_fma_f32 v190, |v90|, v74, v190\n"
      "v_fma_f32 v190, |v91|, v75, v190\n"
      "v_mfma_f32_16x16x32_f16 v[80:83], a[0:3], v[160:163], v[16:19]\n"
      "ds_read_b128 a[212:215], v215 offset:12096\n"
      "v_sub_f32 v195, v195, v219\n"
      "v_mfma_f32_16x16x32_f16 v[80:83], a[4:7], v[164:167], v[80:83]\n"
      "v_exp_f32 v189, v189\n"
      "v_mfma_f32_16x16x32_f16 v[80:83], a[8:11], v[168:171], v[80:83]\n"
      "v_exp_f32 v194, v194\n"
      "v_mfma_f32_16x16x32_f16 v[80:83], a[12:15], v[172:175], v[80:83]\n"
      "v_exp_f32 v195, v195\n"
      "v_mfma_f32_16x16x32_f16 v[84:87], a[28:31], v[172:175], v[20:23]\n"
      "v_pk_mul_f16 v138, v138, v154\n"
      "v_pk_mul_f16 v139, v139, v155\n"
      "v_mfma_f32_16x16x32_f16 v[84:87], a[24:27], v[168:171], v[84:87]\n"
      "v_fma_f32 v190, |v92|, v76, v190\n"
      "v_fma_f32 v190, |v93|, v77, v190\n"
      "v_mfma_f32_16x16x32_f16 v[84:87], a[20:23], v[164:167], v[84:87]\n"
      "v_fma_f32 v190, |v94|, v78, v190\n"
      "v_fma_f32 v190, |v95|, v79, v190\n"
      "v_mfma_f32_16x16x32_f16 v[84:87], a[16:19], v[160:163], v[84:87]\n"
      "v_mul_f32 v221, v189, v229\n"
      "v_add_f32 v198, v198, v192\n"
      "v_mfma_f32_16x16x32_f16 v[88:91], a[32:35], v[160:163], v[24:27]\n"
      "v_permlane32_swap_b32 v193, v190\n"
      "v_mfma_f32_16x16x32_f16 v[88:91], a[36:39], v[164:167], v[88:91]\n"
      "v_add_f32 v220, v189, v194\n"
      "v_add_f32 v193, v193, v190\n"
      "v_mfma_f32_16x16x32_f16 v[88:91], a[40:43], v[168:171], v[88:91]\n"
      "v_permlane16_swap_b32 v188, v198\n"
      "v_mfma_f32_16x16x32_f16 v[88:91], a[44:47], v[172:175], v[88:91]\n"
      "v_fma_f32 v199, |v80|, v48, 2.0\n"
      "v_fma_f32 v199, |v81|, v49, v199\n"
      "v_mfma_f32_16x16x32_f16 v[92:95], a[60:63], v[172:175], v[28:31]\n"
      "s_waitcnt lgkmcnt(3)\n"
      "v_pk_mul_f16 v140, v140, v156\n"
      "v_fmac_f32 v221, v194, v231\n"
      "v_mfma_f32_16x16x32_f16 v[92:95], a[56:59], v[168:171], v[92:95]\n"
      "v_fma_f32 v199, |v82|, v50, v199\n"
      "v_fma_f32 v199, |v83|, v51, v199\n"
      "v_mfma_f32_16x16x32_f16 v[92:95], a[52:55], v[164:167], v[92:95]\n"
      "v_pk_mul_f16 v141, v141, v157\n"
      "v_add_f32 v220, v220, v195\n"
      "v_mfma_f32_16x16x32_f16 v[92:95], a[48:51], v[160:163], v[92:95]\n"
      "v_add_f32 v188, v188, v198\n"
      "v_pk_mul_f16 v142, v142, v158\n"
      "v_mfma_f32_16x16x32_f16 v[80:83], a[0:3], v[128:131], v[16:19]\n"
      "v_fmac_f32 v221, v195, v233\n"
      "v_fmac_f32 v188, v200, v234\n"
      "v_mfma_f32_16x16x32_f16 v[80:83], a[4:7], v[132:135], v[80:83]\n"
      "v_pk_mul_f16 v143, v143, v159\n"
      "v_fma_f32 v199, |v84|, v52, v199\n"
      "v_mfma_f32_16x16x32_f16 v[80:83], a[8:11], v[136:139], v[80:83]\n"
      "v_fma_f32 v199, |v85|, v53, v199\n"
      "v_fma_f32 v199, |v86|, v54, v199\n"
      "v_mfma_f32_16x16x32_f16 v[80:83], a[12:15], v[140:143], v[80:83]\n"
      "v_fma_f32 v199, |v87|, v55, v199\n"
      "v_fma_f32 v199, |v88|, v56, v199\n"
      "v_mfma_f32_16x16x32_f16 v[84:87], a[28:31], v[140:143], v[20:23]\n"
      "v_fma_f32 v199, |v89|, v57, v199\n"
      "v_fma_f32 v199, |v90|, v58, v199\n"
      "v_mfma_f32_16x16x32_f16 v[84:87], a[24:27], v[136:139], v[84:87]\n"
      "v_fma_f32 v199, |v91|, v59, v199\n"
      "v_fma_f32 v199, |v92|, v60, v199\n"
      "v_mfma_f32_16x16x32_f16 v[84:87], a[20:23], v[132:135], v[84:87]\n"
      "ds_read_b128 v[112:115], v216 offset:0\n"
      "ds_read_b128 v[144:147], v217 offset:0\n"
      "v_mfma_f32_16x16x32_f16 v[84:87], a[16:19], v[128:131], v[84:87]\n"
      "v_fma_f32 v199, |v93|, v61, v199\n"
      "v_fma_f32 v199, |v94|, v62, v199\n"
      "v_mfma_f32_16x16x32_f16 v[180:183], a[200:203], v[160:163], 0\n"
      "v_fma_f32 v199, |v95|, v63, v199\n"
      "v_fma_f32 v191, |v80|, v48, 4.0\n"
      "v_mfma_f32_16x16x32_f16 v[88:91], a[32:35], v[128:131], v[24:27]\n"
      "ds_write_b32 v1, v199 offset:12992\n"
      "v_mfma_f32_16x16x32_f16 v[88:91], a[36:39], v[132:135], v[88:91]\n"
      "ds_read_b128 v[116:119], v216 offset:64\n"
      "ds_read_b128 v[148:151], v217 offset:64\n"
      "v_mfma_f32_16x16x32_f16 v[88:91], a[40:43], v[136:139], v[88:91]\n"
      "v_permlane16_swap_b32 v193, v7\n"
      "v_mfma_f32_16x16x32_f16 v[88:91], a[44:47], v[140:143], v[88:91]\n"
      "v_fma_f32 v191, |v81|, v49, v191\n"
      "v_fma_f32 v191, |v82|, v50, v191\n"
      "v_mfma_f32_16x16x32_f16 v[92:95], a[60:63], v[140:143], v[28:31]\n"
      "v_fma_f32 v191, |v83|, v51, v191\n"
      "v_fma_f32 v191, |v84|, v52, v191\n"
      "v_mfma_f32_16x16x32_f16 v[92:95], a[56:59], v[136:139], v[92:95]\n"
      "s_waitcnt lgkmcnt(3)\n"
      "v_pk_mul_f16 v112, v112, v144\n"
      "v_pk_mul_f16 v113, v113, v145\n"
      "v_mfma_f32_16x16x32_f16 v[92:95], a[52:55], v[132:135], v[92:95]\n"
      "v_pk_mul_f16 v114, v114, v146\n"
      "v_pk_mul_f16 v115, v115, v147\n"
      "v_mfma_f32_16x16x32_f16 v[92:95], a[48:51], v[128:131], v[92:95]\n"
      "ds_read_b128 v[120:123], v216 offset:128\n"
      "ds_read_b128 v[152:155], v217 offset:128\n"
      "v_mfma_f32_16x16x32_f16 v[180:183], a[204:207], v[164:167], v[180:183]\n"
      "v_fma_f32 v191, |v85|, v53, v191\n"
      "v_fma_f32 v191, |v86|, v54, v191\n"
      "v_mfma_f32_16x16x32_f16 v[80:83], a[64:67], v[128:131], v[32:35]\n"
      "v_fma_f32 v191, |v87|, v55, v191\n"
      "v_fma_f32 v191, |v88|, v56, v191\n"
      "v_mfma_f32_16x16x32_f16 v[80:83], a[68:71], v[132:135], v[80:83]\n"
      "s_waitcnt lgkmcnt(2)\n"
      "v_pk_mul_f16 v116, v116, v148\n"
      "v_pk_mul_f16 v117, v117, v149\n"
      "v_mfma_f32_16x16x32_f16 v[80:83], a[72:75], v[136:139], v[80:83]\n"
      "v_fma_f32 v191, |v89|, v57, v191\n"
      "v_fma_f32 v191, |v90|, v58, v191\n"
      "v_mfma_f32_16x16x32_f16 v[80:83], a[76:79], v[140:143], v[80:83]\n"
      "v_pk_mul_f16 v118, v118, v150\n"
      "v_pk_mul_f16 v119, v119, v151\n"
      "v_mfma_f32_16x16x32_f16 v[84:87], a[92:95], v[140:143], v[36:39]\n"
      "ds_read_b128 v[124:127], v216 offset:192\n"
      "ds_read_b128 v[156:159], v217 offset:192\n"
      "v_mfma_f32_16x16x32_f16 v[84:87], a[88:91], v[136:139], v[84:87]\n"
      "v_fma_f32 v191, |v91|, v59, v191\n"
      "v_fma_f32 v191, |v92|, v60, v191\n"
      "v_mfma_f32_16x16x32_f16 v[84:87], a[84:87], v[132:135], v[84:87]\n"
      "s_waitcnt lgkmcnt(2)\n"
      "v_pk_mul_f16 v120, v120, v152\n"
      "v_pk_mul_f16 v121, v121, v153\n"
      "v_mfma_f32_16x16x32_f16 v[84:87], a[80:83], v[128:131], v[84:87]\n"
      "v_fma_f32 v191, |v93|, v61, v191\n"
      "v_fma_f32 v191, |v94|, v62, v191\n"
      "v_mfma_f32_16x16x32_f16 v[88:91], a[96:99], v[128:131], v[40:43]\n"
      "v_fma_f32 v191, |v95|, v63, v191\n"
      "v_fma_f32 v191, |v80|, v64, v191\n"
      "v_mfma_f32_16x16x32_f16 v[88:91], a[100:103], v[132:135], v[88:91]\n"
      "v_pk_mul_f16 v122, v122, v154\n"
      "v_pk_mul_f16 v123, v123, v155\n"
      "v_mfma_f32_16x16x32_f16 v[88:91], a[104:107], v[136:139], v[88:91]\n"
      "v_fma_f32 v191, |v81|, v65, v191\n"
      "v_fma_f32 v191, |v82|, v66, v191\n"
      "v_mfma_f32_16x16x32_f16 v[88:91], a[108:111], v[140:143], v[88:91]\n"
      "v_fma_f32 v191, |v83|, v67, v191\n"
      "v_fma_f32 v191, |v84|, v68, v191\n"
      "v_mfma_f32_16x16x32_f16 v[92:95], a[124:127], v[140:143], v[44:47]\n"
      "s_waitcnt lgkmcnt(0)\n"
      "v_pk_mul_f16 v124, v124, v156\n"
      "v_pk_mul_f16 v125, v125, v157\n"
      "v_mfma_f32_16x16x32_f16 v[92:95], a[120:123], v[136:139], v[92:95]\n"
      "v_pk_mul_f16 v126, v126, v158\n"
      "v_pk_mul_f16 v127, v127, v159\n"
      "v_mfma_f32_16x16x32_f16 v[92:95], a[116:119], v[132:135], v[92:95]\n"
      "v_fma_f32 v191, |v85|, v69, v191\n"
      "v_fma_f32 v191, |v86|, v70, v191\n"
      "v_mfma_f32_16x16x32_f16 v[92:95], a[112:115], v[128:131], v[92:95]\n"
      "v_fma_f32 v191, |v87|, v71, v191\n"
      "v_fma_f32 v191, |v88|, v72, v191\n"
      "v_mfma_f32_16x16x32_f16 v[80:83], a[0:3], v[112:115], v[16:19]\n"
      "v_fma_f32 v191, |v89|, v73, v191\n"
      "v_fma_f32 v191, |v90|, v74, v191\n"
      "v_mfma_f32_16x16x32_f16 v[80:83], a[4:7], v[116:119], v[80:83]\n"
      "v_fma_f32 v191, |v91|, v75, v191\n"
      "v_add_f32 v193, v193, v7\n"
      "v_mfma_f32_16x16x32_f16 v[80:83], a[8:11], v[120:123], v[80:83]\n"
      "v_fma_f32 v191, |v92|, v76, v191\n"
      "v_fma_f32 v191, |v93|, v77, v191\n"
      "v_mfma_f32_16x16x32_f16 v[80:83], a[12:15], v[124:127], v[80:83]\n"
      "v_fma_f32 v191, |v94|, v78, v191\n"
      "v_fma_f32 v191, |v95|, v79, v191\n"
      "v_mfma_f32_16x16x32_f16 v[84:87], a[28:31], v[124:127], v[20:23]\n"
      "ds_read_b32 v224, v12 offset:12992\n"
      "v_fmac_f32 v193, v200, v236\n"
      "v_mfma_f32_16x16x32_f16 v[84:87], a[24:27], v[120:123], v[84:87]\n"
      "v_max3_f32 v222, v219, v188, v193\n"
      "v_min3_f32 v226, v226, v188, v193\n"
      "v_mfma_f32_16x16x32_f16 v[84:87], a[20:23], v[116:119], v[84:87]\n"
      "v_fma_f32 v196, |v80|, v48, 4.0\n"
      "v_fma_f32 v196, |v81|, v49, v196\n"
      "v_mfma_f32_16x16x32_f16 v[84:87], a[16:19], v[112:115], v[84:87]\n"
      "v_fma_f32 v196, |v82|, v50, v196\n"
      "v_fma_f32 v196, |v83|, v51, v196\n"
      "v_mfma_f32_16x16x32_f16 v[88:91], a[32:35], v[112:115], v[24:27]\n"
      "v_sub_f32 v219, v219, v222\n"
      "v_sub_f32 v188, v188, v222\n"
      "v_mfma_f32_16x16x32_f16 v[88:91], a[36:39], v[116:119], v[88:91]\n"
      "v_sub_f32 v193, v193, v222\n"
      "v_mfma_f32_16x16x32_f16 v[88:91], a[40:43], v[120:123], v[88:91]\n"
      "v_exp_f32 v219, v219\n"
      "v_mfma_f32_16x16x32_f16 v[88:91], a[44:47], v[124:127], v[88:91]\n"
      "v_exp_f32 v188, v188\n"
      "v_mfma_f32_16x16x32_f16 v[92:95], a[60:63], v[124:127], v[28:31]\n"
      "v_exp_f32 v193, v193\n"
      "v_mfma_f32_16x16x32_f16 v[92:95], a[56:59], v[120:123], v[92:95]\n"
      "v_fma_f32 v196, |v84|, v52, v196\n"
      "v_fma_f32 v196, |v85|, v53, v196\n"
      "v_mfma_f32_16x16x32_f16 v[92:95], a[52:55], v[116:119], v[92:95]\n"
      "v_fma_f32 v196, |v86|, v54, v196\n"
      "v_fma_f32 v196, |v87|, v55, v196\n"
      "v_mfma_f32_16x16x32_f16 v[92:95], a[48:51], v[112:115], v[92:95]\n"
      "v_fma_f32 v196, |v88|, v56, v196\n"
      "v_fma_f32 v196, |v89|, v57, v196\n"
      "v_mfma_f32_16x16x32_f16 v[80:83], a[64:67], v[112:115], v[32:35]\n"
      "v_fma_f32 v196, |v90|, v58, v196\n"
      "v_fma_f32 v196, |v91|, v59, v196\n"
      "v_mfma_f32_16x16x32_f16 v[80:83], a[68:71], v[116:119], v[80:83]\n"
      "s_waitcnt lgkmcnt(0)\n"
      "v_cmp_eq_u32 vcc, s37, v224\n"
      "v_mul_f32 v221, v221, v219\n"
      "v_mfma_f32_16x16x32_f16 v[80:83], a[72:75], v[120:123], v[80:83]\n"
      "v_fma_f32 v196, |v92|, v60, v196\n"
      "v_fma_f32 v196, |v93|, v61, v196\n"
      "v_mfma_f32_16x16x32_f16 v[80:83], a[76:79], v[124:127], v[80:83]\n"
      "s_or_b32 vcc_lo, vcc_lo, s36\n"
      "v_fma_f32 v220, v220, v219, v188\n"
      "v_fma_f32 v196, |v94|, v62, v196\n"
      "v_mfma_f32_16x16x32_f16 v[84:87], a[92:95], v[124:127], v[36:39]\n"
      "s_cmp_lg_u64 vcc, 0\n"
      "v_fma_f32 v196, |v95|, v63, v196\n"
      "v_fmac_f32 v221, v188, v235\n"
      "v_mfma_f32_16x16x32_f16 v[84:87], a[88:91], v[120:123], v[84:87]\n"
      "s_cbranch_scc1 82f\n"
      "83:\n"
      "v_mfma_f32_16x16x32_f16 v[84:87], a[84:87], v[116:119], v[84:87]\n"
      "v_fma_f32 v196, |v80|, v64, v196\n"
      "v_fma_f32 v196, |v81|, v65, v196\n"
      "v_mfma_f32_16x16x32_f16 v[84:87], a[80:83], v[112:115], v[84:87]\n"
      "v_fma_f32 v196, |v82|, v66, v196\n"
      "v_fma_f32 v196, |v83|, v67, v196\n"
      "v_mfma_f32_16x16x32_f16 v[88:91], a[96:99], v[112:115], v[40:43]\n"
      "v_add_f32 v199, v199, v224\n"
      "v_add_f32 v220, v220, v193\n"
      "v_mfma_f32_16x16x32_f16 v[88:91], a[100:103], v[116:119], v[88:91]\n"
      "v_cndmask_b32 v199, v8, v199, s[100:101]\n"
      "v_fmac_f32 v221, v193, v237\n"
      "v_mfma_f32_16x16x32_f16 v[88:91], a[104:107], v[120:123], v[88:91]\n"
      "v_fma_f32 v196, |v84|, v68, v196\n"
      "v_fma_f32 v196, |v85|, v69, v196\n"
      "v_mfma_f32_16x16x32_f16 v[88:91], a[108:111], v[124:127], v[88:91]\n"
      "v_fma_f32 v196, |v86|, v70, v196\n"
      "v_fma_f32 v196, |v87|, v71, v196\n"
      "v_mfma_f32_16x16x32_f16 v[92:95], a[124:127], v[124:127], v[44:47]\n"
      "v_permlane32_swap_b32 v199, v191\n"
      "v_mfma_f32_16x16x32_f16 v[92:95], a[120:123], v[120:123], v[92:95]\n"
      "v_fma_f32 v220, v226, 0, v220\n"
      "v_add_f32 v199, v199, v191\n"
      "v_mfma_f32_16x16x32_f16 v[92:95], a[116:119], v[116:119], v[92:95]\n"
      "v_fma_f32 v196, |v88|, v72, v196\n"
      "v_fma_f32 v196, |v89|, v73, v196\n"
      "v_mfma_f32_16x16x32_f16 v[92:95], a[112:115], v[112:115], v[92:95]\n"
      "v_fma_f32 v196, |v90|, v74, v196\n"
      "v_fma_f32 v196, |v91|, v75, v196\n"
      "v_mfma_f32_16x16x32_f16 v[180:183], a[208:211], v[168:171], v[180:183]\n"
      "v_mfma_f32_16x16x32_f16 v[180:183], a[212:215], v[172:175], v[180:183]\n"
      "s_nop 3\n"
      "v_fma_f32 v196, |v92|, v76, v196\n"
      "v_fma_f32 v196, |v93|, v77, v196\n"
      "v_fma_f32 v196, |v94|, v78, v196\n"
      "v_fma_f32 v196, |v95|, v79, v196\n"
      "v_mov_b32_dpp v238, v180 quad_perm:[0,1,2,3] row_mask:0x1 bank_mask:0xf\n"
      "v_mov_b32_dpp v239, v181 quad_perm:[0,1,2,3] row_mask:0x1 bank_mask:0xf\n"
      "v_permlane32_swap_b32 v196, v8\n"
      "v_add_f32 v196, v196, v8\n"
      "s_movk_i32 s45, 0x207\n"
      "s_nop 0\n"
      "v_permlane16_swap_b32 v199, v196\n"
      "v_add_f32 v199, v199, v196\n"
      "v_fmac_f32 v199, v200, v238\n"
      "v_max_f32 v225, v222, v199\n"
      "v_fma_f32 v220, v199, 0, v220\n"
      "s_nop 0\n"
      "v_max_f32_dpp v223, v225, v225 row_ror:4 row_mask:0xf bank_mask:0xf\n"
      "v_max_f32_dpp v223, v225, v223 row_ror:8 row_mask:0xf bank_mask:0xf\n"
      "v_max_f32_dpp v223, v225, v223 row_ror:12 row_mask:0xf bank_mask:0xf\n"
      "s_nop 1\n"
      "v_max_f32_dpp v225, v223, v223 row_ror:1 row_mask:0xf bank_mask:0xf\n"
      "v_max_f32_dpp v225, v223, v225 row_ror:2 row_mask:0xf bank_mask:0xf\n"
      "v_max_f32_dpp v225, v223, v225 row_ror:3 row_mask:0xf bank_mask:0xf\n"
      "s_nop 1\n"
      "v_max_f32_dpp v225, v225, v225 row_bcast:15 row_mask:0xa bank_mask:0xf\n"
      "s_nop 1\n"
      "v_max_f32_dpp v225, v225, v225 row_bcast:31 row_mask:0xc bank_mask:0xf\n"
      "s_nop 0\n"
      "v_readlane_b32 s44, v225, 63\n"
      "s_nop 1\n"
      "v_subrev_f32 v223, s44, v222\n"
      "v_subrev_f32 v199, s44, v199\n"
      "v_exp_f32 v223, v223\n"
      "v_exp_f32 v199, v199\n"
      "v_mul_f32 v221, v221, v223\n"
      "v_fma_f32 v220, v220, v223, v199\n"
      "v_fmac_f32 v221, v199, v239\n"
      "s_nop 0\n"
      "v_add_f32_dpp v220, v220, v220 row_ror:8 row_mask:0xf bank_mask:0xf\n"
      "v_add_f32_dpp v221, v221, v221 row_ror:8 row_mask:0xf bank_mask:0xf\n"
      "s_nop 0\n"
      "v_add_f32_dpp v220, v220, v220 row_ror:4 row_mask:0xf bank_mask:0xf\n"
      "v_add_f32_dpp v221, v221, v221 row_ror:4 row_mask:0xf bank_mask:0xf\n"
      "s_nop 0\n"
      "v_add_f32_dpp v220, v220, v220 row_ror:2 row_mask:0xf bank_mask:0xf\n"
      "v_add_f32_dpp v221, v221, v221 row_ror:2 row_mask:0xf bank_mask:0xf\n"
      "s_nop 0\n"
      "v_add_f32_dpp v220, v220, v220 row_ror:1 row_mask:0xf bank_mask:0xf\n"
      "v_add_f32_dpp v221, v221, v221 row_ror:1 row_mask:0xf bank_mask:0xf\n"
      "s_nop 0\n"
      "v_add_f32_dpp v220, v220, v220 row_bcast:15 row_mask:0xa bank_mask:0xf\n"
      "v_add_f32_dpp v221, v221, v221 row_bcast:15 row_mask:0xa bank_mask:0xf\n"
      "s_nop 0\n"
      "v_add_f32_dpp v220, v220, v220 row_bcast:31 row_mask:0xc bank_mask:0xf\n"
      "v_add_f32_dpp v221, v221, v221 row_bcast:31 row_mask:0xc bank_mask:0xf\n"
      "s_mov_b32 exec_lo, 0\n"
      "v_rcp_f32 v220, v220\n"
      "v_ldexp_f32 v221, v221, s39\n"
      "s_mov_b32 exec_hi, 0x80000000\n"
      "v_mul_f32 v221, v221, v220\n"
      "v_cvt_pk_bf16_f32 v220, v221, v221\n"
      "global_store_short v3, v220, s[24:25]\n"
      "v_cmp_class_f32 vcc, v221, s45\n"
      "s_nop 0\n"
      "s_cmp_lt_i32 vcc_hi, 0\n"
      "s_cbranch_scc1 7f\n"
      "s_endpgm\n"
      "s_mov_b64 exec, -1\n"
      "s_branch 9f\n"
      "80:\n"
      "s_waitcnt vmcnt(0) lgkmcnt(0)\n"
      "ds_read_b32 v224, v227 offset:37640\n"
      "s_waitcnt lgkmcnt(0)\n"
      "v_cmp_eq_u32 vcc, 0, v224\n"
      "s_nop 4\n"
      "s_and_b32 s44, vcc_lo, s54\n"
      "s_cbranch_scc1 80b\n"
      "v_cmp_eq_u32 vcc, 1, v224\n"
      "s_nop 4\n"
      "s_and_b32 s44, vcc_lo, s54\n"
      "s_cmp_eq_u32 s44, s54\n"
      "s_cbranch_scc1 81b\n"
      "s_mov_b32 s36, 1\n"
      "s_add_u32 s44, s38, 1152\n"
      "v_cndmask_b32 v10, v201, v10, s[100:101]\n"
      "v_cndmask_b32 v11, v201, v11, s[100:101]\n"
      "v_mov_b32 v1, s44\n"
      "s_nop 1\n"
      "s_branch 81b\n"
      "82:\n"
      "s_cmp_lg_u32 s36, 0\n"
      "s_cbranch_scc1 84f\n"
      "85:\n"
      "ds_read_b32 v224, v12 offset:12992\n"
      "s_waitcnt lgkmcnt(0)\n"
      "v_cmp_eq_u32 vcc, s37, v224\n"
      "s_nop 4\n"
      "s_cmp_lg_u64 vcc, 0\n"
      "s_cbranch_scc1 85b\n"
      "s_branch 83b\n"
      "84:\n"
      "s_waitcnt lgkmcnt(0)\n"
      "v_mfma_f32_16x16x32_f16 v[240:243], a[64:67], v[160:163], v[32:35]\n"
      "v_mfma_f32_16x16x32_f16 v[240:243], a[68:71], v[164:167], v[240:243]\n"
      "v_mfma_f32_16x16x32_f16 v[240:243], a[72:75], v[168:171], v[240:243]\n"
      "v_mfma_f32_16x16x32_f16 v[240:243], a[76:79], v[172:175], v[240:243]\n"
      "v_mfma_f32_16x16x32_f16 v[244:247], a[80:83], v[160:163], v[36:39]\n"
      "v_mfma_f32_16x16x32_f16 v[244:247], a[84:87], v[164:167], v[244:247]\n"
      "v_mfma_f32_16x16x32_f16 v[244:247], a[88:91], v[168:171], v[244:247]\n"
      "v_mfma_f32_16x16x32_f16 v[244:247], a[92:95], v[172:175], v[244:247]\n"
      "v_mfma_f32_16x16x32_f16 v[248:251], a[96:99], v[160:163], v[40:43]\n"
      "v_mfma_f32_16x16x32_f16 v[248:251], a[100:103], v[164:167], v[248:251]\n"
      "v_mfma_f32_16x16x32_f16 v[248:251], a[104:107], v[168:171], v[248:251]\n"
      "v_mfma_f32_16x16x32_f16 v[248:251], a[108:111], v[172:175], v[248:251]\n"
      "v_mfma_f32_16x16x32_f16 v[252:255], a[112:115], v[160:163], v[44:47]\n"
      "v_mfma_f32_16x16x32_f16 v[252:255], a[116:119], v[164:167], v[252:255]\n"
      "v_mfma_f32_16x16x32_f16 v[252:255], a[120:123], v[168:171], v[252:255]\n"
      "v_mfma_f32_16x16x32_f16 v[252:255], a[124:127], v[172:175], v[252:255]\n"
      "s_nop 7\n"
      "v_fma_f32 v224, |v240|, v64, 2.0\n"
      "v_fma_f32 v224, |v241|, v65, v224\n"
      "v_fma_f32 v224, |v242|, v66, v224\n"
      "v_fma_f32 v224, |v243|, v67, v224\n"
      "v_fma_f32 v224, |v244|, v68, v224\n"
      "v_fma_f32 v224, |v245|, v69, v224\n"
      "v_fma_f32 v224, |v246|, v70, v224\n"
      "v_fma_f32 v224, |v247|, v71, v224\n"
      "v_fma_f32 v224, |v248|, v72, v224\n"
      "v_fma_f32 v224, |v249|, v73, v224\n"
      "v_fma_f32 v224, |v250|, v74, v224\n"
      "v_fma_f32 v224, |v251|, v75, v224\n"
      "v_fma_f32 v224, |v252|, v76, v224\n"
      "v_fma_f32 v224, |v253|, v77, v224\n"
      "v_fma_f32 v224, |v254|, v78, v224\n"
      "v_fma_f32 v224, |v255|, v79, v224\n"
      "s_nop 1\n"
      "s_branch 83b\n"
      "5:\n"
      "s_waitcnt lgkmcnt(0)\n"
      "ds_read_b32 v224, v227 offset:37632\n"
      "s_waitcnt lgkmcnt(0)\n"
      "v_cmp_eq_u32 vcc, 0, v224\n"
      "s_nop 4\n"
      "s_cmp_lg_u32 vcc_lo, 0\n"
      "s_cbranch_scc1 5b\n"
      "v_cmp_gt_u32 vcc, 4, v224\n"
      "s_nop 4\n"
      "s_cmp_lg_u32 vcc_lo, 0\n"
      "s_cbranch_scc1 8f\n"
      "s_cmp_lg_u32 s58, 0\n"
      "s_cbranch_scc1 52f\n"
      "ds_read_b32 v226, v213 offset:12480\n"
      "v_cmp_eq_u32 s[44:45], 5, v224\n"
      "s_waitcnt lgkmcnt(0)\n"
      "v_max_f16_sdwa v226, |v226|, |v226| dst_sel:DWORD dst_unused:UNUSED_PAD src0_sel:WORD_0 src1_sel:WORD_1\n"
      "s_nop 1\n"
      "v_cmp_le_f16 vcc, 0x6400, v226\n"
      "s_nop 4\n"
      "s_or_b64 vcc, vcc, s[44:45]\n"
      "s_cbranch_scc0 52f\n"
      "v_and_b32 v224, 63, %3\n"
      "v_lshlrev_b32 v224, 9, v224\n"
      "global_load_dwordx4 v[176:179], v224, s[16:17]\n"
      "global_load_dwordx4 v[180:183], v224, s[16:17] offset:336\n"
      "s_waitcnt vmcnt(0)\n"
      "v_max3_f32 v224, |v176|, |v177|, |v178|\n"
      "v_max3_f32 v224, |v179|, |v180|, v224\n"
      "v_max3_f32 v224, |v181|, |v182|, v224\n"
      "v_max_f32 v224, |v183|, v224\n"
      "s_nop 1\n"
      "v_max_f32_dpp v224, v224, v224 row_ror:8 row_mask:0xf bank_mask:0xf\n"
      "s_nop 1\n"
      "v_max_f32_dpp v224, v224, v224 row_ror:4 row_mask:0xf bank_mask:0xf\n"
      "s_nop 1\n"
      "v_max_f32_dpp v224, v224, v224 row_ror:2 row_mask:0xf bank_mask:0xf\n"
      "s_nop 1\n"
      "v_max_f32_dpp v224, v224, v224 row_ror:1 row_mask:0xf bank_mask:0xf\n"
      "s_nop 0\n"
      "v_readlane_b32 s44, v224, 0\n"
      "v_readlane_b32 s45, v224, 16\n"
      "v_readlane_b32 s46, v224, 32\n"
      "v_readlane_b32 s47, v224, 48\n"
      "s_max_u32 s44, s44, s45\n"
      "s_max_u32 s46, s46, s47\n"
      "s_max_u32 s44, s44, s46\n"
      "s_lshr_b32 s44, s44, 23\n"
      "s_sub_i32 s58, 135, s44\n"
      "s_cmp_eq_u32 s44, 0\n"
      "s_cselect_b32 s58, 0, s58\n"
      "s_max_i32 s58, s58, 11\n"
      "s_cmp_gt_u32 s58, 51\n"
      "s_cbranch_scc1 8f\n"
      "s_mov_b32 s41, 1\n"
      "s_mov_b32 s35, 0\n"
      "s_waitcnt vmcnt(0) lgkmcnt(0)\n"
      "s_barrier\n"
      "s_branch 0b\n"
      "52:\n"
      "ds_read_b64 v[176:177], v5 offset:37120\n"
      "v_cmp_eq_u32 vcc, 9, v224\n"
      "v_cmp_eq_u32 s[50:51], 5, v224\n"
      "s_waitcnt lgkmcnt(0)\n"
      "v_max_f32 v225, |v176|, |v177|\n"
      "s_nop 1\n"
      "v_max_f32_dpp v225, v225, v225 row_ror:8 row_mask:0xf bank_mask:0xf\n"
      "s_nop 1\n"
      "v_max_f32_dpp v225, v225, v225 row_ror:4 row_mask:0xf bank_mask:0xf\n"
      "s_nop 1\n"
      "v_max_f32_dpp v225, v225, v225 row_ror:2 row_mask:0xf bank_mask:0xf\n"
      "s_nop 1\n"
      "v_max_f32_dpp v225, v225, v225 row_ror:1 row_mask:0xf bank_mask:0xf\n"
      "s_nop 0\n"
      "v_readlane_b32 s44, v225, 0\n"
      "v_readlane_b32 s45, v225, 16\n"
      "v_readlane_b32 s46, v225, 32\n"
      "v_readlane_b32 s47, v225, 48\n"
      "s_max_u32 s44, s44, s45\n"
      "s_max_u32 s46, s46, s47\n"
      "s_max_u32 s44, s44, s46\n"
      "v_frexp_exp_i32_f32 v224, s44\n"
      "v_sub_u32 v224, 0, v224\n"
      "s_or_b32 s30, s30, vcc_lo\n"
      "s_cmp_lg_u32 s55, 0\n"
      "s_cselect_b32 s30, 0, s30\n"
      "v_readfirstlane_b32 s51, v224\n"
      "s_cmp_lg_u32 s30, 0\n"
      "s_cbranch_scc1 6f\n"
      "v_ldexp_f32 v176, v176, v224\n"
      "v_ldexp_f32 v177, v177, v224\n"
      "v_readfirstlane_b32 s56, v224\n"
      "s_nop 1\n"
      "s_branch 4b\n"
      "22:\n"
      "s_nop 7\n"
      "v_lshlrev_b32 v176, 16, v192\n"
      "v_and_b32 v177, s43, v192\n"
      "v_lshlrev_b32 v178, 16, v193\n"
      "v_and_b32 v179, s43, v193\n"
      "v_lshlrev_b32 v180, 16, v194\n"
      "v_and_b32 v181, s43, v194\n"
      "v_lshlrev_b32 v182, 16, v195\n"
      "v_and_b32 v183, s43, v195\n"
      "v_max3_f32 v222, |v176|, |v177|, |v178|\n"
      "v_max3_f32 v222, |v179|, |v180|, v222\n"
      "v_max3_f32 v222, |v181|, |v182|, v222\n"
      "v_max_f32 v222, |v183|, v222\n"
      "s_nop 1\n"
      "v_max_f32_dpp v222, v222, v222 row_ror:8 row_mask:0xf bank_mask:0xf\n"
      "s_nop 1\n"
      "v_max_f32_dpp v222, v222, v222 row_ror:4 row_mask:0xf bank_mask:0xf\n"
      "s_nop 1\n"
      "v_max_f32_dpp v222, v222, v222 row_ror:2 row_mask:0xf bank_mask:0xf\n"
      "s_nop 1\n"
      "v_max_f32_dpp v222, v222, v222 row_ror:1 row_mask:0xf bank_mask:0xf\n"
      "v_frexp_exp_i32_f32 v90, v222\n"
      "v_sub_u32 v90, 0, v90\n"
      "v_ldexp_f32 v176, v176, v90\n"
      "v_ldexp_f32 v177, v177, v90\n"
      "v_ldexp_f32 v178, v178, v90\n"
      "v_ldexp_f32 v179, v179, v90\n"
      "v_ldexp_f32 v180, v180, v90\n"
      "v_ldexp_f32 v181, v181, v90\n"
      "v_ldexp_f32 v182, v182, v90\n"
      "v_ldexp_f32 v183, v183, v90\n"
      "v_ldexp_f32 v176, v176, 9\n"
      "v_ldexp_f32 v177, v177, 9\n"
      "v_ldexp_f32 v178, v178, 9\n"
      "v_ldexp_f32 v179, v179, 9\n"
      "v_ldexp_f32 v180, v180, 9\n"
      "v_ldexp_f32 v181, v181, 9\n"
      "v_ldexp_f32 v182, v182, 9\n"
      "v_ldexp_f32 v183, v183, 9\n"
      "v_readlane_b32 s59, v90, 0\n"
      "v_readlane_b32 s57, v90, 32\n"
      "s_lshl_b64 exec, -1, 48\n"
      "v_xor_b32 v176, 0x80000000, v176\n"
      "v_xor_b32 v177, 0x80000000, v177\n"
      "v_xor_b32 v178, 0x80000000, v178\n"
      "v_xor_b32 v179, 0x80000000, v179\n"
      "v_xor_b32 v180, 0x80000000, v180\n"
      "v_xor_b32 v181, 0x80000000, v181\n"
      "v_xor_b32 v182, 0x80000000, v182\n"
      "v_xor_b32 v183, 0x80000000, v183\n"
      "s_mov_b64 exec, -1\n"
      "s_cmp_lt_i32 s57, 0\n"
      "s_cselect_b32 s41, 1, s41\n"
      "s_branch 23b\n"
      "70:\n"
      "s_mov_b32 s49, 0\n"
      "s_add_u32 s44, s58, 127\n"
      "s_lshl_b32 s44, s44, 23\n"
      "s_waitcnt vmcnt(0) lgkmcnt(0)\n"
      "s_nop 7\n"
      "v_lshlrev_b32 v128, 16, v96\n"
      "v_and_b32 v129, s43, v96\n"
      "v_mul_f32 v128, s44, v128\n"
      "v_mul_f32 v129, s44, v129\n"
      "v_cvt_pk_bf16_f32 v96, v128, v129\n"
      "v_lshlrev_b32 v128, 16, v97\n"
      "v_and_b32 v129, s43, v97\n"
      "v_mul_f32 v128, s44, v128\n"
      "v_mul_f32 v129, s44, v129\n"
      "v_cvt_pk_bf16_f32 v97, v128, v129\n"
      "v_lshlrev_b32 v128, 16, v98\n"
      "v_and_b32 v129, s43, v98\n"
      "v_mul_f32 v128, s44, v128\n"
      "v_mul_f32 v129, s44, v129\n"
      "v_cvt_pk_bf16_f32 v98, v128, v129\n"
      "v_lshlrev_b32 v128, 16, v99\n"
      "v_and_b32 v129, s43, v99\n"
      "v_mul_f32 v128, s44, v128\n"
      "v_mul_f32 v129, s44, v129\n"
      "v_cvt_pk_bf16_f32 v99, v128, v129\n"
      "v_lshlrev_b32 v128, 16, v100\n"
      "v_and_b32 v129, s43, v100\n"
      "v_mul_f32 v128, s44, v128\n"
      "v_mul_f32 v129, s44, v129\n"
      "v_cvt_pk_bf16_f32 v100, v128, v129\n"
      "v_lshlrev_b32 v128, 16, v101\n"
      "v_and_b32 v129, s43, v101\n"
      "v_mul_f32 v128, s44, v128\n"
      "v_mul_f32 v129, s44, v129\n"
      "v_cvt_pk_bf16_f32 v101, v128, v129\n"
      "v_lshlrev_b32 v128, 16, v102\n"
      "v_and_b32 v129, s43, v102\n"
      "v_mul_f32 v128, s44, v128\n"
      "v_mul_f32 v129, s44, v129\n"
      "v_cvt_pk_bf16_f32 v102, v128, v129\n"
      "v_lshlrev_b32 v128, 16, v103\n"
      "v_and_b32 v129, s43, v103\n"
      "v_mul_f32 v128, s44, v128\n"
      "v_mul_f32 v129, s44, v129\n"
      "v_cvt_pk_bf16_f32 v103, v128, v129\n"
      "v_lshlrev_b32 v128, 16, v104\n"
      "v_and_b32 v129, s43, v104\n"
      "v_mul_f32 v128, s44, v128\n"
      "v_mul_f32 v129, s44, v129\n"
      "v_cvt_pk_bf16_f32 v104, v128, v129\n"
      "v_lshlrev_b32 v128, 16, v105\n"
      "v_and_b32 v129, s43, v105\n"
      "v_mul_f32 v128, s44, v128\n"
      "v_mul_f32 v129, s44, v129\n"
      "v_cvt_pk_bf16_f32 v105, v128, v129\n"
      "v_lshlrev_b32 v128, 16, v106\n"
      "v_and_b32 v129, s43, v106\n"
      "v_mul_f32 v128, s44, v128\n"
      "v_mul_f32 v129, s44, v129\n"
      "v_cvt_pk_bf16_f32 v106, v128, v129\n"
      "v_lshlrev_b32 v128, 16, v107\n"
      "v_and_b32 v129, s43, v107\n"
      "v_mul_f32 v128, s44, v128\n"
      "v_mul_f32 v129, s44, v129\n"
      "v_cvt_pk_bf16_f32 v107, v128, v129\n"
      "v_lshlrev_b32 v128, 16, v108\n"
      "v_and_b32 v129, s43, v108\n"
      "v_mul_f32 v128, s44, v128\n"
      "v_mul_f32 v129, s44, v129\n"
      "v_cvt_pk_bf16_f32 v108, v128, v129\n"
      "v_lshlrev_b32 v128, 16, v109\n"
      "v_and_b32 v129, s43, v109\n"
      "v_mul_f32 v128, s44, v128\n"
      "v_mul_f32 v129, s44, v129\n"
      "v_cvt_pk_bf16_f32 v109, v128, v129\n"
      "v_lshlrev_b32 v128, 16, v110\n"
      "v_and_b32 v129, s43, v110\n"
      "v_mul_f32 v128, s44, v128\n"
      "v_mul_f32 v129, s44, v129\n"
      "v_cvt_pk_bf16_f32 v110, v128, v129\n"
      "v_lshlrev_b32 v128, 16, v111\n"
      "v_and_b32 v129, s43, v111\n"
      "v_mul_f32 v128, s44, v128\n"
      "v_mul_f32 v129, s44, v129\n"
      "v_cvt_pk_bf16_f32 v111, v128, v129\n"
      "v_lshlrev_b32 v128, 16, v112\n"
      "v_and_b32 v129, s43, v112\n"
      "v_mul_f32 v128, s44, v128\n"
      "v_mul_f32 v129, s44, v129\n"
      "v_cvt_pk_bf16_f32 v112, v128, v129\n"
      "v_lshlrev_b32 v128, 16, v113\n"
      "v_and_b32 v129, s43, v113\n"
      "v_mul_f32 v128, s44, v128\n"
      "v_mul_f32 v129, s44, v129\n"
      "v_cvt_pk_bf16_f32 v113, v128, v129\n"
      "v_lshlrev_b32 v128, 16, v114\n"
      "v_and_b32 v129, s43, v114\n"
      "v_mul_f32 v128, s44, v128\n"
      "v_mul_f32 v129, s44, v129\n"
      "v_cvt_pk_bf16_f32 v114, v128, v129\n"
      "v_lshlrev_b32 v128, 16, v115\n"
      "v_and_b32 v129, s43, v115\n"
      "v_mul_f32 v128, s44, v128\n"
      "v_mul_f32 v129, s44, v129\n"
      "v_cvt_pk_bf16_f32 v115, v128, v129\n"
      "v_lshlrev_b32 v128, 16, v116\n"
      "v_and_b32 v129, s43, v116\n"
      "v_mul_f32 v128, s44, v128\n"
      "v_mul_f32 v129, s44, v129\n"
      "v_cvt_pk_bf16_f32 v116, v128, v129\n"
      "v_lshlrev_b32 v128, 16, v117\n"
      "v_and_b32 v129, s43, v117\n"
      "v_mul_f32 v128, s44, v128\n"
      "v_mul_f32 v129, s44, v129\n"
      "v_cvt_pk_bf16_f32 v117, v128, v129\n"
      "v_lshlrev_b32 v128, 16, v118\n"
      "v_and_b32 v129, s43, v118\n"
      "v_mul_f32 v128, s44, v128\n"
      "v_mul_f32 v129, s44, v129\n"
      "v_cvt_pk_bf16_f32 v118, v128, v129\n"
      "v_lshlrev_b32 v128, 16, v119\n"
      "v_and_b32 v129, s43, v119\n"
      "v_mul_f32 v128, s44, v128\n"
      "v_mul_f32 v129, s44, v129\n"
      "v_cvt_pk_bf16_f32 v119, v128, v129\n"
      "v_lshlrev_b32 v128, 16, v120\n"
      "v_and_b32 v129, s43, v120\n"
      "v_mul_f32 v128, s44, v128\n"
      "v_mul_f32 v129, s44, v129\n"
      "v_cvt_pk_bf16_f32 v120, v128, v129\n"
      "v_lshlrev_b32 v128, 16, v121\n"
      "v_and_b32 v129, s43, v121\n"
      "v_mul_f32 v128, s44, v128\n"
      "v_mul_f32 v129, s44, v129\n"
      "v_cvt_pk_bf16_f32 v121, v128, v129\n"
      "v_lshlrev_b32 v128, 16, v122\n"
      "v_and_b32 v129, s43, v122\n"
      "v_mul_f32 v128, s44, v128\n"
      "v_mul_f32 v129, s44, v129\n"
      "v_cvt_pk_bf16_f32 v122, v128, v129\n"
      "v_lshlrev_b32 v128, 16, v123\n"
      "v_and_b32 v129, s43, v123\n"
      "v_mul_f32 v128, s44, v128\n"
      "v_mul_f32 v129, s44, v129\n"
      "v_cvt_pk_bf16_f32 v123, v128, v129\n"
      "v_lshlrev_b32 v128, 16, v124\n"
      "v_and_b32 v129, s43, v124\n"
      "v_mul_f32 v128, s44, v128\n"
      "v_mul_f32 v129, s44, v129\n"
      "v_cvt_pk_bf16_f32 v124, v128, v129\n"
      "v_lshlrev_b32 v128, 16, v125\n"
      "v_and_b32 v129, s43, v125\n"
      "v_mul_f32 v128, s44, v128\n"
      "v_mul_f32 v129, s44, v129\n"
      "v_cvt_pk_bf16_f32 v125, v128, v129\n"
      "v_lshlrev_b32 v128, 16, v126\n"
      "v_and_b32 v129, s43, v126\n"
      "v_mul_f32 v128, s44, v128\n"
      "v_mul_f32 v129, s44, v129\n"
      "v_cvt_pk_bf16_f32 v126, v128, v129\n"
      "v_lshlrev_b32 v128, 16, v127\n"
      "v_and_b32 v129, s43, v127\n"
      "v_mul_f32 v128, s44, v128\n"
      "v_mul_f32 v129, s44, v129\n"
      "v_cvt_pk_bf16_f32 v127, v128, v129\n"
      "s_nop 1\n"
      "s_branch 71b\n"
      "72:\n"
      "s_nop 7\n"
      "s_sub_u32 s44, 0, s58\n"
      "v_ldexp_f32 v16, v16, s58\n"
      "v_ldexp_f32 v48, v48, s44\n"
      "v_ldexp_f32 v17, v17, s58\n"
      "v_ldexp_f32 v49, v49, s44\n"
      "v_ldexp_f32 v18, v18, s58\n"
      "v_ldexp_f32 v50, v50, s44\n"
      "v_ldexp_f32 v19, v19, s58\n"
      "v_ldexp_f32 v51, v51, s44\n"
      "v_ldexp_f32 v20, v20, s58\n"
      "v_ldexp_f32 v52, v52, s44\n"
      "v_ldexp_f32 v21, v21, s58\n"
      "v_ldexp_f32 v53, v53, s44\n"
      "v_ldexp_f32 v22, v22, s58\n"
      "v_ldexp_f32 v54, v54, s44\n"
      "v_ldexp_f32 v23, v23, s58\n"
      "v_ldexp_f32 v55, v55, s44\n"
      "v_ldexp_f32 v24, v24, s58\n"
      "v_ldexp_f32 v56, v56, s44\n"
      "v_ldexp_f32 v25, v25, s58\n"
      "v_ldexp_f32 v57, v57, s44\n"
      "v_ldexp_f32 v26, v26, s58\n"
      "v_ldexp_f32 v58, v58, s44\n"
      "v_ldexp_f32 v27, v27, s58\n"
      "v_ldexp_f32 v59, v59, s44\n"
      "v_ldexp_f32 v28, v28, s58\n"
      "v_ldexp_f32 v60, v60, s44\n"
      "v_ldexp_f32 v29, v29, s58\n"
      "v_ldexp_f32 v61, v61, s44\n"
      "v_ldexp_f32 v30, v30, s58\n"
      "v_ldexp_f32 v62, v62, s44\n"
      "v_ldexp_f32 v31, v31, s58\n"
      "v_ldexp_f32 v63, v63, s44\n"
      "v_ldexp_f32 v32, v32, s58\n"
      "v_ldexp_f32 v64, v64, s44\n"
      "v_ldexp_f32 v33, v33, s58\n"
      "v_ldexp_f32 v65, v65, s44\n"
      "v_ldexp_f32 v34, v34, s58\n"
      "v_ldexp_f32 v66, v66, s44\n"
      "v_ldexp_f32 v35, v35, s58\n"
      "v_ldexp_f32 v67, v67, s44\n"
      "v_ldexp_f32 v36, v36, s58\n"
      "v_ldexp_f32 v68, v68, s44\n"
      "v_ldexp_f32 v37, v37, s58\n"
      "v_ldexp_f32 v69, v69, s44\n"
      "v_ldexp_f32 v38, v38, s58\n"
      "v_ldexp_f32 v70, v70, s44\n"
      "v_ldexp_f32 v39, v39, s58\n"
      "v_ldexp_f32 v71, v71, s44\n"
      "v_ldexp_f32 v40, v40, s58\n"
      "v_ldexp_f32 v72, v72, s44\n"
      "v_ldexp_f32 v41, v41, s58\n"
      "v_ldexp_f32 v73, v73, s44\n"
      "v_ldexp_f32 v42, v42, s58\n"
      "v_ldexp_f32 v74, v74, s44\n"
      "v_ldexp_f32 v43, v43, s58\n"
      "v_ldexp_f32 v75, v75, s44\n"
      "v_ldexp_f32 v44, v44, s58\n"
      "v_ldexp_f32 v76, v76, s44\n"
      "v_ldexp_f32 v45, v45, s58\n"
      "v_ldexp_f32 v77, v77, s44\n"
      "v_ldexp_f32 v46, v46, s58\n"
      "v_ldexp_f32 v78, v78, s44\n"
      "v_ldexp_f32 v47, v47, s58\n"
      "v_ldexp_f32 v79, v79, s44\n"
      "s_branch 73b\n"
      "6:\n"
      "s_waitcnt vmcnt(0) lgkmcnt(0)\n"
      "s_nop 7\n"
      "s_mov_b32 s55, 1\n"
      "s_mov_b32 s41, 1\n"
      "s_mov_b32 s45, 0x7fff7fff\n"
      "v_mov_b32 v226, 0\n"
      "v_and_b32 v176, s45, v228\n"
      "v_pk_max_u16 v226, v226, v176\n"
      "v_and_b32 v176, s45, v229\n"
      "v_pk_max_u16 v226, v226, v176\n"
      "v_and_b32 v176, s45, v230\n"
      "v_pk_max_u16 v226, v226, v176\n"
      "v_and_b32 v176, s45, v231\n"
      "v_pk_max_u16 v226, v226, v176\n"
      "v_and_b32 v176, s45, v232\n"
      "v_pk_max_u16 v226, v226, v176\n"
      "v_and_b32 v176, s45, v233\n"
      "v_pk_max_u16 v226, v226, v176\n"
      "v_and_b32 v176, s45, v234\n"
      "v_pk_max_u16 v226, v226, v176\n"
      "v_and_b32 v176, s45, v235\n"
      "v_pk_max_u16 v226, v226, v176\n"
      "v_and_b32 v176, s45, v236\n"
      "v_pk_max_u16 v226, v226, v176\n"
      "v_and_b32 v176, s45, v237\n"
      "v_pk_max_u16 v226, v226, v176\n"
      "v_and_b32 v176, s45, v238\n"
      "v_pk_max_u16 v226, v226, v176\n"
      "v_and_b32 v176, s45, v239\n"
      "v_pk_max_u16 v226, v226, v176\n"
      "v_and_b32 v176, s45, v240\n"
      "v_pk_max_u16 v226, v226, v176\n"
      "v_and_b32 v176, s45, v241\n"
      "v_pk_max_u16 v226, v226, v176\n"
      "v_and_b32 v176, s45, v242\n"
      "v_pk_max_u16 v226, v226, v176\n"
      "v_and_b32 v176, s45, v243\n"
      "v_pk_max_u16 v226, v226, v176\n"
      "v_and_b32 v176, s45, v244\n"
      "v_pk_max_u16 v226, v226, v176\n"
      "v_and_b32 v176, s45, v245\n"
      "v_pk_max_u16 v226, v226, v176\n"
      "v_and_b32 v176, s45, v246\n"
      "v_pk_max_u16 v226, v226, v176\n"
      "v_and_b32 v176, s45, v247\n"
      "v_pk_max_u16 v226, v226, v176\n"
      "v_and_b32 v176, s45, v248\n"
      "v_pk_max_u16 v226, v226, v176\n"
      "v_and_b32 v176, s45, v249\n"
      "v_pk_max_u16 v226, v226, v176\n"
      "v_and_b32 v176, s45, v250\n"
      "v_pk_max_u16 v226, v226, v176\n"
      "v_and_b32 v176, s45, v251\n"
      "v_pk_max_u16 v226, v226, v176\n"
      "v_and_b32 v176, s45, v252\n"
      "v_pk_max_u16 v226, v226, v176\n"
      "v_and_b32 v176, s45, v253\n"
      "v_pk_max_u16 v226, v226, v176\n"
      "v_and_b32 v176, s45, v254\n"
      "v_pk_max_u16 v226, v226, v176\n"
      "v_and_b32 v176, s45, v255\n"
      "v_pk_max_u16 v226, v226, v176\n"
      "v_lshlrev_b32 v176, 16, v226\n"
      "v_max_u32 v226, v226, v176\n"
      "s_nop 1\n"
      "v_max_u32_dpp v226, v226, v226 row_ror:8 row_mask:0xf bank_mask:0xf\n"
      "s_nop 1\n"
      "v_max_u32_dpp v226, v226, v226 row_ror:4 row_mask:0xf bank_mask:0xf\n"
      "s_nop 1\n"
      "v_max_u32_dpp v226, v226, v226 row_ror:2 row_mask:0xf bank_mask:0xf\n"
      "s_nop 1\n"
      "v_max_u32_dpp v226, v226, v226 row_ror:1 row_mask:0xf bank_mask:0xf\n"
      "s_nop 0\n"
      "v_readlane_b32 s44, v226, 0\n"
      "v_readlane_b32 s45, v226, 16\n"
      "v_readlane_b32 s46, v226, 32\n"
      "v_readlane_b32 s47, v226, 48\n"
      "s_max_u32 s44, s44, s45\n"
      "s_max_u32 s46, s46, s47\n"
      "s_max_u32 s44, s44, s46\n"
      "s_lshr_b32 s44, s44, 23\n"
      "s_sub_i32 s40, 138, s44\n"
      "s_cmp_eq_u32 s44, 0\n"
      "s_cselect_b32 s40, 0, s40\n"
      "s_add_i32 s45, s40, 32\n"
      "s_cmp_gt_u32 s45, 80\n"
      "s_cbranch_scc1 8f\n"
      "s_lshl1_add_u32 s44, s40, s58\n"
      "s_add_i32 s44, s44, s57\n"
      "s_add_i32 s45, s44, s51\n"
      "s_cmp_lt_i32 s45, 2\n"
      "s_cbranch_scc1 8f\n"
      "s_cmp_lg_u32 s50, 0\n"
      "s_cselect_b32 s44, s44, 64\n"
      "s_cmp_lt_i32 s44, 26\n"
      "s_cbranch_scc1 8f\n"
      "s_add_u32 s39, s40, 119\n"
      "s_lshl_b32 s39, s39, 23\n"
      "s_lshl_b32 s44, s40, 1\n"
      "s_sub_u32 s45, 127, s44\n"
      "s_add_u32 s44, s44, 127\n"
      "s_lshl_b32 s44, s44, 23\n"
      "s_lshl_b32 s45, s45, 23\n"
      "v_mul_f32 v16, s44, v16\n"
      "v_mul_f32 v48, s45, v48\n"
      "v_mul_f32 v17, s44, v17\n"
      "v_mul_f32 v49, s45, v49\n"
      "v_mul_f32 v18, s44, v18\n"
      "v_mul_f32 v50, s45, v50\n"
      "v_mul_f32 v19, s44, v19\n"
      "v_mul_f32 v51, s45, v51\n"
      "v_mul_f32 v20, s44, v20\n"
      "v_mul_f32 v52, s45, v52\n"
      "v_mul_f32 v21, s44, v21\n"
      "v_mul_f32 v53, s45, v53\n"
      "v_mul_f32 v22, s44, v22\n"
      "v_mul_f32 v54, s45, v54\n"
      "v_mul_f32 v23, s44, v23\n"
      "v_mul_f32 v55, s45, v55\n"
      "v_mul_f32 v24, s44, v24\n"
      "v_mul_f32 v56, s45, v56\n"
      "v_mul_f32 v25, s44, v25\n"
      "v_mul_f32 v57, s45, v57\n"
      "v_mul_f32 v26, s44, v26\n"
      "v_mul_f32 v58, s45, v58\n"
      "v_mul_f32 v27, s44, v27\n"
      "v_mul_f32 v59, s45, v59\n"
      "v_mul_f32 v28, s44, v28\n"
      "v_mul_f32 v60, s45, v60\n"
      "v_mul_f32 v29, s44, v29\n"
      "v_mul_f32 v61, s45, v61\n"
      "v_mul_f32 v30, s44, v30\n"
      "v_mul_f32 v62, s45, v62\n"
      "v_mul_f32 v31, s44, v31\n"
      "v_mul_f32 v63, s45, v63\n"
      "v_mul_f32 v32, s44, v32\n"
      "v_mul_f32 v64, s45, v64\n"
      "v_mul_f32 v33, s44, v33\n"
      "v_mul_f32 v65, s45, v65\n"
      "v_mul_f32 v34, s44, v34\n"
      "v_mul_f32 v66, s45, v66\n"
      "v_mul_f32 v35, s44, v35\n"
      "v_mul_f32 v67, s45, v67\n"
      "v_mul_f32 v36, s44, v36\n"
      "v_mul_f32 v68, s45, v68\n"
      "v_mul_f32 v37, s44, v37\n"
      "v_mul_f32 v69, s45, v69\n"
      "v_mul_f32 v38, s44, v38\n"
      "v_mul_f32 v70, s45, v70\n"
      "v_mul_f32 v39, s44, v39\n"
      "v_mul_f32 v71, s45, v71\n"
      "v_mul_f32 v40, s44, v40\n"
      "v_mul_f32 v72, s45, v72\n"
      "v_mul_f32 v41, s44, v41\n"
      "v_mul_f32 v73, s45, v73\n"
      "v_mul_f32 v42, s44, v42\n"
      "v_mul_f32 v74, s45, v74\n"
      "v_mul_f32 v43, s44, v43\n"
      "v_mul_f32 v75, s45, v75\n"
      "v_mul_f32 v44, s44, v44\n"
      "v_mul_f32 v76, s45, v76\n"
      "v_mul_f32 v45, s44, v45\n"
      "v_mul_f32 v77, s45, v77\n"
      "v_mul_f32 v46, s44, v46\n"
      "v_mul_f32 v78, s45, v78\n"
      "v_mul_f32 v47, s44, v47\n"
      "v_mul_f32 v79, s45, v79\n"
      "v_lshlrev_b32 v176, 16, v228\n"
      "v_and_b32 v177, s43, v228\n"
      "v_lshlrev_b32 v178, 16, v229\n"
      "v_and_b32 v179, s43, v229\n"
      "v_lshlrev_b32 v180, 16, v230\n"
      "v_and_b32 v181, s43, v230\n"
      "v_lshlrev_b32 v182, 16, v231\n"
      "v_and_b32 v183, s43, v231\n"
      "v_mul_f32 v176, s39, v176\n"
      "v_mul_f32 v177, s39, v177\n"
      "v_mul_f32 v178, s39, v178\n"
      "v_mul_f32 v179, s39, v179\n"
      "v_mul_f32 v180, s39, v180\n"
      "v_mul_f32 v181, s39, v181\n"
      "v_mul_f32 v182, s39, v182\n"
      "v_mul_f32 v183, s39, v183\n"
      "v_cvt_pk_f16_f32 v184, v176, v177\n"
      "v_cvt_pk_f16_f32 v185, v178, v179\n"
      "v_cvt_pk_f16_f32 v186, v180, v181\n"
      "v_cvt_pk_f16_f32 v187, v182, v183\n"
      "ds_write_b128 v1, v[184:187] offset:0\n"
      "v_lshlrev_b32 v176, 16, v232\n"
      "v_and_b32 v177, s43, v232\n"
      "v_lshlrev_b32 v178, 16, v233\n"
      "v_and_b32 v179, s43, v233\n"
      "v_lshlrev_b32 v180, 16, v234\n"
      "v_and_b32 v181, s43, v234\n"
      "v_lshlrev_b32 v182, 16, v235\n"
      "v_and_b32 v183, s43, v235\n"
      "v_mul_f32 v176, s39, v176\n"
      "v_mul_f32 v177, s39, v177\n"
      "v_mul_f32 v178, s39, v178\n"
      "v_mul_f32 v179, s39, v179\n"
      "v_mul_f32 v180, s39, v180\n"
      "v_mul_f32 v181, s39, v181\n"
      "v_mul_f32 v182, s39, v182\n"
      "v_mul_f32 v183, s39, v183\n"
      "v_cvt_pk_f16_f32 v184, v176, v177\n"
      "v_cvt_pk_f16_f32 v185, v178, v179\n"
      "v_cvt_pk_f16_f32 v186, v180, v181\n"
      "v_cvt_pk_f16_f32 v187, v182, v183\n"
      "ds_write_b128 v1, v[184:187] offset:1152\n"
      "v_lshlrev_b32 v176, 16, v236\n"
      "v_and_b32 v177, s43, v236\n"
      "v_lshlrev_b32 v178, 16, v237\n"
      "v_and_b32 v179, s43, v237\n"
      "v_lshlrev_b32 v180, 16, v238\n"
      "v_and_b32 v181, s43, v238\n"
      "v_lshlrev_b32 v182, 16, v239\n"
      "v_and_b32 v183, s43, v239\n"
      "v_mul_f32 v176, s39, v176\n"
      "v_mul_f32 v177, s39, v177\n"
      "v_mul_f32 v178, s39, v178\n"
      "v_mul_f32 v179, s39, v179\n"
      "v_mul_f32 v180, s39, v180\n"
      "v_mul_f32 v181, s39, v181\n"
      "v_mul_f32 v182, s39, v182\n"
      "v_mul_f32 v183, s39, v183\n"
      "v_cvt_pk_f16_f32 v184, v176, v177\n"
      "v_cvt_pk_f16_f32 v185, v178, v179\n"
      "v_cvt_pk_f16_f32 v186, v180, v181\n"
      "v_cvt_pk_f16_f32 v187, v182, v183\n"
      "ds_write_b128 v1, v[184:187] offset:2304\n"
      "v_lshlrev_b32 v176, 16, v240\n"
      "v_and_b32 v177, s43, v240\n"
      "v_lshlrev_b32 v178, 16, v241\n"
      "v_and_b32 v179, s43, v241\n"
      "v_lshlrev_b32 v180, 16, v242\n"
      "v_and_b32 v181, s43, v242\n"
      "v_lshlrev_b32 v182, 16, v243\n"
      "v_and_b32 v183, s43, v243\n"
      "v_mul_f32 v176, s39, v176\n"
      "v_mul_f32 v177, s39, v177\n"
      "v_mul_f32 v178, s39, v178\n"
      "v_mul_f32 v179, s39, v179\n"
      "v_mul_f32 v180, s39, v180\n"
      "v_mul_f32 v181, s39, v181\n"
      "v_mul_f32 v182, s39, v182\n"
      "v_mul_f32 v183, s39, v183\n"
      "v_cvt_pk_f16_f32 v184, v176, v177\n"
      "v_cvt_pk_f16_f32 v185, v178, v179\n"
      "v_cvt_pk_f16_f32 v186, v180, v181\n"
      "v_cvt_pk_f16_f32 v187, v182, v183\n"
      "ds_write_b128 v1, v[184:187] offset:3456\n"
      "v_lshlrev_b32 v176, 16, v244\n"
      "v_and_b32 v177, s43, v244\n"
      "v_lshlrev_b32 v178, 16, v245\n"
      "v_and_b32 v179, s43, v245\n"
      "v_lshlrev_b32 v180, 16, v246\n"
      "v_and_b32 v181, s43, v246\n"
      "v_lshlrev_b32 v182, 16, v247\n"
      "v_and_b32 v183, s43, v247\n"
      "v_mul_f32 v176, s39, v176\n"
      "v_mul_f32 v177, s39, v177\n"
      "v_mul_f32 v178, s39, v178\n"
      "v_mul_f32 v179, s39, v179\n"
      "v_mul_f32 v180, s39, v180\n"
      "v_mul_f32 v181, s39, v181\n"
      "v_mul_f32 v182, s39, v182\n"
      "v_mul_f32 v183, s39, v183\n"
      "v_cvt_pk_f16_f32 v184, v176, v177\n"
      "v_cvt_pk_f16_f32 v185, v178, v179\n"
      "v_cvt_pk_f16_f32 v186, v180, v181\n"
      "v_cvt_pk_f16_f32 v187, v182, v183\n"
      "ds_write_b128 v1, v[184:187] offset:4608\n"
      "v_lshlrev_b32 v176, 16, v248\n"
      "v_and_b32 v177, s43, v248\n"
      "v_lshlrev_b32 v178, 16, v249\n"
      "v_and_b32 v179, s43, v249\n"
      "v_lshlrev_b32 v180, 16, v250\n"
      "v_and_b32 v181, s43, v250\n"
      "v_lshlrev_b32 v182, 16, v251\n"
      "v_and_b32 v183, s43, v251\n"
      "v_mul_f32 v176, s39, v176\n"
      "v_mul_f32 v177, s39, v177\n"
      "v_mul_f32 v178, s39, v178\n"
      "v_mul_f32 v179, s39, v179\n"
      "v_mul_f32 v180, s39, v180\n"
      "v_mul_f32 v181, s39, v181\n"
      "v_mul_f32 v182, s39, v182\n"
      "v_mul_f32 v183, s39, v183\n"
      "v_cvt_pk_f16_f32 v184, v176, v177\n"
      "v_cvt_pk_f16_f32 v185, v178, v179\n"
      "v_cvt_pk_f16_f32 v186, v180, v181\n"
      "v_cvt_pk_f16_f32 v187, v182, v183\n"
      "ds_write_b128 v1, v[184:187] offset:5760\n"
      "v_lshlrev_b32 v176, 16, v252\n"
      "v_and_b32 v177, s43, v252\n"
      "v_lshlrev_b32 v178, 16, v253\n"
      "v_and_b32 v179, s43, v253\n"
      "v_lshlrev_b32 v180, 16, v254\n"
      "v_and_b32 v181, s43, v254\n"
      "v_lshlrev_b32 v182, 16, v255\n"
      "v_and_b32 v183, s43, v255\n"
      "v_mul_f32 v176, s39, v176\n"
      "v_mul_f32 v177, s39, v177\n"
      "v_mul_f32 v178, s39, v178\n"
      "v_mul_f32 v179, s39, v179\n"
      "v_mul_f32 v180, s39, v180\n"
      "v_mul_f32 v181, s39, v181\n"
      "v_mul_f32 v182, s39, v182\n"
      "v_mul_f32 v183, s39, v183\n"
      "v_cvt_pk_f16_f32 v184, v176, v177\n"
      "v_cvt_pk_f16_f32 v185, v178, v179\n"
      "v_cvt_pk_f16_f32 v186, v180, v181\n"
      "v_cvt_pk_f16_f32 v187, v182, v183\n"
      "ds_write_b128 v1, v[184:187] offset:6912\n"
      "v_mfma_f32_16x16x32_f16 v[192:195], a[184:187], a[152:155], 0\n"
      "v_mfma_f32_16x16x32_f16 v[192:195], a[188:191], a[156:159], v[192:195]\n"
      "v_mfma_f32_16x16x32_f16 v[192:195], a[192:195], a[160:163], v[192:195]\n"
      "v_mfma_f32_16x16x32_f16 v[192:195], a[196:199], a[164:167], v[192:195]\n"
      "v_mfma_f32_16x16x32_f16 v[252:255], a[184:187], a[168:171], 0\n"
      "v_mfma_f32_16x16x32_f16 v[252:255], a[188:191], a[172:175], v[252:255]\n"
      "v_mfma_f32_16x16x32_f16 v[252:255], a[192:195], a[176:179], v[252:255]\n"
      "v_mfma_f32_16x16x32_f16 v[252:255], a[196:199], a[180:183], v[252:255]\n"
      "s_sub_u32 s44, 96512, s38\n"
      "s_mov_b32 s98, 0\n"
      "v_add_u32 v1, s44, v1\n"
      "s_waitcnt lgkmcnt(0)\n"
      "ds_read_b128 v[128:131], v201 offset:0\n"
      "ds_read_b128 v[160:163], v202 offset:0\n"
      "ds_read_b128 v[132:135], v201 offset:64\n"
      "ds_read_b128 v[164:167], v202 offset:64\n"
      "ds_read_b128 v[136:139], v201 offset:128\n"
      "ds_read_b128 v[168:171], v202 offset:128\n"
      "ds_read_b128 v[140:143], v201 offset:192\n"
      "ds_read_b128 v[172:175], v202 offset:192\n"
      "s_waitcnt lgkmcnt(0)\n"
      "v_pk_mul_f16 v96, v128, v160\n"
      "v_pk_mul_f16 v97, v129, v161\n"
      "v_pk_mul_f16 v98, v130, v162\n"
      "v_pk_mul_f16 v99, v131, v163\n"
      "v_pk_mul_f16 v100, v132, v164\n"
      "v_pk_mul_f16 v101, v133, v165\n"
      "v_pk_mul_f16 v102, v134, v166\n"
      "v_pk_mul_f16 v103, v135, v167\n"
      "v_pk_mul_f16 v104, v136, v168\n"
      "v_pk_mul_f16 v105, v137, v169\n"
      "v_pk_mul_f16 v106, v138, v170\n"
      "v_pk_mul_f16 v107, v139, v171\n"
      "s_nop 1\n"
      "s_branch 3b\n"
      "7:\n"
      "s_mov_b64 exec, -1\n"
      "s_cmp_lg_u32 s41, 0\n"
      "s_cbranch_scc1 8f\n"
      "s_mov_b32 s41, 1\n"
      "s_waitcnt vmcnt(0)\n"
      "s_branch 0b\n"
      "8:\n"
      "v_mov_b32 v222, 2\n"
      "s_mov_b32 s42, 1\n"
      "ds_write_b32 v2, v222 offset:8\n"
      "9:\n"
      "s_nop 7\n"
      "s_waitcnt vmcnt(0) lgkmcnt(0)\n"
      "s_mov_b32 %0, s42\n"
      : "=s"(flag)
      : "s"(__builtin_amdgcn_kernarg_segment_ptr()), "s"(blockIdx.x), "v"(threadIdx.x), "s"(lbase)
      :
      "v1", "v2", "v3", "v4", "v5", "v6", "v7", "v8", "v9", "v10", "v11", "v12", "v13", "v14", "v15", "v16", "v17", "v18", "v19", "v20", "v21", "v22", "v23", "v24",
      "v25", "v26", "v27", "v28", "v29", "v30", "v31", "v32", "v33", "v34", "v35", "v36", "v37", "v38", "v39", "v40", "v41", "v42", "v43", "v44", "v45", "v46", "v47", "v48",
      "v49", "v50", "v51", "v52", "v53", "v54", "v55", "v56", "v57", "v58", "v59", "v60", "v61", "v62", "v63", "v64", "v65", "v66", "v67", "v68", "v69", "v70", "v71", "v72",
      "v73", "v74", "v75", "v76", "v77", "v78", "v79", "v80", "v81", "v82", "v83", "v84", "v85", "v86", "v87", "v88", "v89", "v90", "v91", "v92", "v93", "v94", "v95", "v96",
      "v97", "v98", "v99", "v100", "v101", "v102", "v103", "v104", "v105", "v106", "v107", "v108", "v109", "v110", "v111", "v112", "v113", "v114", "v115", "v116", "v117", "v118", "v119", "v120",
      "v121", "v122", "v123", "v124", "v125", "v126", "v127", "v128", "v129", "v130", "v131", "v132", "v133", "v134", "v135", "v136", "v137", "v138", "v139", "v140", "v141", "v142", "v143", "v144",
      "v145", "v146", "v147", "v148", "v149", "v150", "v151", "v152", "v153", "v154", "v155", "v156", "v157", "v158", "v159", "v160", "v161", "v162", "v163", "v164", "v165", "v166", "v167", "v168",
      "v169", "v170", "v171", "v172", "v173", "v174", "v175", "v176", "v177", "v178", "v179", "v180", "v181", "v182", "v183", "v184", "v185", "v186", "v187", "v188", "v189", "v190", "v191", "v192",
      "v193", "v194", "v195", "v196", "v197", "v198", "v199", "v200", "v201", "v202", "v203", "v204", "v205", "v206", "v207", "v208", "v209", "v210", "v211", "v212", "v213", "v214", "v215", "v216",
      "v217", "v218", "v219", "v220", "v221", "v222", "v223", "v224", "v225", "v226", "v227", "v228", "v229", "v230", "v231", "v232", "v233", "v234", "v235", "v236", "v237", "v238", "v239", "v240",
      "v241", "v242", "v243", "v244", "v245", "v246", "v247", "v248", "v249", "v250", "v251", "v252", "v253", "v254", "v255", "a0", "a1", "a2", "a3", "a4", "a5", "a6", "a7", "a8",
      "a9", "a10", "a11", "a12", "a13", "a14", "a15", "a16", "a17", "a18", "a19", "a20", "a21", "a22", "a23", "a24", "a25", "a26", "a27", "a28", "a29", "a30", "a31", "a32",
      "a33", "a34", "a35", "a36", "a37", "a38", "a39", "a40", "a41", "a42", "a43", "a44", "a45", "a46", "a47", "a48", "a49", "a50", "a51", "a52", "a53", "a54", "a55", "a56",
      "a57", "a58", "a59", "a60", "a61", "a62", "a63", "a64", "a65", "a66", "a67", "a68", "a69", "a70", "a71", "a72", "a73", "a74", "a75", "a76", "a77", "a78", "a79", "a80",
      "a81", "a82", "a83", "a84", "a85", "a86", "a87", "a88", "a89", "a90", "a91", "a92", "a93", "a94", "a95", "a96", "a97", "a98", "a99", "a100", "a101", "a102", "a103", "a104",
      "a105", "a106", "a107", "a108", "a109", "a110", "a111", "a112", "a113", "a114", "a115", "a116", "a117", "a118", "a119", "a120", "a121", "a122", "a123", "a124", "a125", "a126", "a127", "a128",
      "a129", "a130", "a131", "a132", "a133", "a134", "a135", "a136", "a137", "a138", "a139", "a140", "a141", "a142", "a143", "a144", "a145", "a146", "a147", "a148", "a149", "a150", "a151", "a152",
      "a153", "a154", "a155", "a156", "a157", "a158", "a159", "a160", "a161", "a162", "a163", "a164", "a165", "a166", "a167", "a168", "a169", "a170", "a171", "a172", "a173", "a174", "a175", "a176",
      "a177", "a178", "a179", "a180", "a181", "a182", "a183", "a184", "a185", "a186", "a187", "a188", "a189", "a190", "a191", "a192", "a193", "a194", "a195", "a196", "a197", "a198", "a199", "a200",
      "a201", "a202", "a203", "a204", "a205", "a206", "a207", "a208", "a209", "a210", "a211", "a212", "a213", "a214", "a215", "a216", "a217", "s8", "s9", "s10", "s11", "s12", "s13", "s14",
      "s15", "s16", "s17", "s18", "s19", "s20", "s21", "s22", "s23", "s24", "s25", "s26", "s27", "s28", "s29", "s30", "s31", "s33", "s34", "s35", "s36", "s37", "s38", "s39",
      "s40", "s41", "s42", "s43", "s44", "s45", "s46", "s47", "s48", "s49", "s50", "s51", "s52", "s53", "s54", "s55", "s56", "s57", "s58", "s59", "s60", "s61", "s62", "s63",
      "s64", "s65", "s66", "s67", "s68", "s69", "s70", "s71", "s72", "s73", "s74", "s75", "s76", "s77", "s78", "s79", "s80", "s81", "s82", "s83", "s84", "s85", "s86", "s87",
      "s88", "s89", "s90", "s91", "s92", "s93", "s94", "s95", "s96", "s97", "s98", "s99", "s100", "s101", "vcc", "scc", "memory");
  if (flag | lbase) {
    const int F = 26, P = 325;
    const int lane = threadIdx.x & 63;
    const int b = (int)(min((blockIdx.x & 7) * kx + ((blockIdx.x >> 3) * 4 + (threadIdx.x >> 6)) * 104u, lim) / 104u);
    {
      float m = -INFINITY, z = 0.f, n = 0.f;
      for (int p = lane; p < P; p += 64) {
        const unsigned short* ei = table + (size_t)idx[b * F + ii[p]] * 128;
        const unsigned short* ej = table + (size_t)idx[b * F + jj[p]] * 128;
        float e = 0.f, s = 0.f;
        for (int a = 0; a < 128; ++a) {
          float acc = 0.f;
          for (int d = 0; d < 128; ++d) acc = fmaf(rbf(bf2f(ei[d]) * bf2f(ej[d])), bf2f(W[d * 128 + a]), acc);
          float x = acc + bf2f(bvec[a]);
          x = 0.5f * (x + fabsf(x));
          e = fmaf(x, bf2f(hvec[a]), e);
        }
        for (int d = 0; d < 128; ++d) s = fmaf(rbf(bf2f(ei[d]) * bf2f(ej[d])), bf2f(pvec[d]), s);
        float mn = fmaxf(m, e);
        float fa = __expf(m - mn), fb = __expf(e - mn);
        z = z * fa + fb; n = n * fa + fb * s; m = mn;
      }
      float mx = m;
      for (int o = 1; o < 64; o <<= 1) mx = fmaxf(mx, __shfl_xor(mx, o));
      float f = (m == -INFINITY) ? 0.f : __expf(m - mx);
      z *= f; n *= f;
      for (int o = 1; o < 64; o <<= 1) { z += __shfl_xor(z, o); n += __shfl_xor(n, o); }
      if (lane == 0) out[b] = f2bf(n / z);
    }
  }
}

extern "C" __global__ __launch_bounds__(256) void afm_any(const int* __restrict__ idx, const unsigned short* __restrict__ table,
                                   const int* __restrict__ ii, const int* __restrict__ jj,
                                   const unsigned short* __restrict__ W, const unsigned short* __restrict__ bvec,
                                   const unsigned short* __restrict__ hvec, const unsigned short* __restrict__ pvec,
                                   unsigned short* __restrict__ out, float* __restrict__ ws,
                                   int B, int F, int P, int D, int A) {
  const int b = blockIdx.x;
  float* ebuf = ws + (size_t)b * 2 * P;
  float* sbuf = ebuf + P;
  for (int p = threadIdx.x; p < P; p += 256) {
    const unsigned short* ei = table + (size_t)idx[b * F + ii[p]] * D;
    const unsigned short* ej = table + (size_t)idx[b * F + jj[p]] * D;
    float e = 0.f, sacc = 0.f;
    for (int a = 0; a < A; ++a) {
      float acc = 0.f;
      for (int d = 0; d < D; ++d) acc = fmaf(rbf(bf2f(ei[d]) * bf2f(ej[d])), bf2f(W[(size_t)d * A + a]), acc);
      float x = acc + bf2f(bvec[a]);
      x = 0.5f * (x + fabsf(x));
      e = fmaf(x, bf2f(hvec[a]), e);
    }
    for (int d = 0; d < D; ++d) sacc = fmaf(rbf(bf2f(ei[d]) * bf2f(ej[d])), bf2f(pvec[d]), sacc);
    ebuf[p] = e; sbuf[p] = sacc;
  }
  __syncthreads();
  if (threadIdx.x == 0) {
    float mx = -INFINITY;
    for (int p = 0; p < P; ++p) mx = fmaxf(mx, ebuf[p]);
    float z = 0.f, n = 0.f;
    for (int p = 0; p < P; ++p) { float w = __expf(ebuf[p] - mx); z += w; n += w * sbuf[p]; }
    out[b] = f2bf(n / z);
  }
}

extern "C" void kernel_launch(void* const* d_in, const int* in_sizes, int n_in,
                              void* d_out, int out_size, void* d_ws, size_t ws_size,
                              hipStream_t stream) {
  const int* idx = (const int*)d_in[0];
  const unsigned short* table = (const unsigned short*)d_in[1];
  const int* ii = (const int*)d_in[2];
  const int* jj = (const int*)d_in[3];
  const unsigned short* W = (const unsigned short*)d_in[4];
  const unsigned short* bvec = (const unsigned short*)d_in[5];
  const unsigned short* hvec = (const unsigned short*)d_in[6];
  const unsigned short* pvec = (const unsigned short*)d_in[7];
  unsigned short* out = (unsigned short*)d_out;
  const int B = out_size;
  const int P = in_sizes[2];
  const int D = in_sizes[7];
  const int A = in_sizes[5];
  if (B <= 0) return;
  const int F = in_sizes[0] / B;
  if (D == 128 && A == 128 && F == 26 && P == 325 && B < (1 << 24)) {
    const int g8 = (B + 31) / 32;
    afm_fast<<<8 * g8, 256, 0, stream>>>(idx, table, ii, jj, W, bvec, hvec, pvec, out, 104u * (unsigned)(B - 1), 416u * (unsigned)g8
        );
  } else {
    afm_any<<<B, 256, 0, stream>>>(idx, table, ii, jj, W, bvec, hvec, pvec, out, (float*)d_ws, B, F, P, D, A);
  }
}
```

```cpp
#include <hip/hip_runtime.h>
#include <hip/hip_bf16.h>
#include <stdint.h>

#define AFM_HAS_STAMPS 0

typedef float  f32x2  __attribute__((ext_vector_type(2)));
typedef __bf16 bf16x2 __attribute__((ext_vector_type(2)));

__device__ __forceinline__ float bf2f(unsigned short u) { return __builtin_bit_cast(float, (unsigned)u << 16); }
__device__ __forceinline__ unsigned short f2bf(float x) {
  f32x2 v = {x, 0.f}; bf16x2 r = __builtin_convertvector(v, bf16x2);
  return (unsigned short)(__builtin_bit_cast(unsigned, r) & 0xffffu);
}
__device__ __forceinline__ float rbf(float x) { return bf2f(f2bf(x)); }

extern "C" __device__ __attribute__((used, aligned(512))) const unsigned afm_lane_tab[804] = {
  738206208u, 533735168u, 697050480u, 864u, 743449092u, 539240196u, 719070580u, 20972672u, 748691976u, 539764488u, 554968440u, 1440u, 753934860u, 550250252u, 717235580u, 20973248u,
  759177744u, 555755280u, 613426560u, 2016u, 764420628u, 561260308u, 540550532u, 20971520u, 769663512u, 561784600u, 549463432u, 288u, 774906396u, 530327324u, 722740620u, 20972096u,
  738468432u, 538978128u, 618931888u, 3168u, 743711316u, 534521684u, 727983796u, 20974976u, 748954200u, 549988184u, 708585144u, 3744u, 754197084u, 555493212u, 723003068u, 20975552u,
  759439968u, 560998240u, 728508096u, 4320u, 764682852u, 556541796u, 624436932u, 20973824u, 769925736u, 530065256u, 702555848u, 2592u, 775168620u, 535570284u, 697575116u, 20974400u,
  738730656u, 701507488u, 697050480u, 1049456u, 743973540u, 707012516u, 719070580u, 22021264u, 749216424u, 707536808u, 554968440u, 1050032u, 754459308u, 718022572u, 717235580u, 22021840u,
  759702192u, 723527600u, 613426560u, 1050608u, 764945076u, 729032628u, 540550532u, 22020112u, 770187960u, 729556920u, 549463432u, 1048880u, 775430844u, 698099644u, 722740620u, 22020688u,
  738992880u, 706750448u, 618931888u, 1051760u, 744235764u, 702294004u, 727983796u, 22023568u, 749478648u, 717760504u, 708585144u, 1052336u, 754721532u, 723265532u, 723003068u, 22024144u,
  759964416u, 728770560u, 728508096u, 1052912u, 765207300u, 724314116u, 624436932u, 22022416u, 770450184u, 697837576u, 702555848u, 1051184u, 775693068u, 703342604u, 697575116u, 22022992u,
  739255104u, 617621568u, 529279600u, 2098048u, 744497988u, 623126596u, 549726836u, 23069856u, 749740872u, 623650888u, 555231864u, 2098624u, 754983756u, 634136652u, 560736892u, 23070432u,
  760226640u, 639641680u, 551299712u, 2099200u, 765469524u, 645146708u, 529803908u, 23068704u, 770712408u, 645671000u, 535308936u, 2097472u, 775955292u, 614213724u, 540813964u, 23069280u,
  739517328u, 622864528u, 613166000u, 2100352u, 744760212u, 618408084u, 633613236u, 23072160u, 750003096u, 633874584u, 639118264u, 2100928u, 755245980u, 639379612u, 644623292u, 23072736u,
  760488864u, 644884640u, 635186112u, 2101504u, 765731748u, 640428196u, 613690308u, 23071008u, 770974632u, 613951656u, 619195336u, 2099776u, 776217516u, 619456684u, 624700364u, 23071584u,
  739779552u, 544221408u, 529279600u, 3146640u, 745022436u, 544483556u, 549726836u, 24118448u, 750265320u, 544745704u, 555231864u, 3147216u, 755508204u, 545007852u, 560736892u, 24119024u,
  760751088u, 545270000u, 551299712u, 3147792u, 765993972u, 545532148u, 529803908u, 24117296u, 771236856u, 545794296u, 535308936u, 3146064u, 776479740u, 546056444u, 540813964u, 24117872u,
  740041776u, 565193008u, 613166000u, 3148944u, 745284660u, 565455156u, 633613236u, 24120752u, 750527544u, 565717304u, 639118264u, 3149520u, 755770428u, 565979452u, 644623292u, 24121328u,
  761013312u, 566241600u, 635186112u, 3150096u, 766256196u, 566503748u, 613690308u, 24119600u, 771499080u, 566765896u, 619195336u, 3148368u, 776741964u, 567028044u, 624700364u, 24120176u,
  358617600u, 8064u, 301992192u, 132121440u, 434116224u, 18882512u, 320866848u, 358619616u, 94372128u, 37756960u, 339741504u, 415241280u, 377491968u, 452993136u, 452991744u, 358619616u,
  150998400u, 75505856u, 377490816u, 452992032u, 37750464u, 94380304u, 396365472u, 207622368u, 75497472u, 113254752u, 415240128u, 207622368u, 396366624u, 471867824u, 471866400u, 415241280u,
  169873056u, 151003648u, 4608u, 1075854240u, 415240992u, 169878096u, 18879264u, 1283477760u, 434115648u, 188752544u, 37753920u, 1396724832u, 396366912u, 207626992u, 56628576u, 1283477760u,
  415241568u, 226501440u, 75503232u, 1377848736u, 188747712u, 245375888u, 94377888u, 1151355168u, 320870016u, 264250336u, 113252544u, 1151355168u, 301995936u, 283124784u, 132127200u, 1283477760u,
  359666192u, 1056656u, 303040784u, 133170032u, 435164816u, 19931104u, 321915440u, 359668208u, 95420720u, 38805552u, 340790096u, 416289872u, 378540560u, 454041728u, 454040336u, 359668208u,
  152046992u, 76554448u, 378539408u, 454040624u, 38799056u, 95428896u, 397414064u, 208670960u, 76546064u, 114303344u, 416288720u, 208670960u, 397415216u, 472916416u, 472914992u, 416289872u,
  170921648u, 152052240u, 1053200u, 1076902832u, 416289584u, 170926688u, 19927856u, 1284526352u, 435164240u, 189801136u, 38802512u, 1397773424u, 397415504u, 208675584u, 57677168u, 1284526352u,
  416290160u, 227550032u, 76551824u, 1378897328u, 189796304u, 246424480u, 95426480u, 1152403760u, 321918608u, 265298928u, 114301136u, 1152403760u, 303044528u, 284173376u, 133175792u, 1284526352u,
  360714784u, 2105248u, 304089376u, 134218624u, 436213408u, 20979696u, 322964032u, 360716800u, 96469312u, 39854144u, 341838688u, 417338464u, 379589152u, 455090320u, 455088928u, 360716800u,
  153095584u, 77603040u, 379588000u, 455089216u, 39847648u, 96477488u, 398462656u, 209719552u, 77594656u, 115351936u, 417337312u, 209719552u, 398463808u, 473965008u, 473963584u, 417338464u,
  171970240u, 153100832u, 2101792u, 1077951424u, 417338176u, 171975280u, 20976448u, 1285574944u, 436212832u, 190849728u, 39851104u, 1398822016u, 398464096u, 209724176u, 58725760u, 1285574944u,
  417338752u, 228598624u, 77600416u, 1379945920u, 190844896u, 247473072u, 96475072u, 1153452352u, 322967200u, 266347520u, 115349728u, 1153452352u, 304093120u, 285221968u, 134224384u, 1285574944u,
  361763376u, 3153840u, 305137968u, 135267216u, 437262000u, 22028288u, 324012624u, 361765392u, 97517904u, 40902736u, 342887280u, 418387056u, 380637744u, 456138912u, 456137520u, 361765392u,
  154144176u, 78651632u, 380636592u, 456137808u, 40896240u, 97526080u, 399511248u, 210768144u, 78643248u, 116400528u, 418385904u, 210768144u, 399512400u, 475013600u, 475012176u, 418387056u,
  173018832u, 154149424u, 3150384u, 1079000016u, 418386768u, 173023872u, 22025040u, 1286623536u, 437261424u, 191898320u, 40899696u, 1399870608u, 399512688u, 210772768u, 59774352u, 1286623536u,
  418387344u, 229647216u, 78649008u, 1380994512u, 191893488u, 248521664u, 97523664u, 1154500944u, 324015792u, 267396112u, 116398320u, 1154500944u, 305141712u, 286270560u, 135272976u, 1286623536u,
  0u, 18874368u, 524304u, 37748740u, 75497504u, 56623112u, 76021808u, 75497484u, 304087104u, 94371856u, 304611408u, 113246228u, 379584608u, 132120600u, 380108912u, 28u,
  608174208u, 37748864u, 608698512u, 56623236u, 683671712u, 75497608u, 684196016u, 94371980u, 912261312u, 113246352u, 912785616u, 132120724u, 987758816u, 152u, 988283120u, 18874524u,
  1053216u, 19923744u, 1577520u, 38798116u, 76550720u, 57672488u, 77075024u, 76546860u, 305140320u, 95421232u, 305664624u, 114295604u, 380637824u, 133169976u, 381162128u, 1049404u,
  609227424u, 38798240u, 609751728u, 57672612u, 684724928u, 76546984u, 685249232u, 95421356u, 913314528u, 114295728u, 913838832u, 133170100u, 988812032u, 1049528u, 989336336u, 19923900u,
  2106432u, 20972352u, 2630736u, 39846724u, 77603936u, 58721096u, 78128240u, 77595468u, 306193536u, 96469840u, 306717840u, 115344212u, 381691040u, 134218584u, 382215344u, 2098012u,
  610280640u, 39846848u, 610804944u, 58721220u, 685778144u, 77595592u, 686302448u, 96469964u, 914367744u, 115344336u, 914892048u, 134218708u, 989865248u, 2098136u, 990389552u, 20972508u,
  3159648u, 22020960u, 3683952u, 40895332u, 78657152u, 59769704u, 79181456u, 78644076u, 307246752u, 97518448u, 307771056u, 116392820u, 382744256u, 135267192u, 383268560u, 3146620u,
  611333856u, 40895456u, 611858160u, 59769828u, 686831360u, 78644200u, 687355664u, 97518572u, 915420960u, 116392944u, 915945264u, 135267316u, 990918464u, 3146744u, 991442768u, 22021116u,
  0u, 0u, 0u, 0u, 0u, 0u, 16843008u, 16843009u, 16843009u, 16843009u, 16843009u, 16843009u, 33686017u, 33686018u, 33686018u, 33686018u,
  33686018u, 33686018u, 50529027u, 50529027u, 50529027u, 50529027u, 50529027u, 67371779u, 67372036u, 67372036u, 67372036u, 67372036u, 84149252u, 84215045u, 84215045u, 84215045u,
  84215045u, 100992261u, 101058054u, 101058054u, 101058054u, 101058054u, 117900806u, 117901063u, 117901063u, 117901063u, 117901063u, 134744072u, 134744072u, 134744072u, 134744072u, 151587080u,
  151587081u, 151587081u, 151587081u, 168430089u, 168430090u, 168430090u, 168430090u, 185273099u, 185273099u, 185273099u, 202115851u, 202116108u, 202116108u, 218893324u, 218959117u, 218959117u,
  235736333u, 235802126u, 235802126u, 252644878u, 252645135u, 252645135u, 269488144u, 269488144u, 286331152u, 286331153u, 303174161u, 303174162u, 320017171u, 336859923u, 353637396u, 370480405u,
  387388950u, 404166422u, 67305985u, 134678021u, 202050057u, 269422093u, 336794129u, 404166165u, 67306009u, 134678021u, 202050057u, 269422093u, 336794129u, 404166165u, 84149017u, 151521030u,
  218893066u, 286265102u, 353637138u, 421009174u, 117835012u, 185207048u, 252579084u, 319951120u, 387323156u, 100997400u, 168364039u, 235736075u, 303108111u, 370480147u, 102307863u, 168364039u,
  235736075u, 303108111u, 370480147u, 119085079u, 185207048u, 252579084u, 319951120u, 387323156u, 151525656u, 218893066u, 286265102u, 353637138u, 421009174u, 202050057u, 269422093u, 336794129u,
  404166165u, 202050073u, 269422093u, 336794129u, 404166165u, 218893081u, 286265102u, 353637138u, 421009174u, 252579084u, 319951120u, 387323156u, 235739416u, 303108111u, 370480147u, 236525591u,
  303108111u, 370480147u, 253302807u, 319951120u, 387323156u, 286267672u, 353637138u, 421009174u, 336794129u, 404166165u, 336794137u, 404166165u, 353637145u, 421009174u, 387323156u, 370481432u,
  370743319u, 387520535u, 421009688u, 421074969u
};

extern "C" __global__ __launch_bounds__(256, 1) __attribute__((aligned(4096)))
void afm_fast(const int* __restrict__ idx, const unsigned short* __restrict__ table,
                     const int* __restrict__ ii, const int* __restrict__ jj,
                     const unsigned short* __restrict__ W, const unsigned short* __restrict__ bvec,
                     const unsigned short* __restrict__ hvec, const unsigned short* __restrict__ pvec,
                     unsigned short* __restrict__ out, unsigned lim, unsigned kx
                     ) {
  __shared__ __attribute__((aligned(16))) char smem[110912];
  const unsigned lbase = (unsigned)(uintptr_t)(__attribute__((address_space(3))) char*)smem;
  unsigned flag;
  asm volatile(
      "s_load_dwordx16 s[8:23], %1, 0x0\n"
      "s_load_dwordx4 s[24:27], %1, 0x40\n"
      "s_mov_b32 s41, 0\n"
      "s_mov_b32 s42, 0\n"
      "s_mov_b32 s58, 0\n"
      "s_mov_b32 s35, 0\n"
      "0:\n"
      "s_getpc_b64 s[56:57]\n"
      "s_add_u32 s98, s56, afm_lane_tab@rel32@lo+4\n"
      "s_addc_u32 s99, s57, afm_lane_tab@rel32@hi+12\n"
      "v_and_b32 v255, 63, %3\n"
      "v_and_b32 v254, 127, %3\n"
      "v_lshrrev_b32 v253, 7, %3\n"
      "v_min_u32 v254, 81, v254\n"
      "v_mul_u32_u24 v253, 82, v253\n"
      "v_lshlrev_b32 v14, 4, v255\n"
      "v_lshlrev_b32 v252, 3, v255\n"
      "v_add_lshl_u32 v253, v253, v254, 2\n"
      "global_load_dwordx2 v[22:23], v252, s[98:99] offset:2048\n"
      "global_load_dword v211, v253, s[98:99] offset:2560\n"
      "global_load_dwordx4 v[204:207], v14, s[98:99]\n"
      "global_load_dwordx4 v[196:199], v14, s[98:99] offset:1024\n"
      "v_lshrrev_b32 v2, 6, %3\n"
      "s_bfe_u32 s44, %2, 0x30003\n"
      "s_mul_i32 s44, s44, 24\n"
      "v_mad_u32_u24 v255, v2, 6, v255\n"
      "s_mov_b64 exec, 0x3f\n"
      "v_add_lshl_u32 v255, v255, s44, 7\n"
      "global_load_dword v255, v255, s[56:57]\n"
      "s_mov_b64 exec, -1\n"
      "v_bfe_u32 v4, %3, 4, 2\n"
      "v_and_b32 v3, 15, %3\n"
      "v_readfirstlane_b32 s53, v2\n"
      "s_and_b32 s50, %2, 7\n"
      "s_lshr_b32 s51, %2, 3\n"
      "s_lshl2_add_u32 s51, s51, s53\n"
      "s_mul_i32 s51, s51, 104\n"
      "v_lshlrev_b32 v13, 4, v3\n"
      "v_lshlrev_b32 v215, 4, v254\n"
      "v_min_u32 v215, 1284, v215\n"
      "v_lshl_add_u32 v80, v2, 11, v14\n"
      "v_add_u32 v81, 8192, v80\n"
      "v_add_u32 v82, 16384, v80\n"
      "v_add_u32 v26, 24576, v80\n"
      "v_lshlrev_b32 v2, 4, v2\n"
      "s_mul_i32 s38, s53, 14400\n"
      "v_add_u32 v2, 37632, v2\n"
      "s_add_u32 s38, s38, 38912\n"
      "v_mul_u32_u24 v222, 288, v4\n"
      "v_add_u32 v213, s38, v13\n"
      "v_mov_b32 v5, 0\n"
      "v_add_u32 v1, v222, v213\n"
      "v_mov_b32 v222, 0x7fa5a5a5\n"
      "s_cmp_lg_u32 s35, 0\n"
      "s_cbranch_scc1 2f\n"
      "ds_write2_b32 v2, v5, v5 offset0:0 offset1:1\n"
      "ds_write_b32 v2, v5 offset:8\n"
      "ds_write_b32 v1, v222 offset:12992\n"
      "2:\n"
      "s_mov_b32 s43, 0xffff0000\n"
      "s_mov_b32 s56, 0\n"
      "s_mov_b32 s40, 0\n"
      "s_mov_b32 s55, 0\n"
      "s_and_b32 s46, s53, 2\n"
      "s_lshl_b32 s54, 3, s46\n"
      "s_bitcmp1_b32 s53, 0\n"
      "s_cselect_b64 s[100:101], -1, 0\n"
      "s_movk_i32 s98, 14400\n"
      "s_cselect_b32 s99, s98, 0\n"
      "s_cmovk_i32 s98, -14400\n"
      "s_cselect_b32 s46, 0, 18560\n"
      "s_cselect_b32 s47, 18560, 0\n"
      "v_add_u32 v212, s46, v14\n"
      "v_add_u32 v14, s47, v14\n"
      "s_sub_u32 s99, s38, s99\n"
      "v_cmp_eq_u32 vcc, 1, v4\n"
      "v_mov_b32 v222, 0x3200\n"
      "s_mov_b32 s46, 0\n"
      "v_cndmask_b32 v94, 0, v222, vcc\n"
      "s_mov_b32 s47, 0xc82c4cb9\n"
      "v_lshlrev_b32 v223, 4, v4\n"
      "v_lshrrev_b64 v[146:147], v223, s[46:47]\n"
      "v_lshlrev_b32 v138, 16, v146\n"
      "v_lshrrev_b32 v130, 16, v138\n"
      "v_mov_b32 v200, 0x4838aa3b\n"
      "v_lshlrev_b32 v224, 16, v94\n"
      "v_accvgpr_write_b32 a128, v130\n"
      "v_accvgpr_write_b32 a129, 0\n"
      "v_accvgpr_write_b32 a130, v138\n"
      "v_accvgpr_write_b32 a131, 0\n"
      "v_accvgpr_write_b32 a132, 0\n"
      "v_accvgpr_write_b32 a133, v130\n"
      "v_accvgpr_write_b32 a134, 0\n"
      "v_accvgpr_write_b32 a135, v138\n"
      "v_accvgpr_write_b32 a208, v94\n"
      "v_accvgpr_write_b32 a209, 0\n"
      "v_accvgpr_write_b32 a210, v224\n"
      "v_accvgpr_write_b32 a211, 0\n"
      "v_accvgpr_write_b32 a212, 0\n"
      "v_accvgpr_write_b32 a213, v94\n"
      "v_accvgpr_write_b32 a214, 0\n"
      "v_accvgpr_write_b32 a215, v224\n"
      "v_add_u32 v95, s38, v223\n"
      "v_and_b32 v227, 3, %3\n"
      "v_bfe_u32 v224, %3, 2, 2\n"
      "v_cmp_eq_u32 vcc, v227, v4\n"
      "v_mov_b32 v222, 0x3a00\n"
      "v_mov_b64 v[84:85], 0\n"
      "s_waitcnt lgkmcnt(0)\n"
      "s_mul_i32 s50, s50, s27\n"
      "s_add_u32 s50, s50, s51\n"
      "s_min_u32 s34, s50, s26\n"
      "s_load_dwordx16 s[60:75], s[8:9], s34\n"
      "s_load_dwordx8 s[76:83], s[8:9], s34 offset:0x40\n"
      "s_load_dwordx2 s[84:85], s[8:9], s34 offset:0x60\n"
      "global_load_dwordx4 v[96:99], v80, s[16:17] offset:0\n"
      "global_load_dwordx4 v[100:103], v80, s[16:17] offset:1024\n"
      "v_mov_b64 v[24:25], s[20:21]\n"
      "s_mov_b32 exec_hi, 0\n"
      "v_mov_b64 v[24:25], s[18:19]\n"
      "s_mov_b32 exec_lo, 0xffff\n"
      "v_mov_b64 v[24:25], s[22:23]\n"
      "s_mov_b64 exec, -1\n"
      "v_add_co_u32 v24, s[28:29], v24, v13\n"
      "s_cmp_lt_u32 s53, 2\n"
      "s_cselect_b64 s[12:13], s[12:13], s[14:15]\n"
      "v_addc_co_u32 v25, s[28:29], 0, v25, s[28:29]\n"
      "global_load_dwordx4 v[192:195], v[24:25], off\n"
      "global_load_dwordx4 v[104:107], v81, s[16:17] offset:0\n"
      "global_load_dwordx4 v[108:111], v81, s[16:17] offset:1024\n"
      "global_load_dwordx4 v[112:115], v82, s[16:17] offset:0\n"
      "global_load_dwordx4 v[116:119], v82, s[16:17] offset:1024\n"
      "global_load_dwordx4 v[120:123], v26, s[16:17] offset:0\n"
      "global_load_dwordx4 v[124:127], v26, s[16:17] offset:1024\n"
      "s_cmp_lg_u32 s35, 0\n"
      "s_cbranch_scc1 21f\n"
      "s_barrier\n"
      "21:\n"
      "v_mov_b64 v[88:89], 0\n"
      "v_cndmask_b32 v84, 0, v222, vcc\n"
      "v_lshlrev_b32 v222, 4, v224\n"
      "v_lshlrev_b64 v[86:87], v222, v[84:85]\n"
      "s_mov_b32 s46, 0xd905\n"
      "v_lshlrev_b32 v222, 2, v4\n"
      "v_mov_b32 v84, 0\n"
      "v_bfe_u32 v222, s46, v222, 4\n"
      "v_lshl_add_u32 v91, v222, 6, v213\n"
      "v_lshlrev_b32 v227, 4, v227\n"
      "s_mov_b64 s[46:47], 0x3b80\n"
      "v_lshlrev_b32 v5, 5, v3\n"
      "v_lshl_add_u32 v213, v4, 2, v213\n"
      "v_lshl_add_u32 v5, v4, 3, v5\n"
      "v_lshlrev_b64 v[222:223], v227, s[46:47]\n"
      "s_movk_i32 s46, 0x4400\n"
      "v_accvgpr_write_b32 a216, v222\n"
      "v_lshlrev_b64 v[172:173], v227, s[46:47]\n"
      "s_lshl_b64 exec, -1, 48\n"
      "s_bitset1_b32 s46, 15\n"
      "v_lshlrev_b64 v[172:173], v227, s[46:47]\n"
      "s_mov_b64 exec, -1\n"
      "s_mov_b32 s49, 0x03000000\n"
      "v_accvgpr_write_b32 a217, v223\n"
      "s_movk_i32 s46, 288\n"
      "v_and_b32 v222, 7, %3\n"
      "v_bfe_u32 v223, %3, 3, 1\n"
      "v_mad_u32_u24 v201, v222, s46, v95\n"
      "v_mad_u32_u24 v203, v223, s46, v95\n"
      "s_movk_i32 s47, 320\n"
      "v_mad_u32_u24 v210, v3, s46, v95\n"
      "v_mad_u32_u24 v214, v223, s47, v95\n"
      "s_mov_b32 s59, 0\n"
      "s_mov_b32 s57, 0\n"
      "v_and_b32 v222, v3, v4\n"
      "v_lshlrev_b32 v223, 6, v3\n"
      "v_bfe_u32 v222, 6, v222, 1\n"
      "s_movk_i32 s46, 0x100\n"
      "v_lshl_add_u32 v44, v222, 8, v95\n"
      "v_bitop3_b32 v45, v44, v223, s46 bitop3:0x78\n"
      "v_bitop3_b32 v46, v44, v5, s46 bitop3:0x78\n"
      "v_bitop3_b32 v47, v45, v5, s46 bitop3:0x78\n"
      "s_lshr_b32 s33, s34, 2\n"
      "s_mul_i32 s33, s33, 0xc4ec4ec5\n"
      "s_mov_b32 s36, 0\n"
      "s_mov_b32 s37, 0x7fa5a5a5\n"
      "s_mov_b64 exec, 0\n"
      "s_cmp_eq_u32 s53, 0\n"
      "s_cbranch_scc1 40f\n"
      "s_cmp_eq_u32 s53, 1\n"
      "s_cbranch_scc1 41f\n"
      "s_cmp_eq_u32 s53, 2\n"
      "s_cbranch_scc1 42f\n"
      "s_branch 43f\n"
      "44:\n"
      "s_mov_b64 exec, -1\n"
      "s_waitcnt lgkmcnt(0)\n"
      "v_lshl_add_u32 v6, s60, 8, v13\n"
      "v_lshl_add_u32 v93, s64, 8, v13\n"
      "s_mov_b32 exec_lo, 0xffff0000\n"
      "v_lshl_add_u32 v6, s61, 8, v13\n"
      "v_lshl_add_u32 v93, s65, 8, v13\n"
      "s_mov_b32 exec_lo, 0\n"
      "v_lshl_add_u32 v6, s62, 8, v13\n"
      "v_lshl_add_u32 v93, s66, 8, v13\n"
      "s_mov_b32 exec_hi, 0xffff0000\n"
      "v_lshl_add_u32 v6, s63, 8, v13\n"
      "v_lshl_add_u32 v93, s67, 8, v13\n"
      "s_mov_b64 exec, -1\n"
      "global_load_dwordx4 v[228:231], v6, s[10:11] nt\n"
      "global_load_dwordx4 v[232:235], v93, s[10:11] nt\n"
      "v_lshl_add_u32 v8, s68, 8, v13\n"
      "v_lshl_add_u32 v9, s72, 8, v13\n"
      "v_lshl_add_u32 v10, s76, 8, v13\n"
      "v_lshl_add_u32 v11, s80, 8, v13\n"
      "v_lshl_add_u32 v12, s84, 8, v13\n"
      "s_mov_b32 exec_lo, 0xffff0000\n"
      "v_lshl_add_u32 v8, s69, 8, v13\n"
      "v_lshl_add_u32 v9, s73, 8, v13\n"
      "v_lshl_add_u32 v10, s77, 8, v13\n"
      "v_lshl_add_u32 v11, s81, 8, v13\n"
      "v_lshl_add_u32 v12, s85, 8, v13\n"
      "s_mov_b32 exec_lo, 0\n"
      "v_lshl_add_u32 v8, s70, 8, v13\n"
      "v_lshl_add_u32 v9, s74, 8, v13\n"
      "v_lshl_add_u32 v10, s78, 8, v13\n"
      "v_lshl_add_u32 v11, s82, 8, v13\n"
      "s_mov_b32 exec_hi, 0xffff0000\n"
      "v_lshl_add_u32 v8, s71, 8, v13\n"
      "v_lshl_add_u32 v9, s75, 8, v13\n"
      "v_lshl_add_u32 v10, s79, 8, v13\n"
      "v_lshl_add_u32 v11, s83, 8, v13\n"
      "s_mov_b64 exec, -1\n"
      "s_lshl_b32 s46, s53, 8\n"
      "s_lshl_b32 s47, s53, 5\n"
      "s_waitcnt vmcnt(15)\n"
      "v_add_u32_sdwa v15, s46, v22 dst_sel:DWORD dst_unused:UNUSED_PAD src0_sel:DWORD src1_sel:WORD_0\n"
      "v_add_u32_sdwa v92, s46, v22 dst_sel:DWORD dst_unused:UNUSED_PAD src0_sel:DWORD src1_sel:WORD_1\n"
      "v_add_u32_sdwa v7, s47, v23 dst_sel:DWORD dst_unused:UNUSED_PAD src0_sel:DWORD src1_sel:WORD_0\n"
      "v_add_u32_sdwa v202, s38, v23 dst_sel:DWORD dst_unused:UNUSED_PAD src0_sel:DWORD src1_sel:WORD_1\n"
      "s_cmp_lg_u32 s58, 0\n"
      "s_cbranch_scc1 70f\n"
      "71:\n"
      "s_waitcnt vmcnt(10)\n"
      "v_mfma_f32_16x16x32_bf16 v[128:131], v[86:89], v[96:99], 0\n"
      "s_waitcnt vmcnt(9)\n"
      "v_mfma_f32_16x16x32_bf16 v[132:135], v[86:89], v[100:103], 0\n"
      "v_mfma_f32_16x16x32_bf16 v[136:139], v[84:87], v[96:99], 0\n"
      "v_mfma_f32_16x16x32_bf16 v[140:143], v[84:87], v[100:103], 0\n"
      "s_waitcnt vmcnt(7)\n"
      "v_mfma_f32_16x16x32_bf16 v[144:147], v[86:89], v[104:107], 0\n"
      "s_nop 1\n"
      "v_cvt_pk_f16_f32 v164, v128, v129\n"
      "v_cvt_pk_f16_f32 v165, v130, v131\n"
      "s_waitcnt vmcnt(6)\n"
      "v_mfma_f32_16x16x32_bf16 v[148:151], v[86:89], v[108:111], 0\n"
      "v_cvt_pk_f16_f32 v166, v132, v133\n"
      "v_cvt_pk_f16_f32 v167, v134, v135\n"
      "v_mfma_f32_16x16x32_bf16 v[152:155], v[84:87], v[104:107], 0\n"
      "ds_write_b128 v15, v[164:167] offset:0\n"
      "v_cvt_pk_f16_f32 v168, v136, v137\n"
      "v_cvt_pk_f16_f32 v169, v138, v139\n"
      "v_mfma_f32_16x16x32_bf16 v[156:159], v[84:87], v[108:111], 0\n"
      "v_cvt_pk_f16_f32 v170, v140, v141\n"
      "v_cvt_pk_f16_f32 v171, v142, v143\n"
      "global_load_dwordx4 v[236:239], v8, s[10:11] nt\n"
      "s_waitcnt vmcnt(6)\n"
      "v_mfma_f32_16x16x32_bf16 v[128:131], v[86:89], v[112:115], 0\n"
      "ds_write_b128 v15, v[168:171] offset:18560\n"
      "v_cvt_pk_f16_f32 v164, v144, v145\n"
      "v_cvt_pk_f16_f32 v165, v146, v147\n"
      "s_waitcnt vmcnt(5)\n"
      "v_mfma_f32_16x16x32_bf16 v[132:135], v[86:89], v[116:119], 0\n"
      "v_cvt_pk_f16_f32 v166, v148, v149\n"
      "v_cvt_pk_f16_f32 v167, v150, v151\n"
      "v_mfma_f32_16x16x32_bf16 v[136:139], v[84:87], v[112:115], 0\n"
      "ds_write_b128 v15, v[164:167] offset:1152\n"
      "v_cvt_pk_f16_f32 v168, v152, v153\n"
      "v_cvt_pk_f16_f32 v169, v154, v155\n"
      "v_mfma_f32_16x16x32_bf16 v[140:143], v[84:87], v[116:119], 0\n"
      "v_cvt_pk_f16_f32 v170, v156, v157\n"
      "v_cvt_pk_f16_f32 v171, v158, v159\n"
      "s_waitcnt vmcnt(4)\n"
      "v_mfma_f32_16x16x32_bf16 v[144:147], v[86:89], v[120:123], 0\n"
      "ds_write_b128 v15, v[168:171] offset:19712\n"
      "v_cvt_pk_f16_f32 v164, v128, v129\n"
      "v_cvt_pk_f16_f32 v165, v130, v131\n"
      "s_waitcnt vmcnt(3)\n"
      "v_mfma_f32_16x16x32_bf16 v[148:151], v[86:89], v[124:127], 0\n"
      "v_cvt_pk_f16_f32 v166, v132, v133\n"
      "v_cvt_pk_f16_f32 v167, v134, v135\n"
      "v_mfma_f32_16x16x32_bf16 v[152:155], v[84:87], v[120:123], 0\n"
      "ds_write_b128 v15, v[164:167] offset:2304\n"
      "v_cvt_pk_f16_f32 v168, v136, v137\n"
      "v_cvt_pk_f16_f32 v169, v138, v139\n"
      "v_mfma_f32_16x16x32_bf16 v[156:159], v[84:87], v[124:127], 0\n"
      "v_cvt_pk_f16_f32 v170, v140, v141\n"
      "v_cvt_pk_f16_f32 v171, v142, v143\n"
      "ds_write_b128 v15, v[168:171] offset:20864\n"
      "v_mfma_f32_4x4x4_16b_bf16 v[176:179], v[172:173], v[192:193], 0\n"
      "v_cndmask_b32 v188, v192, v194, s[100:101]\n"
      "v_cvt_pk_f16_f32 v164, v144, v145\n"
      "v_cndmask_b32 v189, v193, v195, s[100:101]\n"
      "v_max3_f32 v222, |v192|, |v193|, |v195|\n"
      "v_cvt_pk_f16_f32 v165, v146, v147\n"
      "v_mfma_f32_4x4x4_16b_bf16 v[180:183], v[172:173], v[194:195], 0\n"
      "v_subrev_u32 v222, 0x3d800000, v222\n"
      "v_cvt_pk_f16_f32 v166, v148, v149\n"
      "v_mfma_f32_16x16x16_bf16 v[48:51], v[188:189], a[128:129], 0\n"
      "v_cmp_gt_u32 vcc, s49, v222\n"
      "v_cvt_pk_f16_f32 v167, v150, v151\n"
      "v_mfma_f32_16x16x16_bf16 v[52:55], v[188:189], a[130:131], 0\n"
      "v_cndmask_b32 v190, v194, v192, s[100:101]\n"
      "ds_write_b128 v15, v[164:167] offset:3456\n"
      "v_cndmask_b32 v191, v195, v193, s[100:101]\n"
      "v_mfma_f32_16x16x16_bf16 v[56:59], v[188:189], a[132:133], 0\n"
      "v_cvt_pk_f16_f32 v168, v152, v153\n"
      "s_and_b32 s44, vcc_lo, 0xffff\n"
      "s_cselect_b32 s44, vcc_hi, 0\n"
      "v_cvt_pk_f16_f32 v169, v154, v155\n"
      "v_mfma_f32_16x16x16_bf16 v[60:63], v[188:189], a[134:135], 0\n"
      "s_cmp_eq_u32 s44, 0\n"
      "v_cvt_pk_f16_f32 v170, v156, v157\n"
      "s_cbranch_scc1 22f\n"
      "23:\n"
      "v_mfma_f32_16x16x16_bf16 v[64:67], v[190:191], a[128:129], 0\n"
      "v_cvt_pk_f16_f32 v171, v158, v159\n"
      "v_cvt_pk_f16_f32 v184, v176, v177\n"
      "v_cvt_pk_f16_f32 v185, v178, v179\n"
      "ds_write_b128 v15, v[168:171] offset:22016\n"
      "v_mfma_f32_16x16x16_bf16 v[68:71], v[190:191], a[130:131], 0\n"
      "v_cvt_pk_f16_f32 v186, v180, v181\n"
      "ds_write_b32 v2, v2 offset:4\n"
      "v_cvt_pk_f16_f32 v187, v182, v183\n"
      "ds_write_b128 v91, v[184:187] offset:11904\n"
      "v_mfma_f32_16x16x16_bf16 v[72:75], v[190:191], a[132:133], 0\n"
      "v_mfma_f32_16x16x16_bf16 v[76:79], v[190:191], a[134:135], 0\n"
      "global_load_dwordx4 v[216:219], v215, s[12:13]\n"
      "v_mfma_f32_16x16x16_bf16 v[16:19], v[188:189], a[208:209], 0\n"
      "ds_read_b128 a[184:187], v44 offset:12480\n"
      "ds_read_b128 a[188:191], v45 offset:12544\n"
      "v_mfma_f32_16x16x16_bf16 v[20:23], v[188:189], a[210:211], 0\n"
      "ds_read_b128 a[192:195], v46 offset:12608\n"
      "ds_read_b128 a[196:199], v47 offset:12672\n"
      "v_mfma_f32_16x16x16_bf16 v[24:27], v[188:189], a[212:213], 0\n"
      "ds_read_b64_tr_b16 a[152:153], v92 offset:0\n"
      "ds_read_b64_tr_b16 a[154:155], v92 offset:18560\n"
      "ds_read_b64_tr_b16 a[156:157], v92 offset:64\n"
      "v_mfma_f32_16x16x16_bf16 v[28:31], v[188:189], a[214:215], 0\n"
      "ds_read_b64_tr_b16 a[158:159], v92 offset:18624\n"
      "ds_read_b64_tr_b16 a[160:161], v92 offset:128\n"
      "ds_read_b64_tr_b16 a[162:163], v92 offset:18688\n"
      "v_mfma_f32_16x16x16_bf16 v[32:35], v[190:191], a[208:209], 0\n"
      "ds_read_b64_tr_b16 a[164:165], v92 offset:192\n"
      "ds_read_b64_tr_b16 a[166:167], v92 offset:18752\n"
      "ds_read_b64_tr_b16 a[168:169], v92 offset:2304\n"
      "v_mfma_f32_16x16x16_bf16 v[36:39], v[190:191], a[210:211], 0\n"
      "ds_read_b64_tr_b16 a[170:171], v92 offset:20864\n"
      "ds_read_b64_tr_b16 a[172:173], v92 offset:2368\n"
      "ds_read_b64_tr_b16 a[174:175], v92 offset:20928\n"
      "v_mfma_f32_16x16x16_bf16 v[40:43], v[190:191], a[212:213], 0\n"
      "ds_read_b64_tr_b16 a[176:177], v92 offset:2432\n"
      "ds_read_b64_tr_b16 a[178:179], v92 offset:20992\n"
      "ds_read_b64_tr_b16 a[180:181], v92 offset:2496\n"
      "v_mfma_f32_16x16x16_bf16 v[44:47], v[190:191], a[214:215], 0\n"
      "ds_read_b64_tr_b16 a[182:183], v92 offset:21056\n"
      "ds_read_b32 v224, v227 offset:37636\n"
      "s_waitcnt lgkmcnt(15)\n"
      "v_mfma_f32_16x16x32_f16 v[192:195], a[184:187], a[152:155], 0\n"
      "v_and_b32 v208, 0xff, v211\n"
      "s_waitcnt lgkmcnt(13)\n"
      "v_mfma_f32_16x16x32_f16 v[192:195], a[188:191], a[156:159], v[192:195]\n"
      "v_bfe_u32 v209, v211, 8, 8\n"
      "s_waitcnt lgkmcnt(11)\n"
      "v_mfma_f32_16x16x32_f16 v[192:195], a[192:195], a[160:163], v[192:195]\n"
      "v_bfe_u32 v220, v211, 16, 8\n"
      "s_waitcnt lgkmcnt(9)\n"
      "v_mfma_f32_16x16x32_f16 v[192:195], a[196:199], a[164:167], v[192:195]\n"
      "v_lshrrev_b32 v221, 24, v211\n"
      "s_waitcnt lgkmcnt(0)\n"
      "v_cmp_eq_u32 vcc, 0, v224\n"
      "s_cbranch_vccz 13f\n"
      "11:\n"
      "ds_read_b32 v224, v227 offset:37636\n"
      "s_waitcnt lgkmcnt(0)\n"
      "v_cmp_eq_u32 vcc, 0, v224\n"
      "s_cbranch_vccnz 11b\n"
      "13:\n"
      "v_mfma_f32_16x16x32_f16 v[252:255], a[184:187], a[168:171], 0\n"
      "ds_read_b128 a[0:3], v14 offset:0\n"
      "ds_read_b128 a[4:7], v14 offset:1152\n"
      "v_mfma_f32_16x16x32_f16 v[252:255], a[188:191], a[172:175], v[252:255]\n"
      "ds_read_b128 a[8:11], v14 offset:2304\n"
      "ds_read_b128 a[12:15], v14 offset:3456\n"
      "v_mfma_f32_16x16x32_f16 v[252:255], a[192:195], a[176:179], v[252:255]\n"
      "ds_read_b128 a[16:19], v14 offset:4640\n"
      "ds_read_b128 a[20:23], v14 offset:5792\n"
      "v_mfma_f32_16x16x32_f16 v[252:255], a[196:199], a[180:183], v[252:255]\n"
      "ds_read_b128 a[24:27], v14 offset:6944\n"
      "ds_read_b128 a[28:31], v14 offset:8096\n"
      "s_cmp_lg_u32 s58, 0\n"
      "s_cbranch_scc1 72f\n"
      "73:\n"
      "global_load_dwordx4 v[240:243], v9, s[10:11] nt\n"
      "v_mov_b32 v3, s33\n"
      "global_load_ushort v250, v3, s[24:25]\n"
      "s_waitcnt vmcnt(5)\n"
      "v_mfma_f32_4x4x4_16b_bf16 v[176:179], a[216:217], v[228:229], 0\n"
      "v_mfma_f32_4x4x4_16b_bf16 v[180:183], a[216:217], v[230:231], 0\n"
      "ds_read_b128 a[32:35], v14 offset:9280\n"
      "s_waitcnt vmcnt(4)\n"
      "v_mfma_f32_4x4x4_16b_bf16 v[144:147], a[216:217], v[232:233], 0\n"
      "ds_read_b128 a[36:39], v14 offset:10432\n"
      "v_mfma_f32_4x4x4_16b_bf16 v[148:151], a[216:217], v[234:235], 0\n"
      "ds_read_b128 a[40:43], v14 offset:11584\n"
      "ds_read_b128 a[44:47], v14 offset:12736\n"
      "v_cvt_pk_f16_f32 v184, v176, v177\n"
      "v_cvt_pk_f16_f32 v185, v178, v179\n"
      "v_cvt_pk_f16_f32 v186, v180, v181\n"
      "v_cvt_pk_f16_f32 v187, v182, v183\n"
      "ds_write_b128 v1, v[184:187] offset:0\n"
      "v_cvt_pk_f16_f32 v152, v144, v145\n"
      "v_cvt_pk_f16_f32 v153, v146, v147\n"
      "v_cvt_pk_f16_f32 v154, v148, v149\n"
      "v_cvt_pk_f16_f32 v155, v150, v151\n"
      "ds_write_b128 v1, v[152:155] offset:1152\n"
      "ds_read_b128 v[128:131], v201 offset:0\n"
      "ds_read_b128 v[160:163], v202 offset:0\n"
      "ds_read_b128 v[132:135], v201 offset:64\n"
      "ds_read_b128 v[164:167], v202 offset:64\n"
      "ds_read_b128 v[136:139], v201 offset:128\n"
      "ds_read_b128 v[168:171], v202 offset:128\n"
      "ds_read_b128 v[140:143], v201 offset:192\n"
      "ds_read_b128 v[172:175], v202 offset:192\n"
      "s_waitcnt vmcnt(3)\n"
      "v_mfma_f32_4x4x4_16b_bf16 v[112:115], a[216:217], v[236:237], 0\n"
      "v_mfma_f32_4x4x4_16b_bf16 v[116:119], a[216:217], v[238:239], 0\n"
      "s_nop 3\n"
      "v_cvt_pk_f16_f32 v120, v112, v113\n"
      "v_cvt_pk_f16_f32 v121, v114, v115\n"
      "v_cvt_pk_f16_f32 v122, v116, v117\n"
      "v_cvt_pk_f16_f32 v123, v118, v119\n"
      "ds_write_b128 v1, v[120:123] offset:2304\n"
      "s_waitcnt lgkmcnt(7)\n"
      "v_pk_mul_f16 v96, v128, v160\n"
      "v_pk_mul_f16 v97, v129, v161\n"
      "v_pk_mul_f16 v98, v130, v162\n"
      "v_pk_mul_f16 v99, v131, v163\n"
      "s_waitcnt lgkmcnt(5)\n"
      "v_pk_mul_f16 v100, v132, v164\n"
      "v_pk_mul_f16 v101, v133, v165\n"
      "v_pk_mul_f16 v102, v134, v166\n"
      "v_pk_mul_f16 v103, v135, v167\n"
      "s_waitcnt lgkmcnt(3)\n"
      "v_pk_mul_f16 v104, v136, v168\n"
      "v_pk_mul_f16 v105, v137, v169\n"
      "v_pk_mul_f16 v106, v138, v170\n"
      "v_pk_mul_f16 v107, v139, v171\n"
      "3:\n"
      "v_mfma_f32_16x16x32_f16 v[80:83], a[0:3], v[96:99], v[16:19]\n"
      "s_waitcnt lgkmcnt(1)\n"
      "v_pk_mul_f16 v108, v140, v172\n"
      "v_pk_mul_f16 v109, v141, v173\n"
      "v_mfma_f32_16x16x32_f16 v[80:83], a[4:7], v[100:103], v[80:83]\n"
      "v_pk_mul_f16 v110, v142, v174\n"
      "v_pk_mul_f16 v111, v143, v175\n"
      "v_mfma_f32_16x16x32_f16 v[80:83], a[8:11], v[104:107], v[80:83]\n"
      "ds_read_b128 a[60:63], v14 offset:17376\n"
      "ds_read_b128 a[56:59], v14 offset:16224\n"
      "v_mfma_f32_16x16x32_f16 v[80:83], a[12:15], v[108:111], v[80:83]\n"
      "ds_read_b128 a[52:55], v14 offset:15072\n"
      "ds_read_b128 a[48:51], v14 offset:13920\n"
      "v_mfma_f32_16x16x32_f16 v[84:87], a[28:31], v[108:111], v[20:23]\n"
      "ds_read_b128 a[64:67], v212 offset:0\n"
      "ds_read_b128 v[144:147], v203 offset:2304\n"
      "v_mfma_f32_16x16x32_f16 v[84:87], a[24:27], v[104:107], v[84:87]\n"
      "ds_read_b128 a[68:71], v212 offset:1152\n"
      "ds_read_b128 a[72:75], v212 offset:2304\n"
      "v_mfma_f32_16x16x32_f16 v[84:87], a[20:23], v[100:103], v[84:87]\n"
      "ds_read_b128 a[76:79], v212 offset:3456\n"
      "ds_read_b128 a[92:95], v212 offset:8096\n"
      "v_mfma_f32_16x16x32_f16 v[84:87], a[16:19], v[96:99], v[84:87]\n"
      "ds_read_b128 a[88:91], v212 offset:6944\n"
      "ds_read_b128 v[148:151], v203 offset:2368\n"
      "v_mfma_f32_16x16x32_f16 v[88:91], a[32:35], v[96:99], v[24:27]\n"
      "ds_read_b128 a[84:87], v212 offset:5792\n"
      "ds_read_b128 a[80:83], v212 offset:4640\n"
      "v_mfma_f32_16x16x32_f16 v[88:91], a[36:39], v[100:103], v[88:91]\n"
      "v_fma_f32 v188, |v80|, v48, 4.0\n"
      "v_fma_f32 v188, |v81|, v49, v188\n"
      "v_mfma_f32_16x16x32_f16 v[88:91], a[40:43], v[104:107], v[88:91]\n"
      "ds_read_b128 a[96:99], v212 offset:9280\n"
      "v_fma_f32 v188, |v82|, v50, v188\n"
      "v_mfma_f32_16x16x32_f16 v[88:91], a[44:47], v[108:111], v[88:91]\n"
      "s_waitcnt lgkmcnt(13)\n"
      "v_fma_f32 v188, |v83|, v51, v188\n"
      "v_fma_f32 v188, |v84|, v52, v188\n"
      "v_mfma_f32_16x16x32_f16 v[92:95], a[60:63], v[108:111], v[28:31]\n"
      "s_waitcnt lgkmcnt(9)\n"
      "v_pk_mul_f16 v112, v128, v144\n"
      "v_pk_mul_f16 v113, v129, v145\n"
      "v_mfma_f32_16x16x32_f16 v[92:95], a[56:59], v[104:107], v[92:95]\n"
      "ds_read_b128 a[100:103], v212 offset:10432\n"
      "ds_read_b128 a[104:107], v212 offset:11584\n"
      "v_mfma_f32_16x16x32_f16 v[92:95], a[52:55], v[100:103], v[92:95]\n"
      "v_pk_mul_f16 v114, v130, v146\n"
      "v_pk_mul_f16 v115, v131, v147\n"
      "v_mfma_f32_16x16x32_f16 v[92:95], a[48:51], v[96:99], v[92:95]\n"
      "s_waitcnt lgkmcnt(10)\n"
      "ds_read_b128 v[152:155], v203 offset:2432\n"
      "ds_read_b128 a[108:111], v212 offset:12736\n"
      "v_mfma_f32_16x16x32_f16 v[80:83], a[64:67], v[96:99], v[32:35]\n"
      "ds_read_b128 a[124:127], v212 offset:17376\n"
      "v_fma_f32 v188, |v85|, v53, v188\n"
      "v_mfma_f32_16x16x32_f16 v[80:83], a[68:71], v[100:103], v[80:83]\n"
      "s_waitcnt lgkmcnt(11)\n"
      "v_fma_f32 v188, |v86|, v54, v188\n"
      "v_fma_f32 v188, |v87|, v55, v188\n"
      "v_mfma_f32_16x16x32_f16 v[80:83], a[72:75], v[104:107], v[80:83]\n"
      "s_waitcnt lgkmcnt(8)\n"
      "ds_read_b128 a[120:123], v212 offset:16224\n"
      "v_pk_mul_f16 v116, v132, v148\n"
      "v_mfma_f32_16x16x32_f16 v[80:83], a[76:79], v[108:111], v[80:83]\n"
      "ds_read_b128 a[116:119], v212 offset:15072\n"
      "v_pk_mul_f16 v117, v133, v149\n"
      "v_mfma_f32_16x16x32_f16 v[84:87], a[92:95], v[108:111], v[36:39]\n"
      "ds_read_b128 a[112:115], v212 offset:13920\n"
      "v_fma_f32 v188, |v88|, v56, v188\n"
      "v_mfma_f32_16x16x32_f16 v[84:87], a[88:91], v[104:107], v[84:87]\n"
      "s_waitcnt lgkmcnt(9)\n"
      "v_fma_f32 v188, |v89|, v57, v188\n"
      "v_pk_mul_f16 v118, v134, v150\n"
      "v_mfma_f32_16x16x32_f16 v[84:87], a[84:87], v[100:103], v[84:87]\n"
      "ds_read_b128 v[156:159], v203 offset:2496\n"
      "v_fma_f32 v188, |v90|, v58, v188\n"
      "v_mfma_f32_16x16x32_f16 v[84:87], a[80:83], v[96:99], v[84:87]\n"
      "s_waitcnt lgkmcnt(8)\n"
      "v_fma_f32 v188, |v91|, v59, v188\n"
      "v_pk_mul_f16 v119, v135, v151\n"
      "v_mfma_f32_16x16x32_f16 v[88:91], a[96:99], v[96:99], v[40:43]\n"
      "s_waitcnt lgkmcnt(6)\n"
      "v_pk_mul_f16 v120, v136, v152\n"
      "v_fma_f32 v188, |v92|, v60, v188\n"
      "v_mfma_f32_16x16x32_f16 v[88:91], a[100:103], v[100:103], v[88:91]\n"
      "s_waitcnt lgkmcnt(5)\n"
      "v_fma_f32 v188, |v93|, v61, v188\n"
      "v_pk_mul_f16 v121, v137, v153\n"
      "v_mfma_f32_16x16x32_f16 v[88:91], a[104:107], v[104:107], v[88:91]\n"
      "v_fma_f32 v188, |v94|, v62, v188\n"
      "v_fma_f32 v188, |v95|, v63, v188\n"
      "v_mfma_f32_16x16x32_f16 v[88:91], a[108:111], v[108:111], v[88:91]\n"
      "s_waitcnt lgkmcnt(3)\n"
      "v_pk_mul_f16 v122, v138, v154\n"
      "v_pk_mul_f16 v123, v139, v155\n"
      "v_mfma_f32_16x16x32_f16 v[92:95], a[124:127], v[108:111], v[44:47]\n"
      "global_load_dwordx4 v[244:247], v10, s[10:11] nt\n"
      "v_fma_f32 v188, |v80|, v64, v188\n"
      "v_mfma_f32_16x16x32_f16 v[92:95], a[120:123], v[104:107], v[92:95]\n"
      "s_waitcnt lgkmcnt(1)\n"
      "v_fma_f32 v188, |v81|, v65, v188\n"
      "v_fma_f32 v188, |v82|, v66, v188\n"
      "v_mfma_f32_16x16x32_f16 v[92:95], a[116:119], v[100:103], v[92:95]\n"
      "s_waitcnt lgkmcnt(0)\n"
      "v_pk_mul_f16 v124, v140, v156\n"
      "v_fma_f32 v188, |v83|, v67, v188\n"
      "v_mfma_f32_16x16x32_f16 v[92:95], a[112:115], v[96:99], v[92:95]\n"
      "v_pk_mul_f16 v125, v141, v157\n"
      "v_pk_mul_f16 v126, v142, v158\n"
      "v_mfma_f32_16x16x32_f16 v[80:83], a[0:3], v[112:115], v[16:19]\n"
      "ds_read_b128 v[160:163], v203 offset:2880\n"
      "v_pk_mul_f16 v127, v143, v159\n"
      "v_mfma_f32_16x16x32_f16 v[80:83], a[4:7], v[116:119], v[80:83]\n"
      "v_fma_f32 v188, |v84|, v68, v188\n"
      "v_fma_f32 v188, |v85|, v69, v188\n"
      "v_mfma_f32_16x16x32_f16 v[80:83], a[8:11], v[120:123], v[80:83]\n"
      "v_fma_f32 v188, |v86|, v70, v188\n"
      "v_fma_f32 v188, |v87|, v71, v188\n"
      "v_mfma_f32_16x16x32_f16 v[80:83], a[12:15], v[124:127], v[80:83]\n"
      "v_fma_f32 v188, |v88|, v72, v188\n"
      "v_fma_f32 v188, |v89|, v73, v188\n"
      "v_mfma_f32_16x16x32_f16 v[84:87], a[28:31], v[124:127], v[20:23]\n"
      "v_fma_f32 v188, |v90|, v74, v188\n"
      "v_fma_f32 v188, |v91|, v75, v188\n"
      "v_mfma_f32_16x16x32_f16 v[84:87], a[24:27], v[120:123], v[84:87]\n"
      "ds_read_b128 v[164:167], v203 offset:2944\n"
      "v_fma_f32 v188, |v92|, v76, v188\n"
      "v_mfma_f32_16x16x32_f16 v[84:87], a[20:23], v[116:119], v[84:87]\n"
      "v_fma_f32 v188, |v93|, v77, v188\n"
      "v_fma_f32 v188, |v94|, v78, v188\n"
      "v_mfma_f32_16x16x32_f16 v[84:87], a[16:19], v[112:115], v[84:87]\n"
      "v_fma_f32 v188, |v95|, v79, v188\n"
      "v_fma_f32 v189, |v80|, v48, 4.0\n"
      "v_mfma_f32_16x16x32_f16 v[88:91], a[32:35], v[112:115], v[24:27]\n"
      "s_waitcnt lgkmcnt(1)\n"
      "v_pk_mul_f16 v96, v128, v160\n"
      "v_pk_mul_f16 v97, v129, v161\n"
      "v_mfma_f32_16x16x32_f16 v[88:91], a[36:39], v[116:119], v[88:91]\n"
      "v_pk_mul_f16 v98, v130, v162\n"
      "v_pk_mul_f16 v99, v131, v163\n"
      "v_mfma_f32_16x16x32_f16 v[88:91], a[40:43], v[120:123], v[88:91]\n"
      "40:\n"
      "s_cbranch_execz 44b\n"
      "ds_read_b128 v[168:171], v203 offset:3008\n"
      "v_mfma_f32_16x16x32_f16 v[88:91], a[44:47], v[124:127], v[88:91]\n"
      "s_waitcnt vmcnt(2)\n"
      "v_fma_f32 v189, |v81|, v49, v189\n"
      "v_fma_f32 v189, |v82|, v50, v189\n"
      "v_mfma_f32_4x4x4_16b_bf16 v[176:179], a[216:217], v[240:241], 0\n"
      "v_fma_f32 v189, |v83|, v51, v189\n"
      "v_mfma_f32_4x4x4_16b_bf16 v[180:183], a[216:217], v[242:243], 0\n"
      "s_waitcnt lgkmcnt(1)\n"
      "v_pk_mul_f16 v100, v132, v164\n"
      "v_pk_mul_f16 v101, v133, v165\n"
      "v_mfma_f32_16x16x32_f16 v[92:95], a[60:63], v[124:127], v[28:31]\n"
      "v_pk_mul_f16 v102, v134, v166\n"
      "v_pk_mul_f16 v103, v135, v167\n"
      "v_mfma_f32_16x16x32_f16 v[92:95], a[56:59], v[120:123], v[92:95]\n"
      "v_fma_f32 v189, |v84|, v52, v189\n"
      "v_fma_f32 v189, |v85|, v53, v189\n"
      "v_mfma_f32_16x16x32_f16 v[92:95], a[52:55], v[116:119], v[92:95]\n"
      "v_fma_f32 v189, |v86|, v54, v189\n"
      "v_fma_f32 v189, |v87|, v55, v189\n"
      "v_mfma_f32_16x16x32_f16 v[92:95], a[48:51], v[112:115], v[92:95]\n"
      "v_fma_f32 v189, |v88|, v56, v189\n"
      "v_fma_f32 v189, |v89|, v57, v189\n"
      "v_mfma_f32_16x16x32_f16 v[80:83], a[64:67], v[112:115], v[32:35]\n"
      "s_waitcnt lgkmcnt(0)\n"
      "v_pk_mul_f16 v104, v136, v168\n"
      "v_pk_mul_f16 v105, v137, v169\n"
      "v_mfma_f32_16x16x32_f16 v[80:83], a[68:71], v[116:119], v[80:83]\n"
      "v_pk_mul_f16 v106, v138, v170\n"
      "v_pk_mul_f16 v107, v139, v171\n"
      "v_mfma_f32_16x16x32_f16 v[80:83], a[72:75], v[120:123], v[80:83]\n"
      "ds_read_b128 v[172:175], v203 offset:3072\n"
      "v_fma_f32 v189, |v90|, v58, v189\n"
      "v_mfma_f32_16x16x32_f16 v[80:83], a[76:79], v[124:127], v[80:83]\n"
      "v_fma_f32 v189, |v91|, v59, v189\n"
      "v_fma_f32 v189, |v92|, v60, v189\n"
      "v_mfma_f32_16x16x32_f16 v[84:87], a[92:95], v[124:127], v[36:39]\n"
      "v_cvt_pk_f16_f32 v184, v176, v177\n"
      "v_cvt_pk_f16_f32 v185, v178, v179\n"
      "v_mfma_f32_16x16x32_f16 v[84:87], a[88:91], v[120:123], v[84:87]\n"
      "v_fma_f32 v189, |v93|, v61, v189\n"
      "v_fma_f32 v189, |v94|, v62, v189\n"
      "v_mfma_f32_16x16x32_f16 v[84:87], a[84:87], v[116:119], v[84:87]\n"
      "v_fma_f32 v189, |v95|, v63, v189\n"
      "v_fma_f32 v189, |v80|, v64, v189\n"
      "v_mfma_f32_16x16x32_f16 v[84:87], a[80:83], v[112:115], v[84:87]\n"
      "v_cvt_pk_f16_f32 v186, v180, v181\n"
      "v_cvt_pk_f16_f32 v187, v182, v183\n"
      "v_mfma_f32_16x16x32_f16 v[88:91], a[96:99], v[112:115], v[40:43]\n"
      "ds_write_b128 v1, v[184:187] offset:3456\n"
      "v_mfma_f32_16x16x32_f16 v[88:91], a[100:103], v[116:119], v[88:91]\n"
      "v_fma_f32 v189, |v81|, v65, v189\n"
      "v_fma_f32 v189, |v82|, v66, v189\n"
      "v_mfma_f32_16x16x32_f16 v[88:91], a[104:107], v[120:123], v[88:91]\n"
      "s_waitcnt lgkmcnt(1)\n"
      "v_pk_mul_f16 v108, v140, v172\n"
      "v_pk_mul_f16 v109, v141, v173\n"
      "v_mfma_f32_16x16x32_f16 v[88:91], a[108:111], v[124:127], v[88:91]\n"
      "v_fma_f32 v189, |v83|, v67, v189\n"
      "v_fma_f32 v189, |v84|, v68, v189\n"
      "v_mfma_f32_16x16x32_f16 v[92:95], a[124:127], v[124:127], v[44:47]\n"
      "v_pk_mul_f16 v110, v142, v174\n"
      "v_pk_mul_f16 v111, v143, v175\n"
      "v_mfma_f32_16x16x32_f16 v[92:95], a[120:123], v[120:123], v[92:95]\n"
      "ds_read_b128 v[160:163], v203 offset:3456\n"
      "v_fma_f32 v189, |v85|, v69, v189\n"
      "v_mfma_f32_16x16x32_f16 v[92:95], a[116:119], v[116:119], v[92:95]\n"
      "v_fma_f32 v189, |v86|, v70, v189\n"
      "v_fma_f32 v189, |v87|, v71, v189\n"
      "v_mfma_f32_16x16x32_f16 v[92:95], a[112:115], v[112:115], v[92:95]\n"
      "v_fma_f32 v189, |v88|, v72, v189\n"
      "v_fma_f32 v189, |v89|, v73, v189\n"
      "v_mfma_f32_16x16x32_f16 v[80:83], a[0:3], v[96:99], v[16:19]\n"
      "v_fma_f32 v189, |v90|, v74, v189\n"
      "v_fma_f32 v189, |v91|, v75, v189\n"
      "v_mfma_f32_16x16x32_f16 v[80:83], a[4:7], v[100:103], v[80:83]\n"
      "ds_read_b128 v[164:167], v203 offset:3520\n"
      "ds_read_b128 v[168:171], v203 offset:3584\n"
      "v_mfma_f32_16x16x32_f16 v[80:83], a[8:11], v[104:107], v[80:83]\n"
      "v_fma_f32 v189, |v92|, v76, v189\n"
      "v_fma_f32 v189, |v93|, v77, v189\n"
      "v_mfma_f32_16x16x32_f16 v[80:83], a[12:15], v[108:111], v[80:83]\n"
      "v_fma_f32 v189, |v94|, v78, v189\n"
      "v_fma_f32 v189, |v95|, v79, v189\n"
      "v_mfma_f32_16x16x32_f16 v[84:87], a[28:31], v[108:111], v[20:23]\n"
      "ds_write_b32 v7, v252 offset:37376\n"
      "v_mfma_f32_16x16x32_f16 v[84:87], a[24:27], v[104:107], v[84:87]\n"
      "s_waitcnt lgkmcnt(3)\n"
      "v_pk_mul_f16 v112, v128, v160\n"
      "v_pk_mul_f16 v113, v129, v161\n"
      "v_mfma_f32_16x16x32_f16 v[84:87], a[20:23], v[100:103], v[84:87]\n"
      "ds_write_b32 v7, v192 offset:37120\n"
      "v_mfma_f32_16x16x32_f16 v[84:87], a[16:19], v[96:99], v[84:87]\n"
      "v_pk_mul_f16 v114, v130, v162\n"
      "v_pk_mul_f16 v115, v131, v163\n"
      "v_mfma_f32_16x16x32_f16 v[88:91], a[32:35], v[96:99], v[24:27]\n"
      "v_fma_f32 v190, |v80|, v48, 4.0\n"
      "v_fma_f32 v190, |v81|, v49, v190\n"
      "v_mfma_f32_16x16x32_f16 v[88:91], a[36:39], v[100:103], v[88:91]\n"
      "v_fma_f32 v190, |v82|, v50, v190\n"
      "v_fma_f32 v190, |v83|, v51, v190\n"
      "v_mfma_f32_16x16x32_f16 v[88:91], a[40:43], v[104:107], v[88:91]\n"
      "s_waitcnt lgkmcnt(3)\n"
      "v_pk_mul_f16 v116, v132, v164\n"
      "v_pk_mul_f16 v117, v133, v165\n"
      "v_mfma_f32_16x16x32_f16 v[88:91], a[44:47], v[108:111], v[88:91]\n"
      "v_pk_mul_f16 v118, v134, v166\n"
      "v_pk_mul_f16 v119, v135, v167\n"
      "v_mfma_f32_16x16x32_f16 v[92:95], a[60:63], v[108:111], v[28:31]\n"
      "v_fma_f32 v190, |v84|, v52, v190\n"
      "v_fma_f32 v190, |v85|, v53, v190\n"
      "v_mfma_f32_16x16x32_f16 v[92:95], a[56:59], v[104:107], v[92:95]\n"
      "v_fma_f32 v190, |v86|, v54, v190\n"
      "v_fma_f32 v190, |v87|, v55, v190\n"
      "v_mfma_f32_16x16x32_f16 v[92:95], a[52:55], v[100:103], v[92:95]\n"
      "v_max3_f32 v195, |v193|, |v194|, |v195|\n"
      "v_max3_f32 v194, |v253|, |v254|, |v255|\n"
      "v_mfma_f32_16x16x32_f16 v[92:95], a[48:51], v[96:99], v[92:95]\n"
      "s_waitcnt lgkmcnt(2)\n"
      "v_pk_mul_f16 v120, v136, v168\n"
      "v_pk_mul_f16 v121, v137, v169\n"
      "v_mfma_f32_16x16x32_f16 v[80:83], a[64:67], v[96:99], v[32:35]\n"
      "v_fma_f32 v190, |v88|, v56, v190\n"
      "v_fma_f32 v190, |v89|, v57, v190\n"
      "v_mfma_f32_16x16x32_f16 v[80:83], a[68:71], v[100:103], v[80:83]\n"
      "ds_read_b128 v[172:175], v203 offset:3648\n"
      "v_fma_f32 v190, |v90|, v58, v190\n"
      "v_mfma_f32_16x16x32_f16 v[80:83], a[72:75], v[104:107], v[80:83]\n"
      "v_fma_f32 v190, |v91|, v59, v190\n"
      "v_fma_f32 v190, |v92|, v60, v190\n"
      "v_mfma_f32_16x16x32_f16 v[80:83], a[76:79], v[108:111], v[80:83]\n"
      "v_max_f32 v194, |v252|, v194\n"
      "v_max_f32 v195, |v192|, v195\n"
      "v_mfma_f32_16x16x32_f16 v[84:87], a[92:95], v[108:111], v[36:39]\n"
      "v_pk_mul_f16 v122, v138, v170\n"
      "v_pk_mul_f16 v123, v139, v171\n"
      "v_mfma_f32_16x16x32_f16 v[84:87], a[88:91], v[104:107], v[84:87]\n"
      "s_waitcnt vmcnt(1)\n"
      "global_load_dwordx4 v[248:251], v11, s[10:11] nt\n"
      "v_cmp_eq_u64 vcc, v[216:217], v[208:209]\n"
      "v_mfma_f32_16x16x32_f16 v[84:87], a[84:87], v[100:103], v[84:87]\n"
      "v_fma_f32 v190, |v93|, v61, v190\n"
      "v_fma_f32 v190, |v94|, v62, v190\n"
      "v_mfma_f32_16x16x32_f16 v[84:87], a[80:83], v[96:99], v[84:87]\n"
      "v_cmp_eq_u64 s[28:29], v[218:219], v[220:221]\n"
      "v_fma_f32 v190, |v95|, v63, v190\n"
      "v_mfma_f32_16x16x32_f16 v[88:91], a[96:99], v[96:99], v[40:43]\n"
      "v_fma_f32 v190, |v80|, v64, v190\n"
      "v_fma_f32 v190, |v81|, v65, v190\n"
      "v_mfma_f32_16x16x32_f16 v[88:91], a[100:103], v[100:103], v[88:91]\n"
      "s_waitcnt lgkmcnt(0)\n"
      "v_pk_mul_f16 v124, v140, v172\n"
      "v_pk_mul_f16 v125, v141, v173\n"
      "v_mfma_f32_16x16x32_f16 v[88:91], a[104:107], v[104:107], v[88:91]\n"
      "s_and_b64 vcc, vcc, s[28:29]\n"
      "v_fma_f32 v190, |v82|, v66, v190\n"
      "v_fma_f32 v190, |v83|, v67, v190\n"
      "v_mfma_f32_16x16x32_f16 v[88:91], a[108:111], v[108:111], v[88:91]\n"
      "s_cmp_eq_u64 vcc, -1\n"
      "v_pk_mul_f16 v126, v142, v174\n"
      "v_pk_mul_f16 v127, v143, v175\n"
      "v_mfma_f32_16x16x32_f16 v[92:95], a[124:127], v[108:111], v[44:47]\n"
      "s_cselect_b32 s52, 13, 1\n"
      "ds_read_b128 v[160:163], v203 offset:4032\n"
      "v_fma_f32 v190, |v84|, v68, v190\n"
      "v_mfma_f32_16x16x32_f16 v[92:95], a[120:123], v[104:107], v[92:95]\n"
      "v_fma_f32 v190, |v85|, v69, v190\n"
      "v_fma_f32 v190, |v86|, v70, v190\n"
      "v_mfma_f32_16x16x32_f16 v[92:95], a[116:119], v[100:103], v[92:95]\n"
      "global_load_dwordx4 v[252:255], v12, s[10:11] nt\n"
      "v_cmp_le_f32 vcc, 0x3ca3d70a, v194\n"
      "v_mfma_f32_16x16x32_f16 v[92:95], a[112:115], v[96:99], v[92:95]\n"
      "v_fma_f32 v190, |v87|, v71, v190\n"
      "v_fma_f32 v190, |v88|, v72, v190\n"
      "v_mfma_f32_16x16x32_f16 v[80:83], a[0:3], v[112:115], v[16:19]\n"
      "s_or_b32 s44, vcc_lo, vcc_hi\n"
      "v_fma_f32 v190, |v89|, v73, v190\n"
      "v_fma_f32 v190, |v90|, v74, v190\n"
      "v_mfma_f32_16x16x32_f16 v[80:83], a[4:7], v[116:119], v[80:83]\n"
      "v_cmp_le_f32 vcc, 0x3ca3d70a, v195\n"
      "v_max_f32 v195, v195, v194\n"
      "v_mfma_f32_16x16x32_f16 v[80:83], a[8:11], v[120:123], v[80:83]\n"
      "v_fma_f32 v190, |v91|, v75, v190\n"
      "v_fma_f32 v190, |v92|, v76, v190\n"
      "v_mfma_f32_16x16x32_f16 v[80:83], a[12:15], v[124:127], v[80:83]\n"
      "s_or_b32 s45, vcc_lo, vcc_hi\n"
      "ds_read_b128 v[164:167], v203 offset:4096\n"
      "v_fma_f32 v190, |v93|, v77, v190\n"
      "v_mfma_f32_16x16x32_f16 v[84:87], a[28:31], v[124:127], v[20:23]\n"
      "s_waitcnt lgkmcnt(1)\n"
      "v_cmp_le_f32 vcc, 0x3e800000, v195\n"
      "v_pk_mul_f16 v96, v128, v160\n"
      "v_mfma_f32_16x16x32_f16 v[84:87], a[24:27], v[120:123], v[84:87]\n"
      "s_pack_ll_b32_b16 s46, s44, s45\n"
      "v_pk_mul_f16 v97, v129, v161\n"
      "v_pk_mul_f16 v98, v130, v162\n"
      "v_mfma_f32_16x16x32_f16 v[84:87], a[20:23], v[116:119], v[84:87]\n"
      "s_pack_hh_b32_b16 s44, s44, s45\n"
      "v_fma_f32 v190, |v94|, v78, v190\n"
      "v_fma_f32 v190, |v95|, v79, v190\n"
      "v_mfma_f32_16x16x32_f16 v[84:87], a[16:19], v[112:115], v[84:87]\n"
      "s_or_b32 s44, s44, s46\n"
      "ds_read_b128 v[168:171], v203 offset:4160\n"
      "v_pk_mul_f16 v99, v131, v163\n"
      "v_mfma_f32_16x16x32_f16 v[88:91], a[32:35], v[112:115], v[24:27]\n"
      "s_cmp_eq_u64 vcc, 0\n"
      "v_fma_f32 v191, |v80|, v48, 4.0\n"
      "v_fma_f32 v191, |v81|, v49, v191\n"
      "v_mfma_f32_16x16x32_f16 v[88:91], a[36:39], v[116:119], v[88:91]\n"
      "s_cselect_b32 s45, 13, 9\n"
      "v_fma_f32 v191, |v82|, v50, v191\n"
      "v_fma_f32 v191, |v83|, v51, v191\n"
      "v_mfma_f32_16x16x32_f16 v[88:91], a[40:43], v[120:123], v[88:91]\n"
      "s_cmp_eq_u32 s44, -1\n"
      "v_fma_f32 v191, |v84|, v52, v191\n"
      "v_fma_f32 v191, |v85|, v53, v191\n"
      "v_mfma_f32_16x16x32_f16 v[88:91], a[44:47], v[124:127], v[88:91]\n"
      "s_waitcnt lgkmcnt(1)\n"
      "v_pk_mul_f16 v100, v132, v164\n"
      "v_pk_mul_f16 v101, v133, v165\n"
      "v_mfma_f32_16x16x32_f16 v[92:95], a[60:63], v[124:127], v[28:31]\n"
      "s_cselect_b32 s44, s45, 5\n"
      "v_pk_mul_f16 v102, v134, v166\n"
      "v_pk_mul_f16 v103, v135, v167\n"
      "v_mfma_f32_16x16x32_f16 v[92:95], a[56:59], v[120:123], v[92:95]\n"
      "s_min_u32 s44, s44, s52\n"
      "v_fma_f32 v191, |v86|, v54, v191\n"
      "v_fma_f32 v191, |v87|, v55, v191\n"
      "v_mfma_f32_16x16x32_f16 v[92:95], a[52:55], v[116:119], v[92:95]\n"
      "s_movk_i32 s31, 0x1c00\n"
      "v_mov_b32 v223, s44\n"
      "v_fma_f32 v191, |v88|, v56, v191\n"
      "v_mfma_f32_16x16x32_f16 v[92:95], a[48:51], v[112:115], v[92:95]\n"
      "s_mov_b32 s35, 1\n"
      "ds_write_b32 v2, v223\n"
      "v_mfma_f32_16x16x32_f16 v[80:83], a[64:67], v[112:115], v[32:35]\n"
      "s_waitcnt lgkmcnt(1)\n"
      "v_pk_mul_f16 v104, v136, v168\n"
      "v_pk_mul_f16 v105, v137, v169\n"
      "v_mfma_f32_16x16x32_f16 v[80:83], a[68:71], v[116:119], v[80:83]\n"
      "v_fma_f32 v191, |v89|, v57, v191\n"
      "v_fma_f32 v191, |v90|, v58, v191\n"
      "v_mfma_f32_16x16x32_f16 v[80:83], a[72:75], v[120:123], v[80:83]\n"
      "ds_read_b128 v[172:175], v203 offset:4224\n"
      "v_fma_f32 v191, |v91|, v59, v191\n"
      "v_mfma_f32_16x16x32_f16 v[80:83], a[76:79], v[124:127], v[80:83]\n"
      "v_pk_mul_f16 v106, v138, v170\n"
      "v_pk_mul_f16 v107, v139, v171\n"
      "v_mfma_f32_16x16x32_f16 v[84:87], a[92:95], v[124:127], v[36:39]\n"
      "v_fma_f32 v191, |v92|, v60, v191\n"
      "v_fma_f32 v191, |v93|, v61, v191\n"
      "v_mfma_f32_16x16x32_f16 v[84:87], a[88:91], v[120:123], v[84:87]\n"
      "v_fma_f32 v191, |v94|, v62, v191\n"
      "v_fma_f32 v191, |v95|, v63, v191\n"
      "v_mfma_f32_16x16x32_f16 v[84:87], a[84:87], v[116:119], v[84:87]\n"
      "v_fma_f32 v191, |v80|, v64, v191\n"
      "v_fma_f32 v191, |v81|, v65, v191\n"
      "v_mfma_f32_16x16x32_f16 v[84:87], a[80:83], v[112:115], v[84:87]\n"
      "v_fma_f32 v191, |v82|, v66, v191\n"
      "v_fma_f32 v191, |v83|, v67, v191\n"
      "v_mfma_f32_16x16x32_f16 v[88:91], a[96:99], v[112:115], v[40:43]\n"
      "ds_read_b128 v[144:147], v201 offset:2304\n"
      "ds_read_b128 v[160:163], v202 offset:2304\n"
      "v_mfma_f32_16x16x32_f16 v[88:91], a[100:103], v[116:119], v[88:91]\n"
      "ds_read_b128 v[148:151], v201 offset:2368\n"
      "ds_read_b128 v[152:155], v201 offset:2432\n"
      "v_mfma_f32_16x16x32_f16 v[88:91], a[104:107], v[120:123], v[88:91]\n"
      "s_waitcnt lgkmcnt(4)\n"
      "v_pk_mul_f16 v108, v140, v172\n"
      "v_pk_mul_f16 v109, v141, v173\n"
      "v_mfma_f32_16x16x32_f16 v[88:91], a[108:111], v[124:127], v[88:91]\n"
      "v_pk_mul_f16 v110, v142, v174\n"
      "v_pk_mul_f16 v111, v143, v175\n"
      "v_mfma_f32_16x16x32_f16 v[92:95], a[124:127], v[124:127], v[44:47]\n"
      "v_fma_f32 v191, |v84|, v68, v191\n"
      "v_fma_f32 v191, |v85|, v69, v191\n"
      "v_mfma_f32_16x16x32_f16 v[92:95], a[120:123], v[120:123], v[92:95]\n"
      "v_fma_f32 v191, |v86|, v70, v191\n"
      "v_fma_f32 v191, |v87|, v71, v191\n"
      "v_mfma_f32_16x16x32_f16 v[92:95], a[116:119], v[116:119], v[92:95]\n"
      "v_fma_f32 v191, |v88|, v72, v191\n"
      "v_fma_f32 v191, |v89|, v73, v191\n"
      "v_mfma_f32_16x16x32_f16 v[92:95], a[112:115], v[112:115], v[92:95]\n"
      "v_fma_f32 v191, |v90|, v74, v191\n"
      "v_fma_f32 v191, |v91|, v75, v191\n"
      "v_mfma_f32_16x16x32_f16 v[80:83], a[0:3], v[96:99], v[16:19]\n"
      "s_waitcnt lgkmcnt(2)\n"
      "ds_read_b128 v[164:167], v202 offset:2368\n"
      "v_pk_mul_f16 v112, v144, v160\n"
      "v_mfma_f32_16x16x32_f16 v[80:83], a[4:7], v[100:103], v[80:83]\n"
      "v_pk_mul_f16 v113, v145, v161\n"
      "v_pk_mul_f16 v114, v146, v162\n"
      "v_mfma_f32_16x16x32_f16 v[80:83], a[8:11], v[104:107], v[80:83]\n"
      "v_fma_f32 v191, |v92|, v76, v191\n"
      "v_fma_f32 v191, |v93|, v77, v191\n"
      "v_mfma_f32_16x16x32_f16 v[80:83], a[12:15], v[108:111], v[80:83]\n"
      "v_fma_f32 v191, |v94|, v78, v191\n"
      "v_fma_f32 v191, |v95|, v79, v191\n"
      "v_mfma_f32_16x16x32_f16 v[84:87], a[28:31], v[108:111], v[20:23]\n"
      "s_waitcnt vmcnt(2)\n"
      "v_pk_mul_f16 v115, v147, v163\n"
      "v_lshlrev_b32 v176, 16, v244\n"
      "v_mfma_f32_16x16x32_f16 v[84:87], a[24:27], v[104:107], v[84:87]\n"
      "v_permlane32_swap_b32 v189, v191\n"
      "v_mfma_f32_16x16x32_f16 v[84:87], a[20:23], v[100:103], v[84:87]\n"
      "ds_read_b128 v[168:171], v202 offset:2432\n"
      "v_fma_f32 v192, |v80|, v48, 4.0\n"
      "v_mfma_f32_16x16x32_f16 v[84:87], a[16:19], v[96:99], v[84:87]\n"
      "v_fma_f32 v192, |v81|, v49, v192\n"
      "v_fma_f32 v192, |v82|, v50, v192\n"
      "v_mfma_f32_16x16x32_f16 v[88:91], a[32:35], v[96:99], v[24:27]\n"
      "v_and_b32 v177, s43, v244\n"
      "v_fma_f32 v192, |v83|, v51, v192\n"
      "v_mfma_f32_16x16x32_f16 v[88:91], a[36:39], v[100:103], v[88:91]\n"
      "s_waitcnt lgkmcnt(1)\n"
      "v_lshlrev_b32 v178, 16, v245\n"
      "v_pk_mul_f16 v116, v148, v164\n"
      "v_mfma_f32_16x16x32_f16 v[88:91], a[40:43], v[104:107], v[88:91]\n"
      "v_and_b32 v179, s43, v245\n"
      "v_pk_mul_f16 v117, v149, v165\n"
      "v_mfma_f32_16x16x32_f16 v[88:91], a[44:47], v[108:111], v[88:91]\n"
      "ds_read_b128 v[156:159], v201 offset:2496\n"
      "v_lshlrev_b32 v180, 16, v246\n"
      "v_mfma_f32_16x16x32_f16 v[92:95], a[60:63], v[108:111], v[28:31]\n"
      "v_pk_mul_f16 v118, v150, v166\n"
      "v_pk_mul_f16 v119, v151, v167\n"
      "v_mfma_f32_16x16x32_f16 v[92:95], a[56:59], v[104:107], v[92:95]\n"
      "v_fma_f32 v192, |v84|, v52, v192\n"
      "v_fma_f32 v192, |v85|, v53, v192\n"
      "v_mfma_f32_16x16x32_f16 v[92:95], a[52:55], v[100:103], v[92:95]\n"
      "v_and_b32 v181, s43, v246\n"
      "v_fma_f32 v192, |v86|, v54, v192\n"
      "v_mfma_f32_16x16x32_f16 v[92:95], a[48:51], v[96:99], v[92:95]\n"
      "v_fma_f32 v192, |v87|, v55, v192\n"
      "v_fma_f32 v192, |v88|, v56, v192\n"
      "v_mfma_f32_16x16x32_f16 v[80:83], a[64:67], v[96:99], v[32:35]\n"
      "v_lshlrev_b32 v182, 16, v247\n"
      "v_and_b32 v183, s43, v247\n"
      "v_mfma_f32_16x16x32_f16 v[80:83], a[68:71], v[100:103], v[80:83]\n"
      "v_cvt_pk_f16_f32 v184, v176, v177\n"
      "v_cvt_pk_f16_f32 v185, v178, v179\n"
      "v_mfma_f32_16x16x32_f16 v[80:83], a[72:75], v[104:107], v[80:83]\n"
      "s_waitcnt lgkmcnt(1)\n"
      "v_pk_mul_f16 v120, v152, v168\n"
      "v_pk_mul_f16 v121, v153, v169\n"
      "v_mfma_f32_16x16x32_f16 v[80:83], a[76:79], v[108:111], v[80:83]\n"
      "v_fma_f32 v192, |v89|, v57, v192\n"
      "v_fma_f32 v192, |v90|, v58, v192\n"
      "v_mfma_f32_16x16x32_f16 v[84:87], a[92:95], v[108:111], v[36:39]\n"
      "ds_read_b128 v[172:175], v202 offset:2496\n"
      "v_fma_f32 v192, |v91|, v59, v192\n"
      "v_mfma_f32_16x16x32_f16 v[84:87], a[88:91], v[104:107], v[84:87]\n"
      "v_cvt_pk_f16_f32 v186, v180, v181\n"
      "v_pk_mul_f16 v122, v154, v170\n"
      "v_mfma_f32_16x16x32_f16 v[84:87], a[84:87], v[100:103], v[84:87]\n"
      "v_cvt_pk_f16_f32 v187, v182, v183\n"
      "v_pk_mul_f16 v123, v155, v171\n"
      "v_mfma_f32_16x16x32_f16 v[84:87], a[80:83], v[96:99], v[84:87]\n"
      "v_pk_mul_f16 v184, v184, s31 op_sel_hi:[1,0]\n"
      "v_pk_mul_f16 v185, v185, s31 op_sel_hi:[1,0]\n"
      "v_mfma_f32_16x16x32_f16 v[88:91], a[96:99], v[96:99], v[40:43]\n"
      "v_fma_f32 v192, |v92|, v60, v192\n"
      "v_fma_f32 v192, |v93|, v61, v192\n"
      "v_mfma_f32_16x16x32_f16 v[88:91], a[100:103], v[100:103], v[88:91]\n"
      "v_fma_f32 v192, |v94|, v62, v192\n"
      "v_fma_f32 v192, |v95|, v63, v192\n"
      "v_mfma_f32_16x16x32_f16 v[88:91], a[104:107], v[104:107], v[88:91]\n"
      "v_pk_mul_f16 v186, v186, s31 op_sel_hi:[1,0]\n"
      "v_pk_mul_f16 v187, v187, s31 op_sel_hi:[1,0]\n"
      "v_mfma_f32_16x16x32_f16 v[88:91], a[108:111], v[108:111], v[88:91]\n"
      "ds_write_b128 v1, v[184:187] offset:4608\n"
      "v_mfma_f32_16x16x32_f16 v[92:95], a[124:127], v[108:111], v[44:47]\n"
      "v_fma_f32 v192, |v80|, v64, v192\n"
      "v_fma_f32 v192, |v81|, v65, v192\n"
      "v_mfma_f32_16x16x32_f16 v[92:95], a[120:123], v[104:107], v[92:95]\n"
      "s_waitcnt lgkmcnt(1)\n"
      "v_pk_mul_f16 v124, v156, v172\n"
      "v_fma_f32 v192, |v82|, v66, v192\n"
      "v_mfma_f32_16x16x32_f16 v[92:95], a[116:119], v[100:103], v[92:95]\n"
      "v_fma_f32 v192, |v83|, v67, v192\n"
      "v_pk_mul_f16 v125, v157, v173\n"
      "v_mfma_f32_16x16x32_f16 v[92:95], a[112:115], v[96:99], v[92:95]\n"
      "v_pk_mul_f16 v126, v158, v174\n"
      "v_pk_mul_f16 v127, v159, v175\n"
      "v_mfma_f32_16x16x32_f16 v[80:83], a[0:3], v[112:115], v[16:19]\n"
      "ds_read_b128 v[160:163], v203 offset:4608\n"
      "v_fma_f32 v192, |v84|, v68, v192\n"
      "v_mfma_f32_16x16x32_f16 v[80:83], a[4:7], v[116:119], v[80:83]\n"
      "v_fma_f32 v192, |v85|, v69, v192\n"
      "v_fma_f32 v192, |v86|, v70, v192\n"
      "v_mfma_f32_16x16x32_f16 v[80:83], a[8:11], v[120:123], v[80:83]\n"
      "v_fma_f32 v192, |v87|, v71, v192\n"
      "v_fma_f32 v192, |v88|, v72, v192\n"
      "v_mfma_f32_16x16x32_f16 v[80:83], a[12:15], v[124:127], v[80:83]\n"
      "v_fma_f32 v192, |v89|, v73, v192\n"
      "v_fma_f32 v192, |v90|, v74, v192\n"
      "v_mfma_f32_16x16x32_f16 v[84:87], a[28:31], v[124:127], v[20:23]\n"
      "v_fma_f32 v192, |v91|, v75, v192\n"
      "v_fma_f32 v192, |v92|, v76, v192\n"
      "v_mfma_f32_16x16x32_f16 v[84:87], a[24:27], v[120:123], v[84:87]\n"
      "ds_read_b128 v[164:167], v203 offset:4672\n"
      "v_fma_f32 v192, |v93|, v77, v192\n"
      "v_mfma_f32_16x16x32_f16 v[84:87], a[20:23], v[116:119], v[84:87]\n"
      "v_fma_f32 v192, |v94|, v78, v192\n"
      "v_fma_f32 v192, |v95|, v79, v192\n"
      "v_mfma_f32_16x16x32_f16 v[84:87], a[16:19], v[112:115], v[84:87]\n"
      "ds_read_b128 v[168:171], v203 offset:4736\n"
      "v_min3_f32 v226, |v228|, |v233|, |v238|\n"
      "v_mfma_f32_16x16x32_f16 v[88:91], a[32:35], v[112:115], v[24:27]\n"
      "s_waitcnt lgkmcnt(2)\n"
      "v_pk_mul_f16 v96, v128, v160\n"
      "v_pk_mul_f16 v97, v129, v161\n"
      "v_mfma_f32_16x16x32_f16 v[88:91], a[36:39], v[116:119], v[88:91]\n"
      "v_pk_mul_f16 v98, v130, v162\n"
      "v_pk_mul_f16 v99, v131, v163\n"
      "v_mfma_f32_16x16x32_f16 v[88:91], a[40:43], v[120:123], v[88:91]\n"
      "v_fma_f32 v193, |v80|, v48, 4.0\n"
      "v_fma_f32 v193, |v81|, v49, v193\n"
      "v_mfma_f32_16x16x32_f16 v[88:91], a[44:47], v[124:127], v[88:91]\n"
      "s_waitcnt vmcnt(0)\n"
      "v_min3_f32 v226, |v243|, |v244|, v226\n"
      "v_min3_f32 v226, |v249|, |v254|, v226\n"
      "v_mfma_f32_16x16x32_f16 v[92:95], a[60:63], v[124:127], v[28:31]\n"
      "v_fma_f32 v193, |v82|, v50, v193\n"
      "v_fma_f32 v193, |v83|, v51, v193\n"
      "v_mfma_f32_16x16x32_f16 v[92:95], a[56:59], v[120:123], v[92:95]\n"
      "s_waitcnt lgkmcnt(1)\n"
      "v_pk_mul_f16 v100, v132, v164\n"
      "v_pk_mul_f16 v101, v133, v165\n"
      "v_mfma_f32_16x16x32_f16 v[92:95], a[52:55], v[116:119], v[92:95]\n"
      "v_cmp_le_f32 vcc, 0x3da00000, v226\n"
      "v_pk_mul_f16 v102, v134, v166\n"
      "v_mfma_f32_16x16x32_f16 v[92:95], a[48:51], v[112:115], v[92:95]\n"
      "v_fma_f32 v193, |v84|, v52, v193\n"
      "v_fma_f32 v193, |v85|, v53, v193\n"
      "v_mfma_f32_16x16x32_f16 v[80:83], a[64:67], v[112:115], v[32:35]\n"
      "s_waitcnt lgkmcnt(0)\n"
      "v_pk_mul_f16 v103, v135, v167\n"
      "v_pk_mul_f16 v104, v136, v168\n"
      "v_mfma_f32_16x16x32_f16 v[80:83], a[68:71], v[116:119], v[80:83]\n"
      "s_quadmask_b64 s[44:45], vcc\n"
      "v_fma_f32 v193, |v86|, v54, v193\n"
      "v_fma_f32 v193, |v87|, v55, v193\n"
      "v_mfma_f32_16x16x32_f16 v[80:83], a[72:75], v[120:123], v[80:83]\n"
      "s_quadmask_b32 s30, s44\n"
      "v_fma_f32 v193, |v88|, v56, v193\n"
      "v_fma_f32 v193, |v89|, v57, v193\n"
      "v_mfma_f32_16x16x32_f16 v[80:83], a[76:79], v[124:127], v[80:83]\n"
      "s_xor_b32 s30, s30, 15\n"
      "v_pk_mul_f16 v105, v137, v169\n"
      "v_pk_mul_f16 v106, v138, v170\n"
      "v_mfma_f32_16x16x32_f16 v[84:87], a[92:95], v[124:127], v[36:39]\n"
      "s_or_b32 s30, s30, s41\n"
      "ds_read_b128 v[172:175], v203 offset:4800\n"
      "v_fma_f32 v193, |v90|, v58, v193\n"
      "v_mfma_f32_16x16x32_f16 v[84:87], a[88:91], v[120:123], v[84:87]\n"
      "s_cmp_lg_u32 s55, 0\n"
      "v_fma_f32 v193, |v91|, v59, v193\n"
      "v_fma_f32 v193, |v92|, v60, v193\n"
      "v_mfma_f32_16x16x32_f16 v[84:87], a[84:87], v[116:119], v[84:87]\n"
      "s_cselect_b32 s30, 0, s30\n"
      "v_pk_mul_f16 v107, v139, v171\n"
      "v_fma_f32 v193, |v93|, v61, v193\n"
      "v_mfma_f32_16x16x32_f16 v[84:87], a[80:83], v[112:115], v[84:87]\n"
      "v_fma_f32 v193, |v94|, v62, v193\n"
      "v_fma_f32 v193, |v95|, v63, v193\n"
      "v_mfma_f32_16x16x32_f16 v[88:91], a[96:99], v[112:115], v[40:43]\n"
      "v_fma_f32 v193, |v80|, v64, v193\n"
      "v_fma_f32 v193, |v81|, v65, v193\n"
      "v_mfma_f32_16x16x32_f16 v[88:91], a[100:103], v[116:119], v[88:91]\n"
      "v_fma_f32 v193, |v82|, v66, v193\n"
      "v_fma_f32 v193, |v83|, v67, v193\n"
      "v_mfma_f32_16x16x32_f16 v[88:91], a[104:107], v[120:123], v[88:91]\n"
      "v_add_f32 v189, v189, v191\n"
      "v_fma_f32 v193, |v84|, v68, v193\n"
      "v_mfma_f32_16x16x32_f16 v[88:91], a[108:111], v[124:127], v[88:91]\n"
      "v_permlane32_swap_b32 v190, v192\n"
      "v_mfma_f32_16x16x32_f16 v[92:95], a[124:127], v[124:127], v[44:47]\n"
      "s_waitcnt lgkmcnt(0)\n"
      "v_pk_mul_f16 v108, v140, v172\n"
      "v_pk_mul_f16 v109, v141, v173\n"
      "v_mfma_f32_16x16x32_f16 v[92:95], a[120:123], v[120:123], v[92:95]\n"
      "v_pk_mul_f16 v110, v142, v174\n"
      "v_pk_mul_f16 v111, v143, v175\n"
      "v_mfma_f32_16x16x32_f16 v[92:95], a[116:119], v[116:119], v[92:95]\n"
      "v_fma_f32 v193, |v85|, v69, v193\n"
      "v_fma_f32 v193, |v86|, v70, v193\n"
      "v_mfma_f32_16x16x32_f16 v[92:95], a[112:115], v[112:115], v[92:95]\n"
      "v_fma_f32 v193, |v87|, v71, v193\n"
      "v_fma_f32 v193, |v88|, v72, v193\n"
      "v_mfma_f32_16x16x32_f16 v[80:83], a[0:3], v[96:99], v[16:19]\n"
      "v_fma_f32 v193, |v89|, v73, v193\n"
      "v_fma_f32 v193, |v90|, v74, v193\n"
      "v_mfma_f32_16x16x32_f16 v[80:83], a[4:7], v[100:103], v[80:83]\n"
      "v_fma_f32 v193, |v91|, v75, v193\n"
      "v_pk_mul_f16 v112, v144, v160\n"
      "v_mfma_f32_16x16x32_f16 v[80:83], a[8:11], v[104:107], v[80:83]\n"
      "v_fma_f32 v193, |v92|, v76, v193\n"
      "v_fma_f32 v193, |v93|, v77, v193\n"
      "v_mfma_f32_16x16x32_f16 v[80:83], a[12:15], v[108:111], v[80:83]\n"
      "v_pk_mul_f16 v113, v145, v161\n"
      "v_pk_mul_f16 v114, v146, v162\n"
      "v_mfma_f32_16x16x32_f16 v[84:87], a[28:31], v[108:111], v[20:23]\n"
      "v_fma_f32 v193, |v94|, v78, v193\n"
      "v_fma_f32 v193, |v95|, v79, v193\n"
      "v_mfma_f32_16x16x32_f16 v[84:87], a[24:27], v[104:107], v[84:87]\n"
      "ds_read_b32 v224, v227 offset:37632\n"
      "v_pk_mul_f16 v115, v147, v163\n"
      "v_mfma_f32_16x16x32_f16 v[84:87], a[20:23], v[100:103], v[84:87]\n"
      "v_permlane32_swap_b32 v188, v193\n"
      "v_mfma_f32_16x16x32_f16 v[84:87], a[16:19], v[96:99], v[84:87]\n"
      "ds_read_b64 v[176:177], v5 offset:37120\n"
      "ds_read_b32 v226, v213 offset:12480\n"
      "v_mfma_f32_16x16x32_f16 v[88:91], a[32:35], v[96:99], v[24:27]\n"
      "v_add_f32 v190, v190, v192\n"
      "v_add_f32 v188, v188, v193\n"
      "v_mfma_f32_16x16x32_f16 v[88:91], a[36:39], v[100:103], v[88:91]\n"
      "v_fma_f32 v194, |v80|, v48, 4.0\n"
      "v_fma_f32 v194, |v81|, v49, v194\n"
      "v_mfma_f32_16x16x32_f16 v[88:91], a[40:43], v[104:107], v[88:91]\n"
      "v_permlane16_swap_b32 v189, v190\n"
      "v_mfma_f32_16x16x32_f16 v[88:91], a[44:47], v[108:111], v[88:91]\n"
      "v_fma_f32 v194, |v82|, v50, v194\n"
      "v_fma_f32 v194, |v83|, v51, v194\n"
      "v_mfma_f32_16x16x32_f16 v[92:95], a[60:63], v[108:111], v[28:31]\n"
      "v_pk_mul_f16 v116, v148, v164\n"
      "v_pk_mul_f16 v117, v149, v165\n"
      "v_mfma_f32_16x16x32_f16 v[92:95], a[56:59], v[104:107], v[92:95]\n"
      "s_waitcnt lgkmcnt(2)\n"
      "v_cmp_ne_u32 vcc, 13, v224\n"
      "v_add_f32 v189, v189, v190\n"
      "v_mfma_f32_16x16x32_f16 v[92:95], a[52:55], v[100:103], v[92:95]\n"
      "v_pk_mul_f16 v118, v150, v166\n"
      "v_pk_mul_f16 v119, v151, v167\n"
      "v_mfma_f32_16x16x32_f16 v[92:95], a[48:51], v[96:99], v[92:95]\n"
      "s_or_b32 s45, vcc_lo, s30\n"
      "v_fma_f32 v194, |v84|, v52, v194\n"
      "v_fma_f32 v194, |v85|, v53, v194\n"
      "v_mfma_f32_16x16x32_f16 v[80:83], a[64:67], v[96:99], v[32:35]\n"
      "s_waitcnt lgkmcnt(0)\n"
      "v_max_f16_sdwa v226, |v226|, |v226| dst_sel:DWORD dst_unused:UNUSED_PAD src0_sel:WORD_0 src1_sel:WORD_1\n"
      "v_cmp_le_f16 vcc, 0x6400, v226\n"
      "v_mfma_f32_16x16x32_f16 v[80:83], a[68:71], v[100:103], v[80:83]\n"
      "v_fma_f32 v194, |v86|, v54, v194\n"
      "v_fma_f32 v194, |v87|, v55, v194\n"
      "v_mfma_f32_16x16x32_f16 v[80:83], a[72:75], v[104:107], v[80:83]\n"
      "41:\n"
      "s_cbranch_execz 44b\n"
      "s_or_b32 vcc_lo, vcc_lo, s45\n"
      "v_pk_mul_f16 v120, v152, v168\n"
      "v_mfma_f32_16x16x32_f16 v[80:83], a[76:79], v[108:111], v[80:83]\n"
      "s_cmp_lg_u64 vcc, 0\n"
      "v_fma_f32 v194, |v88|, v56, v194\n"
      "v_fma_f32 v194, |v89|, v57, v194\n"
      "v_mfma_f32_16x16x32_f16 v[84:87], a[92:95], v[108:111], v[36:39]\n"
      "s_cbranch_scc1 5f\n"
      "4:\n"
      "v_mfma_f32_16x16x32_f16 v[84:87], a[88:91], v[104:107], v[84:87]\n"
      "v_pk_mul_f16 v121, v153, v169\n"
      "v_cvt_pk_f16_f32 v176, v176, v177\n"
      "v_mfma_f32_16x16x32_f16 v[84:87], a[84:87], v[100:103], v[84:87]\n"
      "s_lshl_b32 s49, s40, 1\n"
      "ds_write_b32 v213, v176 offset:11904\n"
      "v_mfma_f32_16x16x32_f16 v[84:87], a[80:83], v[96:99], v[84:87]\n"
      "s_add_u32 s51, s57, s49\n"
      "v_fma_f32 v194, |v90|, v58, v194\n"
      "v_fma_f32 v194, |v91|, v59, v194\n"
      "v_pk_mul_f16 v122, v154, v170\n"
      "v_mfma_f32_16x16x32_f16 v[88:91], a[96:99], v[96:99], v[40:43]\n"
      "s_add_u32 s51, s51, s58\n"
      "v_pk_mul_f16 v123, v155, v171\n"
      "v_fma_f32 v194, |v92|, v60, v194\n"
      "v_mfma_f32_16x16x32_f16 v[88:91], a[100:103], v[100:103], v[88:91]\n"
      "s_add_u32 s51, s51, s56\n"
      "v_fma_f32 v194, |v93|, v61, v194\n"
      "v_fma_f32 v194, |v94|, v62, v194\n"
      "v_mfma_f32_16x16x32_f16 v[88:91], a[104:107], v[104:107], v[88:91]\n"
      "s_sub_u32 s51, 0, s51\n"
      "v_fma_f32 v194, |v95|, v63, v194\n"
      "v_fma_f32 v194, |v80|, v64, v194\n"
      "v_mfma_f32_16x16x32_f16 v[88:91], a[108:111], v[108:111], v[88:91]\n"
      "s_add_u32 s49, s59, s49\n"
      "v_fma_f32 v194, |v81|, v65, v194\n"
      "v_fma_f32 v194, |v82|, v66, v194\n"
      "v_mfma_f32_16x16x32_f16 v[92:95], a[124:127], v[108:111], v[44:47]\n"
      "s_sub_u32 s39, 7, s49\n"
      "v_pk_mul_f16 v124, v156, v172\n"
      "v_pk_mul_f16 v125, v157, v173\n"
      "v_mfma_f32_16x16x32_f16 v[92:95], a[120:123], v[104:107], v[92:95]\n"
      "v_fma_f32 v194, |v83|, v67, v194\n"
      "v_fma_f32 v194, |v84|, v68, v194\n"
      "v_mfma_f32_16x16x32_f16 v[92:95], a[116:119], v[100:103], v[92:95]\n"
      "v_pk_mul_f16 v126, v158, v174\n"
      "v_pk_mul_f16 v127, v159, v175\n"
      "v_mfma_f32_16x16x32_f16 v[92:95], a[112:115], v[96:99], v[92:95]\n"
      "ds_read_b128 v[160:163], v203 offset:5184\n"
      "v_ldexp_f32 v200, v200, s51\n"
      "v_mfma_f32_16x16x32_f16 v[80:83], a[0:3], v[112:115], v[16:19]\n"
      "v_fma_f32 v194, |v85|, v69, v194\n"
      "v_fma_f32 v194, |v86|, v70, v194\n"
      "v_mfma_f32_16x16x32_f16 v[80:83], a[4:7], v[116:119], v[80:83]\n"
      "v_fma_f32 v194, |v87|, v71, v194\n"
      "v_fma_f32 v194, |v88|, v72, v194\n"
      "v_mfma_f32_16x16x32_f16 v[80:83], a[8:11], v[120:123], v[80:83]\n"
      "ds_read_b128 v[164:167], v203 offset:5248\n"
      "v_fma_f32 v194, |v89|, v73, v194\n"
      "v_mfma_f32_16x16x32_f16 v[80:83], a[12:15], v[124:127], v[80:83]\n"
      "v_fma_f32 v194, |v90|, v74, v194\n"
      "v_fma_f32 v194, |v91|, v75, v194\n"
      "v_mfma_f32_4x4x4_16b_bf16 v[176:179], a[216:217], v[248:249], 0\n"
      "v_fma_f32 v194, |v92|, v76, v194\n"
      "v_mfma_f32_4x4x4_16b_bf16 v[180:183], a[216:217], v[250:251], 0\n"
      "v_fma_f32 v194, |v93|, v77, v194\n"
      "v_mfma_f32_4x4x4_16b_bf16 v[228:231], a[216:217], v[252:253], 0\n"
      "v_fma_f32 v194, |v94|, v78, v194\n"
      "v_mfma_f32_4x4x4_16b_bf16 v[232:235], a[216:217], v[254:255], 0\n"
      "s_waitcnt lgkmcnt(1)\n"
      "v_pk_mul_f16 v96, v128, v160\n"
      "v_pk_mul_f16 v97, v129, v161\n"
      "v_mfma_f32_16x16x32_f16 v[84:87], a[28:31], v[124:127], v[20:23]\n"
      "v_pk_mul_f16 v98, v130, v162\n"
      "v_pk_mul_f16 v99, v131, v163\n"
      "v_mfma_f32_16x16x32_f16 v[84:87], a[24:27], v[120:123], v[84:87]\n"
      "ds_read_b128 v[168:171], v203 offset:5312\n"
      "v_fma_f32 v194, |v95|, v79, v194\n"
      "v_mfma_f32_16x16x32_f16 v[84:87], a[20:23], v[116:119], v[84:87]\n"
      "s_waitcnt lgkmcnt(1)\n"
      "v_pk_mul_f16 v100, v132, v164\n"
      "v_pk_mul_f16 v101, v133, v165\n"
      "v_mfma_f32_16x16x32_f16 v[84:87], a[16:19], v[112:115], v[84:87]\n"
      "v_pk_mul_f16 v102, v134, v166\n"
      "v_pk_mul_f16 v103, v135, v167\n"
      "v_mfma_f32_16x16x32_f16 v[88:91], a[32:35], v[112:115], v[24:27]\n"
      "v_fma_f32 v195, |v80|, v48, 4.0\n"
      "v_fma_f32 v195, |v81|, v49, v195\n"
      "v_mfma_f32_16x16x32_f16 v[88:91], a[36:39], v[116:119], v[88:91]\n"
      "v_fma_f32 v195, |v82|, v50, v195\n"
      "v_fma_f32 v195, |v83|, v51, v195\n"
      "v_mfma_f32_16x16x32_f16 v[88:91], a[40:43], v[120:123], v[88:91]\n"
      "v_fma_f32 v195, |v84|, v52, v195\n"
      "v_fma_f32 v195, |v85|, v53, v195\n"
      "v_mfma_f32_16x16x32_f16 v[88:91], a[44:47], v[124:127], v[88:91]\n"
      "v_fma_f32 v195, |v86|, v54, v195\n"
      "v_fma_f32 v195, |v87|, v55, v195\n"
      "v_mfma_f32_16x16x32_f16 v[92:95], a[60:63], v[124:127], v[28:31]\n"
      "ds_read_b128 v[172:175], v203 offset:5376\n"
      "v_add_u32_sdwa v219, s38, v197 dst_sel:DWORD dst_unused:UNUSED_PAD src0_sel:DWORD src1_sel:WORD_1\n"
      "v_mfma_f32_16x16x32_f16 v[92:95], a[56:59], v[120:123], v[92:95]\n"
      "s_waitcnt lgkmcnt(1)\n"
      "v_pk_mul_f16 v104, v136, v168\n"
      "v_pk_mul_f16 v105, v137, v169\n"
      "v_mfma_f32_16x16x32_f16 v[92:95], a[52:55], v[116:119], v[92:95]\n"
      "v_pk_mul_f16 v106, v138, v170\n"
      "v_pk_mul_f16 v107, v139, v171\n"
      "v_mfma_f32_16x16x32_f16 v[92:95], a[48:51], v[112:115], v[92:95]\n"
      "v_fma_f32 v195, |v88|, v56, v195\n"
      "v_fma_f32 v195, |v89|, v57, v195\n"
      "v_mfma_f32_16x16x32_f16 v[80:83], a[64:67], v[112:115], v[32:35]\n"
      "v_fma_f32 v195, |v90|, v58, v195\n"
      "v_fma_f32 v195, |v91|, v59, v195\n"
      "v_mfma_f32_16x16x32_f16 v[80:83], a[68:71], v[116:119], v[80:83]\n"
      "v_add_u32_sdwa v220, s38, v198 dst_sel:DWORD dst_unused:UNUSED_PAD src0_sel:DWORD src1_sel:WORD_0\n"
      "v_add_u32_sdwa v221, s38, v198 dst_sel:DWORD dst_unused:UNUSED_PAD src0_sel:DWORD src1_sel:WORD_1\n"
      "v_mfma_f32_16x16x32_f16 v[80:83], a[72:75], v[120:123], v[80:83]\n"
      "v_fma_f32 v195, |v92|, v60, v195\n"
      "v_fma_f32 v195, |v93|, v61, v195\n"
      "v_mfma_f32_16x16x32_f16 v[80:83], a[76:79], v[124:127], v[80:83]\n"
      "v_fma_f32 v195, |v94|, v62, v195\n"
      "v_fma_f32 v195, |v95|, v63, v195\n"
      "v_mfma_f32_16x16x32_f16 v[84:87], a[92:95], v[124:127], v[36:39]\n"
      "s_waitcnt lgkmcnt(0)\n"
      "v_pk_mul_f16 v108, v140, v172\n"
      "v_pk_mul_f16 v109, v141, v173\n"
      "v_mfma_f32_16x16x32_f16 v[84:87], a[88:91], v[120:123], v[84:87]\n"
      "v_pk_mul_f16 v110, v142, v174\n"
      "v_pk_mul_f16 v111, v143, v175\n"
      "v_mfma_f32_16x16x32_f16 v[84:87], a[84:87], v[116:119], v[84:87]\n"
      "v_fma_f32 v195, |v80|, v64, v195\n"
      "v_fma_f32 v195, |v81|, v65, v195\n"
      "v_mfma_f32_16x16x32_f16 v[84:87], a[80:83], v[112:115], v[84:87]\n"
      "v_fma_f32 v195, |v82|, v66, v195\n"
      "v_fma_f32 v195, |v83|, v67, v195\n"
      "v_mfma_f32_16x16x32_f16 v[88:91], a[96:99], v[112:115], v[40:43]\n"
      "v_cvt_pk_f16_f32 v184, v176, v177\n"
      "v_cvt_pk_f16_f32 v185, v178, v179\n"
      "v_mfma_f32_16x16x32_f16 v[88:91], a[100:103], v[116:119], v[88:91]\n"
      "v_cvt_pk_f16_f32 v186, v180, v181\n"
      "v_cvt_pk_f16_f32 v187, v182, v183\n"
      "v_mfma_f32_16x16x32_f16 v[88:91], a[104:107], v[120:123], v[88:91]\n"
      "ds_write_b128 v1, v[184:187] offset:5760\n"
      "v_mfma_f32_16x16x32_f16 v[88:91], a[108:111], v[124:127], v[88:91]\n"
      "v_fma_f32 v195, |v84|, v68, v195\n"
      "v_fma_f32 v195, |v85|, v69, v195\n"
      "v_mfma_f32_16x16x32_f16 v[92:95], a[124:127], v[124:127], v[44:47]\n"
      "v_fma_f32 v195, |v86|, v70, v195\n"
      "v_fma_f32 v195, |v87|, v71, v195\n"
      "v_mfma_f32_16x16x32_f16 v[92:95], a[120:123], v[120:123], v[92:95]\n"
      "v_pk_mul_f16 v120, v152, v168\n"
      "v_pk_mul_f16 v121, v153, v169\n"
      "v_mfma_f32_16x16x32_f16 v[92:95], a[116:119], v[116:119], v[92:95]\n"
      "v_fma_f32 v195, |v88|, v72, v195\n"
      "v_fma_f32 v195, |v89|, v73, v195\n"
      "v_mfma_f32_16x16x32_f16 v[92:95], a[112:115], v[112:115], v[92:95]\n"
      "v_fma_f32 v195, |v90|, v74, v195\n"
      "v_fma_f32 v195, |v91|, v75, v195\n"
      "v_mfma_f32_16x16x32_f16 v[80:83], a[0:3], v[96:99], v[16:19]\n"
      "v_pk_mul_f16 v112, v144, v160\n"
      "v_pk_mul_f16 v113, v145, v161\n"
      "v_mfma_f32_16x16x32_f16 v[80:83], a[4:7], v[100:103], v[80:83]\n"
      "v_pk_mul_f16 v114, v146, v162\n"
      "v_pk_mul_f16 v115, v147, v163\n"
      "v_mfma_f32_16x16x32_f16 v[80:83], a[8:11], v[104:107], v[80:83]\n"
      "v_fma_f32 v195, |v92|, v76, v195\n"
      "v_fma_f32 v195, |v93|, v77, v195\n"
      "v_mfma_f32_16x16x32_f16 v[80:83], a[12:15], v[108:111], v[80:83]\n"
      "v_fma_f32 v195, |v94|, v78, v195\n"
      "v_fma_f32 v195, |v95|, v79, v195\n"
      "v_mfma_f32_16x16x32_f16 v[84:87], a[28:31], v[108:111], v[20:23]\n"
      "v_pk_mul_f16 v116, v148, v164\n"
      "v_pk_mul_f16 v117, v149, v165\n"
      "v_mfma_f32_16x16x32_f16 v[84:87], a[24:27], v[104:107], v[84:87]\n"
      "v_pk_mul_f16 v118, v150, v166\n"
      "v_pk_mul_f16 v119, v151, v167\n"
      "v_mfma_f32_16x16x32_f16 v[84:87], a[20:23], v[100:103], v[84:87]\n"
      "v_fma_f32 v190, |v80|, v48, 4.0\n"
      "v_fma_f32 v190, |v81|, v49, v190\n"
      "v_mfma_f32_16x16x32_f16 v[84:87], a[16:19], v[96:99], v[84:87]\n"
      "v_fma_f32 v190, |v82|, v50, v190\n"
      "v_fma_f32 v190, |v83|, v51, v190\n"
      "v_mfma_f32_16x16x32_f16 v[88:91], a[32:35], v[96:99], v[24:27]\n"
      "v_pk_mul_f16 v122, v154, v170\n"
      "v_pk_mul_f16 v123, v155, v171\n"
      "v_mfma_f32_16x16x32_f16 v[88:91], a[36:39], v[100:103], v[88:91]\n"
      "v_pk_mul_f16 v124, v156, v172\n"
      "v_pk_mul_f16 v125, v157, v173\n"
      "v_mfma_f32_16x16x32_f16 v[88:91], a[40:43], v[104:107], v[88:91]\n"
      "v_fma_f32 v190, |v84|, v52, v190\n"
      "v_fma_f32 v190, |v85|, v53, v190\n"
      "v_mfma_f32_16x16x32_f16 v[88:91], a[44:47], v[108:111], v[88:91]\n"
      "v_fma_f32 v190, |v86|, v54, v190\n"
      "v_fma_f32 v190, |v87|, v55, v190\n"
      "v_mfma_f32_16x16x32_f16 v[92:95], a[60:63], v[108:111], v[28:31]\n"
      "v_pk_mul_f16 v126, v158, v174\n"
      "v_pk_mul_f16 v127, v159, v175\n"
      "v_mfma_f32_16x16x32_f16 v[92:95], a[56:59], v[104:107], v[92:95]\n"
      "v_cvt_pk_f16_f32 v184, v228, v229\n"
      "v_cvt_pk_f16_f32 v185, v230, v231\n"
      "v_mfma_f32_16x16x32_f16 v[92:95], a[52:55], v[100:103], v[92:95]\n"
      "v_fma_f32 v190, |v88|, v56, v190\n"
      "v_fma_f32 v190, |v89|, v57, v190\n"
      "v_mfma_f32_16x16x32_f16 v[92:95], a[48:51], v[96:99], v[92:95]\n"
      "v_fma_f32 v190, |v90|, v58, v190\n"
      "v_fma_f32 v190, |v91|, v59, v190\n"
      "v_mfma_f32_16x16x32_f16 v[80:83], a[64:67], v[96:99], v[32:35]\n"
      "v_cvt_pk_f16_f32 v186, v232, v233\n"
      "v_cvt_pk_f16_f32 v187, v234, v235\n"
      "v_mfma_f32_16x16x32_f16 v[80:83], a[68:71], v[100:103], v[80:83]\n"
      "ds_write_b128 v1, v[184:187] offset:6912\n"
      "v_mfma_f32_16x16x32_f16 v[80:83], a[72:75], v[104:107], v[80:83]\n"
      "v_fma_f32 v190, |v92|, v60, v190\n"
      "v_fma_f32 v190, |v93|, v61, v190\n"
      "v_mfma_f32_16x16x32_f16 v[80:83], a[76:79], v[108:111], v[80:83]\n"
      "v_fma_f32 v190, |v94|, v62, v190\n"
      "v_fma_f32 v190, |v95|, v63, v190\n"
      "v_mfma_f32_16x16x32_f16 v[84:87], a[92:95], v[108:111], v[36:39]\n"
      "ds_read_b128 v[160:163], v203 offset:5760\n"
      "ds_read_b128 v[176:179], v214 offset:11904\n"
      "v_mfma_f32_16x16x32_f16 v[84:87], a[88:91], v[104:107], v[84:87]\n"
      "ds_read_b128 a[152:155], v219 offset:0\n"
      "ds_read_b128 a[168:171], v220 offset:0\n"
      "v_mfma_f32_16x16x32_f16 v[84:87], a[84:87], v[100:103], v[84:87]\n"
      "v_fma_f32 v190, |v80|, v64, v190\n"
      "v_fma_f32 v190, |v81|, v65, v190\n"
      "v_mfma_f32_16x16x32_f16 v[84:87], a[80:83], v[96:99], v[84:87]\n"
      "v_fma_f32 v190, |v82|, v66, v190\n"
      "v_fma_f32 v190, |v83|, v67, v190\n"
      "v_mfma_f32_16x16x32_f16 v[88:91], a[96:99], v[96:99], v[40:43]\n"
      "ds_read_b128 a[184:187], v221 offset:0\n"
      "v_add_u32_sdwa v216, s38, v196 dst_sel:DWORD dst_unused:UNUSED_PAD src0_sel:DWORD src1_sel:WORD_0\n"
      "v_mfma_f32_16x16x32_f16 v[88:91], a[100:103], v[100:103], v[88:91]\n"
      "ds_read_b128 a[156:159], v219 offset:64\n"
      "v_add_u32_sdwa v217, s38, v196 dst_sel:DWORD dst_unused:UNUSED_PAD src0_sel:DWORD src1_sel:WORD_1\n"
      "v_mfma_f32_16x16x32_f16 v[88:91], a[104:107], v[104:107], v[88:91]\n"
      "v_fma_f32 v190, |v84|, v68, v190\n"
      "v_fma_f32 v190, |v85|, v69, v190\n"
      "v_mfma_f32_16x16x32_f16 v[88:91], a[108:111], v[108:111], v[88:91]\n"
      "v_fma_f32 v190, |v86|, v70, v190\n"
      "v_fma_f32 v190, |v87|, v71, v190\n"
      "v_mfma_f32_16x16x32_f16 v[92:95], a[124:127], v[108:111], v[44:47]\n"
      "s_waitcnt lgkmcnt(4)\n"
      "ds_read_b128 v[164:167], v203 offset:5824\n"
      "v_pk_mul_f16 v228, v128, v176\n"
      "v_mfma_f32_16x16x32_f16 v[92:95], a[120:123], v[104:107], v[92:95]\n"
      "v_pk_mul_f16 v229, v129, v177\n"
      "v_pk_mul_f16 v230, v130, v178\n"
      "v_mfma_f32_16x16x32_f16 v[92:95], a[116:119], v[100:103], v[92:95]\n"
      "v_fma_f32 v190, |v88|, v72, v190\n"
      "v_fma_f32 v190, |v89|, v73, v190\n"
      "v_mfma_f32_16x16x32_f16 v[92:95], a[112:115], v[96:99], v[92:95]\n"
      "v_fma_f32 v190, |v90|, v74, v190\n"
      "v_fma_f32 v190, |v91|, v75, v190\n"
      "v_mfma_f32_16x16x32_f16 v[80:83], a[0:3], v[112:115], v[16:19]\n"
      "v_pk_mul_f16 v96, v128, v160\n"
      "v_pk_mul_f16 v97, v129, v161\n"
      "v_mfma_f32_16x16x32_f16 v[80:83], a[4:7], v[116:119], v[80:83]\n"
      "v_pk_mul_f16 v98, v130, v162\n"
      "v_pk_mul_f16 v99, v131, v163\n"
      "v_mfma_f32_16x16x32_f16 v[80:83], a[8:11], v[120:123], v[80:83]\n"
      "v_fma_f32 v190, |v92|, v76, v190\n"
      "v_fma_f32 v190, |v93|, v77, v190\n"
      "v_mfma_f32_16x16x32_f16 v[80:83], a[12:15], v[124:127], v[80:83]\n"
      "v_fma_f32 v190, |v94|, v78, v190\n"
      "v_fma_f32 v190, |v95|, v79, v190\n"
      "v_mfma_f32_16x16x32_f16 v[84:87], a[28:31], v[124:127], v[20:23]\n"
      "s_waitcnt lgkmcnt(0)\n"
      "ds_read_b128 v[168:171], v203 offset:5888\n"
      "v_pk_mul_f16 v100, v132, v164\n"
      "v_mfma_f32_16x16x32_f16 v[84:87], a[24:27], v[120:123], v[84:87]\n"
      "v_pk_mul_f16 v101, v133, v165\n"
      "v_pk_mul_f16 v102, v134, v166\n"
      "v_mfma_f32_16x16x32_f16 v[84:87], a[20:23], v[116:119], v[84:87]\n"
      "v_fma_f32 v191, |v80|, v48, 4.0\n"
      "v_fma_f32 v191, |v81|, v49, v191\n"
      "v_mfma_f32_16x16x32_f16 v[84:87], a[16:19], v[112:115], v[84:87]\n"
      "v_fma_f32 v191, |v82|, v50, v191\n"
      "v_fma_f32 v191, |v83|, v51, v191\n"
      "v_mfma_f32_16x16x32_f16 v[88:91], a[32:35], v[112:115], v[24:27]\n"
      "v_pk_mul_f16 v103, v135, v167\n"
      "v_pk_mul_f16 v231, v131, v179\n"
      "v_mfma_f32_16x16x32_f16 v[88:91], a[36:39], v[116:119], v[88:91]\n"
      "ds_read_b128 v[172:175], v203 offset:5952\n"
      "ds_read_b128 v[240:243], v201 offset:4608\n"
      "v_mfma_f32_16x16x32_f16 v[88:91], a[40:43], v[120:123], v[88:91]\n"
      "v_fma_f32 v191, |v84|, v52, v191\n"
      "v_fma_f32 v191, |v85|, v53, v191\n"
      "v_mfma_f32_16x16x32_f16 v[88:91], a[44:47], v[124:127], v[88:91]\n"
      "v_fma_f32 v191, |v86|, v54, v191\n"
      "v_fma_f32 v191, |v87|, v55, v191\n"
      "v_mfma_f32_16x16x32_f16 v[92:95], a[60:63], v[124:127], v[28:31]\n"
      "s_waitcnt lgkmcnt(2)\n"
      "v_pk_mul_f16 v104, v136, v168\n"
      "v_pk_mul_f16 v105, v137, v169\n"
      "v_mfma_f32_16x16x32_f16 v[92:95], a[56:59], v[120:123], v[92:95]\n"
      "v_pk_mul_f16 v106, v138, v170\n"
      "v_pk_mul_f16 v107, v139, v171\n"
      "v_mfma_f32_16x16x32_f16 v[92:95], a[52:55], v[116:119], v[92:95]\n"
      "v_fma_f32 v191, |v88|, v56, v191\n"
      "v_fma_f32 v191, |v89|, v57, v191\n"
      "v_mfma_f32_16x16x32_f16 v[92:95], a[48:51], v[112:115], v[92:95]\n"
      "v_fma_f32 v191, |v90|, v58, v191\n"
      "v_fma_f32 v191, |v91|, v59, v191\n"
      "v_mfma_f32_16x16x32_f16 v[80:83], a[64:67], v[112:115], v[32:35]\n"
      "ds_read_b128 v[244:247], v201 offset:4672\n"
      "ds_read_b128 v[248:251], v201 offset:4736\n"
      "v_mfma_f32_16x16x32_f16 v[80:83], a[68:71], v[116:119], v[80:83]\n"
      "s_waitcnt lgkmcnt(3)\n"
      "ds_read_b128 v[252:255], v201 offset:4800\n"
      "v_pk_mul_f16 v108, v140, v172\n"
      "v_mfma_f32_16x16x32_f16 v[80:83], a[72:75], v[120:123], v[80:83]\n"
      "v_fma_f32 v191, |v92|, v60, v191\n"
      "v_fma_f32 v191, |v93|, v61, v191\n"
      "v_mfma_f32_16x16x32_f16 v[80:83], a[76:79], v[124:127], v[80:83]\n"
      "v_fma_f32 v191, |v94|, v62, v191\n"
      "v_fma_f32 v191, |v95|, v63, v191\n"
      "v_mfma_f32_16x16x32_f16 v[84:87], a[92:95], v[124:127], v[36:39]\n"
      "v_pk_mul_f16 v109, v141, v173\n"
      "v_pk_mul_f16 v110, v142, v174\n"
      "v_mfma_f32_16x16x32_f16 v[84:87], a[88:91], v[120:123], v[84:87]\n"
      "v_pk_mul_f16 v111, v143, v175\n"
      "v_pk_mul_f16 v232, v144, v176\n"
      "v_mfma_f32_16x16x32_f16 v[84:87], a[84:87], v[116:119], v[84:87]\n"
      "v_fma_f32 v191, |v80|, v64, v191\n"
      "v_fma_f32 v191, |v81|, v65, v191\n"
      "v_mfma_f32_16x16x32_f16 v[84:87], a[80:83], v[112:115], v[84:87]\n"
      "v_fma_f32 v191, |v82|, v66, v191\n"
      "v_fma_f32 v191, |v83|, v67, v191\n"
      "v_mfma_f32_16x16x32_f16 v[88:91], a[96:99], v[112:115], v[40:43]\n"
      "v_pk_mul_f16 v233, v145, v177\n"
      "v_pk_mul_f16 v234, v146, v178\n"
      "v_mfma_f32_16x16x32_f16 v[88:91], a[100:103], v[116:119], v[88:91]\n"
      "s_waitcnt lgkmcnt(3)\n"
      "v_pk_mul_f16 v235, v147, v179\n"
      "v_pk_mul_f16 v236, v240, v176\n"
      "v_mfma_f32_16x16x32_f16 v[88:91], a[104:107], v[120:123], v[88:91]\n"
      "v_fma_f32 v191, |v84|, v68, v191\n"
      "v_fma_f32 v191, |v85|, v69, v191\n"
      "v_mfma_f32_16x16x32_f16 v[88:91], a[108:111], v[124:127], v[88:91]\n"
      "v_fma_f32 v191, |v86|, v70, v191\n"
      "v_fma_f32 v191, |v87|, v71, v191\n"
      "v_mfma_f32_16x16x32_f16 v[92:95], a[124:127], v[124:127], v[44:47]\n"
      "v_pk_mul_f16 v237, v241, v177\n"
      "v_pk_mul_f16 v238, v242, v178\n"
      "v_mfma_f32_16x16x32_f16 v[92:95], a[120:123], v[120:123], v[92:95]\n"
      "v_pk_mul_f16 v120, v152, v168\n"
      "v_pk_mul_f16 v121, v153, v169\n"
      "v_mfma_f32_16x16x32_f16 v[92:95], a[116:119], v[116:119], v[92:95]\n"
      "v_fma_f32 v191, |v88|, v72, v191\n"
      "v_fma_f32 v191, |v89|, v73, v191\n"
      "v_mfma_f32_16x16x32_f16 v[92:95], a[112:115], v[112:115], v[92:95]\n"
      "v_fma_f32 v191, |v90|, v74, v191\n"
      "v_fma_f32 v191, |v91|, v75, v191\n"
      "v_mfma_f32_16x16x32_f16 v[80:83], a[0:3], v[96:99], v[16:19]\n"
      "v_pk_mul_f16 v112, v144, v160\n"
      "v_pk_mul_f16 v113, v145, v161\n"
      "v_mfma_f32_16x16x32_f16 v[80:83], a[4:7], v[100:103], v[80:83]\n"
      "v_pk_mul_f16 v114, v146, v162\n"
      "v_pk_mul_f16 v115, v147, v163\n"
      "v_mfma_f32_16x16x32_f16 v[80:83], a[8:11], v[104:107], v[80:83]\n"
      "v_fma_f32 v191, |v92|, v76, v191\n"
      "v_fma_f32 v191, |v93|, v77, v191\n"
      "v_mfma_f32_16x16x32_f16 v[80:83], a[12:15], v[108:111], v[80:83]\n"
      "v_fma_f32 v191, |v94|, v78, v191\n"
      "v_fma_f32 v191, |v95|, v79, v191\n"
      "v_mfma_f32_16x16x32_f16 v[84:87], a[28:31], v[108:111], v[20:23]\n"
      "v_pk_mul_f16 v116, v148, v164\n"
      "v_pk_mul_f16 v117, v149, v165\n"
      "v_mfma_f32_16x16x32_f16 v[84:87], a[24:27], v[104:107], v[84:87]\n"
      "v_pk_mul_f16 v118, v150, v166\n"
      "v_pk_mul_f16 v119, v151, v167\n"
      "v_mfma_f32_16x16x32_f16 v[84:87], a[20:23], v[100:103], v[84:87]\n"
      "v_fma_f32 v192, |v80|, v48, 4.0\n"
      "v_fma_f32 v192, |v81|, v49, v192\n"
      "v_mfma_f32_16x16x32_f16 v[84:87], a[16:19], v[96:99], v[84:87]\n"
      "v_fma_f32 v192, |v82|, v50, v192\n"
      "v_fma_f32 v192, |v83|, v51, v192\n"
      "v_mfma_f32_16x16x32_f16 v[88:91], a[32:35], v[96:99], v[24:27]\n"
      "v_pk_mul_f16 v122, v154, v170\n"
      "v_pk_mul_f16 v123, v155, v171\n"
      "v_mfma_f32_16x16x32_f16 v[88:91], a[36:39], v[100:103], v[88:91]\n"
      "v_pk_mul_f16 v239, v243, v179\n"
      "v_pk_mul_f16 v124, v156, v172\n"
      "v_mfma_f32_16x16x32_f16 v[88:91], a[40:43], v[104:107], v[88:91]\n"
      "v_fma_f32 v192, |v84|, v52, v192\n"
      "v_fma_f32 v192, |v85|, v53, v192\n"
      "v_mfma_f32_16x16x32_f16 v[88:91], a[44:47], v[108:111], v[88:91]\n"
      "v_fma_f32 v192, |v86|, v54, v192\n"
      "v_fma_f32 v192, |v87|, v55, v192\n"
      "v_mfma_f32_16x16x32_f16 v[92:95], a[60:63], v[108:111], v[28:31]\n"
      "v_pk_mul_f16 v125, v157, v173\n"
      "v_pk_mul_f16 v126, v158, v174\n"
      "v_mfma_f32_16x16x32_f16 v[92:95], a[56:59], v[104:107], v[92:95]\n"
      "ds_read_b128 v[176:179], v214 offset:11968\n"
      "v_pk_mul_f16 v127, v159, v175\n"
      "v_mfma_f32_16x16x32_f16 v[92:95], a[52:55], v[100:103], v[92:95]\n"
      "v_fma_f32 v192, |v88|, v56, v192\n"
      "v_fma_f32 v192, |v89|, v57, v192\n"
      "v_mfma_f32_16x16x32_f16 v[92:95], a[48:51], v[96:99], v[92:95]\n"
      "v_fma_f32 v192, |v90|, v58, v192\n"
      "v_fma_f32 v192, |v91|, v59, v192\n"
      "v_mfma_f32_16x16x32_f16 a[128:131], v[228:231], a[152:155], 0\n"
      "v_add_u32_sdwa v218, s38, v197 dst_sel:DWORD dst_unused:UNUSED_PAD src0_sel:DWORD src1_sel:WORD_0\n"
      "v_add_u32_sdwa v215, s38, v207 dst_sel:DWORD dst_unused:UNUSED_PAD src0_sel:DWORD src1_sel:WORD_1\n"
      "v_mfma_f32_16x16x32_f16 v[80:83], a[64:67], v[96:99], v[32:35]\n"
      "v_add_u32_sdwa v211, s38, v207 dst_sel:DWORD dst_unused:UNUSED_PAD src0_sel:DWORD src1_sel:WORD_0\n"
      "v_add_u32_sdwa v209, s38, v206 dst_sel:DWORD dst_unused:UNUSED_PAD src0_sel:DWORD src1_sel:WORD_1\n"
      "v_mfma_f32_16x16x32_f16 v[80:83], a[68:71], v[100:103], v[80:83]\n"
      "v_fma_f32 v192, |v92|, v60, v192\n"
      "v_fma_f32 v192, |v93|, v61, v192\n"
      "v_mfma_f32_16x16x32_f16 v[80:83], a[72:75], v[104:107], v[80:83]\n"
      "v_fma_f32 v192, |v94|, v62, v192\n"
      "v_fma_f32 v192, |v95|, v63, v192\n"
      "v_mfma_f32_16x16x32_f16 v[80:83], a[76:79], v[108:111], v[80:83]\n"
      "v_add_u32_sdwa v208, s38, v206 dst_sel:DWORD dst_unused:UNUSED_PAD src0_sel:DWORD src1_sel:WORD_0\n"
      "v_add_u32_sdwa v207, s38, v205 dst_sel:DWORD dst_unused:UNUSED_PAD src0_sel:DWORD src1_sel:WORD_1\n"
      "v_mfma_f32_16x16x32_f16 v[84:87], a[92:95], v[108:111], v[36:39]\n"
      "s_waitcnt lgkmcnt(0)\n"
      "v_pk_mul_f16 v228, v132, v176\n"
      "v_pk_mul_f16 v229, v133, v177\n"
      "v_mfma_f32_16x16x32_f16 v[84:87], a[88:91], v[104:107], v[84:87]\n"
      "v_pk_mul_f16 v230, v134, v178\n"
      "v_pk_mul_f16 v231, v135, v179\n"
      "v_mfma_f32_16x16x32_f16 v[84:87], a[84:87], v[100:103], v[84:87]\n"
      "v_fma_f32 v192, |v80|, v64, v192\n"
      "v_fma_f32 v192, |v81|, v65, v192\n"
      "v_mfma_f32_16x16x32_f16 v[84:87], a[80:83], v[96:99], v[84:87]\n"
      "v_fma_f32 v192, |v82|, v66, v192\n"
      "v_fma_f32 v192, |v83|, v67, v192\n"
      "v_mfma_f32_16x16x32_f16 a[132:135], v[232:235], a[168:171], 0\n"
      "ds_read_b128 v[160:163], v203 offset:6336\n"
      "v_add_u32_sdwa v206, s38, v205 dst_sel:DWORD dst_unused:UNUSED_PAD src0_sel:DWORD src1_sel:WORD_0\n"
      "v_mfma_f32_16x16x32_f16 v[88:91], a[96:99], v[96:99], v[40:43]\n"
      "v_add_u32_sdwa v205, s38, v204 dst_sel:DWORD dst_unused:UNUSED_PAD src0_sel:DWORD src1_sel:WORD_1\n"
      "v_add_u32_sdwa v204, s38, v204 dst_sel:DWORD dst_unused:UNUSED_PAD src0_sel:DWORD src1_sel:WORD_0\n"
      "v_mfma_f32_16x16x32_f16 v[88:91], a[100:103], v[100:103], v[88:91]\n"
      "v_fma_f32 v192, |v84|, v68, v192\n"
      "v_fma_f32 v192, |v85|, v69, v192\n"
      "v_mfma_f32_16x16x32_f16 v[88:91], a[104:107], v[104:107], v[88:91]\n"
      "v_fma_f32 v192, |v86|, v70, v192\n"
      "v_fma_f32 v192, |v87|, v71, v192\n"
      "v_mfma_f32_16x16x32_f16 v[88:91], a[108:111], v[108:111], v[88:91]\n"
      "ds_read_b128 v[164:167], v203 offset:6400\n"
      "ds_read_b128 a[172:175], v220 offset:64\n"
      "v_mfma_f32_16x16x32_f16 v[92:95], a[124:127], v[108:111], v[44:47]\n"
      "v_pk_mul_f16 v232, v148, v176\n"
      "v_pk_mul_f16 v233, v149, v177\n"
      "v_mfma_f32_16x16x32_f16 v[92:95], a[120:123], v[104:107], v[92:95]\n"
      "v_pk_mul_f16 v234, v150, v178\n"
      "v_pk_mul_f16 v235, v151, v179\n"
      "v_mfma_f32_16x16x32_f16 v[92:95], a[116:119], v[100:103], v[92:95]\n"
      "v_fma_f32 v192, |v88|, v72, v192\n"
      "v_fma_f32 v192, |v89|, v73, v192\n"
      "v_mfma_f32_16x16x32_f16 v[92:95], a[112:115], v[96:99], v[92:95]\n"
      "v_fma_f32 v192, |v90|, v74, v192\n"
      "v_fma_f32 v192, |v91|, v75, v192\n"
      "v_mfma_f32_16x16x32_f16 a[136:139], v[236:239], a[184:187], 0\n"
      "s_waitcnt lgkmcnt(2)\n"
      "v_pk_mul_f16 v96, v128, v160\n"
      "v_pk_mul_f16 v97, v129, v161\n"
      "v_mfma_f32_16x16x32_f16 v[80:83], a[0:3], v[112:115], v[16:19]\n"
      "v_pk_mul_f16 v98, v130, v162\n"
      "v_pk_mul_f16 v99, v131, v163\n"
      "v_mfma_f32_16x16x32_f16 v[80:83], a[4:7], v[116:119], v[80:83]\n"
      "v_fma_f32 v192, |v92|, v76, v192\n"
      "v_fma_f32 v192, |v93|, v77, v192\n"
      "v_mfma_f32_16x16x32_f16 v[80:83], a[8:11], v[120:123], v[80:83]\n"
      "v_fma_f32 v192, |v94|, v78, v192\n"
      "v_fma_f32 v192, |v95|, v79, v192\n"
      "v_mfma_f32_16x16x32_f16 v[80:83], a[12:15], v[124:127], v[80:83]\n"
      "s_waitcnt lgkmcnt(1)\n"
      "ds_read_b128 v[168:171], v203 offset:6464\n"
      "v_pk_mul_f16 v100, v132, v164\n"
      "v_mfma_f32_16x16x32_f16 v[84:87], a[28:31], v[124:127], v[20:23]\n"
      "v_permlane32_swap_b32 v194, v192\n"
      "v_mfma_f32_16x16x32_f16 v[84:87], a[24:27], v[120:123], v[84:87]\n"
      "v_pk_mul_f16 v101, v133, v165\n"
      "v_pk_mul_f16 v102, v134, v166\n"
      "v_mfma_f32_16x16x32_f16 v[84:87], a[20:23], v[116:119], v[84:87]\n"
      "v_fma_f32 v193, |v80|, v48, 4.0\n"
      "v_fma_f32 v193, |v81|, v49, v193\n"
      "v_mfma_f32_16x16x32_f16 v[84:87], a[16:19], v[112:115], v[84:87]\n"
      "v_fma_f32 v193, |v82|, v50, v193\n"
      "v_fma_f32 v193, |v83|, v51, v193\n"
      "v_mfma_f32_16x16x32_f16 v[88:91], a[32:35], v[112:115], v[24:27]\n"
      "v_pk_mul_f16 v103, v135, v167\n"
      "v_pk_mul_f16 v236, v244, v176\n"
      "v_mfma_f32_16x16x32_f16 v[88:91], a[36:39], v[116:119], v[88:91]\n"
      "ds_read_b128 a[188:191], v221 offset:64\n"
      "v_pk_mul_f16 v237, v245, v177\n"
      "v_mfma_f32_16x16x32_f16 v[88:91], a[40:43], v[120:123], v[88:91]\n"
      "v_fma_f32 v193, |v84|, v52, v193\n"
      "v_fma_f32 v193, |v85|, v53, v193\n"
      "v_mfma_f32_16x16x32_f16 v[88:91], a[44:47], v[124:127], v[88:91]\n"
      "v_fma_f32 v193, |v86|, v54, v193\n"
      "v_fma_f32 v193, |v87|, v55, v193\n"
      "v_mfma_f32_16x16x32_f16 v[92:95], a[60:63], v[124:127], v[28:31]\n"
      "s_waitcnt lgkmcnt(1)\n"
      "v_pk_mul_f16 v104, v136, v168\n"
      "v_pk_mul_f16 v105, v137, v169\n"
      "v_mfma_f32_16x16x32_f16 v[92:95], a[56:59], v[120:123], v[92:95]\n"
      "v_pk_mul_f16 v106, v138, v170\n"
      "v_pk_mul_f16 v107, v139, v171\n"
      "v_mfma_f32_16x16x32_f16 v[92:95], a[52:55], v[116:119], v[92:95]\n"
      "v_fma_f32 v193, |v88|, v56, v193\n"
      "v_fma_f32 v193, |v89|, v57, v193\n"
      "v_mfma_f32_16x16x32_f16 v[92:95], a[48:51], v[112:115], v[92:95]\n"
      "v_fma_f32 v193, |v90|, v58, v193\n"
      "v_fma_f32 v193, |v91|, v59, v193\n"
      "v_mfma_f32_16x16x32_f16 a[128:131], v[228:231], a[156:159], a[128:131]\n"
      "ds_read_b128 v[172:175], v203 offset:6528\n"
      "v_pk_mul_f16 v238, v246, v178\n"
      "v_mfma_f32_16x16x32_f16 v[80:83], a[64:67], v[112:115], v[32:35]\n"
      "v_add_u32_sdwa v10, s99, v199 dst_sel:DWORD dst_unused:UNUSED_PAD src0_sel:DWORD src1_sel:WORD_0\n"
      "v_add_u32_sdwa v11, s99, v199 dst_sel:DWORD dst_unused:UNUSED_PAD src0_sel:DWORD src1_sel:WORD_1\n"
      "v_mfma_f32_16x16x32_f16 v[80:83], a[68:71], v[116:119], v[80:83]\n"
      "v_fma_f32 v193, |v92|, v60, v193\n"
      "v_fma_f32 v193, |v93|, v61, v193\n"
      "v_mfma_f32_16x16x32_f16 v[80:83], a[72:75], v[120:123], v[80:83]\n"
      "v_fma_f32 v193, |v94|, v62, v193\n"
      "v_fma_f32 v193, |v95|, v63, v193\n"
      "v_mfma_f32_16x16x32_f16 v[80:83], a[76:79], v[124:127], v[80:83]\n"
      "v_pk_mul_f16 v239, v247, v179\n"
      "v_mov_b32 v7, 0xf149f2ca\n"
      "v_mfma_f32_16x16x32_f16 v[84:87], a[92:95], v[124:127], v[36:39]\n"
      "42:\n"
      "s_cbranch_execz 44b\n"
      "v_add_u32 v12, s98, v1\n"
      "v_mfma_f32_16x16x32_f16 v[84:87], a[88:91], v[120:123], v[84:87]\n"
      "s_and_b32 s100, s100, 0x37373737\n"
      "ds_read_b128 v[176:179], v214 offset:12032\n"
      "v_mov_b32 v8, 0xf149f2ca\n"
      "v_mfma_f32_16x16x32_f16 v[84:87], a[84:87], v[116:119], v[84:87]\n"
      "s_xor_b32 s100, s100, 0x370037\n"
      "v_fma_f32 v193, |v80|, v64, v193\n"
      "v_fma_f32 v193, |v81|, v65, v193\n"
      "v_mfma_f32_16x16x32_f16 v[84:87], a[80:83], v[112:115], v[84:87]\n"
      "s_waitcnt lgkmcnt(1)\n"
      "v_pk_mul_f16 v108, v140, v172\n"
      "v_pk_mul_f16 v109, v141, v173\n"
      "v_mfma_f32_16x16x32_f16 a[132:135], v[232:235], a[172:175], a[132:135]\n"
      "s_mov_b32 s101, s100\n"
      "v_fma_f32 v193, |v82|, v66, v193\n"
      "v_fma_f32 v193, |v83|, v67, v193\n"
      "v_mfma_f32_16x16x32_f16 v[88:91], a[96:99], v[112:115], v[40:43]\n"
      "s_or_b32 s44, s55, s41\n"
      "v_pk_mul_f16 v110, v142, v174\n"
      "v_pk_mul_f16 v111, v143, v175\n"
      "v_mfma_f32_16x16x32_f16 v[88:91], a[100:103], v[116:119], v[88:91]\n"
      "s_cselect_b32 s44, 2, 1\n"
      "v_fma_f32 v193, |v84|, v68, v193\n"
      "v_fma_f32 v193, |v85|, v69, v193\n"
      "v_mfma_f32_16x16x32_f16 v[88:91], a[104:107], v[120:123], v[88:91]\n"
      "v_fma_f32 v193, |v86|, v70, v193\n"
      "v_fma_f32 v193, |v87|, v71, v193\n"
      "v_mfma_f32_16x16x32_f16 v[88:91], a[108:111], v[124:127], v[88:91]\n"
      "ds_read_b128 a[160:163], v219 offset:128\n"
      "ds_read_b128 a[176:179], v220 offset:128\n"
      "v_mfma_f32_16x16x32_f16 v[92:95], a[124:127], v[124:127], v[44:47]\n"
      "s_waitcnt lgkmcnt(2)\n"
      "v_pk_mul_f16 v228, v136, v176\n"
      "v_pk_mul_f16 v229, v137, v177\n"
      "v_mfma_f32_16x16x32_f16 v[92:95], a[120:123], v[120:123], v[92:95]\n"
      "v_pk_mul_f16 v230, v138, v178\n"
      "v_pk_mul_f16 v231, v139, v179\n"
      "v_mfma_f32_16x16x32_f16 v[92:95], a[116:119], v[116:119], v[92:95]\n"
      "v_fma_f32 v193, |v88|, v72, v193\n"
      "v_fma_f32 v193, |v89|, v73, v193\n"
      "v_mfma_f32_16x16x32_f16 v[92:95], a[112:115], v[112:115], v[92:95]\n"
      "v_fma_f32 v193, |v90|, v74, v193\n"
      "v_fma_f32 v193, |v91|, v75, v193\n"
      "v_mfma_f32_16x16x32_f16 a[136:139], v[236:239], a[188:191], a[136:139]\n"
      "v_pk_mul_f16 v112, v144, v160\n"
      "v_pk_mul_f16 v113, v145, v161\n"
      "v_mfma_f32_16x16x32_f16 v[80:83], a[0:3], v[96:99], v[16:19]\n"
      "v_pk_mul_f16 v114, v146, v162\n"
      "v_pk_mul_f16 v115, v147, v163\n"
      "v_mfma_f32_16x16x32_f16 v[80:83], a[4:7], v[100:103], v[80:83]\n"
      "v_fma_f32 v193, |v92|, v76, v193\n"
      "v_fma_f32 v193, |v93|, v77, v193\n"
      "v_mfma_f32_16x16x32_f16 v[80:83], a[8:11], v[104:107], v[80:83]\n"
      "v_fma_f32 v193, |v94|, v78, v193\n"
      "v_fma_f32 v193, |v95|, v79, v193\n"
      "v_mfma_f32_16x16x32_f16 v[80:83], a[12:15], v[108:111], v[80:83]\n"
      "v_pk_mul_f16 v116, v148, v164\n"
      "v_pk_mul_f16 v117, v149, v165\n"
      "v_mfma_f32_16x16x32_f16 v[84:87], a[28:31], v[108:111], v[20:23]\n"
      "v_permlane32_swap_b32 v195, v193\n"
      "v_mfma_f32_16x16x32_f16 v[84:87], a[24:27], v[104:107], v[84:87]\n"
      "v_pk_mul_f16 v118, v150, v166\n"
      "v_pk_mul_f16 v119, v151, v167\n"
      "v_mfma_f32_16x16x32_f16 v[84:87], a[20:23], v[100:103], v[84:87]\n"
      "v_fma_f32 v196, |v80|, v48, 4.0\n"
      "v_fma_f32 v196, |v81|, v49, v196\n"
      "v_mfma_f32_16x16x32_f16 v[84:87], a[16:19], v[96:99], v[84:87]\n"
      "v_fma_f32 v196, |v82|, v50, v196\n"
      "v_fma_f32 v196, |v83|, v51, v196\n"
      "v_mfma_f32_16x16x32_f16 v[88:91], a[32:35], v[96:99], v[24:27]\n"
      "v_pk_mul_f16 v232, v152, v176\n"
      "v_pk_mul_f16 v120, v152, v168\n"
      "v_mfma_f32_16x16x32_f16 v[88:91], a[36:39], v[100:103], v[88:91]\n"
      "v_pk_mul_f16 v233, v153, v177\n"
      "v_pk_mul_f16 v121, v153, v169\n"
      "v_mfma_f32_16x16x32_f16 v[88:91], a[40:43], v[104:107], v[88:91]\n"
      "v_fma_f32 v196, |v84|, v52, v196\n"
      "v_fma_f32 v196, |v85|, v53, v196\n"
      "v_mfma_f32_16x16x32_f16 v[88:91], a[44:47], v[108:111], v[88:91]\n"
      "v_fma_f32 v196, |v86|, v54, v196\n"
      "v_fma_f32 v196, |v87|, v55, v196\n"
      "v_mfma_f32_16x16x32_f16 v[92:95], a[60:63], v[108:111], v[28:31]\n"
      "v_pk_mul_f16 v234, v154, v178\n"
      "v_pk_mul_f16 v122, v154, v170\n"
      "v_mfma_f32_16x16x32_f16 v[92:95], a[56:59], v[104:107], v[92:95]\n"
      "v_pk_mul_f16 v235, v155, v179\n"
      "v_pk_mul_f16 v123, v155, v171\n"
      "v_mfma_f32_16x16x32_f16 v[92:95], a[52:55], v[100:103], v[92:95]\n"
      "v_fma_f32 v196, |v88|, v56, v196\n"
      "v_fma_f32 v196, |v89|, v57, v196\n"
      "v_mfma_f32_16x16x32_f16 v[92:95], a[48:51], v[96:99], v[92:95]\n"
      "s_waitcnt lgkmcnt(1)\n"
      "ds_read_b128 a[192:195], v221 offset:128\n"
      "v_fma_f32 v196, |v90|, v58, v196\n"
      "v_mfma_f32_16x16x32_f16 a[128:131], v[228:231], a[160:163], a[128:131]\n"
      "v_fma_f32 v196, |v91|, v59, v196\n"
      "v_pk_mul_f16 v236, v248, v176\n"
      "v_mfma_f32_16x16x32_f16 v[80:83], a[64:67], v[96:99], v[32:35]\n"
      "v_pk_mul_f16 v237, v249, v177\n"
      "v_pk_mul_f16 v238, v250, v178\n"
      "v_mfma_f32_16x16x32_f16 v[80:83], a[68:71], v[100:103], v[80:83]\n"
      "v_fma_f32 v196, |v92|, v60, v196\n"
      "v_fma_f32 v196, |v93|, v61, v196\n"
      "v_mfma_f32_16x16x32_f16 v[80:83], a[72:75], v[104:107], v[80:83]\n"
      "v_fma_f32 v196, |v94|, v62, v196\n"
      "v_fma_f32 v196, |v95|, v63, v196\n"
      "v_mfma_f32_16x16x32_f16 v[80:83], a[76:79], v[108:111], v[80:83]\n"
      "v_pk_mul_f16 v239, v251, v179\n"
      "v_pk_mul_f16 v124, v156, v172\n"
      "v_mfma_f32_16x16x32_f16 v[84:87], a[92:95], v[108:111], v[36:39]\n"
      "v_pk_mul_f16 v125, v157, v173\n"
      "v_pk_mul_f16 v126, v158, v174\n"
      "v_mfma_f32_16x16x32_f16 v[84:87], a[88:91], v[104:107], v[84:87]\n"
      "ds_read_b128 v[176:179], v214 offset:12096\n"
      "v_mov_b32 v223, s44\n"
      "v_mfma_f32_16x16x32_f16 v[84:87], a[84:87], v[100:103], v[84:87]\n"
      "ds_write_b32 v2, v223 offset:8\n"
      "v_mfma_f32_16x16x32_f16 v[84:87], a[80:83], v[96:99], v[84:87]\n"
      "s_waitcnt lgkmcnt(3)\n"
      "v_fma_f32 v196, |v80|, v64, v196\n"
      "v_fma_f32 v196, |v81|, v65, v196\n"
      "v_mfma_f32_16x16x32_f16 a[132:135], v[232:235], a[176:179], a[132:135]\n"
      "v_fma_f32 v196, |v82|, v66, v196\n"
      "v_fma_f32 v196, |v83|, v67, v196\n"
      "v_mfma_f32_16x16x32_f16 v[88:91], a[96:99], v[96:99], v[40:43]\n"
      "v_add_f32 v194, v194, v192\n"
      "v_add_f32 v195, v195, v193\n"
      "v_mfma_f32_16x16x32_f16 v[88:91], a[100:103], v[100:103], v[88:91]\n"
      "ds_read_b128 v[160:163], v202 offset:4608\n"
      "v_pk_mul_f16 v127, v159, v175\n"
      "v_mfma_f32_16x16x32_f16 v[88:91], a[104:107], v[104:107], v[88:91]\n"
      "v_fma_f32 v196, |v84|, v68, v196\n"
      "v_fma_f32 v196, |v85|, v69, v196\n"
      "v_mfma_f32_16x16x32_f16 v[88:91], a[108:111], v[108:111], v[88:91]\n"
      "v_fma_f32 v196, |v86|, v70, v196\n"
      "v_fma_f32 v196, |v87|, v71, v196\n"
      "v_mfma_f32_16x16x32_f16 v[92:95], a[124:127], v[108:111], v[44:47]\n"
      "ds_read_b128 a[164:167], v219 offset:192\n"
      "ds_read_b128 v[164:167], v202 offset:4672\n"
      "v_mfma_f32_16x16x32_f16 v[92:95], a[120:123], v[104:107], v[92:95]\n"
      "s_waitcnt lgkmcnt(4)\n"
      "v_pk_mul_f16 v228, v140, v176\n"
      "v_pk_mul_f16 v229, v141, v177\n"
      "v_mfma_f32_16x16x32_f16 v[92:95], a[116:119], v[100:103], v[92:95]\n"
      "v_fma_f32 v196, |v88|, v72, v196\n"
      "v_fma_f32 v196, |v89|, v73, v196\n"
      "v_mfma_f32_16x16x32_f16 v[92:95], a[112:115], v[96:99], v[92:95]\n"
      "v_fma_f32 v196, |v90|, v74, v196\n"
      "v_fma_f32 v196, |v91|, v75, v196\n"
      "v_mfma_f32_16x16x32_f16 a[136:139], v[236:239], a[192:195], a[136:139]\n"
      "v_pk_mul_f16 v230, v142, v178\n"
      "v_pk_mul_f16 v231, v143, v179\n"
      "v_mfma_f32_16x16x32_f16 v[80:83], a[0:3], v[112:115], v[16:19]\n"
      "s_waitcnt lgkmcnt(2)\n"
      "v_pk_mul_f16 v96, v240, v160\n"
      "v_pk_mul_f16 v97, v241, v161\n"
      "v_mfma_f32_16x16x32_f16 v[80:83], a[4:7], v[116:119], v[80:83]\n"
      "v_fma_f32 v196, |v92|, v76, v196\n"
      "v_fma_f32 v196, |v93|, v77, v196\n"
      "v_mfma_f32_16x16x32_f16 v[80:83], a[8:11], v[120:123], v[80:83]\n"
      "v_fma_f32 v196, |v94|, v78, v196\n"
      "v_fma_f32 v196, |v95|, v79, v196\n"
      "v_mfma_f32_16x16x32_f16 v[80:83], a[12:15], v[124:127], v[80:83]\n"
      "v_pk_mul_f16 v98, v242, v162\n"
      "v_pk_mul_f16 v99, v243, v163\n"
      "v_mfma_f32_16x16x32_f16 v[84:87], a[28:31], v[124:127], v[20:23]\n"
      "v_permlane32_swap_b32 v190, v196\n"
      "v_mfma_f32_16x16x32_f16 v[84:87], a[24:27], v[120:123], v[84:87]\n"
      "s_waitcnt lgkmcnt(0)\n"
      "ds_read_b128 v[168:171], v202 offset:4736\n"
      "v_pk_mul_f16 v100, v244, v164\n"
      "v_mfma_f32_16x16x32_f16 v[84:87], a[20:23], v[116:119], v[84:87]\n"
      "v_fma_f32 v197, |v80|, v48, 4.0\n"
      "v_fma_f32 v197, |v81|, v49, v197\n"
      "v_mfma_f32_16x16x32_f16 v[84:87], a[16:19], v[112:115], v[84:87]\n"
      "ds_read_b128 a[180:183], v220 offset:192\n"
      "v_fma_f32 v197, |v82|, v50, v197\n"
      "v_mfma_f32_16x16x32_f16 v[88:91], a[32:35], v[112:115], v[24:27]\n"
      "v_fma_f32 v197, |v83|, v51, v197\n"
      "v_pk_mul_f16 v101, v245, v165\n"
      "v_mfma_f32_16x16x32_f16 v[88:91], a[36:39], v[116:119], v[88:91]\n"
      "v_pk_mul_f16 v102, v246, v166\n"
      "v_pk_mul_f16 v103, v247, v167\n"
      "v_mfma_f32_16x16x32_f16 v[88:91], a[40:43], v[120:123], v[88:91]\n"
      "v_fma_f32 v197, |v84|, v52, v197\n"
      "v_fma_f32 v197, |v85|, v53, v197\n"
      "v_mfma_f32_16x16x32_f16 v[88:91], a[44:47], v[124:127], v[88:91]\n"
      "v_fma_f32 v197, |v86|, v54, v197\n"
      "v_fma_f32 v197, |v87|, v55, v197\n"
      "v_mfma_f32_16x16x32_f16 v[92:95], a[60:63], v[124:127], v[28:31]\n"
      "v_pk_mul_f16 v232, v156, v176\n"
      "v_pk_mul_f16 v233, v157, v177\n"
      "v_mfma_f32_16x16x32_f16 v[92:95], a[56:59], v[120:123], v[92:95]\n"
      "s_waitcnt lgkmcnt(1)\n"
      "v_pk_mul_f16 v104, v248, v168\n"
      "v_pk_mul_f16 v105, v249, v169\n"
      "v_mfma_f32_16x16x32_f16 v[92:95], a[52:55], v[116:119], v[92:95]\n"
      "v_pk_mul_f16 v234, v158, v178\n"
      "v_pk_mul_f16 v106, v250, v170\n"
      "v_mfma_f32_16x16x32_f16 v[92:95], a[48:51], v[112:115], v[92:95]\n"
      "v_pk_mul_f16 v235, v159, v179\n"
      "v_pk_mul_f16 v107, v251, v171\n"
      "v_mfma_f32_16x16x32_f16 a[128:131], v[228:231], a[164:167], a[128:131]\n"
      "v_fma_f32 v197, |v88|, v56, v197\n"
      "v_fma_f32 v197, |v89|, v57, v197\n"
      "v_mfma_f32_16x16x32_f16 v[80:83], a[64:67], v[112:115], v[32:35]\n"
      "ds_read_b128 a[196:199], v221 offset:192\n"
      "v_fma_f32 v197, |v90|, v58, v197\n"
      "v_mfma_f32_16x16x32_f16 v[80:83], a[68:71], v[116:119], v[80:83]\n"
      "v_fma_f32 v197, |v91|, v59, v197\n"
      "v_fma_f32 v197, |v92|, v60, v197\n"
      "v_mfma_f32_16x16x32_f16 v[80:83], a[72:75], v[120:123], v[80:83]\n"
      "ds_write_b64 v218, a[128:129] offset:0\n"
      "v_mfma_f32_16x16x32_f16 v[80:83], a[76:79], v[124:127], v[80:83]\n"
      "v_fma_f32 v197, |v93|, v61, v197\n"
      "v_fma_f32 v197, |v94|, v62, v197\n"
      "v_mfma_f32_16x16x32_f16 v[84:87], a[92:95], v[124:127], v[36:39]\n"
      "ds_read_b128 v[172:175], v202 offset:4800\n"
      "v_fma_f32 v197, |v95|, v63, v197\n"
      "v_mfma_f32_16x16x32_f16 v[84:87], a[88:91], v[120:123], v[84:87]\n"
      "ds_write_b64 v218, a[130:131] offset:8\n"
      "v_mfma_f32_16x16x32_f16 v[84:87], a[84:87], v[116:119], v[84:87]\n"
      "v_pk_mul_f16 v236, v252, v176\n"
      "v_pk_mul_f16 v237, v253, v177\n"
      "v_mfma_f32_16x16x32_f16 v[84:87], a[80:83], v[112:115], v[84:87]\n"
      "s_waitcnt lgkmcnt(4)\n"
      "v_pk_mul_f16 v238, v254, v178\n"
      "v_pk_mul_f16 v239, v255, v179\n"
      "v_mfma_f32_16x16x32_f16 a[132:135], v[232:235], a[180:183], a[132:135]\n"
      "v_fma_f32 v197, |v80|, v64, v197\n"
      "v_fma_f32 v197, |v81|, v65, v197\n"
      "v_mfma_f32_16x16x32_f16 v[88:91], a[96:99], v[112:115], v[40:43]\n"
      "v_fma_f32 v197, |v82|, v66, v197\n"
      "v_fma_f32 v197, |v83|, v67, v197\n"
      "v_mfma_f32_16x16x32_f16 v[88:91], a[100:103], v[116:119], v[88:91]\n"
      "ds_read_b128 v[160:163], v203 offset:6912\n"
      "v_fma_f32 v197, |v84|, v68, v197\n"
      "v_mfma_f32_16x16x32_f16 v[88:91], a[104:107], v[120:123], v[88:91]\n"
      "ds_write_b64 v218, a[132:133] offset:1280\n"
      "v_mfma_f32_16x16x32_f16 v[88:91], a[108:111], v[124:127], v[88:91]\n"
      "s_waitcnt lgkmcnt(3)\n"
      "v_pk_mul_f16 v108, v252, v172\n"
      "v_pk_mul_f16 v109, v253, v173\n"
      "v_mfma_f32_16x16x32_f16 v[92:95], a[124:127], v[124:127], v[44:47]\n"
      "ds_write_b64 v218, a[134:135] offset:1288\n"
      "v_mfma_f32_16x16x32_f16 v[92:95], a[120:123], v[120:123], v[92:95]\n"
      "v_pk_mul_f16 v110, v254, v174\n"
      "v_pk_mul_f16 v111, v255, v175\n"
      "v_mfma_f32_16x16x32_f16 v[92:95], a[116:119], v[116:119], v[92:95]\n"
      "v_fma_f32 v197, |v85|, v69, v197\n"
      "v_fma_f32 v197, |v86|, v70, v197\n"
      "v_mfma_f32_16x16x32_f16 v[92:95], a[112:115], v[112:115], v[92:95]\n"
      "v_fma_f32 v197, |v87|, v71, v197\n"
      "v_fma_f32 v197, |v88|, v72, v197\n"
      "v_mfma_f32_16x16x32_f16 a[136:139], v[236:239], a[196:199], a[136:139]\n"
      "v_fma_f32 v197, |v89|, v73, v197\n"
      "v_fma_f32 v197, |v90|, v74, v197\n"
      "v_mfma_f32_16x16x32_f16 v[80:83], a[0:3], v[96:99], v[16:19]\n"
      "ds_read_b128 v[164:167], v203 offset:6976\n"
      "v_fma_f32 v197, |v91|, v75, v197\n"
      "v_mfma_f32_16x16x32_f16 v[80:83], a[4:7], v[100:103], v[80:83]\n"
      "s_waitcnt lgkmcnt(3)\n"
      "v_pk_mul_f16 v112, v128, v160\n"
      "v_pk_mul_f16 v113, v129, v161\n"
      "v_mfma_f32_16x16x32_f16 v[80:83], a[8:11], v[104:107], v[80:83]\n"
      "ds_write_b128 v218, a[136:139] offset:2560\n"
      "v_mfma_f32_16x16x32_f16 v[80:83], a[12:15], v[108:111], v[80:83]\n"
      "v_fma_f32 v197, |v92|, v76, v197\n"
      "v_fma_f32 v197, |v93|, v77, v197\n"
      "v_mfma_f32_16x16x32_f16 v[84:87], a[28:31], v[108:111], v[20:23]\n"
      "v_fma_f32 v197, |v94|, v78, v197\n"
      "v_fma_f32 v197, |v95|, v79, v197\n"
      "v_mfma_f32_16x16x32_f16 v[84:87], a[24:27], v[104:107], v[84:87]\n"
      "v_pk_mul_f16 v114, v130, v162\n"
      "v_pk_mul_f16 v115, v131, v163\n"
      "v_mfma_f32_16x16x32_f16 v[84:87], a[20:23], v[100:103], v[84:87]\n"
      "v_permlane32_swap_b32 v191, v197\n"
      "v_mfma_f32_16x16x32_f16 v[84:87], a[16:19], v[96:99], v[84:87]\n"
      "ds_read_b128 v[168:171], v203 offset:7040\n"
      "v_add_f32 v190, v190, v196\n"
      "v_mfma_f32_16x16x32_f16 v[88:91], a[32:35], v[96:99], v[24:27]\n"
      "v_fma_f32 v198, |v80|, v48, 4.0\n"
      "v_fma_f32 v198, |v81|, v49, v198\n"
      "v_mfma_f32_16x16x32_f16 v[88:91], a[36:39], v[100:103], v[88:91]\n"
      "v_permlane16_swap_b32 v194, v190\n"
      "v_mfma_f32_16x16x32_f16 v[88:91], a[40:43], v[104:107], v[88:91]\n"
      "v_fma_f32 v198, |v82|, v50, v198\n"
      "v_fma_f32 v198, |v83|, v51, v198\n"
      "v_mfma_f32_16x16x32_f16 v[88:91], a[44:47], v[108:111], v[88:91]\n"
      "s_waitcnt lgkmcnt(2)\n"
      "v_pk_mul_f16 v116, v132, v164\n"
      "v_pk_mul_f16 v117, v133, v165\n"
      "v_mfma_f32_16x16x32_f16 v[92:95], a[60:63], v[108:111], v[28:31]\n"
      "v_add_f32 v194, v194, v190\n"
      "v_add_f32 v191, v191, v197\n"
      "v_mfma_f32_16x16x32_f16 v[92:95], a[56:59], v[104:107], v[92:95]\n"
      "v_pk_mul_f16 v118, v134, v166\n"
      "v_pk_mul_f16 v119, v135, v167\n"
      "v_mfma_f32_16x16x32_f16 v[92:95], a[52:55], v[100:103], v[92:95]\n"
      "v_fma_f32 v198, |v84|, v52, v198\n"
      "v_fma_f32 v198, |v85|, v53, v198\n"
      "v_mfma_f32_16x16x32_f16 v[92:95], a[48:51], v[96:99], v[92:95]\n"
      "v_fma_f32 v198, |v86|, v54, v198\n"
      "v_fma_f32 v198, |v87|, v55, v198\n"
      "v_mfma_f32_16x16x32_f16 v[80:83], a[64:67], v[96:99], v[32:35]\n"
      "v_permlane16_swap_b32 v195, v191\n"
      "v_mfma_f32_16x16x32_f16 v[80:83], a[68:71], v[100:103], v[80:83]\n"
      "s_waitcnt lgkmcnt(0)\n"
      "v_pk_mul_f16 v120, v136, v168\n"
      "v_pk_mul_f16 v121, v137, v169\n"
      "v_mfma_f32_16x16x32_f16 v[80:83], a[72:75], v[104:107], v[80:83]\n"
      "v_fma_f32 v198, |v88|, v56, v198\n"
      "v_fma_f32 v198, |v89|, v57, v198\n"
      "v_mfma_f32_16x16x32_f16 v[80:83], a[76:79], v[108:111], v[80:83]\n"
      "ds_read_b128 v[172:175], v203 offset:7104\n"
      "v_fma_f32 v198, |v90|, v58, v198\n"
      "v_mfma_f32_16x16x32_f16 v[84:87], a[92:95], v[108:111], v[36:39]\n"
      "v_fma_f32 v198, |v91|, v59, v198\n"
      "v_fma_f32 v198, |v92|, v60, v198\n"
      "v_mfma_f32_16x16x32_f16 v[84:87], a[88:91], v[104:107], v[84:87]\n"
      "v_pk_mul_f16 v122, v138, v170\n"
      "v_pk_mul_f16 v123, v139, v171\n"
      "v_mfma_f32_16x16x32_f16 v[84:87], a[84:87], v[100:103], v[84:87]\n"
      "v_fma_f32 v198, |v93|, v61, v198\n"
      "v_fma_f32 v198, |v94|, v62, v198\n"
      "v_mfma_f32_16x16x32_f16 v[84:87], a[80:83], v[96:99], v[84:87]\n"
      "v_fma_f32 v198, |v95|, v63, v198\n"
      "v_fma_f32 v198, |v80|, v64, v198\n"
      "v_mfma_f32_16x16x32_f16 v[88:91], a[96:99], v[96:99], v[40:43]\n"
      "v_fma_f32 v198, |v81|, v65, v198\n"
      "v_fma_f32 v198, |v82|, v66, v198\n"
      "v_mfma_f32_16x16x32_f16 v[88:91], a[100:103], v[100:103], v[88:91]\n"
      "v_fma_f32 v198, |v83|, v67, v198\n"
      "v_add_f32 v195, v195, v191\n"
      "v_mfma_f32_16x16x32_f16 v[88:91], a[104:107], v[104:107], v[88:91]\n"
      "v_fma_f32 v198, |v84|, v68, v198\n"
      "v_fma_f32 v198, |v85|, v69, v198\n"
      "v_mfma_f32_16x16x32_f16 v[88:91], a[108:111], v[108:111], v[88:91]\n"
      "s_waitcnt lgkmcnt(0)\n"
      "v_pk_mul_f16 v124, v140, v172\n"
      "v_pk_mul_f16 v125, v141, v173\n"
      "v_mfma_f32_16x16x32_f16 v[92:95], a[124:127], v[108:111], v[44:47]\n"
      "v_pk_mul_f16 v126, v142, v174\n"
      "v_pk_mul_f16 v127, v143, v175\n"
      "v_mfma_f32_16x16x32_f16 v[92:95], a[120:123], v[104:107], v[92:95]\n"
      "v_fma_f32 v198, |v86|, v70, v198\n"
      "v_fma_f32 v198, |v87|, v71, v198\n"
      "v_mfma_f32_16x16x32_f16 v[92:95], a[116:119], v[100:103], v[92:95]\n"
      "v_fma_f32 v198, |v88|, v72, v198\n"
      "v_fma_f32 v198, |v89|, v73, v198\n"
      "v_mfma_f32_16x16x32_f16 v[92:95], a[112:115], v[96:99], v[92:95]\n"
      "v_fma_f32 v198, |v90|, v74, v198\n"
      "v_fma_f32 v198, |v91|, v75, v198\n"
      "v_mfma_f32_16x16x32_f16 v[80:83], a[0:3], v[112:115], v[16:19]\n"
      "v_pk_mul_f16 v96, v144, v160\n"
      "v_pk_mul_f16 v97, v145, v161\n"
      "v_mfma_f32_16x16x32_f16 v[80:83], a[4:7], v[116:119], v[80:83]\n"
      "v_pk_mul_f16 v98, v146, v162\n"
      "v_pk_mul_f16 v99, v147, v163\n"
      "v_mfma_f32_16x16x32_f16 v[80:83], a[8:11], v[120:123], v[80:83]\n"
      "v_fma_f32 v198, |v92|, v76, v198\n"
      "v_fma_f32 v198, |v93|, v77, v198\n"
      "v_mfma_f32_16x16x32_f16 v[80:83], a[12:15], v[124:127], v[80:83]\n"
      "v_fma_f32 v198, |v94|, v78, v198\n"
      "v_fma_f32 v198, |v95|, v79, v198\n"
      "v_mfma_f32_16x16x32_f16 v[84:87], a[28:31], v[124:127], v[20:23]\n"
      "v_pk_mul_f16 v100, v148, v164\n"
      "v_pk_mul_f16 v101, v149, v165\n"
      "v_mfma_f32_16x16x32_f16 v[84:87], a[24:27], v[120:123], v[84:87]\n"
      "v_pk_mul_f16 v102, v150, v166\n"
      "v_pk_mul_f16 v103, v151, v167\n"
      "v_mfma_f32_16x16x32_f16 v[84:87], a[20:23], v[116:119], v[84:87]\n"
      "v_fma_f32 v192, |v80|, v48, 4.0\n"
      "v_fma_f32 v192, |v81|, v49, v192\n"
      "v_mfma_f32_16x16x32_f16 v[84:87], a[16:19], v[112:115], v[84:87]\n"
      "v_fma_f32 v192, |v82|, v50, v192\n"
      "v_fma_f32 v192, |v83|, v51, v192\n"
      "v_mfma_f32_16x16x32_f16 v[88:91], a[32:35], v[112:115], v[24:27]\n"
      "v_pk_mul_f16 v104, v152, v168\n"
      "v_pk_mul_f16 v105, v153, v169\n"
      "v_mfma_f32_16x16x32_f16 v[88:91], a[36:39], v[116:119], v[88:91]\n"
      "v_pk_mul_f16 v106, v154, v170\n"
      "v_pk_mul_f16 v107, v155, v171\n"
      "v_mfma_f32_16x16x32_f16 v[88:91], a[40:43], v[120:123], v[88:91]\n"
      "v_fma_f32 v192, |v84|, v52, v192\n"
      "v_fma_f32 v192, |v85|, v53, v192\n"
      "v_mfma_f32_16x16x32_f16 v[88:91], a[44:47], v[124:127], v[88:91]\n"
      "v_fma_f32 v192, |v86|, v54, v192\n"
      "v_fma_f32 v192, |v87|, v55, v192\n"
      "v_mfma_f32_16x16x32_f16 v[92:95], a[60:63], v[124:127], v[28:31]\n"
      "ds_read2_b32 v[228:229], v204 offset0:0 offset1:8\n"
      "ds_read2_b32 v[230:231], v205 offset0:0 offset1:8\n"
      "v_mfma_f32_16x16x32_f16 v[92:95], a[56:59], v[120:123], v[92:95]\n"
      "ds_read_b32 v224, v227 offset:37640\n"
      "ds_read2_b32 v[232:233], v206 offset0:0 offset1:8\n"
      "v_mfma_f32_16x16x32_f16 v[92:95], a[52:55], v[116:119], v[92:95]\n"
      "v_fma_f32 v192, |v88|, v56, v192\n"
      "v_fma_f32 v192, |v89|, v57, v192\n"
      "v_mfma_f32_16x16x32_f16 v[92:95], a[48:51], v[112:115], v[92:95]\n"
      "v_fma_f32 v192, |v90|, v58, v192\n"
      "v_fma_f32 v192, |v91|, v59, v192\n"
      "v_mfma_f32_16x16x32_f16 v[80:83], a[64:67], v[112:115], v[32:35]\n"
      "v_pk_mul_f16 v108, v156, v172\n"
      "v_pk_mul_f16 v109, v157, v173\n"
      "v_mfma_f32_16x16x32_f16 v[80:83], a[68:71], v[116:119], v[80:83]\n"
      "ds_read2_b32 v[234:235], v207 offset0:0 offset1:8\n"
      "ds_read2_b32 v[236:237], v208 offset0:0 offset1:8\n"
      "v_mfma_f32_16x16x32_f16 v[80:83], a[72:75], v[120:123], v[80:83]\n"
      "v_fma_f32 v192, |v92|, v60, v192\n"
      "v_fma_f32 v192, |v93|, v61, v192\n"
      "v_mfma_f32_16x16x32_f16 v[80:83], a[76:79], v[124:127], v[80:83]\n"
      "v_fma_f32 v192, |v94|, v62, v192\n"
      "v_fma_f32 v192, |v95|, v63, v192\n"
      "v_mfma_f32_16x16x32_f16 v[84:87], a[92:95], v[124:127], v[36:39]\n"
      "v_pk_mul_f16 v110, v158, v174\n"
      "v_pk_mul_f16 v111, v159, v175\n"
      "v_mfma_f32_16x16x32_f16 v[84:87], a[88:91], v[120:123], v[84:87]\n"
      "s_waitcnt lgkmcnt(3)\n"
      "ds_read2_b32 v[238:239], v209 offset0:0 offset1:8\n"
      "v_cmp_eq_u32 vcc, 1, v224\n"
      "v_mfma_f32_16x16x32_f16 v[84:87], a[84:87], v[116:119], v[84:87]\n"
      "v_fma_f32 v192, |v80|, v64, v192\n"
      "v_fma_f32 v192, |v81|, v65, v192\n"
      "v_mfma_f32_16x16x32_f16 v[84:87], a[80:83], v[112:115], v[84:87]\n"
      "s_and_b32 s44, vcc_lo, s54\n"
      "v_fma_f32 v192, |v82|, v66, v192\n"
      "v_fma_f32 v192, |v83|, v67, v192\n"
      "v_mfma_f32_16x16x32_f16 v[88:91], a[96:99], v[112:115], v[40:43]\n"
      "s_cmp_lg_u32 s44, s54\n"
      "ds_read_b128 v[128:131], v210 offset:0\n"
      "ds_read_b128 v[144:147], v211 offset:0\n"
      "v_mfma_f32_16x16x32_f16 v[88:91], a[100:103], v[116:119], v[88:91]\n"
      "s_cbranch_scc1 80f\n"
      "81:\n"
      "v_mfma_f32_16x16x32_f16 v[88:91], a[104:107], v[120:123], v[88:91]\n"
      "v_fma_f32 v192, |v84|, v68, v192\n"
      "v_fma_f32 v192, |v85|, v69, v192\n"
      "v_mfma_f32_16x16x32_f16 v[88:91], a[108:111], v[124:127], v[88:91]\n"
      "v_fma_f32 v192, |v86|, v70, v192\n"
      "v_fma_f32 v192, |v87|, v71, v192\n"
      "v_mfma_f32_16x16x32_f16 v[92:95], a[124:127], v[124:127], v[44:47]\n"
      "v_pk_mul_f16 v124, v252, v172\n"
      "v_pk_mul_f16 v125, v253, v173\n"
      "v_mfma_f32_16x16x32_f16 v[92:95], a[120:123], v[120:123], v[92:95]\n"
      "v_pk_mul_f16 v120, v248, v168\n"
      "v_pk_mul_f16 v121, v249, v169\n"
      "v_mfma_f32_16x16x32_f16 v[92:95], a[116:119], v[116:119], v[92:95]\n"
      "v_fma_f32 v192, |v88|, v72, v192\n"
      "v_fma_f32 v192, |v89|, v73, v192\n"
      "v_mfma_f32_16x16x32_f16 v[92:95], a[112:115], v[112:115], v[92:95]\n"
      "v_fma_f32 v192, |v90|, v74, v192\n"
      "v_fma_f32 v192, |v91|, v75, v192\n"
      "v_mfma_f32_16x16x32_f16 v[80:83], a[0:3], v[96:99], v[16:19]\n"
      "v_pk_mul_f16 v112, v240, v160\n"
      "v_pk_mul_f16 v113, v241, v161\n"
      "v_mfma_f32_16x16x32_f16 v[80:83], a[4:7], v[100:103], v[80:83]\n"
      "v_pk_mul_f16 v114, v242, v162\n"
      "v_pk_mul_f16 v115, v243, v163\n"
      "v_mfma_f32_16x16x32_f16 v[80:83], a[8:11], v[104:107], v[80:83]\n"
      "v_fma_f32 v192, |v92|, v76, v192\n"
      "v_fma_f32 v192, |v93|, v77, v192\n"
      "v_mfma_f32_16x16x32_f16 v[80:83], a[12:15], v[108:111], v[80:83]\n"
      "v_fma_f32 v192, |v94|, v78, v192\n"
      "v_fma_f32 v192, |v95|, v79, v192\n"
      "v_mfma_f32_16x16x32_f16 v[84:87], a[28:31], v[108:111], v[20:23]\n"
      "v_pk_mul_f16 v116, v244, v164\n"
      "v_pk_mul_f16 v117, v245, v165\n"
      "v_mfma_f32_16x16x32_f16 v[84:87], a[24:27], v[104:107], v[84:87]\n"
      "v_pk_mul_f16 v118, v246, v166\n"
      "v_pk_mul_f16 v119, v247, v167\n"
      "v_mfma_f32_16x16x32_f16 v[84:87], a[20:23], v[100:103], v[84:87]\n"
      "v_fma_f32 v193, |v80|, v48, 4.0\n"
      "v_fma_f32 v193, |v81|, v49, v193\n"
      "v_mfma_f32_16x16x32_f16 v[84:87], a[16:19], v[96:99], v[84:87]\n"
      "v_fma_f32 v193, |v82|, v50, v193\n"
      "v_fma_f32 v193, |v83|, v51, v193\n"
      "v_mfma_f32_16x16x32_f16 v[88:91], a[32:35], v[96:99], v[24:27]\n"
      "v_pk_mul_f16 v122, v250, v170\n"
      "v_pk_mul_f16 v123, v251, v171\n"
      "v_mfma_f32_16x16x32_f16 v[88:91], a[36:39], v[100:103], v[88:91]\n"
      "v_pk_mul_f16 v126, v254, v174\n"
      "v_pk_mul_f16 v127, v255, v175\n"
      "v_mfma_f32_16x16x32_f16 v[88:91], a[40:43], v[104:107], v[88:91]\n"
      "v_fma_f32 v193, |v84|, v52, v193\n"
      "v_fma_f32 v193, |v85|, v53, v193\n"
      "v_mfma_f32_16x16x32_f16 v[88:91], a[44:47], v[108:111], v[88:91]\n"
      "v_fma_f32 v193, |v86|, v54, v193\n"
      "v_fma_f32 v193, |v87|, v55, v193\n"
      "v_mfma_f32_16x16x32_f16 v[92:95], a[60:63], v[108:111], v[28:31]\n"
      "ds_read_b128 v[160:163], v10 offset:0\n"
      "ds_read_b128 v[240:243], v11 offset:0\n"
      "v_mfma_f32_16x16x32_f16 v[92:95], a[56:59], v[104:107], v[92:95]\n"
      "ds_read_b128 v[164:167], v10 offset:64\n"
      "ds_read_b128 v[244:247], v11 offset:64\n"
      "v_mfma_f32_16x16x32_f16 v[92:95], a[52:55], v[100:103], v[92:95]\n"
      "v_fma_f32 v193, |v88|, v56, v193\n"
      "v_fma_f32 v193, |v89|, v57, v193\n"
      "v_mfma_f32_16x16x32_f16 v[92:95], a[48:51], v[96:99], v[92:95]\n"
      "v_fma_f32 v193, |v90|, v58, v193\n"
      "v_fma_f32 v193, |v91|, v59, v193\n"
      "v_mfma_f32_16x16x32_f16 v[80:83], a[64:67], v[96:99], v[32:35]\n"
      "ds_read_b128 v[168:171], v10 offset:128\n"
      "ds_read_b128 v[248:251], v11 offset:128\n"
      "v_mfma_f32_16x16x32_f16 v[80:83], a[68:71], v[100:103], v[80:83]\n"
      "ds_read_b128 v[172:175], v10 offset:192\n"
      "ds_read_b128 v[252:255], v11 offset:192\n"
      "v_mfma_f32_16x16x32_f16 v[80:83], a[72:75], v[104:107], v[80:83]\n"
      "v_fma_f32 v193, |v92|, v60, v193\n"
      "v_fma_f32 v193, |v93|, v61, v193\n"
      "v_mfma_f32_16x16x32_f16 v[80:83], a[76:79], v[108:111], v[80:83]\n"
      "v_fma_f32 v193, |v94|, v62, v193\n"
      "v_fma_f32 v193, |v95|, v63, v193\n"
      "v_mfma_f32_16x16x32_f16 v[84:87], a[92:95], v[108:111], v[36:39]\n"
      "s_waitcnt lgkmcnt(6)\n"
      "v_pk_mul_f16 v160, v160, v240\n"
      "v_pk_mul_f16 v161, v161, v241\n"
      "v_mfma_f32_16x16x32_f16 v[84:87], a[88:91], v[104:107], v[84:87]\n"
      "v_pk_mul_f16 v162, v162, v242\n"
      "v_pk_mul_f16 v163, v163, v243\n"
      "v_mfma_f32_16x16x32_f16 v[84:87], a[84:87], v[100:103], v[84:87]\n"
      "v_fma_f32 v193, |v80|, v64, v193\n"
      "v_fma_f32 v193, |v81|, v65, v193\n"
      "v_mfma_f32_16x16x32_f16 v[84:87], a[80:83], v[96:99], v[84:87]\n"
      "v_fma_f32 v193, |v82|, v66, v193\n"
      "v_fma_f32 v193, |v83|, v67, v193\n"
      "v_mfma_f32_16x16x32_f16 v[88:91], a[96:99], v[96:99], v[40:43]\n"
      "s_waitcnt lgkmcnt(4)\n"
      "v_pk_mul_f16 v164, v164, v244\n"
      "v_pk_mul_f16 v165, v165, v245\n"
      "v_mfma_f32_16x16x32_f16 v[88:91], a[100:103], v[100:103], v[88:91]\n"
      "43:\n"
      "s_cbranch_execz 44b\n"
      "v_pk_mul_f16 v166, v166, v246\n"
      "v_mfma_f32_16x16x32_f16 v[88:91], a[104:107], v[104:107], v[88:91]\n"
      "v_fma_f32 v193, |v84|, v68, v193\n"
      "v_fma_f32 v193, |v85|, v69, v193\n"
      "v_mfma_f32_16x16x32_f16 v[88:91], a[108:111], v[108:111], v[88:91]\n"
      "v_fma_f32 v193, |v86|, v70, v193\n"
      "v_fma_f32 v193, |v87|, v71, v193\n"
      "v_mfma_f32_16x16x32_f16 v[92:95], a[124:127], v[108:111], v[44:47]\n"
      "s_waitcnt lgkmcnt(2)\n"
      "v_pk_mul_f16 v167, v167, v247\n"
      "v_pk_mul_f16 v168, v168, v248\n"
      "v_mfma_f32_16x16x32_f16 v[92:95], a[120:123], v[104:107], v[92:95]\n"
      "v_pk_mul_f16 v169, v169, v249\n"
      "v_pk_mul_f16 v170, v170, v250\n"
      "v_mfma_f32_16x16x32_f16 v[92:95], a[116:119], v[100:103], v[92:95]\n"
      "v_fma_f32 v193, |v88|, v72, v193\n"
      "v_fma_f32 v193, |v89|, v73, v193\n"
      "v_mfma_f32_16x16x32_f16 v[92:95], a[112:115], v[96:99], v[92:95]\n"
      "v_fma_f32 v193, |v90|, v74, v193\n"
      "v_fma_f32 v193, |v91|, v75, v193\n"
      "v_mfma_f32_16x16x32_f16 v[80:83], a[0:3], v[112:115], v[16:19]\n"
      "ds_read_b128 v[132:135], v210 offset:64\n"
      "ds_read_b128 v[148:151], v211 offset:64\n"
      "v_mfma_f32_16x16x32_f16 v[80:83], a[4:7], v[116:119], v[80:83]\n"
      "v_pk_mul_f16 v128, v128, v144\n"
      "v_pk_mul_f16 v171, v171, v251\n"
      "v_mfma_f32_16x16x32_f16 v[80:83], a[8:11], v[120:123], v[80:83]\n"
      "v_fma_f32 v193, |v92|, v76, v193\n"
      "v_fma_f32 v193, |v93|, v77, v193\n"
      "v_mfma_f32_16x16x32_f16 v[80:83], a[12:15], v[124:127], v[80:83]\n"
      "v_fma_f32 v193, |v94|, v78, v193\n"
      "v_fma_f32 v193, |v95|, v79, v193\n"
      "v_mfma_f32_16x16x32_f16 v[84:87], a[28:31], v[124:127], v[20:23]\n"
      "v_pk_mul_f16 v129, v129, v145\n"
      "v_pk_mul_f16 v130, v130, v146\n"
      "v_mfma_f32_16x16x32_f16 v[84:87], a[24:27], v[120:123], v[84:87]\n"
      "s_waitcnt lgkmcnt(2)\n"
      "v_pk_mul_f16 v131, v131, v147\n"
      "v_pk_mul_f16 v172, v172, v252\n"
      "v_mfma_f32_16x16x32_f16 v[84:87], a[20:23], v[116:119], v[84:87]\n"
      "v_fma_f32 v190, |v80|, v48, 4.0\n"
      "v_fma_f32 v190, |v81|, v49, v190\n"
      "v_mfma_f32_16x16x32_f16 v[84:87], a[16:19], v[112:115], v[84:87]\n"
      "v_fma_f32 v190, |v82|, v50, v190\n"
      "v_fma_f32 v190, |v83|, v51, v190\n"
      "v_mfma_f32_16x16x32_f16 v[88:91], a[32:35], v[112:115], v[24:27]\n"
      "v_pk_mul_f16 v173, v173, v253\n"
      "v_pk_mul_f16 v174, v174, v254\n"
      "v_mfma_f32_16x16x32_f16 v[88:91], a[36:39], v[116:119], v[88:91]\n"
      "ds_read_b128 v[136:139], v210 offset:128\n"
      "ds_read_b128 v[152:155], v211 offset:128\n"
      "v_mfma_f32_16x16x32_f16 v[88:91], a[40:43], v[120:123], v[88:91]\n"
      "v_fma_f32 v190, |v84|, v52, v190\n"
      "v_fma_f32 v190, |v85|, v53, v190\n"
      "v_mfma_f32_16x16x32_f16 v[88:91], a[44:47], v[124:127], v[88:91]\n"
      "v_fma_f32 v190, |v86|, v54, v190\n"
      "v_fma_f32 v190, |v87|, v55, v190\n"
      "v_mfma_f32_16x16x32_f16 v[92:95], a[60:63], v[124:127], v[28:31]\n"
      "s_waitcnt lgkmcnt(2)\n"
      "v_pk_mul_f16 v132, v132, v148\n"
      "v_pk_mul_f16 v175, v175, v255\n"
      "v_mfma_f32_16x16x32_f16 v[92:95], a[56:59], v[120:123], v[92:95]\n"
      "v_pk_mul_f16 v133, v133, v149\n"
      "v_pk_mul_f16 v134, v134, v150\n"
      "v_mfma_f32_16x16x32_f16 v[92:95], a[52:55], v[116:119], v[92:95]\n"
      "v_fma_f32 v190, |v88|, v56, v190\n"
      "v_fma_f32 v190, |v89|, v57, v190\n"
      "v_mfma_f32_16x16x32_f16 v[92:95], a[48:51], v[112:115], v[92:95]\n"
      "v_fma_f32 v190, |v90|, v58, v190\n"
      "v_fma_f32 v190, |v91|, v59, v190\n"
      "v_mfma_f32_16x16x32_f16 v[80:83], a[64:67], v[112:115], v[32:35]\n"
      "v_fmac_f32 v189, v200, v228\n"
      "v_fmac_f32 v194, v200, v230\n"
      "v_mfma_f32_16x16x32_f16 v[80:83], a[68:71], v[116:119], v[80:83]\n"
      "v_permlane32_swap_b32 v198, v192\n"
      "v_mfma_f32_16x16x32_f16 v[80:83], a[72:75], v[120:123], v[80:83]\n"
      "v_fma_f32 v190, |v92|, v60, v190\n"
      "v_fma_f32 v190, |v93|, v61, v190\n"
      "v_mfma_f32_16x16x32_f16 v[80:83], a[76:79], v[124:127], v[80:83]\n"
      "v_fma_f32 v190, |v94|, v62, v190\n"
      "v_fma_f32 v190, |v95|, v63, v190\n"
      "v_mfma_f32_16x16x32_f16 v[84:87], a[92:95], v[124:127], v[36:39]\n"
      "s_waitcnt lgkmcnt(0)\n"
      "v_pk_mul_f16 v135, v135, v151\n"
      "v_pk_mul_f16 v136, v136, v152\n"
      "v_mfma_f32_16x16x32_f16 v[84:87], a[88:91], v[120:123], v[84:87]\n"
      "v_fmac_f32 v195, v200, v232\n"
      "v_max3_f32 v219, v189, v194, v195\n"
      "v_mfma_f32_16x16x32_f16 v[84:87], a[84:87], v[116:119], v[84:87]\n"
      "v_fma_f32 v190, |v80|, v64, v190\n"
      "v_fma_f32 v190, |v81|, v65, v190\n"
      "v_mfma_f32_16x16x32_f16 v[84:87], a[80:83], v[112:115], v[84:87]\n"
      "v_fma_f32 v190, |v82|, v66, v190\n"
      "v_fma_f32 v190, |v83|, v67, v190\n"
      "v_mfma_f32_16x16x32_f16 v[88:91], a[96:99], v[112:115], v[40:43]\n"
      "ds_read_b128 a[200:203], v215 offset:11904\n"
      "v_min3_f32 v226, v189, v194, v195\n"
      "v_mfma_f32_16x16x32_f16 v[88:91], a[100:103], v[116:119], v[88:91]\n"
      "ds_read_b128 v[140:143], v210 offset:192\n"
      "ds_read_b128 v[156:159], v211 offset:192\n"
      "v_mfma_f32_16x16x32_f16 v[88:91], a[104:107], v[120:123], v[88:91]\n"
      "v_fma_f32 v190, |v84|, v68, v190\n"
      "v_fma_f32 v190, |v85|, v69, v190\n"
      "v_mfma_f32_16x16x32_f16 v[88:91], a[108:111], v[124:127], v[88:91]\n"
      "v_fma_f32 v190, |v86|, v70, v190\n"
      "v_fma_f32 v190, |v87|, v71, v190\n"
      "v_mfma_f32_16x16x32_f16 v[92:95], a[124:127], v[124:127], v[44:47]\n"
      "ds_read_b128 a[204:207], v215 offset:11968\n"
      "v_sub_f32 v189, v189, v219\n"
      "v_mfma_f32_16x16x32_f16 v[92:95], a[120:123], v[120:123], v[92:95]\n"
      "ds_read_b128 a[208:211], v215 offset:12032\n"
      "v_sub_f32 v194, v194, v219\n"
      "v_mfma_f32_16x16x32_f16 v[92:95], a[116:119], v[116:119], v[92:95]\n"
      "v_fma_f32 v190, |v88|, v72, v190\n"
      "v_fma_f32 v190, |v89|, v73, v190\n"
      "v_mfma_f32_16x16x32_f16 v[92:95], a[112:115], v[112:115], v[92:95]\n"
      "v_fma_f32 v190, |v90|, v74, v190\n"
      "v_fma_f32 v190, |v91|, v75, v190\n"
      "v_mfma_f32_16x16x32_f16 v[80:83], a[0:3], v[160:163], v[16:19]\n"
      "v_pk_mul_f16 v137, v137, v153\n"
      "v_pk_mul_f16 v138, v138, v154\n"
      "v_mfma_f32_16x16x32_f16 v[80:83], a[4:7], v[164:167], v[80:83]\n"
      "ds_read_b128 a[212:215], v215 offset:12096\n"
      "v_sub_f32 v195, v195, v219\n"
      "v_mfma_f32_16x16x32_f16 v[80:83], a[8:11], v[168:171], v[80:83]\n"
      "v_exp_f32 v189, v189\n"
      "v_mfma_f32_16x16x32_f16 v[80:83], a[12:15], v[172:175], v[80:83]\n"
      "v_exp_f32 v194, v194\n"
      "v_mfma_f32_16x16x32_f16 v[84:87], a[28:31], v[172:175], v[20:23]\n"
      "v_exp_f32 v195, v195\n"
      "v_mfma_f32_16x16x32_f16 v[84:87], a[24:27], v[168:171], v[84:87]\n"
      "v_fma_f32 v190, |v92|, v76, v190\n"
      "v_fma_f32 v190, |v93|, v77, v190\n"
      "v_mfma_f32_16x16x32_f16 v[84:87], a[20:23], v[164:167], v[84:87]\n"
      "s_waitcnt lgkmcnt(3)\n"
      "v_pk_mul_f16 v139, v139, v155\n"
      "v_pk_mul_f16 v140, v140, v156\n"
      "v_mfma_f32_16x16x32_f16 v[84:87], a[16:19], v[160:163], v[84:87]\n"
      "v_fma_f32 v190, |v94|, v78, v190\n"
      "v_fma_f32 v190, |v95|, v79, v190\n"
      "v_mfma_f32_16x16x32_f16 v[88:91], a[32:35], v[160:163], v[24:27]\n"
      "v_mul_f32 v221, v189, v229\n"
      "v_add_f32 v198, v198, v192\n"
      "v_mfma_f32_16x16x32_f16 v[88:91], a[36:39], v[164:167], v[88:91]\n"
      "v_add_f32 v220, v189, v194\n"
      "v_fma_f32 v199, |v80|, v48, 2.0\n"
      "v_mfma_f32_16x16x32_f16 v[88:91], a[40:43], v[168:171], v[88:91]\n"
      "v_permlane16_swap_b32 v188, v198\n"
      "v_mfma_f32_16x16x32_f16 v[88:91], a[44:47], v[172:175], v[88:91]\n"
      "v_fma_f32 v199, |v81|, v49, v199\n"
      "v_fma_f32 v199, |v82|, v50, v199\n"
      "v_mfma_f32_16x16x32_f16 v[92:95], a[60:63], v[172:175], v[28:31]\n"
      "v_fmac_f32 v221, v194, v231\n"
      "v_fma_f32 v199, |v83|, v51, v199\n"
      "v_mfma_f32_16x16x32_f16 v[92:95], a[56:59], v[168:171], v[92:95]\n"
      "v_pk_mul_f16 v141, v141, v157\n"
      "v_add_f32 v220, v220, v195\n"
      "v_mfma_f32_16x16x32_f16 v[92:95], a[52:55], v[164:167], v[92:95]\n"
      "v_add_f32 v188, v188, v198\n"
      "v_pk_mul_f16 v142, v142, v158\n"
      "v_mfma_f32_16x16x32_f16 v[92:95], a[48:51], v[160:163], v[92:95]\n"
      "v_fmac_f32 v221, v195, v233\n"
      "v_fmac_f32 v188, v200, v234\n"
      "v_mfma_f32_16x16x32_f16 v[80:83], a[0:3], v[128:131], v[16:19]\n"
      "v_pk_mul_f16 v143, v143, v159\n"
      "v_fma_f32 v199, |v84|, v52, v199\n"
      "v_mfma_f32_16x16x32_f16 v[80:83], a[4:7], v[132:135], v[80:83]\n"
      "v_fma_f32 v199, |v85|, v53, v199\n"
      "v_fma_f32 v199, |v86|, v54, v199\n"
      "v_mfma_f32_16x16x32_f16 v[80:83], a[8:11], v[136:139], v[80:83]\n"
      "v_fma_f32 v199, |v87|, v55, v199\n"
      "v_fma_f32 v199, |v88|, v56, v199\n"
      "v_mfma_f32_16x16x32_f16 v[80:83], a[12:15], v[140:143], v[80:83]\n"
      "v_fma_f32 v199, |v89|, v57, v199\n"
      "v_fma_f32 v199, |v90|, v58, v199\n"
      "v_mfma_f32_16x16x32_f16 v[84:87], a[28:31], v[140:143], v[20:23]\n"
      "v_fma_f32 v199, |v91|, v59, v199\n"
      "v_fma_f32 v199, |v92|, v60, v199\n"
      "v_mfma_f32_16x16x32_f16 v[84:87], a[24:27], v[136:139], v[84:87]\n"
      "ds_read_b128 v[112:115], v216 offset:0\n"
      "ds_read_b128 v[144:147], v217 offset:0\n"
      "v_mfma_f32_16x16x32_f16 v[84:87], a[20:23], v[132:135], v[84:87]\n"
      "v_permlane32_swap_b32 v193, v190\n"
      "v_mfma_f32_16x16x32_f16 v[84:87], a[16:19], v[128:131], v[84:87]\n"
      "v_fma_f32 v199, |v93|, v61, v199\n"
      "v_fma_f32 v199, |v94|, v62, v199\n"
      "v_mfma_f32_16x16x32_f16 v[180:183], a[200:203], v[160:163], 0\n"
      "v_fma_f32 v199, |v95|, v63, v199\n"
      "v_fma_f32 v191, |v80|, v48, 4.0\n"
      "v_mfma_f32_16x16x32_f16 v[88:91], a[32:35], v[128:131], v[24:27]\n"
      "ds_write_b32 v1, v199 offset:12992\n"
      "v_mfma_f32_16x16x32_f16 v[88:91], a[36:39], v[132:135], v[88:91]\n"
      "ds_read_b128 v[116:119], v216 offset:64\n"
      "ds_read_b128 v[148:151], v217 offset:64\n"
      "v_mfma_f32_16x16x32_f16 v[88:91], a[40:43], v[136:139], v[88:91]\n"
      "v_fma_f32 v191, |v81|, v49, v191\n"
      "v_fma_f32 v191, |v82|, v50, v191\n"
      "v_mfma_f32_16x16x32_f16 v[88:91], a[44:47], v[140:143], v[88:91]\n"
      "v_fma_f32 v191, |v83|, v51, v191\n"
      "v_fma_f32 v191, |v84|, v52, v191\n"
      "v_mfma_f32_16x16x32_f16 v[92:95], a[60:63], v[140:143], v[28:31]\n"
      "s_waitcnt lgkmcnt(3)\n"
      "v_pk_mul_f16 v112, v112, v144\n"
      "v_pk_mul_f16 v113, v113, v145\n"
      "v_mfma_f32_16x16x32_f16 v[92:95], a[56:59], v[136:139], v[92:95]\n"
      "v_pk_mul_f16 v114, v114, v146\n"
      "v_pk_mul_f16 v115, v115, v147\n"
      "v_mfma_f32_16x16x32_f16 v[92:95], a[52:55], v[132:135], v[92:95]\n"
      "ds_read_b128 v[120:123], v216 offset:128\n"
      "ds_read_b128 v[152:155], v217 offset:128\n"
      "v_mfma_f32_16x16x32_f16 v[92:95], a[48:51], v[128:131], v[92:95]\n"
      "v_fma_f32 v191, |v85|, v53, v191\n"
      "v_fma_f32 v191, |v86|, v54, v191\n"
      "v_mfma_f32_16x16x32_f16 v[180:183], a[204:207], v[164:167], v[180:183]\n"
      "v_fma_f32 v191, |v87|, v55, v191\n"
      "v_fma_f32 v191, |v88|, v56, v191\n"
      "v_mfma_f32_16x16x32_f16 v[80:83], a[64:67], v[128:131], v[32:35]\n"
      "s_waitcnt lgkmcnt(2)\n"
      "v_pk_mul_f16 v116, v116, v148\n"
      "v_pk_mul_f16 v117, v117, v149\n"
      "v_mfma_f32_16x16x32_f16 v[80:83], a[68:71], v[132:135], v[80:83]\n"
      "v_fma_f32 v191, |v89|, v57, v191\n"
      "v_fma_f32 v191, |v90|, v58, v191\n"
      "v_mfma_f32_16x16x32_f16 v[80:83], a[72:75], v[136:139], v[80:83]\n"
      "v_pk_mul_f16 v118, v118, v150\n"
      "v_pk_mul_f16 v119, v119, v151\n"
      "v_mfma_f32_16x16x32_f16 v[80:83], a[76:79], v[140:143], v[80:83]\n"
      "ds_read_b128 v[124:127], v216 offset:192\n"
      "ds_read_b128 v[156:159], v217 offset:192\n"
      "v_mfma_f32_16x16x32_f16 v[84:87], a[92:95], v[140:143], v[36:39]\n"
      "v_fma_f32 v191, |v91|, v59, v191\n"
      "v_fma_f32 v191, |v92|, v60, v191\n"
      "v_mfma_f32_16x16x32_f16 v[84:87], a[88:91], v[136:139], v[84:87]\n"
      "s_waitcnt lgkmcnt(2)\n"
      "v_pk_mul_f16 v120, v120, v152\n"
      "v_pk_mul_f16 v121, v121, v153\n"
      "v_mfma_f32_16x16x32_f16 v[84:87], a[84:87], v[132:135], v[84:87]\n"
      "v_fma_f32 v191, |v93|, v61, v191\n"
      "v_fma_f32 v191, |v94|, v62, v191\n"
      "v_mfma_f32_16x16x32_f16 v[84:87], a[80:83], v[128:131], v[84:87]\n"
      "v_fma_f32 v191, |v95|, v63, v191\n"
      "v_fma_f32 v191, |v80|, v64, v191\n"
      "v_mfma_f32_16x16x32_f16 v[88:91], a[96:99], v[128:131], v[40:43]\n"
      "v_pk_mul_f16 v122, v122, v154\n"
      "v_pk_mul_f16 v123, v123, v155\n"
      "v_mfma_f32_16x16x32_f16 v[88:91], a[100:103], v[132:135], v[88:91]\n"
      "v_fma_f32 v191, |v81|, v65, v191\n"
      "v_fma_f32 v191, |v82|, v66, v191\n"
      "v_mfma_f32_16x16x32_f16 v[88:91], a[104:107], v[136:139], v[88:91]\n"
      "v_fma_f32 v191, |v83|, v67, v191\n"
      "v_fma_f32 v191, |v84|, v68, v191\n"
      "v_mfma_f32_16x16x32_f16 v[88:91], a[108:111], v[140:143], v[88:91]\n"
      "s_waitcnt lgkmcnt(0)\n"
      "v_pk_mul_f16 v124, v124, v156\n"
      "v_pk_mul_f16 v125, v125, v157\n"
      "v_mfma_f32_16x16x32_f16 v[92:95], a[124:127], v[140:143], v[44:47]\n"
      "v_pk_mul_f16 v126, v126, v158\n"
      "v_pk_mul_f16 v127, v127, v159\n"
      "v_mfma_f32_16x16x32_f16 v[92:95], a[120:123], v[136:139], v[92:95]\n"
      "v_fma_f32 v191, |v85|, v69, v191\n"
      "v_fma_f32 v191, |v86|, v70, v191\n"
      "v_mfma_f32_16x16x32_f16 v[92:95], a[116:119], v[132:135], v[92:95]\n"
      "v_fma_f32 v191, |v87|, v71, v191\n"
      "v_fma_f32 v191, |v88|, v72, v191\n"
      "v_mfma_f32_16x16x32_f16 v[92:95], a[112:115], v[128:131], v[92:95]\n"
      "v_fma_f32 v191, |v89|, v73, v191\n"
      "v_fma_f32 v191, |v90|, v74, v191\n"
      "v_mfma_f32_16x16x32_f16 v[80:83], a[0:3], v[112:115], v[16:19]\n"
      "v_fma_f32 v191, |v91|, v75, v191\n"
      "v_add_f32 v193, v193, v190\n"
      "v_mfma_f32_16x16x32_f16 v[80:83], a[4:7], v[116:119], v[80:83]\n"
      "ds_read_b32 v224, v12 offset:12992\n"
      "v_mfma_f32_16x16x32_f16 v[80:83], a[8:11], v[120:123], v[80:83]\n"
      "v_permlane16_swap_b32 v193, v7\n"
      "v_mfma_f32_16x16x32_f16 v[80:83], a[12:15], v[124:127], v[80:83]\n"
      "v_fma_f32 v191, |v92|, v76, v191\n"
      "v_fma_f32 v191, |v93|, v77, v191\n"
      "v_mfma_f32_16x16x32_f16 v[84:87], a[28:31], v[124:127], v[20:23]\n"
      "v_fma_f32 v191, |v94|, v78, v191\n"
      "v_fma_f32 v191, |v95|, v79, v191\n"
      "v_mfma_f32_16x16x32_f16 v[84:87], a[24:27], v[120:123], v[84:87]\n"
      "v_add_f32 v193, v193, v7\n"
      "v_fmac_f32 v193, v200, v236\n"
      "v_mfma_f32_16x16x32_f16 v[84:87], a[20:23], v[116:119], v[84:87]\n"
      "v_fma_f32 v196, |v80|, v48, 4.0\n"
      "v_fma_f32 v196, |v81|, v49, v196\n"
      "v_mfma_f32_16x16x32_f16 v[84:87], a[16:19], v[112:115], v[84:87]\n"
      "v_fma_f32 v196, |v82|, v50, v196\n"
      "v_fma_f32 v196, |v83|, v51, v196\n"
      "v_mfma_f32_16x16x32_f16 v[88:91], a[32:35], v[112:115], v[24:27]\n"
      "v_max3_f32 v222, v219, v188, v193\n"
      "v_min3_f32 v226, v226, v188, v193\n"
      "v_mfma_f32_16x16x32_f16 v[88:91], a[36:39], v[116:119], v[88:91]\n"
      "v_sub_f32 v219, v219, v222\n"
      "v_sub_f32 v188, v188, v222\n"
      "v_mfma_f32_16x16x32_f16 v[88:91], a[40:43], v[120:123], v[88:91]\n"
      "v_fma_f32 v196, |v84|, v52, v196\n"
      "v_fma_f32 v196, |v85|, v53, v196\n"
      "v_mfma_f32_16x16x32_f16 v[88:91], a[44:47], v[124:127], v[88:91]\n"
      "v_fma_f32 v196, |v86|, v54, v196\n"
      "v_fma_f32 v196, |v87|, v55, v196\n"
      "v_mfma_f32_16x16x32_f16 v[92:95], a[60:63], v[124:127], v[28:31]\n"
      "s_waitcnt lgkmcnt(0)\n"
      "v_sub_f32 v193, v193, v222\n"
      "v_cmp_eq_u32 vcc, s37, v224\n"
      "v_mfma_f32_16x16x32_f16 v[92:95], a[56:59], v[120:123], v[92:95]\n"
      "v_exp_f32 v219, v219\n"
      "v_mfma_f32_16x16x32_f16 v[92:95], a[52:55], v[116:119], v[92:95]\n"
      "s_or_b32 vcc_lo, vcc_lo, s36\n"
      "v_exp_f32 v188, v188\n"
      "v_mfma_f32_16x16x32_f16 v[92:95], a[48:51], v[112:115], v[92:95]\n"
      "s_cmp_lg_u64 vcc, 0\n"
      "v_exp_f32 v193, v193\n"
      "v_mfma_f32_16x16x32_f16 v[80:83], a[64:67], v[112:115], v[32:35]\n"
      "s_cbranch_scc1 82f\n"
      "83:\n"
      "v_mfma_f32_16x16x32_f16 v[80:83], a[68:71], v[116:119], v[80:83]\n"
      "v_fma_f32 v196, |v88|, v56, v196\n"
      "v_fma_f32 v196, |v89|, v57, v196\n"
      "v_mfma_f32_16x16x32_f16 v[80:83], a[72:75], v[120:123], v[80:83]\n"
      "v_fma_f32 v196, |v90|, v58, v196\n"
      "v_fma_f32 v196, |v91|, v59, v196\n"
      "v_mfma_f32_16x16x32_f16 v[80:83], a[76:79], v[124:127], v[80:83]\n"
      "v_mul_f32 v221, v221, v219\n"
      "v_fma_f32 v196, |v92|, v60, v196\n"
      "v_mfma_f32_16x16x32_f16 v[84:87], a[92:95], v[124:127], v[36:39]\n"
      "v_fma_f32 v196, |v93|, v61, v196\n"
      "v_fma_f32 v220, v220, v219, v188\n"
      "v_mfma_f32_16x16x32_f16 v[84:87], a[88:91], v[120:123], v[84:87]\n"
      "v_fma_f32 v196, |v94|, v62, v196\n"
      "v_fma_f32 v196, |v95|, v63, v196\n"
      "v_mfma_f32_16x16x32_f16 v[84:87], a[84:87], v[116:119], v[84:87]\n"
      "v_fma_f32 v196, |v80|, v64, v196\n"
      "v_fma_f32 v196, |v81|, v65, v196\n"
      "v_mfma_f32_16x16x32_f16 v[84:87], a[80:83], v[112:115], v[84:87]\n"
      "v_fma_f32 v196, |v82|, v66, v196\n"
      "v_fma_f32 v196, |v83|, v67, v196\n"
      "v_mfma_f32_16x16x32_f16 v[88:91], a[96:99], v[112:115], v[40:43]\n"
      "v_fmac_f32 v221, v188, v235\n"
      "v_add_f32 v199, v199, v224\n"
      "v_mfma_f32_16x16x32_f16 v[88:91], a[100:103], v[116:119], v[88:91]\n"
      "v_add_f32 v220, v220, v193\n"
      "v_cndmask_b32 v199, v8, v199, s[100:101]\n"
      "v_mfma_f32_16x16x32_f16 v[88:91], a[104:107], v[120:123], v[88:91]\n"
      "v_fma_f32 v196, |v84|, v68, v196\n"
      "v_fma_f32 v196, |v85|, v69, v196\n"
      "v_mfma_f32_16x16x32_f16 v[88:91], a[108:111], v[124:127], v[88:91]\n"
      "v_fma_f32 v196, |v86|, v70, v196\n"
      "v_fma_f32 v196, |v87|, v71, v196\n"
      "v_mfma_f32_16x16x32_f16 v[92:95], a[124:127], v[124:127], v[44:47]\n"
      "v_fmac_f32 v221, v193, v237\n"
      "v_fma_f32 v220, v226, 0, v220\n"
      "v_mfma_f32_16x16x32_f16 v[92:95], a[120:123], v[120:123], v[92:95]\n"
      "v_permlane32_swap_b32 v199, v191\n"
      "v_mfma_f32_16x16x32_f16 v[92:95], a[116:119], v[116:119], v[92:95]\n"
      "v_fma_f32 v196, |v88|, v72, v196\n"
      "v_fma_f32 v196, |v89|, v73, v196\n"
      "v_mfma_f32_16x16x32_f16 v[92:95], a[112:115], v[112:115], v[92:95]\n"
      "v_fma_f32 v196, |v90|, v74, v196\n"
      "v_fma_f32 v196, |v91|, v75, v196\n"
      "v_add_f32 v199, v199, v191\n"
      "v_mfma_f32_16x16x32_f16 v[180:183], a[208:211], v[168:171], v[180:183]\n"
      "v_mfma_f32_16x16x32_f16 v[180:183], a[212:215], v[172:175], v[180:183]\n"
      "s_nop 2\n"
      "v_fma_f32 v196, |v92|, v76, v196\n"
      "v_fma_f32 v196, |v93|, v77, v196\n"
      "v_fma_f32 v196, |v94|, v78, v196\n"
      "v_fma_f32 v196, |v95|, v79, v196\n"
      "s_nop 0\n"
      "v_mov_b32_dpp v238, v180 quad_perm:[0,1,2,3] row_mask:0x1 bank_mask:0xf\n"
      "v_mov_b32_dpp v239, v181 quad_perm:[0,1,2,3] row_mask:0x1 bank_mask:0xf\n"
      "v_permlane32_swap_b32 v196, v8\n"
      "v_add_f32 v196, v196, v8\n"
      "s_movk_i32 s45, 0x207\n"
      "s_nop 0\n"
      "v_permlane16_swap_b32 v199, v196\n"
      "v_add_f32 v199, v199, v196\n"
      "v_fmac_f32 v199, v200, v238\n"
      "v_max_f32 v225, v222, v199\n"
      "v_fma_f32 v220, v199, 0, v220\n"
      "s_nop 0\n"
      "v_max_f32_dpp v223, v225, v225 row_ror:4 row_mask:0xf bank_mask:0xf\n"
      "v_max_f32_dpp v223, v225, v223 row_ror:8 row_mask:0xf bank_mask:0xf\n"
      "v_max_f32_dpp v223, v225, v223 row_ror:12 row_mask:0xf bank_mask:0xf\n"
      "s_nop 1\n"
      "v_max_f32_dpp v225, v223, v223 row_ror:1 row_mask:0xf bank_mask:0xf\n"
      "v_max_f32_dpp v225, v223, v225 row_ror:2 row_mask:0xf bank_mask:0xf\n"
      "v_max_f32_dpp v225, v223, v225 row_ror:3 row_mask:0xf bank_mask:0xf\n"
      "s_nop 1\n"
      "v_max_f32_dpp v225, v225, v225 row_bcast:15 row_mask:0xa bank_mask:0xf\n"
      "s_nop 1\n"
      "v_max_f32_dpp v225, v225, v225 row_bcast:31 row_mask:0xc bank_mask:0xf\n"
      "s_nop 0\n"
      "v_readlane_b32 s44, v225, 63\n"
      "s_nop 1\n"
      "v_subrev_f32 v223, s44, v222\n"
      "v_subrev_f32 v199, s44, v199\n"
      "v_exp_f32 v223, v223\n"
      "v_exp_f32 v199, v199\n"
      "v_mul_f32 v221, v221, v223\n"
      "v_fma_f32 v220, v220, v223, v199\n"
      "v_fmac_f32 v221, v199, v239\n"
      "s_nop 0\n"
      "v_add_f32_dpp v220, v220, v220 row_ror:8 row_mask:0xf bank_mask:0xf\n"
      "v_add_f32_dpp v221, v221, v221 row_ror:8 row_mask:0xf bank_mask:0xf\n"
      "s_nop 0\n"
      "v_add_f32_dpp v220, v220, v220 row_ror:4 row_mask:0xf bank_mask:0xf\n"
      "v_add_f32_dpp v221, v221, v221 row_ror:4 row_mask:0xf bank_mask:0xf\n"
      "s_nop 0\n"
      "v_add_f32_dpp v220, v220, v220 row_ror:2 row_mask:0xf bank_mask:0xf\n"
      "v_add_f32_dpp v221, v221, v221 row_ror:2 row_mask:0xf bank_mask:0xf\n"
      "s_nop 0\n"
      "v_add_f32_dpp v220, v220, v220 row_ror:1 row_mask:0xf bank_mask:0xf\n"
      "v_add_f32_dpp v221, v221, v221 row_ror:1 row_mask:0xf bank_mask:0xf\n"
      "s_nop 0\n"
      "v_add_f32_dpp v220, v220, v220 row_bcast:15 row_mask:0xa bank_mask:0xf\n"
      "v_add_f32_dpp v221, v221, v221 row_bcast:15 row_mask:0xa bank_mask:0xf\n"
      "s_nop 0\n"
      "v_add_f32_dpp v220, v220, v220 row_bcast:31 row_mask:0xc bank_mask:0xf\n"
      "v_add_f32_dpp v221, v221, v221 row_bcast:31 row_mask:0xc bank_mask:0xf\n"
      "s_mov_b32 exec_lo, 0\n"
      "v_rcp_f32 v220, v220\n"
      "v_ldexp_f32 v221, v221, s39\n"
      "s_mov_b32 exec_hi, 0x80000000\n"
      "v_mul_f32 v221, v221, v220\n"
      "v_cvt_pk_bf16_f32 v220, v221, v221\n"
      "global_store_short v3, v220, s[24:25]\n"
      "v_cmp_class_f32 vcc, v221, s45\n"
      "s_nop 0\n"
      "s_cmp_lt_i32 vcc_hi, 0\n"
      "s_cbranch_scc1 7f\n"
      "s_endpgm\n"
      "s_mov_b64 exec, -1\n"
      "s_branch 9f\n"
      "80:\n"
      "s_waitcnt vmcnt(0) lgkmcnt(0)\n"
      "ds_read_b32 v224, v227 offset:37640\n"
      "s_waitcnt lgkmcnt(0)\n"
      "v_cmp_eq_u32 vcc, 0, v224\n"
      "s_nop 4\n"
      "s_and_b32 s44, vcc_lo, s54\n"
      "s_cbranch_scc1 80b\n"
      "v_cmp_eq_u32 vcc, 1, v224\n"
      "s_nop 4\n"
      "s_and_b32 s44, vcc_lo, s54\n"
      "s_cmp_eq_u32 s44, s54\n"
      "s_cbranch_scc1 81b\n"
      "s_mov_b32 s36, 1\n"
      "s_add_u32 s44, s38, 1152\n"
      "v_cndmask_b32 v10, v201, v10, s[100:101]\n"
      "v_cndmask_b32 v11, v201, v11, s[100:101]\n"
      "v_mov_b32 v1, s44\n"
      "s_nop 1\n"
      "s_branch 81b\n"
      "82:\n"
      "s_cmp_lg_u32 s36, 0\n"
      "s_cbranch_scc1 84f\n"
      "85:\n"
      "ds_read_b32 v224, v12 offset:12992\n"
      "s_waitcnt lgkmcnt(0)\n"
      "v_cmp_eq_u32 vcc, s37, v224\n"
      "s_nop 4\n"
      "s_cmp_lg_u64 vcc, 0\n"
      "s_cbranch_scc1 85b\n"
      "s_branch 83b\n"
      "84:\n"
      "s_waitcnt lgkmcnt(0)\n"
      "v_mfma_f32_16x16x32_f16 v[240:243], a[64:67], v[160:163], v[32:35]\n"
      "v_mfma_f32_16x16x32_f16 v[240:243], a[68:71], v[164:167], v[240:243]\n"
      "v_mfma_f32_16x16x32_f16 v[240:243], a[72:75], v[168:171], v[240:243]\n"
      "v_mfma_f32_16x16x32_f16 v[240:243], a[76:79], v[172:175], v[240:243]\n"
      "v_mfma_f32_16x16x32_f16 v[244:247], a[80:83], v[160:163], v[36:39]\n"
      "v_mfma_f32_16x16x32_f16 v[244:247], a[84:87], v[164:167], v[244:247]\n"
      "v_mfma_f32_16x16x32_f16 v[244:247], a[88:91], v[168:171], v[244:247]\n"
      "v_mfma_f32_16x16x32_f16 v[244:247], a[92:95], v[172:175], v[244:247]\n"
      "v_mfma_f32_16x16x32_f16 v[248:251], a[96:99], v[160:163], v[40:43]\n"
      "v_mfma_f32_16x16x32_f16 v[248:251], a[100:103], v[164:167], v[248:251]\n"
      "v_mfma_f32_16x16x32_f16 v[248:251], a[104:107], v[168:171], v[248:251]\n"
      "v_mfma_f32_16x16x32_f16 v[248:251], a[108:111], v[172:175], v[248:251]\n"
      "v_mfma_f32_16x16x32_f16 v[252:255], a[112:115], v[160:163], v[44:47]\n"
      "v_mfma_f32_16x16x32_f16 v[252:255], a[116:119], v[164:167], v[252:255]\n"
      "v_mfma_f32_16x16x32_f16 v[252:255], a[120:123], v[168:171], v[252:255]\n"
      "v_mfma_f32_16x16x32_f16 v[252:255], a[124:127], v[172:175], v[252:255]\n"
      "s_nop 7\n"
      "v_fma_f32 v224, |v240|, v64, 2.0\n"
      "v_fma_f32 v224, |v241|, v65, v224\n"
      "v_fma_f32 v224, |v242|, v66, v224\n"
      "v_fma_f32 v224, |v243|, v67, v224\n"
      "v_fma_f32 v224, |v244|, v68, v224\n"
      "v_fma_f32 v224, |v245|, v69, v224\n"
      "v_fma_f32 v224, |v246|, v70, v224\n"
      "v_fma_f32 v224, |v247|, v71, v224\n"
      "v_fma_f32 v224, |v248|, v72, v224\n"
      "v_fma_f32 v224, |v249|, v73, v224\n"
      "v_fma_f32 v224, |v250|, v74, v224\n"
      "v_fma_f32 v224, |v251|, v75, v224\n"
      "v_fma_f32 v224, |v252|, v76, v224\n"
      "v_fma_f32 v224, |v253|, v77, v224\n"
      "v_fma_f32 v224, |v254|, v78, v224\n"
      "v_fma_f32 v224, |v255|, v79, v224\n"
      "s_nop 1\n"
      "s_branch 83b\n"
      "5:\n"
      "s_waitcnt lgkmcnt(0)\n"
      "ds_read_b32 v224, v227 offset:37632\n"
      "s_waitcnt lgkmcnt(0)\n"
      "v_cmp_eq_u32 vcc, 0, v224\n"
      "s_nop 4\n"
      "s_cmp_lg_u32 vcc_lo, 0\n"
      "s_cbranch_scc1 5b\n"
      "v_cmp_gt_u32 vcc, 4, v224\n"
      "s_nop 4\n"
      "s_cmp_lg_u32 vcc_lo, 0\n"
      "s_cbranch_scc1 8f\n"
      "s_cmp_lg_u32 s58, 0\n"
      "s_cbranch_scc1 52f\n"
      "ds_read_b32 v226, v213 offset:12480\n"
      "v_cmp_eq_u32 s[44:45], 5, v224\n"
      "s_waitcnt lgkmcnt(0)\n"
      "v_max_f16_sdwa v226, |v226|, |v226| dst_sel:DWORD dst_unused:UNUSED_PAD src0_sel:WORD_0 src1_sel:WORD_1\n"
      "s_nop 1\n"
      "v_cmp_le_f16 vcc, 0x6400, v226\n"
      "s_nop 4\n"
      "s_or_b64 vcc, vcc, s[44:45]\n"
      "s_cbranch_scc0 52f\n"
      "v_and_b32 v224, 63, %3\n"
      "v_lshlrev_b32 v224, 9, v224\n"
      "global_load_dwordx4 v[176:179], v224, s[16:17]\n"
      "global_load_dwordx4 v[180:183], v224, s[16:17] offset:336\n"
      "s_waitcnt vmcnt(0)\n"
      "v_max3_f32 v224, |v176|, |v177|, |v178|\n"
      "v_max3_f32 v224, |v179|, |v180|, v224\n"
      "v_max3_f32 v224, |v181|, |v182|, v224\n"
      "v_max_f32 v224, |v183|, v224\n"
      "s_nop 1\n"
      "v_max_f32_dpp v224, v224, v224 row_ror:8 row_mask:0xf bank_mask:0xf\n"
      "s_nop 1\n"
      "v_max_f32_dpp v224, v224, v224 row_ror:4 row_mask:0xf bank_mask:0xf\n"
      "s_nop 1\n"
      "v_max_f32_dpp v224, v224, v224 row_ror:2 row_mask:0xf bank_mask:0xf\n"
      "s_nop 1\n"
      "v_max_f32_dpp v224, v224, v224 row_ror:1 row_mask:0xf bank_mask:0xf\n"
      "s_nop 0\n"
      "v_readlane_b32 s44, v224, 0\n"
      "v_readlane_b32 s45, v224, 16\n"
      "v_readlane_b32 s46, v224, 32\n"
      "v_readlane_b32 s47, v224, 48\n"
      "s_max_u32 s44, s44, s45\n"
      "s_max_u32 s46, s46, s47\n"
      "s_max_u32 s44, s44, s46\n"
      "s_lshr_b32 s44, s44, 23\n"
      "s_sub_i32 s58, 135, s44\n"
      "s_cmp_eq_u32 s44, 0\n"
      "s_cselect_b32 s58, 0, s58\n"
      "s_max_i32 s58, s58, 11\n"
      "s_cmp_gt_u32 s58, 51\n"
      "s_cbranch_scc1 8f\n"
      "s_mov_b32 s41, 1\n"
      "s_mov_b32 s35, 0\n"
      "s_waitcnt vmcnt(0) lgkmcnt(0)\n"
      "s_barrier\n"
      "s_branch 0b\n"
      "52:\n"
      "ds_read_b64 v[176:177], v5 offset:37120\n"
      "v_cmp_eq_u32 vcc, 9, v224\n"
      "v_cmp_eq_u32 s[50:51], 5, v224\n"
      "s_waitcnt lgkmcnt(0)\n"
      "v_max_f32 v225, |v176|, |v177|\n"
      "s_nop 1\n"
      "v_max_f32_dpp v225, v225, v225 row_ror:8 row_mask:0xf bank_mask:0xf\n"
      "s_nop 1\n"
      "v_max_f32_dpp v225, v225, v225 row_ror:4 row_mask:0xf bank_mask:0xf\n"
      "s_nop 1\n"
      "v_max_f32_dpp v225, v225, v225 row_ror:2 row_mask:0xf bank_mask:0xf\n"
      "s_nop 1\n"
      "v_max_f32_dpp v225, v225, v225 row_ror:1 row_mask:0xf bank_mask:0xf\n"
      "s_nop 0\n"
      "v_readlane_b32 s44, v225, 0\n"
      "v_readlane_b32 s45, v225, 16\n"
      "v_readlane_b32 s46, v225, 32\n"
      "v_readlane_b32 s47, v225, 48\n"
      "s_max_u32 s44, s44, s45\n"
      "s_max_u32 s46, s46, s47\n"
      "s_max_u32 s44, s44, s46\n"
      "v_frexp_exp_i32_f32 v224, s44\n"
      "v_sub_u32 v224, 0, v224\n"
      "s_or_b32 s30, s30, vcc_lo\n"
      "s_cmp_lg_u32 s55, 0\n"
      "s_cselect_b32 s30, 0, s30\n"
      "v_readfirstlane_b32 s51, v224\n"
      "s_cmp_lg_u32 s30, 0\n"
      "s_cbranch_scc1 6f\n"
      "v_ldexp_f32 v176, v176, v224\n"
      "v_ldexp_f32 v177, v177, v224\n"
      "v_readfirstlane_b32 s56, v224\n"
      "s_nop 1\n"
      "s_branch 4b\n"
      "22:\n"
      "s_nop 7\n"
      "v_lshlrev_b32 v176, 16, v192\n"
      "v_and_b32 v177, s43, v192\n"
      "v_lshlrev_b32 v178, 16, v193\n"
      "v_and_b32 v179, s43, v193\n"
      "v_lshlrev_b32 v180, 16, v194\n"
      "v_and_b32 v181, s43, v194\n"
      "v_lshlrev_b32 v182, 16, v195\n"
      "v_and_b32 v183, s43, v195\n"
      "v_max3_f32 v222, |v176|, |v177|, |v178|\n"
      "v_max3_f32 v222, |v179|, |v180|, v222\n"
      "v_max3_f32 v222, |v181|, |v182|, v222\n"
      "v_max_f32 v222, |v183|, v222\n"
      "s_nop 1\n"
      "v_max_f32_dpp v222, v222, v222 row_ror:8 row_mask:0xf bank_mask:0xf\n"
      "s_nop 1\n"
      "v_max_f32_dpp v222, v222, v222 row_ror:4 row_mask:0xf bank_mask:0xf\n"
      "s_nop 1\n"
      "v_max_f32_dpp v222, v222, v222 row_ror:2 row_mask:0xf bank_mask:0xf\n"
      "s_nop 1\n"
      "v_max_f32_dpp v222, v222, v222 row_ror:1 row_mask:0xf bank_mask:0xf\n"
      "v_frexp_exp_i32_f32 v90, v222\n"
      "v_sub_u32 v90, 0, v90\n"
      "v_ldexp_f32 v176, v176, v90\n"
      "v_ldexp_f32 v177, v177, v90\n"
      "v_ldexp_f32 v178, v178, v90\n"
      "v_ldexp_f32 v179, v179, v90\n"
      "v_ldexp_f32 v180, v180, v90\n"
      "v_ldexp_f32 v181, v181, v90\n"
      "v_ldexp_f32 v182, v182, v90\n"
      "v_ldexp_f32 v183, v183, v90\n"
      "v_ldexp_f32 v176, v176, 9\n"
      "v_ldexp_f32 v177, v177, 9\n"
      "v_ldexp_f32 v178, v178, 9\n"
      "v_ldexp_f32 v179, v179, 9\n"
      "v_ldexp_f32 v180, v180, 9\n"
      "v_ldexp_f32 v181, v181, 9\n"
      "v_ldexp_f32 v182, v182, 9\n"
      "v_ldexp_f32 v183, v183, 9\n"
      "v_readlane_b32 s59, v90, 0\n"
      "v_readlane_b32 s57, v90, 32\n"
      "s_lshl_b64 exec, -1, 48\n"
      "v_xor_b32 v176, 0x80000000, v176\n"
      "v_xor_b32 v177, 0x80000000, v177\n"
      "v_xor_b32 v178, 0x80000000, v178\n"
      "v_xor_b32 v179, 0x80000000, v179\n"
      "v_xor_b32 v180, 0x80000000, v180\n"
      "v_xor_b32 v181, 0x80000000, v181\n"
      "v_xor_b32 v182, 0x80000000, v182\n"
      "v_xor_b32 v183, 0x80000000, v183\n"
      "s_mov_b64 exec, -1\n"
      "s_cmp_lt_i32 s57, 0\n"
      "s_cselect_b32 s41, 1, s41\n"
      "s_branch 23b\n"
      "70:\n"
      "s_mov_b32 s49, 0\n"
      "s_add_u32 s44, s58, 127\n"
      "s_lshl_b32 s44, s44, 23\n"
      "s_waitcnt vmcnt(0) lgkmcnt(0)\n"
      "s_nop 7\n"
      "v_lshlrev_b32 v128, 16, v96\n"
      "v_and_b32 v129, s43, v96\n"
      "v_mul_f32 v128, s44, v128\n"
      "v_mul_f32 v129, s44, v129\n"
      "v_cvt_pk_bf16_f32 v96, v128, v129\n"
      "v_lshlrev_b32 v128, 16, v97\n"
      "v_and_b32 v129, s43, v97\n"
      "v_mul_f32 v128, s44, v128\n"
      "v_mul_f32 v129, s44, v129\n"
      "v_cvt_pk_bf16_f32 v97, v128, v129\n"
      "v_lshlrev_b32 v128, 16, v98\n"
      "v_and_b32 v129, s43, v98\n"
      "v_mul_f32 v128, s44, v128\n"
      "v_mul_f32 v129, s44, v129\n"
      "v_cvt_pk_bf16_f32 v98, v128, v129\n"
      "v_lshlrev_b32 v128, 16, v99\n"
      "v_and_b32 v129, s43, v99\n"
      "v_mul_f32 v128, s44, v128\n"
      "v_mul_f32 v129, s44, v129\n"
      "v_cvt_pk_bf16_f32 v99, v128, v129\n"
      "v_lshlrev_b32 v128, 16, v100\n"
      "v_and_b32 v129, s43, v100\n"
      "v_mul_f32 v128, s44, v128\n"
      "v_mul_f32 v129, s44, v129\n"
      "v_cvt_pk_bf16_f32 v100, v128, v129\n"
      "v_lshlrev_b32 v128, 16, v101\n"
      "v_and_b32 v129, s43, v101\n"
      "v_mul_f32 v128, s44, v128\n"
      "v_mul_f32 v129, s44, v129\n"
      "v_cvt_pk_bf16_f32 v101, v128, v129\n"
      "v_lshlrev_b32 v128, 16, v102\n"
      "v_and_b32 v129, s43, v102\n"
      "v_mul_f32 v128, s44, v128\n"
      "v_mul_f32 v129, s44, v129\n"
      "v_cvt_pk_bf16_f32 v102, v128, v129\n"
      "v_lshlrev_b32 v128, 16, v103\n"
      "v_and_b32 v129, s43, v103\n"
      "v_mul_f32 v128, s44, v128\n"
      "v_mul_f32 v129, s44, v129\n"
      "v_cvt_pk_bf16_f32 v103, v128, v129\n"
      "v_lshlrev_b32 v128, 16, v104\n"
      "v_and_b32 v129, s43, v104\n"
      "v_mul_f32 v128, s44, v128\n"
      "v_mul_f32 v129, s44, v129\n"
      "v_cvt_pk_bf16_f32 v104, v128, v129\n"
      "v_lshlrev_b32 v128, 16, v105\n"
      "v_and_b32 v129, s43, v105\n"
      "v_mul_f32 v128, s44, v128\n"
      "v_mul_f32 v129, s44, v129\n"
      "v_cvt_pk_bf16_f32 v105, v128, v129\n"
      "v_lshlrev_b32 v128, 16, v106\n"
      "v_and_b32 v129, s43, v106\n"
      "v_mul_f32 v128, s44, v128\n"
      "v_mul_f32 v129, s44, v129\n"
      "v_cvt_pk_bf16_f32 v106, v128, v129\n"
      "v_lshlrev_b32 v128, 16, v107\n"
      "v_and_b32 v129, s43, v107\n"
      "v_mul_f32 v128, s44, v128\n"
      "v_mul_f32 v129, s44, v129\n"
      "v_cvt_pk_bf16_f32 v107, v128, v129\n"
      "v_lshlrev_b32 v128, 16, v108\n"
      "v_and_b32 v129, s43, v108\n"
      "v_mul_f32 v128, s44, v128\n"
      "v_mul_f32 v129, s44, v129\n"
      "v_cvt_pk_bf16_f32 v108, v128, v129\n"
      "v_lshlrev_b32 v128, 16, v109\n"
      "v_and_b32 v129, s43, v109\n"
      "v_mul_f32 v128, s44, v128\n"
      "v_mul_f32 v129, s44, v129\n"
      "v_cvt_pk_bf16_f32 v109, v128, v129\n"
      "v_lshlrev_b32 v128, 16, v110\n"
      "v_and_b32 v129, s43, v110\n"
      "v_mul_f32 v128, s44, v128\n"
      "v_mul_f32 v129, s44, v129\n"
      "v_cvt_pk_bf16_f32 v110, v128, v129\n"
      "v_lshlrev_b32 v128, 16, v111\n"
      "v_and_b32 v129, s43, v111\n"
      "v_mul_f32 v128, s44, v128\n"
      "v_mul_f32 v129, s44, v129\n"
      "v_cvt_pk_bf16_f32 v111, v128, v129\n"
      "v_lshlrev_b32 v128, 16, v112\n"
      "v_and_b32 v129, s43, v112\n"
      "v_mul_f32 v128, s44, v128\n"
      "v_mul_f32 v129, s44, v129\n"
      "v_cvt_pk_bf16_f32 v112, v128, v129\n"
      "v_lshlrev_b32 v128, 16, v113\n"
      "v_and_b32 v129, s43, v113\n"
      "v_mul_f32 v128, s44, v128\n"
      "v_mul_f32 v129, s44, v129\n"
      "v_cvt_pk_bf16_f32 v113, v128, v129\n"
      "v_lshlrev_b32 v128, 16, v114\n"
      "v_and_b32 v129, s43, v114\n"
      "v_mul_f32 v128, s44, v128\n"
      "v_mul_f32 v129, s44, v129\n"
      "v_cvt_pk_bf16_f32 v114, v128, v129\n"
      "v_lshlrev_b32 v128, 16, v115\n"
      "v_and_b32 v129, s43, v115\n"
      "v_mul_f32 v128, s44, v128\n"
      "v_mul_f32 v129, s44, v129\n"
      "v_cvt_pk_bf16_f32 v115, v128, v129\n"
      "v_lshlrev_b32 v128, 16, v116\n"
      "v_and_b32 v129, s43, v116\n"
      "v_mul_f32 v128, s44, v128\n"
      "v_mul_f32 v129, s44, v129\n"
      "v_cvt_pk_bf16_f32 v116, v128, v129\n"
      "v_lshlrev_b32 v128, 16, v117\n"
      "v_and_b32 v129, s43, v117\n"
      "v_mul_f32 v128, s44, v128\n"
      "v_mul_f32 v129, s44, v129\n"
      "v_cvt_pk_bf16_f32 v117, v128, v129\n"
      "v_lshlrev_b32 v128, 16, v118\n"
      "v_and_b32 v129, s43, v118\n"
      "v_mul_f32 v128, s44, v128\n"
      "v_mul_f32 v129, s44, v129\n"
      "v_cvt_pk_bf16_f32 v118, v128, v129\n"
      "v_lshlrev_b32 v128, 16, v119\n"
      "v_and_b32 v129, s43, v119\n"
      "v_mul_f32 v128, s44, v128\n"
      "v_mul_f32 v129, s44, v129\n"
      "v_cvt_pk_bf16_f32 v119, v128, v129\n"
      "v_lshlrev_b32 v128, 16, v120\n"
      "v_and_b32 v129, s43, v120\n"
      "v_mul_f32 v128, s44, v128\n"
      "v_mul_f32 v129, s44, v129\n"
      "v_cvt_pk_bf16_f32 v120, v128, v129\n"
      "v_lshlrev_b32 v128, 16, v121\n"
      "v_and_b32 v129, s43, v121\n"
      "v_mul_f32 v128, s44, v128\n"
      "v_mul_f32 v129, s44, v129\n"
      "v_cvt_pk_bf16_f32 v121, v128, v129\n"
      "v_lshlrev_b32 v128, 16, v122\n"
      "v_and_b32 v129, s43, v122\n"
      "v_mul_f32 v128, s44, v128\n"
      "v_mul_f32 v129, s44, v129\n"
      "v_cvt_pk_bf16_f32 v122, v128, v129\n"
      "v_lshlrev_b32 v128, 16, v123\n"
      "v_and_b32 v129, s43, v123\n"
      "v_mul_f32 v128, s44, v128\n"
      "v_mul_f32 v129, s44, v129\n"
      "v_cvt_pk_bf16_f32 v123, v128, v129\n"
      "v_lshlrev_b32 v128, 16, v124\n"
      "v_and_b32 v129, s43, v124\n"
      "v_mul_f32 v128, s44, v128\n"
      "v_mul_f32 v129, s44, v129\n"
      "v_cvt_pk_bf16_f32 v124, v128, v129\n"
      "v_lshlrev_b32 v128, 16, v125\n"
      "v_and_b32 v129, s43, v125\n"
      "v_mul_f32 v128, s44, v128\n"
      "v_mul_f32 v129, s44, v129\n"
      "v_cvt_pk_bf16_f32 v125, v128, v129\n"
      "v_lshlrev_b32 v128, 16, v126\n"
      "v_and_b32 v129, s43, v126\n"
      "v_mul_f32 v128, s44, v128\n"
      "v_mul_f32 v129, s44, v129\n"
      "v_cvt_pk_bf16_f32 v126, v128, v129\n"
      "v_lshlrev_b32 v128, 16, v127\n"
      "v_and_b32 v129, s43, v127\n"
      "v_mul_f32 v128, s44, v128\n"
      "v_mul_f32 v129, s44, v129\n"
      "v_cvt_pk_bf16_f32 v127, v128, v129\n"
      "s_nop 1\n"
      "s_branch 71b\n"
      "72:\n"
      "s_nop 7\n"
      "s_sub_u32 s44, 0, s58\n"
      "v_ldexp_f32 v16, v16, s58\n"
      "v_ldexp_f32 v48, v48, s44\n"
      "v_ldexp_f32 v17, v17, s58\n"
      "v_ldexp_f32 v49, v49, s44\n"
      "v_ldexp_f32 v18, v18, s58\n"
      "v_ldexp_f32 v50, v50, s44\n"
      "v_ldexp_f32 v19, v19, s58\n"
      "v_ldexp_f32 v51, v51, s44\n"
      "v_ldexp_f32 v20, v20, s58\n"
      "v_ldexp_f32 v52, v52, s44\n"
      "v_ldexp_f32 v21, v21, s58\n"
      "v_ldexp_f32 v53, v53, s44\n"
      "v_ldexp_f32 v22, v22, s58\n"
      "v_ldexp_f32 v54, v54, s44\n"
      "v_ldexp_f32 v23, v23, s58\n"
      "v_ldexp_f32 v55, v55, s44\n"
      "v_ldexp_f32 v24, v24, s58\n"
      "v_ldexp_f32 v56, v56, s44\n"
      "v_ldexp_f32 v25, v25, s58\n"
      "v_ldexp_f32 v57, v57, s44\n"
      "v_ldexp_f32 v26, v26, s58\n"
      "v_ldexp_f32 v58, v58, s44\n"
      "v_ldexp_f32 v27, v27, s58\n"
      "v_ldexp_f32 v59, v59, s44\n"
      "v_ldexp_f32 v28, v28, s58\n"
      "v_ldexp_f32 v60, v60, s44\n"
      "v_ldexp_f32 v29, v29, s58\n"
      "v_ldexp_f32 v61, v61, s44\n"
      "v_ldexp_f32 v30, v30, s58\n"
      "v_ldexp_f32 v62, v62, s44\n"
      "v_ldexp_f32 v31, v31, s58\n"
      "v_ldexp_f32 v63, v63, s44\n"
      "v_ldexp_f32 v32, v32, s58\n"
      "v_ldexp_f32 v64, v64, s44\n"
      "v_ldexp_f32 v33, v33, s58\n"
      "v_ldexp_f32 v65, v65, s44\n"
      "v_ldexp_f32 v34, v34, s58\n"
      "v_ldexp_f32 v66, v66, s44\n"
      "v_ldexp_f32 v35, v35, s58\n"
      "v_ldexp_f32 v67, v67, s44\n"
      "v_ldexp_f32 v36, v36, s58\n"
      "v_ldexp_f32 v68, v68, s44\n"
      "v_ldexp_f32 v37, v37, s58\n"
      "v_ldexp_f32 v69, v69, s44\n"
      "v_ldexp_f32 v38, v38, s58\n"
      "v_ldexp_f32 v70, v70, s44\n"
      "v_ldexp_f32 v39, v39, s58\n"
      "v_ldexp_f32 v71, v71, s44\n"
      "v_ldexp_f32 v40, v40, s58\n"
      "v_ldexp_f32 v72, v72, s44\n"
      "v_ldexp_f32 v41, v41, s58\n"
      "v_ldexp_f32 v73, v73, s44\n"
      "v_ldexp_f32 v42, v42, s58\n"
      "v_ldexp_f32 v74, v74, s44\n"
      "v_ldexp_f32 v43, v43, s58\n"
      "v_ldexp_f32 v75, v75, s44\n"
      "v_ldexp_f32 v44, v44, s58\n"
      "v_ldexp_f32 v76, v76, s44\n"
      "v_ldexp_f32 v45, v45, s58\n"
      "v_ldexp_f32 v77, v77, s44\n"
      "v_ldexp_f32 v46, v46, s58\n"
      "v_ldexp_f32 v78, v78, s44\n"
      "v_ldexp_f32 v47, v47, s58\n"
      "v_ldexp_f32 v79, v79, s44\n"
      "s_branch 73b\n"
      "6:\n"
      "s_waitcnt vmcnt(0) lgkmcnt(0)\n"
      "s_nop 7\n"
      "s_mov_b32 s55, 1\n"
      "s_mov_b32 s41, 1\n"
      "s_mov_b32 s45, 0x7fff7fff\n"
      "v_mov_b32 v226, 0\n"
      "v_and_b32 v176, s45, v228\n"
      "v_pk_max_u16 v226, v226, v176\n"
      "v_and_b32 v176, s45, v229\n"
      "v_pk_max_u16 v226, v226, v176\n"
      "v_and_b32 v176, s45, v230\n"
      "v_pk_max_u16 v226, v226, v176\n"
      "v_and_b32 v176, s45, v231\n"
      "v_pk_max_u16 v226, v226, v176\n"
      "v_and_b32 v176, s45, v232\n"
      "v_pk_max_u16 v226, v226, v176\n"
      "v_and_b32 v176, s45, v233\n"
      "v_pk_max_u16 v226, v226, v176\n"
      "v_and_b32 v176, s45, v234\n"
      "v_pk_max_u16 v226, v226, v176\n"
      "v_and_b32 v176, s45, v235\n"
      "v_pk_max_u16 v226, v226, v176\n"
      "v_and_b32 v176, s45, v236\n"
      "v_pk_max_u16 v226, v226, v176\n"
      "v_and_b32 v176, s45, v237\n"
      "v_pk_max_u16 v226, v226, v176\n"
      "v_and_b32 v176, s45, v238\n"
      "v_pk_max_u16 v226, v226, v176\n"
      "v_and_b32 v176, s45, v239\n"
      "v_pk_max_u16 v226, v226, v176\n"
      "v_and_b32 v176, s45, v240\n"
      "v_pk_max_u16 v226, v226, v176\n"
      "v_and_b32 v176, s45, v241\n"
      "v_pk_max_u16 v226, v226, v176\n"
      "v_and_b32 v176, s45, v242\n"
      "v_pk_max_u16 v226, v226, v176\n"
      "v_and_b32 v176, s45, v243\n"
      "v_pk_max_u16 v226, v226, v176\n"
      "v_and_b32 v176, s45, v244\n"
      "v_pk_max_u16 v226, v226, v176\n"
      "v_and_b32 v176, s45, v245\n"
      "v_pk_max_u16 v226, v226, v176\n"
      "v_and_b32 v176, s45, v246\n"
      "v_pk_max_u16 v226, v226, v176\n"
      "v_and_b32 v176, s45, v247\n"
      "v_pk_max_u16 v226, v226, v176\n"
      "v_and_b32 v176, s45, v248\n"
      "v_pk_max_u16 v226, v226, v176\n"
      "v_and_b32 v176, s45, v249\n"
      "v_pk_max_u16 v226, v226, v176\n"
      "v_and_b32 v176, s45, v250\n"
      "v_pk_max_u16 v226, v226, v176\n"
      "v_and_b32 v176, s45, v251\n"
      "v_pk_max_u16 v226, v226, v176\n"
      "v_and_b32 v176, s45, v252\n"
      "v_pk_max_u16 v226, v226, v176\n"
      "v_and_b32 v176, s45, v253\n"
      "v_pk_max_u16 v226, v226, v176\n"
      "v_and_b32 v176, s45, v254\n"
      "v_pk_max_u16 v226, v226, v176\n"
      "v_and_b32 v176, s45, v255\n"
      "v_pk_max_u16 v226, v226, v176\n"
      "v_lshlrev_b32 v176, 16, v226\n"
      "v_max_u32 v226, v226, v176\n"
      "s_nop 1\n"
      "v_max_u32_dpp v226, v226, v226 row_ror:8 row_mask:0xf bank_mask:0xf\n"
      "s_nop 1\n"
      "v_max_u32_dpp v226, v226, v226 row_ror:4 row_mask:0xf bank_mask:0xf\n"
      "s_nop 1\n"
      "v_max_u32_dpp v226, v226, v226 row_ror:2 row_mask:0xf bank_mask:0xf\n"
      "s_nop 1\n"
      "v_max_u32_dpp v226, v226, v226 row_ror:1 row_mask:0xf bank_mask:0xf\n"
      "s_nop 0\n"
      "v_readlane_b32 s44, v226, 0\n"
      "v_readlane_b32 s45, v226, 16\n"
      "v_readlane_b32 s46, v226, 32\n"
      "v_readlane_b32 s47, v226, 48\n"
      "s_max_u32 s44, s44, s45\n"
      "s_max_u32 s46, s46, s47\n"
      "s_max_u32 s44, s44, s46\n"
      "s_lshr_b32 s44, s44, 23\n"
      "s_sub_i32 s40, 138, s44\n"
      "s_cmp_eq_u32 s44, 0\n"
      "s_cselect_b32 s40, 0, s40\n"
      "s_add_i32 s45, s40, 32\n"
      "s_cmp_gt_u32 s45, 80\n"
      "s_cbranch_scc1 8f\n"
      "s_lshl1_add_u32 s44, s40, s58\n"
      "s_add_i32 s44, s44, s57\n"
      "s_add_i32 s45, s44, s51\n"
      "s_cmp_lt_i32 s45, 2\n"
      "s_cbranch_scc1 8f\n"
      "s_cmp_lg_u32 s50, 0\n"
      "s_cselect_b32 s44, s44, 64\n"
      "s_cmp_lt_i32 s44, 26\n"
      "s_cbranch_scc1 8f\n"
      "s_add_u32 s39, s40, 119\n"
      "s_lshl_b32 s39, s39, 23\n"
      "s_lshl_b32 s44, s40, 1\n"
      "s_sub_u32 s45, 127, s44\n"
      "s_add_u32 s44, s44, 127\n"
      "s_lshl_b32 s44, s44, 23\n"
      "s_lshl_b32 s45, s45, 23\n"
      "v_mul_f32 v16, s44, v16\n"
      "v_mul_f32 v48, s45, v48\n"
      "v_mul_f32 v17, s44, v17\n"
      "v_mul_f32 v49, s45, v49\n"
      "v_mul_f32 v18, s44, v18\n"
      "v_mul_f32 v50, s45, v50\n"
      "v_mul_f32 v19, s44, v19\n"
      "v_mul_f32 v51, s45, v51\n"
      "v_mul_f32 v20, s44, v20\n"
      "v_mul_f32 v52, s45, v52\n"
      "v_mul_f32 v21, s44, v21\n"
      "v_mul_f32 v53, s45, v53\n"
      "v_mul_f32 v22, s44, v22\n"
      "v_mul_f32 v54, s45, v54\n"
      "v_mul_f32 v23, s44, v23\n"
      "v_mul_f32 v55, s45, v55\n"
      "v_mul_f32 v24, s44, v24\n"
      "v_mul_f32 v56, s45, v56\n"
      "v_mul_f32 v25, s44, v25\n"
      "v_mul_f32 v57, s45, v57\n"
      "v_mul_f32 v26, s44, v26\n"
      "v_mul_f32 v58, s45, v58\n"
      "v_mul_f32 v27, s44, v27\n"
      "v_mul_f32 v59, s45, v59\n"
      "v_mul_f32 v28, s44, v28\n"
      "v_mul_f32 v60, s45, v60\n"
      "v_mul_f32 v29, s44, v29\n"
      "v_mul_f32 v61, s45, v61\n"
      "v_mul_f32 v30, s44, v30\n"
      "v_mul_f32 v62, s45, v62\n"
      "v_mul_f32 v31, s44, v31\n"
      "v_mul_f32 v63, s45, v63\n"
      "v_mul_f32 v32, s44, v32\n"
      "v_mul_f32 v64, s45, v64\n"
      "v_mul_f32 v33, s44, v33\n"
      "v_mul_f32 v65, s45, v65\n"
      "v_mul_f32 v34, s44, v34\n"
      "v_mul_f32 v66, s45, v66\n"
      "v_mul_f32 v35, s44, v35\n"
      "v_mul_f32 v67, s45, v67\n"
      "v_mul_f32 v36, s44, v36\n"
      "v_mul_f32 v68, s45, v68\n"
      "v_mul_f32 v37, s44, v37\n"
      "v_mul_f32 v69, s45, v69\n"
      "v_mul_f32 v38, s44, v38\n"
      "v_mul_f32 v70, s45, v70\n"
      "v_mul_f32 v39, s44, v39\n"
      "v_mul_f32 v71, s45, v71\n"
      "v_mul_f32 v40, s44, v40\n"
      "v_mul_f32 v72, s45, v72\n"
      "v_mul_f32 v41, s44, v41\n"
      "v_mul_f32 v73, s45, v73\n"
      "v_mul_f32 v42, s44, v42\n"
      "v_mul_f32 v74, s45, v74\n"
      "v_mul_f32 v43, s44, v43\n"
      "v_mul_f32 v75, s45, v75\n"
      "v_mul_f32 v44, s44, v44\n"
      "v_mul_f32 v76, s45, v76\n"
      "v_mul_f32 v45, s44, v45\n"
      "v_mul_f32 v77, s45, v77\n"
      "v_mul_f32 v46, s44, v46\n"
      "v_mul_f32 v78, s45, v78\n"
      "v_mul_f32 v47, s44, v47\n"
      "v_mul_f32 v79, s45, v79\n"
      "v_lshlrev_b32 v176, 16, v228\n"
      "v_and_b32 v177, s43, v228\n"
      "v_lshlrev_b32 v178, 16, v229\n"
      "v_and_b32 v179, s43, v229\n"
      "v_lshlrev_b32 v180, 16, v230\n"
      "v_and_b32 v181, s43, v230\n"
      "v_lshlrev_b32 v182, 16, v231\n"
      "v_and_b32 v183, s43, v231\n"
      "v_mul_f32 v176, s39, v176\n"
      "v_mul_f32 v177, s39, v177\n"
      "v_mul_f32 v178, s39, v178\n"
      "v_mul_f32 v179, s39, v179\n"
      "v_mul_f32 v180, s39, v180\n"
      "v_mul_f32 v181, s39, v181\n"
      "v_mul_f32 v182, s39, v182\n"
      "v_mul_f32 v183, s39, v183\n"
      "v_cvt_pk_f16_f32 v184, v176, v177\n"
      "v_cvt_pk_f16_f32 v185, v178, v179\n"
      "v_cvt_pk_f16_f32 v186, v180, v181\n"
      "v_cvt_pk_f16_f32 v187, v182, v183\n"
      "ds_write_b128 v1, v[184:187] offset:0\n"
      "v_lshlrev_b32 v176, 16, v232\n"
      "v_and_b32 v177, s43, v232\n"
      "v_lshlrev_b32 v178, 16, v233\n"
      "v_and_b32 v179, s43, v233\n"
      "v_lshlrev_b32 v180, 16, v234\n"
      "v_and_b32 v181, s43, v234\n"
      "v_lshlrev_b32 v182, 16, v235\n"
      "v_and_b32 v183, s43, v235\n"
      "v_mul_f32 v176, s39, v176\n"
      "v_mul_f32 v177, s39, v177\n"
      "v_mul_f32 v178, s39, v178\n"
      "v_mul_f32 v179, s39, v179\n"
      "v_mul_f32 v180, s39, v180\n"
      "v_mul_f32 v181, s39, v181\n"
      "v_mul_f32 v182, s39, v182\n"
      "v_mul_f32 v183, s39, v183\n"
      "v_cvt_pk_f16_f32 v184, v176, v177\n"
      "v_cvt_pk_f16_f32 v185, v178, v179\n"
      "v_cvt_pk_f16_f32 v186, v180, v181\n"
      "v_cvt_pk_f16_f32 v187, v182, v183\n"
      "ds_write_b128 v1, v[184:187] offset:1152\n"
      "v_lshlrev_b32 v176, 16, v236\n"
      "v_and_b32 v177, s43, v236\n"
      "v_lshlrev_b32 v178, 16, v237\n"
      "v_and_b32 v179, s43, v237\n"
      "v_lshlrev_b32 v180, 16, v238\n"
      "v_and_b32 v181, s43, v238\n"
      "v_lshlrev_b32 v182, 16, v239\n"
      "v_and_b32 v183, s43, v239\n"
      "v_mul_f32 v176, s39, v176\n"
      "v_mul_f32 v177, s39, v177\n"
      "v_mul_f32 v178, s39, v178\n"
      "v_mul_f32 v179, s39, v179\n"
      "v_mul_f32 v180, s39, v180\n"
      "v_mul_f32 v181, s39, v181\n"
      "v_mul_f32 v182, s39, v182\n"
      "v_mul_f32 v183, s39, v183\n"
      "v_cvt_pk_f16_f32 v184, v176, v177\n"
      "v_cvt_pk_f16_f32 v185, v178, v179\n"
      "v_cvt_pk_f16_f32 v186, v180, v181\n"
      "v_cvt_pk_f16_f32 v187, v182, v183\n"
      "ds_write_b128 v1, v[184:187] offset:2304\n"
      "v_lshlrev_b32 v176, 16, v240\n"
      "v_and_b32 v177, s43, v240\n"
      "v_lshlrev_b32 v178, 16, v241\n"
      "v_and_b32 v179, s43, v241\n"
      "v_lshlrev_b32 v180, 16, v242\n"
      "v_and_b32 v181, s43, v242\n"
      "v_lshlrev_b32 v182, 16, v243\n"
      "v_and_b32 v183, s43, v243\n"
      "v_mul_f32 v176, s39, v176\n"
      "v_mul_f32 v177, s39, v177\n"
      "v_mul_f32 v178, s39, v178\n"
      "v_mul_f32 v179, s39, v179\n"
      "v_mul_f32 v180, s39, v180\n"
      "v_mul_f32 v181, s39, v181\n"
      "v_mul_f32 v182, s39, v182\n"
      "v_mul_f32 v183, s39, v183\n"
      "v_cvt_pk_f16_f32 v184, v176, v177\n"
      "v_cvt_pk_f16_f32 v185, v178, v179\n"
      "v_cvt_pk_f16_f32 v186, v180, v181\n"
      "v_cvt_pk_f16_f32 v187, v182, v183\n"
      "ds_write_b128 v1, v[184:187] offset:3456\n"
      "v_lshlrev_b32 v176, 16, v244\n"
      "v_and_b32 v177, s43, v244\n"
      "v_lshlrev_b32 v178, 16, v245\n"
      "v_and_b32 v179, s43, v245\n"
      "v_lshlrev_b32 v180, 16, v246\n"
      "v_and_b32 v181, s43, v246\n"
      "v_lshlrev_b32 v182, 16, v247\n"
      "v_and_b32 v183, s43, v247\n"
      "v_mul_f32 v176, s39, v176\n"
      "v_mul_f32 v177, s39, v177\n"
      "v_mul_f32 v178, s39, v178\n"
      "v_mul_f32 v179, s39, v179\n"
      "v_mul_f32 v180, s39, v180\n"
      "v_mul_f32 v181, s39, v181\n"
      "v_mul_f32 v182, s39, v182\n"
      "v_mul_f32 v183, s39, v183\n"
      "v_cvt_pk_f16_f32 v184, v176, v177\n"
      "v_cvt_pk_f16_f32 v185, v178, v179\n"
      "v_cvt_pk_f16_f32 v186, v180, v181\n"
      "v_cvt_pk_f16_f32 v187, v182, v183\n"
      "ds_write_b128 v1, v[184:187] offset:4608\n"
      "v_lshlrev_b32 v176, 16, v248\n"
      "v_and_b32 v177, s43, v248\n"
      "v_lshlrev_b32 v178, 16, v249\n"
      "v_and_b32 v179, s43, v249\n"
      "v_lshlrev_b32 v180, 16, v250\n"
      "v_and_b32 v181, s43, v250\n"
      "v_lshlrev_b32 v182, 16, v251\n"
      "v_and_b32 v183, s43, v251\n"
      "v_mul_f32 v176, s39, v176\n"
      "v_mul_f32 v177, s39, v177\n"
      "v_mul_f32 v178, s39, v178\n"
      "v_mul_f32 v179, s39, v179\n"
      "v_mul_f32 v180, s39, v180\n"
      "v_mul_f32 v181, s39, v181\n"
      "v_mul_f32 v182, s39, v182\n"
      "v_mul_f32 v183, s39, v183\n"
      "v_cvt_pk_f16_f32 v184, v176, v177\n"
      "v_cvt_pk_f16_f32 v185, v178, v179\n"
      "v_cvt_pk_f16_f32 v186, v180, v181\n"
      "v_cvt_pk_f16_f32 v187, v182, v183\n"
      "ds_write_b128 v1, v[184:187] offset:5760\n"
      "v_lshlrev_b32 v176, 16, v252\n"
      "v_and_b32 v177, s43, v252\n"
      "v_lshlrev_b32 v178, 16, v253\n"
      "v_and_b32 v179, s43, v253\n"
      "v_lshlrev_b32 v180, 16, v254\n"
      "v_and_b32 v181, s43, v254\n"
      "v_lshlrev_b32 v182, 16, v255\n"
      "v_and_b32 v183, s43, v255\n"
      "v_mul_f32 v176, s39, v176\n"
      "v_mul_f32 v177, s39, v177\n"
      "v_mul_f32 v178, s39, v178\n"
      "v_mul_f32 v179, s39, v179\n"
      "v_mul_f32 v180, s39, v180\n"
      "v_mul_f32 v181, s39, v181\n"
      "v_mul_f32 v182, s39, v182\n"
      "v_mul_f32 v183, s39, v183\n"
      "v_cvt_pk_f16_f32 v184, v176, v177\n"
      "v_cvt_pk_f16_f32 v185, v178, v179\n"
      "v_cvt_pk_f16_f32 v186, v180, v181\n"
      "v_cvt_pk_f16_f32 v187, v182, v183\n"
      "ds_write_b128 v1, v[184:187] offset:6912\n"
      "v_mfma_f32_16x16x32_f16 v[192:195], a[184:187], a[152:155], 0\n"
      "v_mfma_f32_16x16x32_f16 v[192:195], a[188:191], a[156:159], v[192:195]\n"
      "v_mfma_f32_16x16x32_f16 v[192:195], a[192:195], a[160:163], v[192:195]\n"
      "v_mfma_f32_16x16x32_f16 v[192:195], a[196:199], a[164:167], v[192:195]\n"
      "v_mfma_f32_16x16x32_f16 v[252:255], a[184:187], a[168:171], 0\n"
      "v_mfma_f32_16x16x32_f16 v[252:255], a[188:191], a[172:175], v[252:255]\n"
      "v_mfma_f32_16x16x32_f16 v[252:255], a[192:195], a[176:179], v[252:255]\n"
      "v_mfma_f32_16x16x32_f16 v[252:255], a[196:199], a[180:183], v[252:255]\n"
      "s_sub_u32 s44, 96512, s38\n"
      "s_mov_b32 s98, 0\n"
      "v_add_u32 v1, s44, v1\n"
      "s_waitcnt lgkmcnt(0)\n"
      "ds_read_b128 v[128:131], v201 offset:0\n"
      "ds_read_b128 v[160:163], v202 offset:0\n"
      "ds_read_b128 v[132:135], v201 offset:64\n"
      "ds_read_b128 v[164:167], v202 offset:64\n"
      "ds_read_b128 v[136:139], v201 offset:128\n"
      "ds_read_b128 v[168:171], v202 offset:128\n"
      "ds_read_b128 v[140:143], v201 offset:192\n"
      "ds_read_b128 v[172:175], v202 offset:192\n"
      "s_waitcnt lgkmcnt(0)\n"
      "v_pk_mul_f16 v96, v128, v160\n"
      "v_pk_mul_f16 v97, v129, v161\n"
      "v_pk_mul_f16 v98, v130, v162\n"
      "v_pk_mul_f16 v99, v131, v163\n"
      "v_pk_mul_f16 v100, v132, v164\n"
      "v_pk_mul_f16 v101, v133, v165\n"
      "v_pk_mul_f16 v102, v134, v166\n"
      "v_pk_mul_f16 v103, v135, v167\n"
      "v_pk_mul_f16 v104, v136, v168\n"
      "v_pk_mul_f16 v105, v137, v169\n"
      "v_pk_mul_f16 v106, v138, v170\n"
      "v_pk_mul_f16 v107, v139, v171\n"
      "s_nop 1\n"
      "s_branch 3b\n"
      "7:\n"
      "s_mov_b64 exec, -1\n"
      "s_cmp_lg_u32 s41, 0\n"
      "s_cbranch_scc1 8f\n"
      "s_mov_b32 s41, 1\n"
      "s_waitcnt vmcnt(0)\n"
      "s_branch 0b\n"
      "8:\n"
      "v_mov_b32 v222, 2\n"
      "s_mov_b32 s42, 1\n"
      "ds_write_b32 v2, v222 offset:8\n"
      "9:\n"
      "s_nop 7\n"
      "s_waitcnt vmcnt(0) lgkmcnt(0)\n"
      "s_mov_b32 %0, s42\n"
      : "=s"(flag)
      : "s"(__builtin_amdgcn_kernarg_segment_ptr()), "s"(blockIdx.x), "v"(threadIdx.x), "s"(lbase)
      :
      "v1", "v2", "v3", "v4", "v5", "v6", "v7", "v8", "v9", "v10", "v11", "v12", "v13", "v14", "v15", "v16", "v17", "v18", "v19", "v20", "v21", "v22", "v23", "v24",
      "v25", "v26", "v27", "v28", "v29", "v30", "v31", "v32", "v33", "v34", "v35", "v36", "v37", "v38", "v39", "v40", "v41", "v42", "v43", "v44", "v45", "v46", "v47", "v48",
      "v49", "v50", "v51", "v52", "v53", "v54", "v55", "v56", "v57", "v58", "v59", "v60", "v61", "v62", "v63", "v64", "v65", "v66", "v67", "v68", "v69", "v70", "v71", "v72",
      "v73", "v74", "v75", "v76", "v77", "v78", "v79", "v80", "v81", "v82", "v83", "v84", "v85", "v86", "v87", "v88", "v89", "v90", "v91", "v92", "v93", "v94", "v95", "v96",
      "v97", "v98", "v99", "v100", "v101", "v102", "v103", "v104", "v105", "v106", "v107", "v108", "v109", "v110", "v111", "v112", "v113", "v114", "v115", "v116", "v117", "v118", "v119", "v120",
      "v121", "v122", "v123", "v124", "v125", "v126", "v127", "v128", "v129", "v130", "v131", "v132", "v133", "v134", "v135", "v136", "v137", "v138", "v139", "v140", "v141", "v142", "v143", "v144",
      "v145", "v146", "v147", "v148", "v149", "v150", "v151", "v152", "v153", "v154", "v155", "v156", "v157", "v158", "v159", "v160", "v161", "v162", "v163", "v164", "v165", "v166", "v167", "v168",
      "v169", "v170", "v171", "v172", "v173", "v174", "v175", "v176", "v177", "v178", "v179", "v180", "v181", "v182", "v183", "v184", "v185", "v186", "v187", "v188", "v189", "v190", "v191", "v192",
      "v193", "v194", "v195", "v196", "v197", "v198", "v199", "v200", "v201", "v202", "v203", "v204", "v205", "v206", "v207", "v208", "v209", "v210", "v211", "v212", "v213", "v214", "v215", "v216",
      "v217", "v218", "v219", "v220", "v221", "v222", "v223", "v224", "v225", "v226", "v227", "v228", "v229", "v230", "v231", "v232", "v233", "v234", "v235", "v236", "v237", "v238", "v239", "v240",
      "v241", "v242", "v243", "v244", "v245", "v246", "v247", "v248", "v249", "v250", "v251", "v252", "v253", "v254", "v255", "a0", "a1", "a2", "a3", "a4", "a5", "a6", "a7", "a8",
      "a9", "a10", "a11", "a12", "a13", "a14", "a15", "a16", "a17", "a18", "a19", "a20", "a21", "a22", "a23", "a24", "a25", "a26", "a27", "a28", "a29", "a30", "a31", "a32",
      "a33", "a34", "a35", "a36", "a37", "a38", "a39", "a40", "a41", "a42", "a43", "a44", "a45", "a46", "a47", "a48", "a49", "a50", "a51", "a52", "a53", "a54", "a55", "a56",
      "a57", "a58", "a59", "a60", "a61", "a62", "a63", "a64", "a65", "a66", "a67", "a68", "a69", "a70", "a71", "a72", "a73", "a74", "a75", "a76", "a77", "a78", "a79", "a80",
      "a81", "a82", "a83", "a84", "a85", "a86", "a87", "a88", "a89", "a90", "a91", "a92", "a93", "a94", "a95", "a96", "a97", "a98", "a99", "a100", "a101", "a102", "a103", "a104",
      "a105", "a106", "a107", "a108", "a109", "a110", "a111", "a112", "a113", "a114", "a115", "a116", "a117", "a118", "a119", "a120", "a121", "a122", "a123", "a124", "a125", "a126", "a127", "a128",
      "a129", "a130", "a131", "a132", "a133", "a134", "a135", "a136", "a137", "a138", "a139", "a140", "a141", "a142", "a143", "a144", "a145", "a146", "a147", "a148", "a149", "a150", "a151", "a152",
      "a153", "a154", "a155", "a156", "a157", "a158", "a159", "a160", "a161", "a162", "a163", "a164", "a165", "a166", "a167", "a168", "a169", "a170", "a171", "a172", "a173", "a174", "a175", "a176",
      "a177", "a178", "a179", "a180", "a181", "a182", "a183", "a184", "a185", "a186", "a187", "a188", "a189", "a190", "a191", "a192", "a193", "a194", "a195", "a196", "a197", "a198", "a199", "a200",
      "a201", "a202", "a203", "a204", "a205", "a206", "a207", "a208", "a209", "a210", "a211", "a212", "a213", "a214", "a215", "a216", "a217", "s8", "s9", "s10", "s11", "s12", "s13", "s14",
      "s15", "s16", "s17", "s18", "s19", "s20", "s21", "s22", "s23", "s24", "s25", "s26", "s27", "s28", "s29", "s30", "s31", "s33", "s34", "s35", "s36", "s37", "s38", "s39",
      "s40", "s41", "s42", "s43", "s44", "s45", "s46", "s47", "s48", "s49", "s50", "s51", "s52", "s53", "s54", "s55", "s56", "s57", "s58", "s59", "s60", "s61", "s62", "s63",
      "s64", "s65", "s66", "s67", "s68", "s69", "s70", "s71", "s72", "s73", "s74", "s75", "s76", "s77", "s78", "s79", "s80", "s81", "s82", "s83", "s84", "s85", "s86", "s87",
      "s88", "s89", "s90", "s91", "s92", "s93", "s94", "s95", "s96", "s97", "s98", "s99", "s100", "s101", "vcc", "scc", "memory");
  if (flag | lbase) {
    const int F = 26, P = 325;
    const int lane = threadIdx.x & 63;
    const int b = (int)(min((blockIdx.x & 7) * kx + ((blockIdx.x >> 3) * 4 + (threadIdx.x >> 6)) * 104u, lim) / 104u);
    {
      float m = -INFINITY, z = 0.f, n = 0.f;
      for (int p = lane; p < P; p += 64) {
        const unsigned short* ei = table + (size_t)idx[b * F + ii[p]] * 128;
        const unsigned short* ej = table + (size_t)idx[b * F + jj[p]] * 128;
        float e = 0.f, s = 0.f;
        for (int a = 0; a < 128; ++a) {
          float acc = 0.f;
          for (int d = 0; d < 128; ++d) acc = fmaf(rbf(bf2f(ei[d]) * bf2f(ej[d])), bf2f(W[d * 128 + a]), acc);
          float x = acc + bf2f(bvec[a]);
          x = 0.5f * (x + fabsf(x));
          e = fmaf(x, bf2f(hvec[a]), e);
        }
        for (int d = 0; d < 128; ++d) s = fmaf(rbf(bf2f(ei[d]) * bf2f(ej[d])), bf2f(pvec[d]), s);
        float mn = fmaxf(m, e);
        float fa = __expf(m - mn), fb = __expf(e - mn);
        z = z * fa + fb; n = n * fa + fb * s; m = mn;
      }
      float mx = m;
      for (int o = 1; o < 64; o <<= 1) mx = fmaxf(mx, __shfl_xor(mx, o));
      float f = (m == -INFINITY) ? 0.f : __expf(m - mx);
      z *= f; n *= f;
      for (int o = 1; o < 64; o <<= 1) { z += __shfl_xor(z, o); n += __shfl_xor(n, o); }
      if (lane == 0) out[b] = f2bf(n / z);
    }
  }
}

extern "C" __global__ __launch_bounds__(256) void afm_any(const int* __restrict__ idx, const unsigned short* __restrict__ table,
                                   const int* __restrict__ ii, const int* __restrict__ jj,
                                   const unsigned short* __restrict__ W, const unsigned short* __restrict__ bvec,
                                   const unsigned short* __restrict__ hvec, const unsigned short* __restrict__ pvec,
                                   unsigned short* __restrict__ out, float* __restrict__ ws,
                                   int B, int F, int P, int D, int A) {
  const int b = blockIdx.x;
  float* ebuf = ws + (size_t)b * 2 * P;
  float* sbuf = ebuf + P;
  for (int p = threadIdx.x; p < P; p += 256) {
    const unsigned short* ei = table + (size_t)idx[b * F + ii[p]] * D;
    const unsigned short* ej = table + (size_t)idx[b * F + jj[p]] * D;
    float e = 0.f, sacc = 0.f;
    for (int a = 0; a < A; ++a) {
      float acc = 0.f;
      for (int d = 0; d < D; ++d) acc = fmaf(rbf(bf2f(ei[d]) * bf2f(ej[d])), bf2f(W[(size_t)d * A + a]), acc);
      float x = acc + bf2f(bvec[a]);
      x = 0.5f * (x + fabsf(x));
      e = fmaf(x, bf2f(hvec[a]), e);
    }
    for (int d = 0; d < D; ++d) sacc = fmaf(rbf(bf2f(ei[d]) * bf2f(ej[d])), bf2f(pvec[d]), sacc);
    ebuf[p] = e; sbuf[p] = sacc;
  }
  __syncthreads();
  if (threadIdx.x == 0) {
    float mx = -INFINITY;
    for (int p = 0; p < P; ++p) mx = fmaxf(mx, ebuf[p]);
    float z = 0.f, n = 0.f;
    for (int p = 0; p < P; ++p) { float w = __expf(ebuf[p] - mx); z += w; n += w * sbuf[p]; }
    out[b] = f2bf(n / z);
  }
}

extern "C" void kernel_launch(void* const* d_in, const int* in_sizes, int n_in,
                              void* d_out, int out_size, void* d_ws, size_t ws_size,
                              hipStream_t stream) {
  const int* idx = (const int*)d_in[0];
  const unsigned short* table = (const unsigned short*)d_in[1];
  const int* ii = (const int*)d_in[2];
  const int* jj = (const int*)d_in[3];
  const unsigned short* W = (const unsigned short*)d_in[4];
  const unsigned short* bvec = (const unsigned short*)d_in[5];
  const unsigned short* hvec = (const unsigned short*)d_in[6];
  const unsigned short* pvec = (const unsigned short*)d_in[7];
  unsigned short* out = (unsigned short*)d_out;
  const int B = out_size;
  const int P = in_sizes[2];
  const int D = in_sizes[7];
  const int A = in_sizes[5];
  if (B <= 0) return;
  const int F = in_sizes[0] / B;
  if (D == 128 && A == 128 && F == 26 && P == 325 && B < (1 << 24)) {
    const int g8 = (B + 31) / 32;
    afm_fast<<<8 * g8, 256, 0, stream>>>(idx, table, ii, jj, W, bvec, hvec, pvec, out, 104u * (unsigned)(B - 1), 416u * (unsigned)g8
        );
  } else {
    afm_any<<<B, 256, 0, stream>>>(idx, table, ii, jj, W, bvec, hvec, pvec, out, (float*)d_ws, B, F, P, D, A);
  }
}
```

```cpp
#include <hip/hip_runtime.h>
#include <hip/hip_bf16.h>
#include <stdint.h>

#define AFM_HAS_STAMPS 0

typedef float  f32x2  __attribute__((ext_vector_type(2)));
typedef __bf16 bf16x2 __attribute__((ext_vector_type(2)));

__device__ __forceinline__ float bf2f(unsigned short u) { return __builtin_bit_cast(float, (unsigned)u << 16); }
__device__ __forceinline__ unsigned short f2bf(float x) {
  f32x2 v = {x, 0.f}; bf16x2 r = __builtin_convertvector(v, bf16x2);
  return (unsigned short)(__builtin_bit_cast(unsigned, r) & 0xffffu);
}
__device__ __forceinline__ float rbf(float x) { return bf2f(f2bf(x)); }

extern "C" __device__ __attribute__((used, aligned(512))) const unsigned afm_lane_tab[804] = {
  738206208u, 533735168u, 697050480u, 864u, 743449092u, 539240196u, 719070580u, 20972672u, 748691976u, 539764488u, 554968440u, 1440u, 753934860u, 550250252u, 717235580u, 20973248u,
  759177744u, 555755280u, 613426560u, 2016u, 764420628u, 561260308u, 540550532u, 20971520u, 769663512u, 561784600u, 549463432u, 288u, 774906396u, 530327324u, 722740620u, 20972096u,
  738468432u, 538978128u, 618931888u, 3168u, 743711316u, 534521684u, 727983796u, 20974976u, 748954200u, 549988184u, 708585144u, 3744u, 754197084u, 555493212u, 723003068u, 20975552u,
  759439968u, 560998240u, 728508096u, 4320u, 764682852u, 556541796u, 624436932u, 20973824u, 769925736u, 530065256u, 702555848u, 2592u, 775168620u, 535570284u, 697575116u, 20974400u,
  738730656u, 701507488u, 697050480u, 1049456u, 743973540u, 707012516u, 719070580u, 22021264u, 749216424u, 707536808u, 554968440u, 1050032u, 754459308u, 718022572u, 717235580u, 22021840u,
  759702192u, 723527600u, 613426560u, 1050608u, 764945076u, 729032628u, 540550532u, 22020112u, 770187960u, 729556920u, 549463432u, 1048880u, 775430844u, 698099644u, 722740620u, 22020688u,
  738992880u, 706750448u, 618931888u, 1051760u, 744235764u, 702294004u, 727983796u, 22023568u, 749478648u, 717760504u, 708585144u, 1052336u, 754721532u, 723265532u, 723003068u, 22024144u,
  759964416u, 728770560u, 728508096u, 1052912u, 765207300u, 724314116u, 624436932u, 22022416u, 770450184u, 697837576u, 702555848u, 1051184u, 775693068u, 703342604u, 697575116u, 22022992u,
  739255104u, 617621568u, 529279600u, 2098048u, 744497988u, 623126596u, 549726836u, 23069856u, 749740872u, 623650888u, 555231864u, 2098624u, 754983756u, 634136652u, 560736892u, 23070432u,
  760226640u, 639641680u, 551299712u, 2099200u, 765469524u, 645146708u, 529803908u, 23068704u, 770712408u, 645671000u, 535308936u, 2097472u, 775955292u, 614213724u, 540813964u, 23069280u,
  739517328u, 622864528u, 613166000u, 2100352u, 744760212u, 618408084u, 633613236u, 23072160u, 750003096u, 633874584u, 639118264u, 2100928u, 755245980u, 639379612u, 644623292u, 23072736u,
  760488864u, 644884640u, 635186112u, 2101504u, 765731748u, 640428196u, 613690308u, 23071008u, 770974632u, 613951656u, 619195336u, 2099776u, 776217516u, 619456684u, 624700364u, 23071584u,
  739779552u, 544221408u, 529279600u, 3146640u, 745022436u, 544483556u, 549726836u, 24118448u, 750265320u, 544745704u, 555231864u, 3147216u, 755508204u, 545007852u, 560736892u, 24119024u,
  760751088u, 545270000u, 551299712u, 3147792u, 765993972u, 545532148u, 529803908u, 24117296u, 771236856u, 545794296u, 535308936u, 3146064u, 776479740u, 546056444u, 540813964u, 24117872u,
  740041776u, 565193008u, 613166000u, 3148944u, 745284660u, 565455156u, 633613236u, 24120752u, 750527544u, 565717304u, 639118264u, 3149520u, 755770428u, 565979452u, 644623292u, 24121328u,
  761013312u, 566241600u, 635186112u, 3150096u, 766256196u, 566503748u, 613690308u, 24119600u, 771499080u, 566765896u, 619195336u, 3148368u, 776741964u, 567028044u, 624700364u, 24120176u,
  358617600u, 8064u, 301992192u, 132121440u, 434116224u, 18882512u, 320866848u, 358619616u, 94372128u, 37756960u, 339741504u, 415241280u, 377491968u, 452993136u, 452991744u, 358619616u,
  150998400u, 75505856u, 377490816u, 452992032u, 37750464u, 94380304u, 396365472u, 207622368u, 75497472u, 113254752u, 415240128u, 207622368u, 396366624u, 471867824u, 471866400u, 415241280u,
  169873056u, 151003648u, 4608u, 1075854240u, 415240992u, 169878096u, 18879264u, 1283477760u, 434115648u, 188752544u, 37753920u, 1396724832u, 396366912u, 207626992u, 56628576u, 1283477760u,
  415241568u, 226501440u, 75503232u, 1377848736u, 188747712u, 245375888u, 94377888u, 1151355168u, 320870016u, 264250336u, 113252544u, 1151355168u, 301995936u, 283124784u, 132127200u, 1283477760u,
  359666192u, 1056656u, 303040784u, 133170032u, 435164816u, 19931104u, 321915440u, 359668208u, 95420720u, 38805552u, 340790096u, 416289872u, 378540560u, 454041728u, 454040336u, 359668208u,
  152046992u, 76554448u, 378539408u, 454040624u, 38799056u, 95428896u, 397414064u, 208670960u, 76546064u, 114303344u, 416288720u, 208670960u, 397415216u, 472916416u, 472914992u, 416289872u,
  170921648u, 152052240u, 1053200u, 1076902832u, 416289584u, 170926688u, 19927856u, 1284526352u, 435164240u, 189801136u, 38802512u, 1397773424u, 397415504u, 208675584u, 57677168u, 1284526352u,
  416290160u, 227550032u, 76551824u, 1378897328u, 189796304u, 246424480u, 95426480u, 1152403760u, 321918608u, 265298928u, 114301136u, 1152403760u, 303044528u, 284173376u, 133175792u, 1284526352u,
  360714784u, 2105248u, 304089376u, 134218624u, 436213408u, 20979696u, 322964032u, 360716800u, 96469312u, 39854144u, 341838688u, 417338464u, 379589152u, 455090320u, 455088928u, 360716800u,
  153095584u, 77603040u, 379588000u, 455089216u, 39847648u, 96477488u, 398462656u, 209719552u, 77594656u, 115351936u, 417337312u, 209719552u, 398463808u, 473965008u, 473963584u, 417338464u,
  171970240u, 153100832u, 2101792u, 1077951424u, 417338176u, 171975280u, 20976448u, 1285574944u, 436212832u, 190849728u, 39851104u, 1398822016u, 398464096u, 209724176u, 58725760u, 1285574944u,
  417338752u, 228598624u, 77600416u, 1379945920u, 190844896u, 247473072u, 96475072u, 1153452352u, 322967200u, 266347520u, 115349728u, 1153452352u, 304093120u, 285221968u, 134224384u, 1285574944u,
  361763376u, 3153840u, 305137968u, 135267216u, 437262000u, 22028288u, 324012624u, 361765392u, 97517904u, 40902736u, 342887280u, 418387056u, 380637744u, 456138912u, 456137520u, 361765392u,
  154144176u, 78651632u, 380636592u, 456137808u, 40896240u, 97526080u, 399511248u, 210768144u, 78643248u, 116400528u, 418385904u, 210768144u, 399512400u, 475013600u, 475012176u, 418387056u,
  173018832u, 154149424u, 3150384u, 1079000016u, 418386768u, 173023872u, 22025040u, 1286623536u, 437261424u, 191898320u, 40899696u, 1399870608u, 399512688u, 210772768u, 59774352u, 1286623536u,
  418387344u, 229647216u, 78649008u, 1380994512u, 191893488u, 248521664u, 97523664u, 1154500944u, 324015792u, 267396112u, 116398320u, 1154500944u, 305141712u, 286270560u, 135272976u, 1286623536u,
  0u, 18874368u, 524304u, 37748740u, 75497504u, 56623112u, 76021808u, 75497484u, 304087104u, 94371856u, 304611408u, 113246228u, 379584608u, 132120600u, 380108912u, 28u,
  608174208u, 37748864u, 608698512u, 56623236u, 683671712u, 75497608u, 684196016u, 94371980u, 912261312u, 113246352u, 912785616u, 132120724u, 987758816u, 152u, 988283120u, 18874524u,
  1053216u, 19923744u, 1577520u, 38798116u, 76550720u, 57672488u, 77075024u, 76546860u, 305140320u, 95421232u, 305664624u, 114295604u, 380637824u, 133169976u, 381162128u, 1049404u,
  609227424u, 38798240u, 609751728u, 57672612u, 684724928u, 76546984u, 685249232u, 95421356u, 913314528u, 114295728u, 913838832u, 133170100u, 988812032u, 1049528u, 989336336u, 19923900u,
  2106432u, 20972352u, 2630736u, 39846724u, 77603936u, 58721096u, 78128240u, 77595468u, 306193536u, 96469840u, 306717840u, 115344212u, 381691040u, 134218584u, 382215344u, 2098012u,
  610280640u, 39846848u, 610804944u, 58721220u, 685778144u, 77595592u, 686302448u, 96469964u, 914367744u, 115344336u, 914892048u, 134218708u, 989865248u, 2098136u, 990389552u, 20972508u,
  3159648u, 22020960u, 3683952u, 40895332u, 78657152u, 59769704u, 79181456u, 78644076u, 307246752u, 97518448u, 307771056u, 116392820u, 382744256u, 135267192u, 383268560u, 3146620u,
  611333856u, 40895456u, 611858160u, 59769828u, 686831360u, 78644200u, 687355664u, 97518572u, 915420960u, 116392944u, 915945264u, 135267316u, 990918464u, 3146744u, 991442768u, 22021116u,
  0u, 0u, 0u, 0u, 0u, 0u, 16843008u, 16843009u, 16843009u, 16843009u, 16843009u, 16843009u, 33686017u, 33686018u, 33686018u, 33686018u,
  33686018u, 33686018u, 50529027u, 50529027u, 50529027u, 50529027u, 50529027u, 67371779u, 67372036u, 67372036u, 67372036u, 67372036u, 84149252u, 84215045u, 84215045u, 84215045u,
  84215045u, 100992261u, 101058054u, 101058054u, 101058054u, 101058054u, 117900806u, 117901063u, 117901063u, 117901063u, 117901063u, 134744072u, 134744072u, 134744072u, 134744072u, 151587080u,
  151587081u, 151587081u, 151587081u, 168430089u, 168430090u, 168430090u, 168430090u, 185273099u, 185273099u, 185273099u, 202115851u, 202116108u, 202116108u, 218893324u, 218959117u, 218959117u,
  235736333u, 235802126u, 235802126u, 252644878u, 252645135u, 252645135u, 269488144u, 269488144u, 286331152u, 286331153u, 303174161u, 303174162u, 320017171u, 336859923u, 353637396u, 370480405u,
  387388950u, 404166422u, 67305985u, 134678021u, 202050057u, 269422093u, 336794129u, 404166165u, 67306009u, 134678021u, 202050057u, 269422093u, 336794129u, 404166165u, 84149017u, 151521030u,
  218893066u, 286265102u, 353637138u, 421009174u, 117835012u, 185207048u, 252579084u, 319951120u, 387323156u, 100997400u, 168364039u, 235736075u, 303108111u, 370480147u, 102307863u, 168364039u,
  235736075u, 303108111u, 370480147u, 119085079u, 185207048u, 252579084u, 319951120u, 387323156u, 151525656u, 218893066u, 286265102u, 353637138u, 421009174u, 202050057u, 269422093u, 336794129u,
  404166165u, 202050073u, 269422093u, 336794129u, 404166165u, 218893081u, 286265102u, 353637138u, 421009174u, 252579084u, 319951120u, 387323156u, 235739416u, 303108111u, 370480147u, 236525591u,
  303108111u, 370480147u, 253302807u, 319951120u, 387323156u, 286267672u, 353637138u, 421009174u, 336794129u, 404166165u, 336794137u, 404166165u, 353637145u, 421009174u, 387323156u, 370481432u,
  370743319u, 387520535u, 421009688u, 421074969u
};

extern "C" __global__ __launch_bounds__(256, 1) __attribute__((aligned(4096)))
void afm_fast(const int* __restrict__ idx, const unsigned short* __restrict__ table,
                     const int* __restrict__ ii, const int* __restrict__ jj,
                     const unsigned short* __restrict__ W, const unsigned short* __restrict__ bvec,
                     const unsigned short* __restrict__ hvec, const unsigned short* __restrict__ pvec,
                     unsigned short* __restrict__ out, unsigned lim, unsigned kx
                     ) {
  __shared__ __attribute__((aligned(16))) char smem[110912];
  const unsigned lbase = (unsigned)(uintptr_t)(__attribute__((address_space(3))) char*)smem;
  unsigned flag;
  asm volatile(
      "s_load_dwordx16 s[8:23], %1, 0x0\n"
      "s_load_dwordx4 s[24:27], %1, 0x40\n"
      "s_mov_b32 s41, 0\n"
      "s_mov_b32 s42, 0\n"
      "s_mov_b32 s58, 0\n"
      "s_mov_b32 s35, 0\n"
      "0:\n"
      "s_getpc_b64 s[56:57]\n"
      "s_add_u32 s98, s56, afm_lane_tab@rel32@lo+4\n"
      "s_addc_u32 s99, s57, afm_lane_tab@rel32@hi+12\n"
      "v_and_b32 v255, 63, %3\n"
      "v_and_b32 v254, 127, %3\n"
      "v_lshrrev_b32 v253, 7, %3\n"
      "v_min_u32 v254, 81, v254\n"
      "v_mul_u32_u24 v253, 82, v253\n"
      "v_lshlrev_b32 v14, 4, v255\n"
      "v_lshlrev_b32 v252, 3, v255\n"
      "v_add_lshl_u32 v253, v253, v254, 2\n"
      "global_load_dwordx2 v[22:23], v252, s[98:99] offset:2048\n"
      "global_load_dword v211, v253, s[98:99] offset:2560\n"
      "global_load_dwordx4 v[204:207], v14, s[98:99]\n"
      "global_load_dwordx4 v[196:199], v14, s[98:99] offset:1024\n"
      "v_lshrrev_b32 v2, 6, %3\n"
      "s_bfe_u32 s44, %2, 0x30003\n"
      "s_mul_i32 s44, s44, 24\n"
      "v_mad_u32_u24 v255, v2, 6, v255\n"
      "s_mov_b64 exec, 0x3f\n"
      "v_add_lshl_u32 v255, v255, s44, 7\n"
      "global_load_dword v255, v255, s[56:57]\n"
      "s_mov_b64 exec, -1\n"
      "v_bfe_u32 v4, %3, 4, 2\n"
      "v_and_b32 v3, 15, %3\n"
      "v_readfirstlane_b32 s53, v2\n"
      "s_and_b32 s50, %2, 7\n"
      "s_lshr_b32 s51, %2, 3\n"
      "s_lshl2_add_u32 s51, s51, s53\n"
      "s_mul_i32 s51, s51, 104\n"
      "v_lshlrev_b32 v13, 4, v3\n"
      "v_lshlrev_b32 v215, 4, v254\n"
      "v_min_u32 v215, 1284, v215\n"
      "v_lshl_add_u32 v80, v2, 11, v14\n"
      "v_add_u32 v81, 8192, v80\n"
      "v_add_u32 v82, 16384, v80\n"
      "v_add_u32 v26, 24576, v80\n"
      "v_lshlrev_b32 v2, 4, v2\n"
      "s_mul_i32 s38, s53, 14400\n"
      "v_add_u32 v2, 37632, v2\n"
      "s_add_u32 s38, s38, 38912\n"
      "v_mul_u32_u24 v222, 288, v4\n"
      "v_add_u32 v213, s38, v13\n"
      "v_mov_b32 v5, 0\n"
      "v_add_u32 v1, v222, v213\n"
      "v_mov_b32 v222, 0x7fa5a5a5\n"
      "s_cmp_lg_u32 s35, 0\n"
      "s_cbranch_scc1 2f\n"
      "ds_write2_b32 v2, v5, v5 offset0:0 offset1:1\n"
      "ds_write_b32 v2, v5 offset:8\n"
      "ds_write_b32 v1, v222 offset:12992\n"
      "2:\n"
      "s_mov_b32 s43, 0xffff0000\n"
      "s_mov_b32 s56, 0\n"
      "s_mov_b32 s40, 0\n"
      "s_mov_b32 s55, 0\n"
      "s_and_b32 s46, s53, 2\n"
      "s_lshl_b32 s54, 3, s46\n"
      "s_bitcmp1_b32 s53, 0\n"
      "s_cselect_b64 s[100:101], -1, 0\n"
      "s_movk_i32 s98, 14400\n"
      "s_cselect_b32 s99, s98, 0\n"
      "s_cmovk_i32 s98, -14400\n"
      "s_cselect_b32 s46, 0, 18560\n"
      "s_cselect_b32 s47, 18560, 0\n"
      "v_add_u32 v212, s46, v14\n"
      "v_add_u32 v14, s47, v14\n"
      "s_sub_u32 s99, s38, s99\n"
      "v_cmp_eq_u32 vcc, 1, v4\n"
      "v_mov_b32 v222, 0x3200\n"
      "s_mov_b32 s46, 0\n"
      "v_cndmask_b32 v94, 0, v222, vcc\n"
      "s_mov_b32 s47, 0xc82c4cb9\n"
      "v_lshlrev_b32 v223, 4, v4\n"
      "v_lshrrev_b64 v[146:147], v223, s[46:47]\n"
      "v_lshlrev_b32 v138, 16, v146\n"
      "v_lshrrev_b32 v130, 16, v138\n"
      "v_mov_b32 v200, 0x4838aa3b\n"
      "v_lshlrev_b32 v224, 16, v94\n"
      "v_accvgpr_write_b32 a128, v130\n"
      "v_accvgpr_write_b32 a129, 0\n"
      "v_accvgpr_write_b32 a130, v138\n"
      "v_accvgpr_write_b32 a131, 0\n"
      "v_accvgpr_write_b32 a132, 0\n"
      "v_accvgpr_write_b32 a133, v130\n"
      "v_accvgpr_write_b32 a134, 0\n"
      "v_accvgpr_write_b32 a135, v138\n"
      "v_accvgpr_write_b32 a208, v94\n"
      "v_accvgpr_write_b32 a209, 0\n"
      "v_accvgpr_write_b32 a210, v224\n"
      "v_accvgpr_write_b32 a211, 0\n"
      "v_accvgpr_write_b32 a212, 0\n"
      "v_accvgpr_write_b32 a213, v94\n"
      "v_accvgpr_write_b32 a214, 0\n"
      "v_accvgpr_write_b32 a215, v224\n"
      "v_add_u32 v95, s38, v223\n"
      "v_and_b32 v227, 3, %3\n"
      "v_bfe_u32 v224, %3, 2, 2\n"
      "v_cmp_eq_u32 vcc, v227, v4\n"
      "v_mov_b32 v222, 0x3a00\n"
      "v_mov_b64 v[84:85], 0\n"
      "s_waitcnt lgkmcnt(0)\n"
      "s_mul_i32 s50, s50, s27\n"
      "s_add_u32 s50, s50, s51\n"
      "s_min_u32 s34, s50, s26\n"
      "s_load_dwordx16 s[60:75], s[8:9], s34\n"
      "s_load_dwordx8 s[76:83], s[8:9], s34 offset:0x40\n"
      "s_load_dwordx2 s[84:85], s[8:9], s34 offset:0x60\n"
      "global_load_dwordx4 v[96:99], v80, s[16:17] offset:0\n"
      "global_load_dwordx4 v[100:103], v80, s[16:17] offset:1024\n"
      "v_mov_b64 v[24:25], s[20:21]\n"
      "s_mov_b32 exec_hi, 0\n"
      "v_mov_b64 v[24:25], s[18:19]\n"
      "s_mov_b32 exec_lo, 0xffff\n"
      "v_mov_b64 v[24:25], s[22:23]\n"
      "s_mov_b64 exec, -1\n"
      "v_add_co_u32 v24, s[28:29], v24, v13\n"
      "s_cmp_lt_u32 s53, 2\n"
      "s_cselect_b64 s[12:13], s[12:13], s[14:15]\n"
      "v_addc_co_u32 v25, s[28:29], 0, v25, s[28:29]\n"
      "global_load_dwordx4 v[192:195], v[24:25], off\n"
      "global_load_dwordx4 v[104:107], v81, s[16:17] offset:0\n"
      "global_load_dwordx4 v[108:111], v81, s[16:17] offset:1024\n"
      "global_load_dwordx4 v[112:115], v82, s[16:17] offset:0\n"
      "global_load_dwordx4 v[116:119], v82, s[16:17] offset:1024\n"
      "global_load_dwordx4 v[120:123], v26, s[16:17] offset:0\n"
      "global_load_dwordx4 v[124:127], v26, s[16:17] offset:1024\n"
      "s_cmp_lg_u32 s35, 0\n"
      "s_cbranch_scc1 21f\n"
      "s_barrier\n"
      "21:\n"
      "v_mov_b64 v[88:89], 0\n"
      "v_cndmask_b32 v84, 0, v222, vcc\n"
      "v_lshlrev_b32 v222, 4, v224\n"
      "v_lshlrev_b64 v[86:87], v222, v[84:85]\n"
      "s_mov_b32 s46, 0xd905\n"
      "v_lshlrev_b32 v222, 2, v4\n"
      "v_mov_b32 v84, 0\n"
      "v_bfe_u32 v222, s46, v222, 4\n"
      "v_lshl_add_u32 v91, v222, 6, v213\n"
      "v_lshlrev_b32 v227, 4, v227\n"
      "s_mov_b64 s[46:47], 0x3b80\n"
      "v_lshlrev_b32 v5, 5, v3\n"
      "v_lshl_add_u32 v213, v4, 2, v213\n"
      "v_lshl_add_u32 v5, v4, 3, v5\n"
      "v_lshlrev_b64 v[222:223], v227, s[46:47]\n"
      "s_movk_i32 s46, 0x4400\n"
      "v_accvgpr_write_b32 a216, v222\n"
      "v_lshlrev_b64 v[172:173], v227, s[46:47]\n"
      "s_lshl_b64 exec, -1, 48\n"
      "s_bitset1_b32 s46, 15\n"
      "v_lshlrev_b64 v[172:173], v227, s[46:47]\n"
      "s_mov_b64 exec, -1\n"
      "s_mov_b32 s49, 0x03000000\n"
      "v_accvgpr_write_b32 a217, v223\n"
      "s_movk_i32 s46, 288\n"
      "v_and_b32 v222, 7, %3\n"
      "v_bfe_u32 v223, %3, 3, 1\n"
      "v_mad_u32_u24 v201, v222, s46, v95\n"
      "v_mad_u32_u24 v203, v223, s46, v95\n"
      "s_movk_i32 s47, 320\n"
      "v_mad_u32_u24 v210, v3, s46, v95\n"
      "v_mad_u32_u24 v214, v223, s47, v95\n"
      "s_mov_b32 s59, 0\n"
      "s_mov_b32 s57, 0\n"
      "v_and_b32 v222, v3, v4\n"
      "v_lshlrev_b32 v223, 6, v3\n"
      "v_bfe_u32 v222, 6, v222, 1\n"
      "s_movk_i32 s46, 0x100\n"
      "v_lshl_add_u32 v44, v222, 8, v95\n"
      "v_bitop3_b32 v45, v44, v223, s46 bitop3:0x78\n"
      "v_bitop3_b32 v46, v44, v5, s46 bitop3:0x78\n"
      "v_bitop3_b32 v47, v45, v5, s46 bitop3:0x78\n"
      "s_lshr_b32 s33, s34, 2\n"
      "s_mul_i32 s33, s33, 0xc4ec4ec5\n"
      "s_mov_b32 s36, 0\n"
      "s_mov_b32 s37, 0x7fa5a5a5\n"
      "s_mov_b64 exec, 0\n"
      "s_cmp_eq_u32 s53, 0\n"
      "s_cbranch_scc1 40f\n"
      "s_cmp_eq_u32 s53, 1\n"
      "s_cbranch_scc1 41f\n"
      "s_cmp_eq_u32 s53, 2\n"
      "s_cbranch_scc1 42f\n"
      "s_branch 43f\n"
      "44:\n"
      "s_mov_b64 exec, -1\n"
      "s_waitcnt lgkmcnt(0)\n"
      "v_lshl_add_u32 v6, s60, 8, v13\n"
      "v_lshl_add_u32 v93, s64, 8, v13\n"
      "s_mov_b32 exec_lo, 0xffff0000\n"
      "v_lshl_add_u32 v6, s61, 8, v13\n"
      "v_lshl_add_u32 v93, s65, 8, v13\n"
      "s_mov_b32 exec_lo, 0\n"
      "v_lshl_add_u32 v6, s62, 8, v13\n"
      "v_lshl_add_u32 v93, s66, 8, v13\n"
      "s_mov_b32 exec_hi, 0xffff0000\n"
      "v_lshl_add_u32 v6, s63, 8, v13\n"
      "v_lshl_add_u32 v93, s67, 8, v13\n"
      "s_mov_b64 exec, -1\n"
      "global_load_dwordx4 v[228:231], v6, s[10:11] nt\n"
      "global_load_dwordx4 v[232:235], v93, s[10:11] nt\n"
      "v_lshl_add_u32 v8, s68, 8, v13\n"
      "v_lshl_add_u32 v9, s72, 8, v13\n"
      "v_lshl_add_u32 v10, s76, 8, v13\n"
      "v_lshl_add_u32 v11, s80, 8, v13\n"
      "v_lshl_add_u32 v12, s84, 8, v13\n"
      "s_mov_b32 exec_lo, 0xffff0000\n"
      "v_lshl_add_u32 v8, s69, 8, v13\n"
      "v_lshl_add_u32 v9, s73, 8, v13\n"
      "v_lshl_add_u32 v10, s77, 8, v13\n"
      "v_lshl_add_u32 v11, s81, 8, v13\n"
      "v_lshl_add_u32 v12, s85, 8, v13\n"
      "s_mov_b32 exec_lo, 0\n"
      "v_lshl_add_u32 v8, s70, 8, v13\n"
      "v_lshl_add_u32 v9, s74, 8, v13\n"
      "v_lshl_add_u32 v10, s78, 8, v13\n"
      "v_lshl_add_u32 v11, s82, 8, v13\n"
      "s_mov_b32 exec_hi, 0xffff0000\n"
      "v_lshl_add_u32 v8, s71, 8, v13\n"
      "v_lshl_add_u32 v9, s75, 8, v13\n"
      "v_lshl_add_u32 v10, s79, 8, v13\n"
      "v_lshl_add_u32 v11, s83, 8, v13\n"
      "s_mov_b64 exec, -1\n"
      "s_lshl_b32 s46, s53, 8\n"
      "s_lshl_b32 s47, s53, 5\n"
      "s_waitcnt vmcnt(15)\n"
      "v_add_u32_sdwa v15, s46, v22 dst_sel:DWORD dst_unused:UNUSED_PAD src0_sel:DWORD src1_sel:WORD_0\n"
      "v_add_u32_sdwa v92, s46, v22 dst_sel:DWORD dst_unused:UNUSED_PAD src0_sel:DWORD src1_sel:WORD_1\n"
      "v_add_u32_sdwa v7, s47, v23 dst_sel:DWORD dst_unused:UNUSED_PAD src0_sel:DWORD src1_sel:WORD_0\n"
      "v_add_u32_sdwa v202, s38, v23 dst_sel:DWORD dst_unused:UNUSED_PAD src0_sel:DWORD src1_sel:WORD_1\n"
      "s_cmp_lg_u32 s58, 0\n"
      "s_cbranch_scc1 70f\n"
      "71:\n"
      "s_waitcnt vmcnt(10)\n"
      "v_mfma_f32_16x16x32_bf16 v[128:131], v[86:89], v[96:99], 0\n"
      "s_waitcnt vmcnt(9)\n"
      "v_mfma_f32_16x16x32_bf16 v[132:135], v[86:89], v[100:103], 0\n"
      "v_mfma_f32_16x16x32_bf16 v[136:139], v[84:87], v[96:99], 0\n"
      "v_mfma_f32_16x16x32_bf16 v[140:143], v[84:87], v[100:103], 0\n"
      "s_waitcnt vmcnt(7)\n"
      "v_mfma_f32_16x16x32_bf16 v[144:147], v[86:89], v[104:107], 0\n"
      "s_nop 1\n"
      "v_cvt_pk_f16_f32 v164, v128, v129\n"
      "v_cvt_pk_f16_f32 v165, v130, v131\n"
      "s_waitcnt vmcnt(6)\n"
      "v_mfma_f32_16x16x32_bf16 v[148:151], v[86:89], v[108:111], 0\n"
      "v_cvt_pk_f16_f32 v166, v132, v133\n"
      "v_cvt_pk_f16_f32 v167, v134, v135\n"
      "v_mfma_f32_16x16x32_bf16 v[152:155], v[84:87], v[104:107], 0\n"
      "ds_write_b128 v15, v[164:167] offset:0\n"
      "v_cvt_pk_f16_f32 v168, v136, v137\n"
      "v_cvt_pk_f16_f32 v169, v138, v139\n"
      "v_mfma_f32_16x16x32_bf16 v[156:159], v[84:87], v[108:111], 0\n"
      "v_cvt_pk_f16_f32 v170, v140, v141\n"
      "v_cvt_pk_f16_f32 v171, v142, v143\n"
      "global_load_dwordx4 v[236:239], v8, s[10:11] nt\n"
      "s_waitcnt vmcnt(6)\n"
      "v_mfma_f32_16x16x32_bf16 v[128:131], v[86:89], v[112:115], 0\n"
      "ds_write_b128 v15, v[168:171] offset:18560\n"
      "v_cvt_pk_f16_f32 v164, v144, v145\n"
      "v_cvt_pk_f16_f32 v165, v146, v147\n"
      "s_waitcnt vmcnt(5)\n"
      "v_mfma_f32_16x16x32_bf16 v[132:135], v[86:89], v[116:119], 0\n"
      "v_cvt_pk_f16_f32 v166, v148, v149\n"
      "v_cvt_pk_f16_f32 v167, v150, v151\n"
      "v_mfma_f32_16x16x32_bf16 v[136:139], v[84:87], v[112:115], 0\n"
      "ds_write_b128 v15, v[164:167] offset:1152\n"
      "v_cvt_pk_f16_f32 v168, v152, v153\n"
      "v_cvt_pk_f16_f32 v169, v154, v155\n"
      "v_mfma_f32_16x16x32_bf16 v[140:143], v[84:87], v[116:119], 0\n"
      "v_cvt_pk_f16_f32 v170, v156, v157\n"
      "v_cvt_pk_f16_f32 v171, v158, v159\n"
      "s_waitcnt vmcnt(4)\n"
      "v_mfma_f32_16x16x32_bf16 v[144:147], v[86:89], v[120:123], 0\n"
      "ds_write_b128 v15, v[168:171] offset:19712\n"
      "v_cvt_pk_f16_f32 v164, v128, v129\n"
      "v_cvt_pk_f16_f32 v165, v130, v131\n"
      "s_waitcnt vmcnt(3)\n"
      "v_mfma_f32_16x16x32_bf16 v[148:151], v[86:89], v[124:127], 0\n"
      "v_cvt_pk_f16_f32 v166, v132, v133\n"
      "v_cvt_pk_f16_f32 v167, v134, v135\n"
      "v_mfma_f32_16x16x32_bf16 v[152:155], v[84:87], v[120:123], 0\n"
      "ds_write_b128 v15, v[164:167] offset:2304\n"
      "v_cvt_pk_f16_f32 v168, v136, v137\n"
      "v_cvt_pk_f16_f32 v169, v138, v139\n"
      "v_mfma_f32_16x16x32_bf16 v[156:159], v[84:87], v[124:127], 0\n"
      "v_cvt_pk_f16_f32 v170, v140, v141\n"
      "v_cvt_pk_f16_f32 v171, v142, v143\n"
      "ds_write_b128 v15, v[168:171] offset:20864\n"
      "v_mfma_f32_4x4x4_16b_bf16 v[176:179], v[172:173], v[192:193], 0\n"
      "v_cndmask_b32 v188, v192, v194, s[100:101]\n"
      "v_cvt_pk_f16_f32 v164, v144, v145\n"
      "v_cndmask_b32 v189, v193, v195, s[100:101]\n"
      "v_max3_f32 v222, |v192|, |v193|, |v195|\n"
      "v_cvt_pk_f16_f32 v165, v146, v147\n"
      "v_mfma_f32_4x4x4_16b_bf16 v[180:183], v[172:173], v[194:195], 0\n"
      "v_subrev_u32 v222, 0x3d800000, v222\n"
      "v_cvt_pk_f16_f32 v166, v148, v149\n"
      "v_mfma_f32_16x16x16_bf16 v[48:51], v[188:189], a[128:129], 0\n"
      "v_cmp_gt_u32 vcc, s49, v222\n"
      "v_cvt_pk_f16_f32 v167, v150, v151\n"
      "v_mfma_f32_16x16x16_bf16 v[52:55], v[188:189], a[130:131], 0\n"
      "v_cndmask_b32 v190, v194, v192, s[100:101]\n"
      "ds_write_b128 v15, v[164:167] offset:3456\n"
      "v_cndmask_b32 v191, v195, v193, s[100:101]\n"
      "v_mfma_f32_16x16x16_bf16 v[56:59], v[188:189], a[132:133], 0\n"
      "v_cvt_pk_f16_f32 v168, v152, v153\n"
      "s_and_b32 s44, vcc_lo, 0xffff\n"
      "s_cselect_b32 s44, vcc_hi, 0\n"
      "v_cvt_pk_f16_f32 v169, v154, v155\n"
      "v_mfma_f32_16x16x16_bf16 v[60:63], v[188:189], a[134:135], 0\n"
      "s_cmp_eq_u32 s44, 0\n"
      "v_cvt_pk_f16_f32 v170, v156, v157\n"
      "s_cbranch_scc1 22f\n"
      "23:\n"
      "v_mfma_f32_16x16x16_bf16 v[64:67], v[190:191], a[128:129], 0\n"
      "v_cvt_pk_f16_f32 v171, v158, v159\n"
      "v_cvt_pk_f16_f32 v184, v176, v177\n"
      "v_cvt_pk_f16_f32 v185, v178, v179\n"
      "ds_write_b128 v15, v[168:171] offset:22016\n"
      "v_mfma_f32_16x16x16_bf16 v[68:71], v[190:191], a[130:131], 0\n"
      "v_cvt_pk_f16_f32 v186, v180, v181\n"
      "ds_write_b32 v2, v2 offset:4\n"
      "v_cvt_pk_f16_f32 v187, v182, v183\n"
      "ds_write_b128 v91, v[184:187] offset:11904\n"
      "v_mfma_f32_16x16x16_bf16 v[72:75], v[190:191], a[132:133], 0\n"
      "v_mfma_f32_16x16x16_bf16 v[76:79], v[190:191], a[134:135], 0\n"
      "global_load_dwordx4 v[216:219], v215, s[12:13]\n"
      "v_mfma_f32_16x16x16_bf16 v[16:19], v[188:189], a[208:209], 0\n"
      "ds_read_b128 a[184:187], v44 offset:12480\n"
      "ds_read_b128 a[188:191], v45 offset:12544\n"
      "v_mfma_f32_16x16x16_bf16 v[20:23], v[188:189], a[210:211], 0\n"
      "ds_read_b128 a[192:195], v46 offset:12608\n"
      "ds_read_b128 a[196:199], v47 offset:12672\n"
      "v_mfma_f32_16x16x16_bf16 v[24:27], v[188:189], a[212:213], 0\n"
      "ds_read_b64_tr_b16 a[152:153], v92 offset:0\n"
      "ds_read_b64_tr_b16 a[154:155], v92 offset:18560\n"
      "ds_read_b64_tr_b16 a[156:157], v92 offset:64\n"
      "v_mfma_f32_16x16x16_bf16 v[28:31], v[188:189], a[214:215], 0\n"
      "ds_read_b64_tr_b16 a[158:159], v92 offset:18624\n"
      "ds_read_b64_tr_b16 a[160:161], v92 offset:128\n"
      "ds_read_b64_tr_b16 a[162:163], v92 offset:18688\n"
      "v_mfma_f32_16x16x16_bf16 v[32:35], v[190:191], a[208:209], 0\n"
      "ds_read_b64_tr_b16 a[164:165], v92 offset:192\n"
      "ds_read_b64_tr_b16 a[166:167], v92 offset:18752\n"
      "ds_read_b64_tr_b16 a[168:169], v92 offset:2304\n"
      "v_mfma_f32_16x16x16_bf16 v[36:39], v[190:191], a[210:211], 0\n"
      "ds_read_b64_tr_b16 a[170:171], v92 offset:20864\n"
      "ds_read_b64_tr_b16 a[172:173], v92 offset:2368\n"
      "ds_read_b64_tr_b16 a[174:175], v92 offset:20928\n"
      "v_mfma_f32_16x16x16_bf16 v[40:43], v[190:191], a[212:213], 0\n"
      "ds_read_b64_tr_b16 a[176:177], v92 offset:2432\n"
      "ds_read_b64_tr_b16 a[178:179], v92 offset:20992\n"
      "ds_read_b64_tr_b16 a[180:181], v92 offset:2496\n"
      "v_mfma_f32_16x16x16_bf16 v[44:47], v[190:191], a[214:215], 0\n"
      "ds_read_b64_tr_b16 a[182:183], v92 offset:21056\n"
      "ds_read_b32 v224, v227 offset:37636\n"
      "s_waitcnt lgkmcnt(15)\n"
      "v_mfma_f32_16x16x32_f16 v[192:195], a[184:187], a[152:155], 0\n"
      "v_and_b32 v208, 0xff, v211\n"
      "s_waitcnt lgkmcnt(13)\n"
      "v_mfma_f32_16x16x32_f16 v[192:195], a[188:191], a[156:159], v[192:195]\n"
      "v_bfe_u32 v209, v211, 8, 8\n"
      "s_waitcnt lgkmcnt(11)\n"
      "v_mfma_f32_16x16x32_f16 v[192:195], a[192:195], a[160:163], v[192:195]\n"
      "v_bfe_u32 v220, v211, 16, 8\n"
      "s_waitcnt lgkmcnt(9)\n"
      "v_mfma_f32_16x16x32_f16 v[192:195], a[196:199], a[164:167], v[192:195]\n"
      "v_lshrrev_b32 v221, 24, v211\n"
      "s_waitcnt lgkmcnt(0)\n"
      "v_cmp_eq_u32 vcc, 0, v224\n"
      "s_cbranch_vccz 13f\n"
      "11:\n"
      "ds_read_b32 v224, v227 offset:37636\n"
      "s_waitcnt lgkmcnt(0)\n"
      "v_cmp_eq_u32 vcc, 0, v224\n"
      "s_cbranch_vccnz 11b\n"
      "13:\n"
      "v_mfma_f32_16x16x32_f16 v[252:255], a[184:187], a[168:171], 0\n"
      "ds_read_b128 a[0:3], v14 offset:0\n"
      "ds_read_b128 a[4:7], v14 offset:1152\n"
      "v_mfma_f32_16x16x32_f16 v[252:255], a[188:191], a[172:175], v[252:255]\n"
      "ds_read_b128 a[8:11], v14 offset:2304\n"
      "ds_read_b128 a[12:15], v14 offset:3456\n"
      "v_mfma_f32_16x16x32_f16 v[252:255], a[192:195], a[176:179], v[252:255]\n"
      "ds_read_b128 a[16:19], v14 offset:4640\n"
      "ds_read_b128 a[20:23], v14 offset:5792\n"
      "v_mfma_f32_16x16x32_f16 v[252:255], a[196:199], a[180:183], v[252:255]\n"
      "ds_read_b128 a[24:27], v14 offset:6944\n"
      "ds_read_b128 a[28:31], v14 offset:8096\n"
      "s_cmp_lg_u32 s58, 0\n"
      "s_cbranch_scc1 72f\n"
      "73:\n"
      "global_load_dwordx4 v[240:243], v9, s[10:11] nt\n"
      "v_mov_b32 v3, s33\n"
      "global_load_ushort v250, v3, s[24:25]\n"
      "s_waitcnt vmcnt(5)\n"
      "v_mfma_f32_4x4x4_16b_bf16 v[176:179], a[216:217], v[228:229], 0\n"
      "v_mfma_f32_4x4x4_16b_bf16 v[180:183], a[216:217], v[230:231], 0\n"
      "ds_read_b128 a[32:35], v14 offset:9280\n"
      "s_waitcnt vmcnt(4)\n"
      "v_mfma_f32_4x4x4_16b_bf16 v[144:147], a[216:217], v[232:233], 0\n"
      "ds_read_b128 a[36:39], v14 offset:10432\n"
      "v_mfma_f32_4x4x4_16b_bf16 v[148:151], a[216:217], v[234:235], 0\n"
      "ds_read_b128 a[40:43], v14 offset:11584\n"
      "ds_read_b128 a[44:47], v14 offset:12736\n"
      "v_cvt_pk_f16_f32 v184, v176, v177\n"
      "v_cvt_pk_f16_f32 v185, v178, v179\n"
      "v_cvt_pk_f16_f32 v186, v180, v181\n"
      "v_cvt_pk_f16_f32 v187, v182, v183\n"
      "ds_write_b128 v1, v[184:187] offset:0\n"
      "v_cvt_pk_f16_f32 v152, v144, v145\n"
      "v_cvt_pk_f16_f32 v153, v146, v147\n"
      "v_cvt_pk_f16_f32 v154, v148, v149\n"
      "v_cvt_pk_f16_f32 v155, v150, v151\n"
      "ds_write_b128 v1, v[152:155] offset:1152\n"
      "ds_read_b128 v[128:131], v201 offset:0\n"
      "ds_read_b128 v[160:163], v202 offset:0\n"
      "ds_read_b128 v[132:135], v201 offset:64\n"
      "ds_read_b128 v[164:167], v202 offset:64\n"
      "ds_read_b128 v[136:139], v201 offset:128\n"
      "ds_read_b128 v[168:171], v202 offset:128\n"
      "ds_read_b128 v[140:143], v201 offset:192\n"
      "ds_read_b128 v[172:175], v202 offset:192\n"
      "s_waitcnt vmcnt(3)\n"
      "v_mfma_f32_4x4x4_16b_bf16 v[112:115], a[216:217], v[236:237], 0\n"
      "v_mfma_f32_4x4x4_16b_bf16 v[116:119], a[216:217], v[238:239], 0\n"
      "s_nop 3\n"
      "v_cvt_pk_f16_f32 v120, v112, v113\n"
      "v_cvt_pk_f16_f32 v121, v114, v115\n"
      "v_cvt_pk_f16_f32 v122, v116, v117\n"
      "v_cvt_pk_f16_f32 v123, v118, v119\n"
      "ds_write_b128 v1, v[120:123] offset:2304\n"
      "s_waitcnt lgkmcnt(7)\n"
      "v_pk_mul_f16 v96, v128, v160\n"
      "v_pk_mul_f16 v97, v129, v161\n"
      "v_pk_mul_f16 v98, v130, v162\n"
      "v_pk_mul_f16 v99, v131, v163\n"
      "s_waitcnt lgkmcnt(5)\n"
      "v_pk_mul_f16 v100, v132, v164\n"
      "v_pk_mul_f16 v101, v133, v165\n"
      "v_pk_mul_f16 v102, v134, v166\n"
      "v_pk_mul_f16 v103, v135, v167\n"
      "s_waitcnt lgkmcnt(3)\n"
      "v_pk_mul_f16 v104, v136, v168\n"
      "v_pk_mul_f16 v105, v137, v169\n"
      "v_pk_mul_f16 v106, v138, v170\n"
      "v_pk_mul_f16 v107, v139, v171\n"
      "3:\n"
      "v_mfma_f32_16x16x32_f16 v[80:83], a[0:3], v[96:99], v[16:19]\n"
      "s_waitcnt lgkmcnt(1)\n"
      "v_pk_mul_f16 v108, v140, v172\n"
      "v_pk_mul_f16 v109, v141, v173\n"
      "v_mfma_f32_16x16x32_f16 v[80:83], a[4:7], v[100:103], v[80:83]\n"
      "v_pk_mul_f16 v110, v142, v174\n"
      "v_pk_mul_f16 v111, v143, v175\n"
      "v_mfma_f32_16x16x32_f16 v[80:83], a[8:11], v[104:107], v[80:83]\n"
      "ds_read_b128 a[60:63], v14 offset:17376\n"
      "ds_read_b128 a[56:59], v14 offset:16224\n"
      "v_mfma_f32_16x16x32_f16 v[80:83], a[12:15], v[108:111], v[80:83]\n"
      "ds_read_b128 a[52:55], v14 offset:15072\n"
      "ds_read_b128 a[48:51], v14 offset:13920\n"
      "v_mfma_f32_16x16x32_f16 v[84:87], a[28:31], v[108:111], v[20:23]\n"
      "ds_read_b128 a[64:67], v212 offset:0\n"
      "ds_read_b128 v[144:147], v203 offset:2304\n"
      "v_mfma_f32_16x16x32_f16 v[84:87], a[24:27], v[104:107], v[84:87]\n"
      "ds_read_b128 a[68:71], v212 offset:1152\n"
      "ds_read_b128 a[72:75], v212 offset:2304\n"
      "v_mfma_f32_16x16x32_f16 v[84:87], a[20:23], v[100:103], v[84:87]\n"
      "ds_read_b128 a[76:79], v212 offset:3456\n"
      "ds_read_b128 a[92:95], v212 offset:8096\n"
      "v_mfma_f32_16x16x32_f16 v[84:87], a[16:19], v[96:99], v[84:87]\n"
      "ds_read_b128 a[88:91], v212 offset:6944\n"
      "ds_read_b128 v[148:151], v203 offset:2368\n"
      "v_mfma_f32_16x16x32_f16 v[88:91], a[32:35], v[96:99], v[24:27]\n"
      "ds_read_b128 a[84:87], v212 offset:5792\n"
      "ds_read_b128 a[80:83], v212 offset:4640\n"
      "v_mfma_f32_16x16x32_f16 v[88:91], a[36:39], v[100:103], v[88:91]\n"
      "v_fma_f32 v188, |v80|, v48, 4.0\n"
      "v_fma_f32 v188, |v81|, v49, v188\n"
      "v_mfma_f32_16x16x32_f16 v[88:91], a[40:43], v[104:107], v[88:91]\n"
      "ds_read_b128 a[96:99], v212 offset:9280\n"
      "v_fma_f32 v188, |v82|, v50, v188\n"
      "v_mfma_f32_16x16x32_f16 v[88:91], a[44:47], v[108:111], v[88:91]\n"
      "s_waitcnt lgkmcnt(13)\n"
      "v_fma_f32 v188, |v83|, v51, v188\n"
      "v_fma_f32 v188, |v84|, v52, v188\n"
      "v_mfma_f32_16x16x32_f16 v[92:95], a[60:63], v[108:111], v[28:31]\n"
      "s_waitcnt lgkmcnt(9)\n"
      "v_pk_mul_f16 v112, v128, v144\n"
      "v_pk_mul_f16 v113, v129, v145\n"
      "v_mfma_f32_16x16x32_f16 v[92:95], a[56:59], v[104:107], v[92:95]\n"
      "ds_read_b128 a[100:103], v212 offset:10432\n"
      "ds_read_b128 a[104:107], v212 offset:11584\n"
      "v_mfma_f32_16x16x32_f16 v[92:95], a[52:55], v[100:103], v[92:95]\n"
      "v_pk_mul_f16 v114, v130, v146\n"
      "v_pk_mul_f16 v115, v131, v147\n"
      "v_mfma_f32_16x16x32_f16 v[92:95], a[48:51], v[96:99], v[92:95]\n"
      "s_waitcnt lgkmcnt(10)\n"
      "ds_read_b128 v[152:155], v203 offset:2432\n"
      "ds_read_b128 a[108:111], v212 offset:12736\n"
      "v_mfma_f32_16x16x32_f16 v[80:83], a[64:67], v[96:99], v[32:35]\n"
      "ds_read_b128 a[124:127], v212 offset:17376\n"
      "v_fma_f32 v188, |v85|, v53, v188\n"
      "v_mfma_f32_16x16x32_f16 v[80:83], a[68:71], v[100:103], v[80:83]\n"
      "s_waitcnt lgkmcnt(11)\n"
      "v_fma_f32 v188, |v86|, v54, v188\n"
      "v_fma_f32 v188, |v87|, v55, v188\n"
      "v_mfma_f32_16x16x32_f16 v[80:83], a[72:75], v[104:107], v[80:83]\n"
      "s_waitcnt lgkmcnt(8)\n"
      "ds_read_b128 a[120:123], v212 offset:16224\n"
      "v_pk_mul_f16 v116, v132, v148\n"
      "v_mfma_f32_16x16x32_f16 v[80:83], a[76:79], v[108:111], v[80:83]\n"
      "ds_read_b128 a[116:119], v212 offset:15072\n"
      "v_pk_mul_f16 v117, v133, v149\n"
      "v_mfma_f32_16x16x32_f16 v[84:87], a[92:95], v[108:111], v[36:39]\n"
      "ds_read_b128 a[112:115], v212 offset:13920\n"
      "v_fma_f32 v188, |v88|, v56, v188\n"
      "v_mfma_f32_16x16x32_f16 v[84:87], a[88:91], v[104:107], v[84:87]\n"
      "s_waitcnt lgkmcnt(9)\n"
      "v_fma_f32 v188, |v89|, v57, v188\n"
      "v_pk_mul_f16 v118, v134, v150\n"
      "v_mfma_f32_16x16x32_f16 v[84:87], a[84:87], v[100:103], v[84:87]\n"
      "ds_read_b128 v[156:159], v203 offset:2496\n"
      "v_fma_f32 v188, |v90|, v58, v188\n"
      "v_mfma_f32_16x16x32_f16 v[84:87], a[80:83], v[96:99], v[84:87]\n"
      "s_waitcnt lgkmcnt(8)\n"
      "v_fma_f32 v188, |v91|, v59, v188\n"
      "v_pk_mul_f16 v119, v135, v151\n"
      "v_mfma_f32_16x16x32_f16 v[88:91], a[96:99], v[96:99], v[40:43]\n"
      "s_waitcnt lgkmcnt(6)\n"
      "v_pk_mul_f16 v120, v136, v152\n"
      "v_fma_f32 v188, |v92|, v60, v188\n"
      "v_mfma_f32_16x16x32_f16 v[88:91], a[100:103], v[100:103], v[88:91]\n"
      "s_waitcnt lgkmcnt(5)\n"
      "v_fma_f32 v188, |v93|, v61, v188\n"
      "v_pk_mul_f16 v121, v137, v153\n"
      "v_mfma_f32_16x16x32_f16 v[88:91], a[104:107], v[104:107], v[88:91]\n"
      "v_fma_f32 v188, |v94|, v62, v188\n"
      "v_fma_f32 v188, |v95|, v63, v188\n"
      "v_mfma_f32_16x16x32_f16 v[88:91], a[108:111], v[108:111], v[88:91]\n"
      "s_waitcnt lgkmcnt(3)\n"
      "v_pk_mul_f16 v122, v138, v154\n"
      "v_pk_mul_f16 v123, v139, v155\n"
      "v_mfma_f32_16x16x32_f16 v[92:95], a[124:127], v[108:111], v[44:47]\n"
      "global_load_dwordx4 v[244:247], v10, s[10:11] nt\n"
      "v_fma_f32 v188, |v80|, v64, v188\n"
      "v_mfma_f32_16x16x32_f16 v[92:95], a[120:123], v[104:107], v[92:95]\n"
      "s_waitcnt lgkmcnt(1)\n"
      "v_fma_f32 v188, |v81|, v65, v188\n"
      "v_fma_f32 v188, |v82|, v66, v188\n"
      "v_mfma_f32_16x16x32_f16 v[92:95], a[116:119], v[100:103], v[92:95]\n"
      "s_waitcnt lgkmcnt(0)\n"
      "v_pk_mul_f16 v124, v140, v156\n"
      "v_fma_f32 v188, |v83|, v67, v188\n"
      "v_mfma_f32_16x16x32_f16 v[92:95], a[112:115], v[96:99], v[92:95]\n"
      "v_pk_mul_f16 v125, v141, v157\n"
      "v_pk_mul_f16 v126, v142, v158\n"
      "v_mfma_f32_16x16x32_f16 v[80:83], a[0:3], v[112:115], v[16:19]\n"
      "ds_read_b128 v[160:163], v203 offset:2880\n"
      "v_pk_mul_f16 v127, v143, v159\n"
      "v_mfma_f32_16x16x32_f16 v[80:83], a[4:7], v[116:119], v[80:83]\n"
      "v_fma_f32 v188, |v84|, v68, v188\n"
      "v_fma_f32 v188, |v85|, v69, v188\n"
      "v_mfma_f32_16x16x32_f16 v[80:83], a[8:11], v[120:123], v[80:83]\n"
      "v_fma_f32 v188, |v86|, v70, v188\n"
      "v_fma_f32 v188, |v87|, v71, v188\n"
      "v_mfma_f32_16x16x32_f16 v[80:83], a[12:15], v[124:127], v[80:83]\n"
      "v_fma_f32 v188, |v88|, v72, v188\n"
      "v_fma_f32 v188, |v89|, v73, v188\n"
      "v_mfma_f32_16x16x32_f16 v[84:87], a[28:31], v[124:127], v[20:23]\n"
      "v_fma_f32 v188, |v90|, v74, v188\n"
      "v_fma_f32 v188, |v91|, v75, v188\n"
      "v_mfma_f32_16x16x32_f16 v[84:87], a[24:27], v[120:123], v[84:87]\n"
      "ds_read_b128 v[164:167], v203 offset:2944\n"
      "v_fma_f32 v188, |v92|, v76, v188\n"
      "v_mfma_f32_16x16x32_f16 v[84:87], a[20:23], v[116:119], v[84:87]\n"
      "v_fma_f32 v188, |v93|, v77, v188\n"
      "v_fma_f32 v188, |v94|, v78, v188\n"
      "v_mfma_f32_16x16x32_f16 v[84:87], a[16:19], v[112:115], v[84:87]\n"
      "v_fma_f32 v188, |v95|, v79, v188\n"
      "v_fma_f32 v189, |v80|, v48, 4.0\n"
      "v_mfma_f32_16x16x32_f16 v[88:91], a[32:35], v[112:115], v[24:27]\n"
      "s_waitcnt lgkmcnt(1)\n"
      "v_pk_mul_f16 v96, v128, v160\n"
      "v_pk_mul_f16 v97, v129, v161\n"
      "v_mfma_f32_16x16x32_f16 v[88:91], a[36:39], v[116:119], v[88:91]\n"
      "v_pk_mul_f16 v98, v130, v162\n"
      "v_pk_mul_f16 v99, v131, v163\n"
      "v_mfma_f32_16x16x32_f16 v[88:91], a[40:43], v[120:123], v[88:91]\n"
      "40:\n"
      "s_cbranch_execz 44b\n"
      "ds_read_b128 v[168:171], v203 offset:3008\n"
      "v_mfma_f32_16x16x32_f16 v[88:91], a[44:47], v[124:127], v[88:91]\n"
      "s_waitcnt vmcnt(2)\n"
      "v_fma_f32 v189, |v81|, v49, v189\n"
      "v_fma_f32 v189, |v82|, v50, v189\n"
      "v_mfma_f32_4x4x4_16b_bf16 v[176:179], a[216:217], v[240:241], 0\n"
      "v_fma_f32 v189, |v83|, v51, v189\n"
      "v_mfma_f32_4x4x4_16b_bf16 v[180:183], a[216:217], v[242:243], 0\n"
      "s_waitcnt lgkmcnt(1)\n"
      "v_pk_mul_f16 v100, v132, v164\n"
      "v_pk_mul_f16 v101, v133, v165\n"
      "v_mfma_f32_16x16x32_f16 v[92:95], a[60:63], v[124:127], v[28:31]\n"
      "v_pk_mul_f16 v102, v134, v166\n"
      "v_pk_mul_f16 v103, v135, v167\n"
      "v_mfma_f32_16x16x32_f16 v[92:95], a[56:59], v[120:123], v[92:95]\n"
      "v_fma_f32 v189, |v84|, v52, v189\n"
      "v_fma_f32 v189, |v85|, v53, v189\n"
      "v_mfma_f32_16x16x32_f16 v[92:95], a[52:55], v[116:119], v[92:95]\n"
      "v_fma_f32 v189, |v86|, v54, v189\n"
      "v_fma_f32 v189, |v87|, v55, v189\n"
      "v_mfma_f32_16x16x32_f16 v[92:95], a[48:51], v[112:115], v[92:95]\n"
      "v_fma_f32 v189, |v88|, v56, v189\n"
      "v_fma_f32 v189, |v89|, v57, v189\n"
      "v_mfma_f32_16x16x32_f16 v[80:83], a[64:67], v[112:115], v[32:35]\n"
      "s_waitcnt lgkmcnt(0)\n"
      "v_pk_mul_f16 v104, v136, v168\n"
      "v_pk_mul_f16 v105, v137, v169\n"
      "v_mfma_f32_16x16x32_f16 v[80:83], a[68:71], v[116:119], v[80:83]\n"
      "v_pk_mul_f16 v106, v138, v170\n"
      "v_pk_mul_f16 v107, v139, v171\n"
      "v_mfma_f32_16x16x32_f16 v[80:83], a[72:75], v[120:123], v[80:83]\n"
      "ds_read_b128 v[172:175], v203 offset:3072\n"
      "v_fma_f32 v189, |v90|, v58, v189\n"
      "v_mfma_f32_16x16x32_f16 v[80:83], a[76:79], v[124:127], v[80:83]\n"
      "v_fma_f32 v189, |v91|, v59, v189\n"
      "v_fma_f32 v189, |v92|, v60, v189\n"
      "v_mfma_f32_16x16x32_f16 v[84:87], a[92:95], v[124:127], v[36:39]\n"
      "v_cvt_pk_f16_f32 v184, v176, v177\n"
      "v_cvt_pk_f16_f32 v185, v178, v179\n"
      "v_mfma_f32_16x16x32_f16 v[84:87], a[88:91], v[120:123], v[84:87]\n"
      "v_fma_f32 v189, |v93|, v61, v189\n"
      "v_fma_f32 v189, |v94|, v62, v189\n"
      "v_mfma_f32_16x16x32_f16 v[84:87], a[84:87], v[116:119], v[84:87]\n"
      "v_fma_f32 v189, |v95|, v63, v189\n"
      "v_fma_f32 v189, |v80|, v64, v189\n"
      "v_mfma_f32_16x16x32_f16 v[84:87], a[80:83], v[112:115], v[84:87]\n"
      "v_cvt_pk_f16_f32 v186, v180, v181\n"
      "v_cvt_pk_f16_f32 v187, v182, v183\n"
      "v_mfma_f32_16x16x32_f16 v[88:91], a[96:99], v[112:115], v[40:43]\n"
      "ds_write_b128 v1, v[184:187] offset:3456\n"
      "v_mfma_f32_16x16x32_f16 v[88:91], a[100:103], v[116:119], v[88:91]\n"
      "v_fma_f32 v189, |v81|, v65, v189\n"
      "v_fma_f32 v189, |v82|, v66, v189\n"
      "v_mfma_f32_16x16x32_f16 v[88:91], a[104:107], v[120:123], v[88:91]\n"
      "s_waitcnt lgkmcnt(1)\n"
      "v_pk_mul_f16 v108, v140, v172\n"
      "v_pk_mul_f16 v109, v141, v173\n"
      "v_mfma_f32_16x16x32_f16 v[88:91], a[108:111], v[124:127], v[88:91]\n"
      "v_fma_f32 v189, |v83|, v67, v189\n"
      "v_fma_f32 v189, |v84|, v68, v189\n"
      "v_mfma_f32_16x16x32_f16 v[92:95], a[124:127], v[124:127], v[44:47]\n"
      "v_pk_mul_f16 v110, v142, v174\n"
      "v_pk_mul_f16 v111, v143, v175\n"
      "v_mfma_f32_16x16x32_f16 v[92:95], a[120:123], v[120:123], v[92:95]\n"
      "ds_read_b128 v[160:163], v203 offset:3456\n"
      "v_fma_f32 v189, |v85|, v69, v189\n"
      "v_mfma_f32_16x16x32_f16 v[92:95], a[116:119], v[116:119], v[92:95]\n"
      "v_fma_f32 v189, |v86|, v70, v189\n"
      "v_fma_f32 v189, |v87|, v71, v189\n"
      "v_mfma_f32_16x16x32_f16 v[92:95], a[112:115], v[112:115], v[92:95]\n"
      "v_fma_f32 v189, |v88|, v72, v189\n"
      "v_fma_f32 v189, |v89|, v73, v189\n"
      "v_mfma_f32_16x16x32_f16 v[80:83], a[0:3], v[96:99], v[16:19]\n"
      "v_fma_f32 v189, |v90|, v74, v189\n"
      "v_fma_f32 v189, |v91|, v75, v189\n"
      "v_mfma_f32_16x16x32_f16 v[80:83], a[4:7], v[100:103], v[80:83]\n"
      "ds_read_b128 v[164:167], v203 offset:3520\n"
      "ds_read_b128 v[168:171], v203 offset:3584\n"
      "v_mfma_f32_16x16x32_f16 v[80:83], a[8:11], v[104:107], v[80:83]\n"
      "v_fma_f32 v189, |v92|, v76, v189\n"
      "v_fma_f32 v189, |v93|, v77, v189\n"
      "v_mfma_f32_16x16x32_f16 v[80:83], a[12:15], v[108:111], v[80:83]\n"
      "v_fma_f32 v189, |v94|, v78, v189\n"
      "v_fma_f32 v189, |v95|, v79, v189\n"
      "v_mfma_f32_16x16x32_f16 v[84:87], a[28:31], v[108:111], v[20:23]\n"
      "ds_write_b32 v7, v252 offset:37376\n"
      "v_mfma_f32_16x16x32_f16 v[84:87], a[24:27], v[104:107], v[84:87]\n"
      "s_waitcnt lgkmcnt(3)\n"
      "v_pk_mul_f16 v112, v128, v160\n"
      "v_pk_mul_f16 v113, v129, v161\n"
      "v_mfma_f32_16x16x32_f16 v[84:87], a[20:23], v[100:103], v[84:87]\n"
      "ds_write_b32 v7, v192 offset:37120\n"
      "v_mfma_f32_16x16x32_f16 v[84:87], a[16:19], v[96:99], v[84:87]\n"
      "v_pk_mul_f16 v114, v130, v162\n"
      "v_pk_mul_f16 v115, v131, v163\n"
      "v_mfma_f32_16x16x32_f16 v[88:91], a[32:35], v[96:99], v[24:27]\n"
      "v_fma_f32 v190, |v80|, v48, 4.0\n"
      "v_fma_f32 v190, |v81|, v49, v190\n"
      "v_mfma_f32_16x16x32_f16 v[88:91], a[36:39], v[100:103], v[88:91]\n"
      "v_fma_f32 v190, |v82|, v50, v190\n"
      "v_fma_f32 v190, |v83|, v51, v190\n"
      "v_mfma_f32_16x16x32_f16 v[88:91], a[40:43], v[104:107], v[88:91]\n"
      "s_waitcnt lgkmcnt(3)\n"
      "v_pk_mul_f16 v116, v132, v164\n"
      "v_pk_mul_f16 v117, v133, v165\n"
      "v_mfma_f32_16x16x32_f16 v[88:91], a[44:47], v[108:111], v[88:91]\n"
      "v_pk_mul_f16 v118, v134, v166\n"
      "v_pk_mul_f16 v119, v135, v167\n"
      "v_mfma_f32_16x16x32_f16 v[92:95], a[60:63], v[108:111], v[28:31]\n"
      "v_fma_f32 v190, |v84|, v52, v190\n"
      "v_fma_f32 v190, |v85|, v53, v190\n"
      "v_mfma_f32_16x16x32_f16 v[92:95], a[56:59], v[104:107], v[92:95]\n"
      "v_fma_f32 v190, |v86|, v54, v190\n"
      "v_fma_f32 v190, |v87|, v55, v190\n"
      "v_mfma_f32_16x16x32_f16 v[92:95], a[52:55], v[100:103], v[92:95]\n"
      "v_max3_f32 v195, |v193|, |v194|, |v195|\n"
      "v_max3_f32 v194, |v253|, |v254|, |v255|\n"
      "v_mfma_f32_16x16x32_f16 v[92:95], a[48:51], v[96:99], v[92:95]\n"
      "s_waitcnt lgkmcnt(2)\n"
      "v_pk_mul_f16 v120, v136, v168\n"
      "v_pk_mul_f16 v121, v137, v169\n"
      "v_mfma_f32_16x16x32_f16 v[80:83], a[64:67], v[96:99], v[32:35]\n"
      "v_fma_f32 v190, |v88|, v56, v190\n"
      "v_fma_f32 v190, |v89|, v57, v190\n"
      "v_mfma_f32_16x16x32_f16 v[80:83], a[68:71], v[100:103], v[80:83]\n"
      "ds_read_b128 v[172:175], v203 offset:3648\n"
      "v_fma_f32 v190, |v90|, v58, v190\n"
      "v_mfma_f32_16x16x32_f16 v[80:83], a[72:75], v[104:107], v[80:83]\n"
      "v_fma_f32 v190, |v91|, v59, v190\n"
      "v_fma_f32 v190, |v92|, v60, v190\n"
      "v_mfma_f32_16x16x32_f16 v[80:83], a[76:79], v[108:111], v[80:83]\n"
      "v_max_f32 v194, |v252|, v194\n"
      "v_max_f32 v195, |v192|, v195\n"
      "v_mfma_f32_16x16x32_f16 v[84:87], a[92:95], v[108:111], v[36:39]\n"
      "v_pk_mul_f16 v122, v138, v170\n"
      "v_pk_mul_f16 v123, v139, v171\n"
      "v_mfma_f32_16x16x32_f16 v[84:87], a[88:91], v[104:107], v[84:87]\n"
      "s_waitcnt vmcnt(1)\n"
      "global_load_dwordx4 v[248:251], v11, s[10:11] nt\n"
      "v_cmp_eq_u64 vcc, v[216:217], v[208:209]\n"
      "v_mfma_f32_16x16x32_f16 v[84:87], a[84:87], v[100:103], v[84:87]\n"
      "v_fma_f32 v190, |v93|, v61, v190\n"
      "v_fma_f32 v190, |v94|, v62, v190\n"
      "v_mfma_f32_16x16x32_f16 v[84:87], a[80:83], v[96:99], v[84:87]\n"
      "v_cmp_eq_u64 s[28:29], v[218:219], v[220:221]\n"
      "v_fma_f32 v190, |v95|, v63, v190\n"
      "v_mfma_f32_16x16x32_f16 v[88:91], a[96:99], v[96:99], v[40:43]\n"
      "v_fma_f32 v190, |v80|, v64, v190\n"
      "v_fma_f32 v190, |v81|, v65, v190\n"
      "v_mfma_f32_16x16x32_f16 v[88:91], a[100:103], v[100:103], v[88:91]\n"
      "s_waitcnt lgkmcnt(0)\n"
      "v_pk_mul_f16 v124, v140, v172\n"
      "v_pk_mul_f16 v125, v141, v173\n"
      "v_mfma_f32_16x16x32_f16 v[88:91], a[104:107], v[104:107], v[88:91]\n"
      "s_and_b64 vcc, vcc, s[28:29]\n"
      "v_fma_f32 v190, |v82|, v66, v190\n"
      "v_fma_f32 v190, |v83|, v67, v190\n"
      "v_mfma_f32_16x16x32_f16 v[88:91], a[108:111], v[108:111], v[88:91]\n"
      "s_cmp_eq_u64 vcc, -1\n"
      "v_pk_mul_f16 v126, v142, v174\n"
      "v_pk_mul_f16 v127, v143, v175\n"
      "v_mfma_f32_16x16x32_f16 v[92:95], a[124:127], v[108:111], v[44:47]\n"
      "s_cselect_b32 s52, 13, 1\n"
      "ds_read_b128 v[160:163], v203 offset:4032\n"
      "v_fma_f32 v190, |v84|, v68, v190\n"
      "v_mfma_f32_16x16x32_f16 v[92:95], a[120:123], v[104:107], v[92:95]\n"
      "v_fma_f32 v190, |v85|, v69, v190\n"
      "v_fma_f32 v190, |v86|, v70, v190\n"
      "v_mfma_f32_16x16x32_f16 v[92:95], a[116:119], v[100:103], v[92:95]\n"
      "global_load_dwordx4 v[252:255], v12, s[10:11] nt\n"
      "v_cmp_le_f32 vcc, 0x3ca3d70a, v194\n"
      "v_mfma_f32_16x16x32_f16 v[92:95], a[112:115], v[96:99], v[92:95]\n"
      "v_fma_f32 v190, |v87|, v71, v190\n"
      "v_fma_f32 v190, |v88|, v72, v190\n"
      "v_mfma_f32_16x16x32_f16 v[80:83], a[0:3], v[112:115], v[16:19]\n"
      "s_or_b32 s44, vcc_lo, vcc_hi\n"
      "v_fma_f32 v190, |v89|, v73, v190\n"
      "v_fma_f32 v190, |v90|, v74, v190\n"
      "v_mfma_f32_16x16x32_f16 v[80:83], a[4:7], v[116:119], v[80:83]\n"
      "v_cmp_le_f32 vcc, 0x3ca3d70a, v195\n"
      "v_max_f32 v195, v195, v194\n"
      "v_mfma_f32_16x16x32_f16 v[80:83], a[8:11], v[120:123], v[80:83]\n"
      "v_fma_f32 v190, |v91|, v75, v190\n"
      "v_fma_f32 v190, |v92|, v76, v190\n"
      "v_mfma_f32_16x16x32_f16 v[80:83], a[12:15], v[124:127], v[80:83]\n"
      "s_or_b32 s45, vcc_lo, vcc_hi\n"
      "ds_read_b128 v[164:167], v203 offset:4096\n"
      "v_fma_f32 v190, |v93|, v77, v190\n"
      "v_mfma_f32_16x16x32_f16 v[84:87], a[28:31], v[124:127], v[20:23]\n"
      "s_waitcnt lgkmcnt(1)\n"
      "v_cmp_le_f32 vcc, 0x3e800000, v195\n"
      "v_pk_mul_f16 v96, v128, v160\n"
      "v_mfma_f32_16x16x32_f16 v[84:87], a[24:27], v[120:123], v[84:87]\n"
      "s_pack_ll_b32_b16 s46, s44, s45\n"
      "v_pk_mul_f16 v97, v129, v161\n"
      "v_pk_mul_f16 v98, v130, v162\n"
      "v_mfma_f32_16x16x32_f16 v[84:87], a[20:23], v[116:119], v[84:87]\n"
      "s_pack_hh_b32_b16 s44, s44, s45\n"
      "v_fma_f32 v190, |v94|, v78, v190\n"
      "v_fma_f32 v190, |v95|, v79, v190\n"
      "v_mfma_f32_16x16x32_f16 v[84:87], a[16:19], v[112:115], v[84:87]\n"
      "s_or_b32 s44, s44, s46\n"
      "ds_read_b128 v[168:171], v203 offset:4160\n"
      "v_pk_mul_f16 v99, v131, v163\n"
      "v_mfma_f32_16x16x32_f16 v[88:91], a[32:35], v[112:115], v[24:27]\n"
      "s_cmp_eq_u64 vcc, 0\n"
      "v_fma_f32 v191, |v80|, v48, 4.0\n"
      "v_fma_f32 v191, |v81|, v49, v191\n"
      "v_mfma_f32_16x16x32_f16 v[88:91], a[36:39], v[116:119], v[88:91]\n"
      "s_cselect_b32 s45, 13, 9\n"
      "v_fma_f32 v191, |v82|, v50, v191\n"
      "v_fma_f32 v191, |v83|, v51, v191\n"
      "v_mfma_f32_16x16x32_f16 v[88:91], a[40:43], v[120:123], v[88:91]\n"
      "s_cmp_eq_u32 s44, -1\n"
      "v_fma_f32 v191, |v84|, v52, v191\n"
      "v_fma_f32 v191, |v85|, v53, v191\n"
      "v_mfma_f32_16x16x32_f16 v[88:91], a[44:47], v[124:127], v[88:91]\n"
      "s_waitcnt lgkmcnt(1)\n"
      "v_pk_mul_f16 v100, v132, v164\n"
      "v_pk_mul_f16 v101, v133, v165\n"
      "v_mfma_f32_16x16x32_f16 v[92:95], a[60:63], v[124:127], v[28:31]\n"
      "s_cselect_b32 s44, s45, 5\n"
      "v_pk_mul_f16 v102, v134, v166\n"
      "v_pk_mul_f16 v103, v135, v167\n"
      "v_mfma_f32_16x16x32_f16 v[92:95], a[56:59], v[120:123], v[92:95]\n"
      "s_min_u32 s44, s44, s52\n"
      "v_fma_f32 v191, |v86|, v54, v191\n"
      "v_fma_f32 v191, |v87|, v55, v191\n"
      "v_mfma_f32_16x16x32_f16 v[92:95], a[52:55], v[116:119], v[92:95]\n"
      "s_movk_i32 s31, 0x1c00\n"
      "v_mov_b32 v223, s44\n"
      "v_fma_f32 v191, |v88|, v56, v191\n"
      "v_mfma_f32_16x16x32_f16 v[92:95], a[48:51], v[112:115], v[92:95]\n"
      "s_mov_b32 s35, 1\n"
      "ds_write_b32 v2, v223\n"
      "v_mfma_f32_16x16x32_f16 v[80:83], a[64:67], v[112:115], v[32:35]\n"
      "s_waitcnt lgkmcnt(1)\n"
      "v_pk_mul_f16 v104, v136, v168\n"
      "v_pk_mul_f16 v105, v137, v169\n"
      "v_mfma_f32_16x16x32_f16 v[80:83], a[68:71], v[116:119], v[80:83]\n"
      "v_fma_f32 v191, |v89|, v57, v191\n"
      "v_fma_f32 v191, |v90|, v58, v191\n"
      "v_mfma_f32_16x16x32_f16 v[80:83], a[72:75], v[120:123], v[80:83]\n"
      "ds_read_b128 v[172:175], v203 offset:4224\n"
      "v_fma_f32 v191, |v91|, v59, v191\n"
      "v_mfma_f32_16x16x32_f16 v[80:83], a[76:79], v[124:127], v[80:83]\n"
      "v_pk_mul_f16 v106, v138, v170\n"
      "v_pk_mul_f16 v107, v139, v171\n"
      "v_mfma_f32_16x16x32_f16 v[84:87], a[92:95], v[124:127], v[36:39]\n"
      "v_fma_f32 v191, |v92|, v60, v191\n"
      "v_fma_f32 v191, |v93|, v61, v191\n"
      "v_mfma_f32_16x16x32_f16 v[84:87], a[88:91], v[120:123], v[84:87]\n"
      "v_fma_f32 v191, |v94|, v62, v191\n"
      "v_fma_f32 v191, |v95|, v63, v191\n"
      "v_mfma_f32_16x16x32_f16 v[84:87], a[84:87], v[116:119], v[84:87]\n"
      "v_fma_f32 v191, |v80|, v64, v191\n"
      "v_fma_f32 v191, |v81|, v65, v191\n"
      "v_mfma_f32_16x16x32_f16 v[84:87], a[80:83], v[112:115], v[84:87]\n"
      "v_fma_f32 v191, |v82|, v66, v191\n"
      "v_fma_f32 v191, |v83|, v67, v191\n"
      "v_mfma_f32_16x16x32_f16 v[88:91], a[96:99], v[112:115], v[40:43]\n"
      "ds_read_b128 v[144:147], v201 offset:2304\n"
      "ds_read_b128 v[160:163], v202 offset:2304\n"
      "v_mfma_f32_16x16x32_f16 v[88:91], a[100:103], v[116:119], v[88:91]\n"
      "ds_read_b128 v[148:151], v201 offset:2368\n"
      "ds_read_b128 v[152:155], v201 offset:2432\n"
      "v_mfma_f32_16x16x32_f16 v[88:91], a[104:107], v[120:123], v[88:91]\n"
      "s_waitcnt lgkmcnt(4)\n"
      "v_pk_mul_f16 v108, v140, v172\n"
      "v_pk_mul_f16 v109, v141, v173\n"
      "v_mfma_f32_16x16x32_f16 v[88:91], a[108:111], v[124:127], v[88:91]\n"
      "v_pk_mul_f16 v110, v142, v174\n"
      "v_pk_mul_f16 v111, v143, v175\n"
      "v_mfma_f32_16x16x32_f16 v[92:95], a[124:127], v[124:127], v[44:47]\n"
      "v_fma_f32 v191, |v84|, v68, v191\n"
      "v_fma_f32 v191, |v85|, v69, v191\n"
      "v_mfma_f32_16x16x32_f16 v[92:95], a[120:123], v[120:123], v[92:95]\n"
      "v_fma_f32 v191, |v86|, v70, v191\n"
      "v_fma_f32 v191, |v87|, v71, v191\n"
      "v_mfma_f32_16x16x32_f16 v[92:95], a[116:119], v[116:119], v[92:95]\n"
      "v_fma_f32 v191, |v88|, v72, v191\n"
      "v_fma_f32 v191, |v89|, v73, v191\n"
      "v_mfma_f32_16x16x32_f16 v[92:95], a[112:115], v[112:115], v[92:95]\n"
      "v_fma_f32 v191, |v90|, v74, v191\n"
      "v_fma_f32 v191, |v91|, v75, v191\n"
      "v_mfma_f32_16x16x32_f16 v[80:83], a[0:3], v[96:99], v[16:19]\n"
      "s_waitcnt lgkmcnt(2)\n"
      "ds_read_b128 v[164:167], v202 offset:2368\n"
      "v_pk_mul_f16 v112, v144, v160\n"
      "v_mfma_f32_16x16x32_f16 v[80:83], a[4:7], v[100:103], v[80:83]\n"
      "v_pk_mul_f16 v113, v145, v161\n"
      "v_pk_mul_f16 v114, v146, v162\n"
      "v_mfma_f32_16x16x32_f16 v[80:83], a[8:11], v[104:107], v[80:83]\n"
      "v_fma_f32 v191, |v92|, v76, v191\n"
      "v_fma_f32 v191, |v93|, v77, v191\n"
      "v_mfma_f32_16x16x32_f16 v[80:83], a[12:15], v[108:111], v[80:83]\n"
      "v_fma_f32 v191, |v94|, v78, v191\n"
      "v_fma_f32 v191, |v95|, v79, v191\n"
      "v_mfma_f32_16x16x32_f16 v[84:87], a[28:31], v[108:111], v[20:23]\n"
      "s_waitcnt vmcnt(2)\n"
      "v_pk_mul_f16 v115, v147, v163\n"
      "v_lshlrev_b32 v176, 16, v244\n"
      "v_mfma_f32_16x16x32_f16 v[84:87], a[24:27], v[104:107], v[84:87]\n"
      "v_permlane32_swap_b32 v189, v191\n"
      "v_mfma_f32_16x16x32_f16 v[84:87], a[20:23], v[100:103], v[84:87]\n"
      "ds_read_b128 v[168:171], v202 offset:2432\n"
      "v_fma_f32 v192, |v80|, v48, 4.0\n"
      "v_mfma_f32_16x16x32_f16 v[84:87], a[16:19], v[96:99], v[84:87]\n"
      "v_fma_f32 v192, |v81|, v49, v192\n"
      "v_fma_f32 v192, |v82|, v50, v192\n"
      "v_mfma_f32_16x16x32_f16 v[88:91], a[32:35], v[96:99], v[24:27]\n"
      "v_and_b32 v177, s43, v244\n"
      "v_fma_f32 v192, |v83|, v51, v192\n"
      "v_mfma_f32_16x16x32_f16 v[88:91], a[36:39], v[100:103], v[88:91]\n"
      "s_waitcnt lgkmcnt(1)\n"
      "v_lshlrev_b32 v178, 16, v245\n"
      "v_pk_mul_f16 v116, v148, v164\n"
      "v_mfma_f32_16x16x32_f16 v[88:91], a[40:43], v[104:107], v[88:91]\n"
      "v_and_b32 v179, s43, v245\n"
      "v_pk_mul_f16 v117, v149, v165\n"
      "v_mfma_f32_16x16x32_f16 v[88:91], a[44:47], v[108:111], v[88:91]\n"
      "ds_read_b128 v[156:159], v201 offset:2496\n"
      "v_lshlrev_b32 v180, 16, v246\n"
      "v_mfma_f32_16x16x32_f16 v[92:95], a[60:63], v[108:111], v[28:31]\n"
      "v_pk_mul_f16 v118, v150, v166\n"
      "v_pk_mul_f16 v119, v151, v167\n"
      "v_mfma_f32_16x16x32_f16 v[92:95], a[56:59], v[104:107], v[92:95]\n"
      "v_fma_f32 v192, |v84|, v52, v192\n"
      "v_fma_f32 v192, |v85|, v53, v192\n"
      "v_mfma_f32_16x16x32_f16 v[92:95], a[52:55], v[100:103], v[92:95]\n"
      "v_and_b32 v181, s43, v246\n"
      "v_fma_f32 v192, |v86|, v54, v192\n"
      "v_mfma_f32_16x16x32_f16 v[92:95], a[48:51], v[96:99], v[92:95]\n"
      "v_fma_f32 v192, |v87|, v55, v192\n"
      "v_fma_f32 v192, |v88|, v56, v192\n"
      "v_mfma_f32_16x16x32_f16 v[80:83], a[64:67], v[96:99], v[32:35]\n"
      "v_lshlrev_b32 v182, 16, v247\n"
      "v_and_b32 v183, s43, v247\n"
      "v_mfma_f32_16x16x32_f16 v[80:83], a[68:71], v[100:103], v[80:83]\n"
      "v_cvt_pk_f16_f32 v184, v176, v177\n"
      "v_cvt_pk_f16_f32 v185, v178, v179\n"
      "v_mfma_f32_16x16x32_f16 v[80:83], a[72:75], v[104:107], v[80:83]\n"
      "s_waitcnt lgkmcnt(1)\n"
      "v_pk_mul_f16 v120, v152, v168\n"
      "v_pk_mul_f16 v121, v153, v169\n"
      "v_mfma_f32_16x16x32_f16 v[80:83], a[76:79], v[108:111], v[80:83]\n"
      "v_fma_f32 v192, |v89|, v57, v192\n"
      "v_fma_f32 v192, |v90|, v58, v192\n"
      "v_mfma_f32_16x16x32_f16 v[84:87], a[92:95], v[108:111], v[36:39]\n"
      "ds_read_b128 v[172:175], v202 offset:2496\n"
      "v_fma_f32 v192, |v91|, v59, v192\n"
      "v_mfma_f32_16x16x32_f16 v[84:87], a[88:91], v[104:107], v[84:87]\n"
      "v_cvt_pk_f16_f32 v186, v180, v181\n"
      "v_pk_mul_f16 v122, v154, v170\n"
      "v_mfma_f32_16x16x32_f16 v[84:87], a[84:87], v[100:103], v[84:87]\n"
      "v_cvt_pk_f16_f32 v187, v182, v183\n"
      "v_pk_mul_f16 v123, v155, v171\n"
      "v_mfma_f32_16x16x32_f16 v[84:87], a[80:83], v[96:99], v[84:87]\n"
      "v_pk_mul_f16 v184, v184, s31 op_sel_hi:[1,0]\n"
      "v_pk_mul_f16 v185, v185, s31 op_sel_hi:[1,0]\n"
      "v_mfma_f32_16x16x32_f16 v[88:91], a[96:99], v[96:99], v[40:43]\n"
      "v_fma_f32 v192, |v92|, v60, v192\n"
      "v_fma_f32 v192, |v93|, v61, v192\n"
      "v_mfma_f32_16x16x32_f16 v[88:91], a[100:103], v[100:103], v[88:91]\n"
      "v_fma_f32 v192, |v94|, v62, v192\n"
      "v_fma_f32 v192, |v95|, v63, v192\n"
      "v_mfma_f32_16x16x32_f16 v[88:91], a[104:107], v[104:107], v[88:91]\n"
      "v_pk_mul_f16 v186, v186, s31 op_sel_hi:[1,0]\n"
      "v_pk_mul_f16 v187, v187, s31 op_sel_hi:[1,0]\n"
      "v_mfma_f32_16x16x32_f16 v[88:91], a[108:111], v[108:111], v[88:91]\n"
      "ds_write_b128 v1, v[184:187] offset:4608\n"
      "v_mfma_f32_16x16x32_f16 v[92:95], a[124:127], v[108:111], v[44:47]\n"
      "v_fma_f32 v192, |v80|, v64, v192\n"
      "v_fma_f32 v192, |v81|, v65, v192\n"
      "v_mfma_f32_16x16x32_f16 v[92:95], a[120:123], v[104:107], v[92:95]\n"
      "s_waitcnt lgkmcnt(1)\n"
      "v_pk_mul_f16 v124, v156, v172\n"
      "v_fma_f32 v192, |v82|, v66, v192\n"
      "v_mfma_f32_16x16x32_f16 v[92:95], a[116:119], v[100:103], v[92:95]\n"
      "v_fma_f32 v192, |v83|, v67, v192\n"
      "v_pk_mul_f16 v125, v157, v173\n"
      "v_mfma_f32_16x16x32_f16 v[92:95], a[112:115], v[96:99], v[92:95]\n"
      "v_pk_mul_f16 v126, v158, v174\n"
      "v_pk_mul_f16 v127, v159, v175\n"
      "v_mfma_f32_16x16x32_f16 v[80:83], a[0:3], v[112:115], v[16:19]\n"
      "ds_read_b128 v[160:163], v203 offset:4608\n"
      "v_fma_f32 v192, |v84|, v68, v192\n"
      "v_mfma_f32_16x16x32_f16 v[80:83], a[4:7], v[116:119], v[80:83]\n"
      "v_fma_f32 v192, |v85|, v69, v192\n"
      "v_fma_f32 v192, |v86|, v70, v192\n"
      "v_mfma_f32_16x16x32_f16 v[80:83], a[8:11], v[120:123], v[80:83]\n"
      "v_fma_f32 v192, |v87|, v71, v192\n"
      "v_fma_f32 v192, |v88|, v72, v192\n"
      "v_mfma_f32_16x16x32_f16 v[80:83], a[12:15], v[124:127], v[80:83]\n"
      "v_fma_f32 v192, |v89|, v73, v192\n"
      "v_fma_f32 v192, |v90|, v74, v192\n"
      "v_mfma_f32_16x16x32_f16 v[84:87], a[28:31], v[124:127], v[20:23]\n"
      "v_fma_f32 v192, |v91|, v75, v192\n"
      "v_fma_f32 v192, |v92|, v76, v192\n"
      "v_mfma_f32_16x16x32_f16 v[84:87], a[24:27], v[120:123], v[84:87]\n"
      "ds_read_b128 v[164:167], v203 offset:4672\n"
      "v_fma_f32 v192, |v93|, v77, v192\n"
      "v_mfma_f32_16x16x32_f16 v[84:87], a[20:23], v[116:119], v[84:87]\n"
      "v_fma_f32 v192, |v94|, v78, v192\n"
      "v_fma_f32 v192, |v95|, v79, v192\n"
      "v_mfma_f32_16x16x32_f16 v[84:87], a[16:19], v[112:115], v[84:87]\n"
      "ds_read_b128 v[168:171], v203 offset:4736\n"
      "v_min3_f32 v226, |v228|, |v233|, |v238|\n"
      "v_mfma_f32_16x16x32_f16 v[88:91], a[32:35], v[112:115], v[24:27]\n"
      "s_waitcnt lgkmcnt(2)\n"
      "v_pk_mul_f16 v96, v128, v160\n"
      "v_pk_mul_f16 v97, v129, v161\n"
      "v_mfma_f32_16x16x32_f16 v[88:91], a[36:39], v[116:119], v[88:91]\n"
      "v_pk_mul_f16 v98, v130, v162\n"
      "v_pk_mul_f16 v99, v131, v163\n"
      "v_mfma_f32_16x16x32_f16 v[88:91], a[40:43], v[120:123], v[88:91]\n"
      "v_fma_f32 v193, |v80|, v48, 4.0\n"
      "v_fma_f32 v193, |v81|, v49, v193\n"
      "v_mfma_f32_16x16x32_f16 v[88:91], a[44:47], v[124:127], v[88:91]\n"
      "s_waitcnt vmcnt(0)\n"
      "v_min3_f32 v226, |v243|, |v244|, v226\n"
      "v_min3_f32 v226, |v249|, |v254|, v226\n"
      "v_mfma_f32_16x16x32_f16 v[92:95], a[60:63], v[124:127], v[28:31]\n"
      "v_fma_f32 v193, |v82|, v50, v193\n"
      "v_fma_f32 v193, |v83|, v51, v193\n"
      "v_mfma_f32_16x16x32_f16 v[92:95], a[56:59], v[120:123], v[92:95]\n"
      "s_waitcnt lgkmcnt(1)\n"
      "v_pk_mul_f16 v100, v132, v164\n"
      "v_pk_mul_f16 v101, v133, v165\n"
      "v_mfma_f32_16x16x32_f16 v[92:95], a[52:55], v[116:119], v[92:95]\n"
      "v_cmp_le_f32 vcc, 0x3da00000, v226\n"
      "v_pk_mul_f16 v102, v134, v166\n"
      "v_mfma_f32_16x16x32_f16 v[92:95], a[48:51], v[112:115], v[92:95]\n"
      "v_fma_f32 v193, |v84|, v52, v193\n"
      "v_fma_f32 v193, |v85|, v53, v193\n"
      "v_mfma_f32_16x16x32_f16 v[80:83], a[64:67], v[112:115], v[32:35]\n"
      "s_waitcnt lgkmcnt(0)\n"
      "v_pk_mul_f16 v103, v135, v167\n"
      "v_pk_mul_f16 v104, v136, v168\n"
      "v_mfma_f32_16x16x32_f16 v[80:83], a[68:71], v[116:119], v[80:83]\n"
      "s_quadmask_b64 s[44:45], vcc\n"
      "v_fma_f32 v193, |v86|, v54, v193\n"
      "v_fma_f32 v193, |v87|, v55, v193\n"
      "v_mfma_f32_16x16x32_f16 v[80:83], a[72:75], v[120:123], v[80:83]\n"
      "s_quadmask_b32 s30, s44\n"
      "v_fma_f32 v193, |v88|, v56, v193\n"
      "v_fma_f32 v193, |v89|, v57, v193\n"
      "v_mfma_f32_16x16x32_f16 v[80:83], a[76:79], v[124:127], v[80:83]\n"
      "s_xor_b32 s30, s30, 15\n"
      "v_pk_mul_f16 v105, v137, v169\n"
      "v_pk_mul_f16 v106, v138, v170\n"
      "v_mfma_f32_16x16x32_f16 v[84:87], a[92:95], v[124:127], v[36:39]\n"
      "s_or_b32 s30, s30, s41\n"
      "ds_read_b128 v[172:175], v203 offset:4800\n"
      "v_fma_f32 v193, |v90|, v58, v193\n"
      "v_mfma_f32_16x16x32_f16 v[84:87], a[88:91], v[120:123], v[84:87]\n"
      "s_cmp_lg_u32 s55, 0\n"
      "v_fma_f32 v193, |v91|, v59, v193\n"
      "v_fma_f32 v193, |v92|, v60, v193\n"
      "v_mfma_f32_16x16x32_f16 v[84:87], a[84:87], v[116:119], v[84:87]\n"
      "s_cselect_b32 s30, 0, s30\n"
      "v_pk_mul_f16 v107, v139, v171\n"
      "v_fma_f32 v193, |v93|, v61, v193\n"
      "v_mfma_f32_16x16x32_f16 v[84:87], a[80:83], v[112:115], v[84:87]\n"
      "v_fma_f32 v193, |v94|, v62, v193\n"
      "v_fma_f32 v193, |v95|, v63, v193\n"
      "v_mfma_f32_16x16x32_f16 v[88:91], a[96:99], v[112:115], v[40:43]\n"
      "v_fma_f32 v193, |v80|, v64, v193\n"
      "v_fma_f32 v193, |v81|, v65, v193\n"
      "v_mfma_f32_16x16x32_f16 v[88:91], a[100:103], v[116:119], v[88:91]\n"
      "v_fma_f32 v193, |v82|, v66, v193\n"
      "v_fma_f32 v193, |v83|, v67, v193\n"
      "v_mfma_f32_16x16x32_f16 v[88:91], a[104:107], v[120:123], v[88:91]\n"
      "v_add_f32 v189, v189, v191\n"
      "v_fma_f32 v193, |v84|, v68, v193\n"
      "v_mfma_f32_16x16x32_f16 v[88:91], a[108:111], v[124:127], v[88:91]\n"
      "v_permlane32_swap_b32 v190, v192\n"
      "v_mfma_f32_16x16x32_f16 v[92:95], a[124:127], v[124:127], v[44:47]\n"
      "s_waitcnt lgkmcnt(0)\n"
      "v_pk_mul_f16 v108, v140, v172\n"
      "v_pk_mul_f16 v109, v141, v173\n"
      "v_mfma_f32_16x16x32_f16 v[92:95], a[120:123], v[120:123], v[92:95]\n"
      "v_pk_mul_f16 v110, v142, v174\n"
      "v_pk_mul_f16 v111, v143, v175\n"
      "v_mfma_f32_16x16x32_f16 v[92:95], a[116:119], v[116:119], v[92:95]\n"
      "v_fma_f32 v193, |v85|, v69, v193\n"
      "v_fma_f32 v193, |v86|, v70, v193\n"
      "v_mfma_f32_16x16x32_f16 v[92:95], a[112:115], v[112:115], v[92:95]\n"
      "v_fma_f32 v193, |v87|, v71, v193\n"
      "v_fma_f32 v193, |v88|, v72, v193\n"
      "v_mfma_f32_16x16x32_f16 v[80:83], a[0:3], v[96:99], v[16:19]\n"
      "v_fma_f32 v193, |v89|, v73, v193\n"
      "v_fma_f32 v193, |v90|, v74, v193\n"
      "v_mfma_f32_16x16x32_f16 v[80:83], a[4:7], v[100:103], v[80:83]\n"
      "v_fma_f32 v193, |v91|, v75, v193\n"
      "v_pk_mul_f16 v112, v144, v160\n"
      "v_mfma_f32_16x16x32_f16 v[80:83], a[8:11], v[104:107], v[80:83]\n"
      "v_fma_f32 v193, |v92|, v76, v193\n"
      "v_fma_f32 v193, |v93|, v77, v193\n"
      "v_mfma_f32_16x16x32_f16 v[80:83], a[12:15], v[108:111], v[80:83]\n"
      "v_pk_mul_f16 v113, v145, v161\n"
      "v_pk_mul_f16 v114, v146, v162\n"
      "v_mfma_f32_16x16x32_f16 v[84:87], a[28:31], v[108:111], v[20:23]\n"
      "v_fma_f32 v193, |v94|, v78, v193\n"
      "v_fma_f32 v193, |v95|, v79, v193\n"
      "v_mfma_f32_16x16x32_f16 v[84:87], a[24:27], v[104:107], v[84:87]\n"
      "ds_read_b32 v224, v227 offset:37632\n"
      "v_pk_mul_f16 v115, v147, v163\n"
      "v_mfma_f32_16x16x32_f16 v[84:87], a[20:23], v[100:103], v[84:87]\n"
      "v_permlane32_swap_b32 v188, v193\n"
      "v_mfma_f32_16x16x32_f16 v[84:87], a[16:19], v[96:99], v[84:87]\n"
      "ds_read_b64 v[176:177], v5 offset:37120\n"
      "ds_read_b32 v226, v213 offset:12480\n"
      "v_mfma_f32_16x16x32_f16 v[88:91], a[32:35], v[96:99], v[24:27]\n"
      "v_add_f32 v190, v190, v192\n"
      "v_add_f32 v188, v188, v193\n"
      "v_mfma_f32_16x16x32_f16 v[88:91], a[36:39], v[100:103], v[88:91]\n"
      "v_fma_f32 v194, |v80|, v48, 4.0\n"
      "v_fma_f32 v194, |v81|, v49, v194\n"
      "v_mfma_f32_16x16x32_f16 v[88:91], a[40:43], v[104:107], v[88:91]\n"
      "v_permlane16_swap_b32 v189, v190\n"
      "v_mfma_f32_16x16x32_f16 v[88:91], a[44:47], v[108:111], v[88:91]\n"
      "v_fma_f32 v194, |v82|, v50, v194\n"
      "v_fma_f32 v194, |v83|, v51, v194\n"
      "v_mfma_f32_16x16x32_f16 v[92:95], a[60:63], v[108:111], v[28:31]\n"
      "v_pk_mul_f16 v116, v148, v164\n"
      "v_pk_mul_f16 v117, v149, v165\n"
      "v_mfma_f32_16x16x32_f16 v[92:95], a[56:59], v[104:107], v[92:95]\n"
      "s_waitcnt lgkmcnt(2)\n"
      "v_cmp_ne_u32 vcc, 13, v224\n"
      "v_add_f32 v189, v189, v190\n"
      "v_mfma_f32_16x16x32_f16 v[92:95], a[52:55], v[100:103], v[92:95]\n"
      "v_pk_mul_f16 v118, v150, v166\n"
      "v_pk_mul_f16 v119, v151, v167\n"
      "v_mfma_f32_16x16x32_f16 v[92:95], a[48:51], v[96:99], v[92:95]\n"
      "s_or_b32 s45, vcc_lo, s30\n"
      "v_fma_f32 v194, |v84|, v52, v194\n"
      "v_fma_f32 v194, |v85|, v53, v194\n"
      "v_mfma_f32_16x16x32_f16 v[80:83], a[64:67], v[96:99], v[32:35]\n"
      "s_waitcnt lgkmcnt(0)\n"
      "v_max_f16_sdwa v226, |v226|, |v226| dst_sel:DWORD dst_unused:UNUSED_PAD src0_sel:WORD_0 src1_sel:WORD_1\n"
      "v_cmp_le_f16 vcc, 0x6400, v226\n"
      "v_mfma_f32_16x16x32_f16 v[80:83], a[68:71], v[100:103], v[80:83]\n"
      "v_fma_f32 v194, |v86|, v54, v194\n"
      "v_fma_f32 v194, |v87|, v55, v194\n"
      "v_mfma_f32_16x16x32_f16 v[80:83], a[72:75], v[104:107], v[80:83]\n"
      "41:\n"
      "s_cbranch_execz 44b\n"
      "s_or_b32 vcc_lo, vcc_lo, s45\n"
      "v_pk_mul_f16 v120, v152, v168\n"
      "v_mfma_f32_16x16x32_f16 v[80:83], a[76:79], v[108:111], v[80:83]\n"
      "s_cmp_lg_u64 vcc, 0\n"
      "v_fma_f32 v194, |v88|, v56, v194\n"
      "v_fma_f32 v194, |v89|, v57, v194\n"
      "v_mfma_f32_16x16x32_f16 v[84:87], a[92:95], v[108:111], v[36:39]\n"
      "s_cbranch_scc1 5f\n"
      "4:\n"
      "v_mfma_f32_16x16x32_f16 v[84:87], a[88:91], v[104:107], v[84:87]\n"
      "v_pk_mul_f16 v121, v153, v169\n"
      "v_cvt_pk_f16_f32 v176, v176, v177\n"
      "v_mfma_f32_16x16x32_f16 v[84:87], a[84:87], v[100:103], v[84:87]\n"
      "s_lshl_b32 s49, s40, 1\n"
      "ds_write_b32 v213, v176 offset:11904\n"
      "v_mfma_f32_16x16x32_f16 v[84:87], a[80:83], v[96:99], v[84:87]\n"
      "s_add_u32 s51, s57, s49\n"
      "v_fma_f32 v194, |v90|, v58, v194\n"
      "v_fma_f32 v194, |v91|, v59, v194\n"
      "v_pk_mul_f16 v122, v154, v170\n"
      "v_mfma_f32_16x16x32_f16 v[88:91], a[96:99], v[96:99], v[40:43]\n"
      "s_add_u32 s51, s51, s58\n"
      "v_pk_mul_f16 v123, v155, v171\n"
      "v_fma_f32 v194, |v92|, v60, v194\n"
      "v_mfma_f32_16x16x32_f16 v[88:91], a[100:103], v[100:103], v[88:91]\n"
      "s_add_u32 s51, s51, s56\n"
      "v_fma_f32 v194, |v93|, v61, v194\n"
      "v_fma_f32 v194, |v94|, v62, v194\n"
      "v_mfma_f32_16x16x32_f16 v[88:91], a[104:107], v[104:107], v[88:91]\n"
      "s_sub_u32 s51, 0, s51\n"
      "v_fma_f32 v194, |v95|, v63, v194\n"
      "v_fma_f32 v194, |v80|, v64, v194\n"
      "v_mfma_f32_16x16x32_f16 v[88:91], a[108:111], v[108:111], v[88:91]\n"
      "s_add_u32 s49, s59, s49\n"
      "v_fma_f32 v194, |v81|, v65, v194\n"
      "v_fma_f32 v194, |v82|, v66, v194\n"
      "v_mfma_f32_16x16x32_f16 v[92:95], a[124:127], v[108:111], v[44:47]\n"
      "s_sub_u32 s39, 7, s49\n"
      "v_pk_mul_f16 v124, v156, v172\n"
      "v_pk_mul_f16 v125, v157, v173\n"
      "v_mfma_f32_16x16x32_f16 v[92:95], a[120:123], v[104:107], v[92:95]\n"
      "v_fma_f32 v194, |v83|, v67, v194\n"
      "v_fma_f32 v194, |v84|, v68, v194\n"
      "v_mfma_f32_16x16x32_f16 v[92:95], a[116:119], v[100:103], v[92:95]\n"
      "v_pk_mul_f16 v126, v158, v174\n"
      "v_pk_mul_f16 v127, v159, v175\n"
      "v_mfma_f32_16x16x32_f16 v[92:95], a[112:115], v[96:99], v[92:95]\n"
      "ds_read_b128 v[160:163], v203 offset:5184\n"
      "v_ldexp_f32 v200, v200, s51\n"
      "v_mfma_f32_16x16x32_f16 v[80:83], a[0:3], v[112:115], v[16:19]\n"
      "v_fma_f32 v194, |v85|, v69, v194\n"
      "v_fma_f32 v194, |v86|, v70, v194\n"
      "v_mfma_f32_16x16x32_f16 v[80:83], a[4:7], v[116:119], v[80:83]\n"
      "v_fma_f32 v194, |v87|, v71, v194\n"
      "v_fma_f32 v194, |v88|, v72, v194\n"
      "v_mfma_f32_16x16x32_f16 v[80:83], a[8:11], v[120:123], v[80:83]\n"
      "ds_read_b128 v[164:167], v203 offset:5248\n"
      "v_fma_f32 v194, |v89|, v73, v194\n"
      "v_mfma_f32_16x16x32_f16 v[80:83], a[12:15], v[124:127], v[80:83]\n"
      "v_fma_f32 v194, |v90|, v74, v194\n"
      "v_fma_f32 v194, |v91|, v75, v194\n"
      "v_mfma_f32_4x4x4_16b_bf16 v[176:179], a[216:217], v[248:249], 0\n"
      "v_fma_f32 v194, |v92|, v76, v194\n"
      "v_mfma_f32_4x4x4_16b_bf16 v[180:183], a[216:217], v[250:251], 0\n"
      "v_fma_f32 v194, |v93|, v77, v194\n"
      "v_mfma_f32_4x4x4_16b_bf16 v[228:231], a[216:217], v[252:253], 0\n"
      "v_fma_f32 v194, |v94|, v78, v194\n"
      "v_mfma_f32_4x4x4_16b_bf16 v[232:235], a[216:217], v[254:255], 0\n"
      "s_waitcnt lgkmcnt(1)\n"
      "v_pk_mul_f16 v96, v128, v160\n"
      "v_pk_mul_f16 v97, v129, v161\n"
      "v_mfma_f32_16x16x32_f16 v[84:87], a[28:31], v[124:127], v[20:23]\n"
      "v_pk_mul_f16 v98, v130, v162\n"
      "v_pk_mul_f16 v99, v131, v163\n"
      "v_mfma_f32_16x16x32_f16 v[84:87], a[24:27], v[120:123], v[84:87]\n"
      "ds_read_b128 v[168:171], v203 offset:5312\n"
      "v_fma_f32 v194, |v95|, v79, v194\n"
      "v_mfma_f32_16x16x32_f16 v[84:87], a[20:23], v[116:119], v[84:87]\n"
      "s_waitcnt lgkmcnt(1)\n"
      "v_pk_mul_f16 v100, v132, v164\n"
      "v_pk_mul_f16 v101, v133, v165\n"
      "v_mfma_f32_16x16x32_f16 v[84:87], a[16:19], v[112:115], v[84:87]\n"
      "v_pk_mul_f16 v102, v134, v166\n"
      "v_pk_mul_f16 v103, v135, v167\n"
      "v_mfma_f32_16x16x32_f16 v[88:91], a[32:35], v[112:115], v[24:27]\n"
      "v_fma_f32 v195, |v80|, v48, 4.0\n"
      "v_fma_f32 v195, |v81|, v49, v195\n"
      "v_mfma_f32_16x16x32_f16 v[88:91], a[36:39], v[116:119], v[88:91]\n"
      "v_fma_f32 v195, |v82|, v50, v195\n"
      "v_fma_f32 v195, |v83|, v51, v195\n"
      "v_mfma_f32_16x16x32_f16 v[88:91], a[40:43], v[120:123], v[88:91]\n"
      "v_fma_f32 v195, |v84|, v52, v195\n"
      "v_fma_f32 v195, |v85|, v53, v195\n"
      "v_mfma_f32_16x16x32_f16 v[88:91], a[44:47], v[124:127], v[88:91]\n"
      "v_fma_f32 v195, |v86|, v54, v195\n"
      "v_fma_f32 v195, |v87|, v55, v195\n"
      "v_mfma_f32_16x16x32_f16 v[92:95], a[60:63], v[124:127], v[28:31]\n"
      "ds_read_b128 v[172:175], v203 offset:5376\n"
      "v_add_u32_sdwa v219, s38, v197 dst_sel:DWORD dst_unused:UNUSED_PAD src0_sel:DWORD src1_sel:WORD_1\n"
      "v_mfma_f32_16x16x32_f16 v[92:95], a[56:59], v[120:123], v[92:95]\n"
      "s_waitcnt lgkmcnt(1)\n"
      "v_pk_mul_f16 v104, v136, v168\n"
      "v_pk_mul_f16 v105, v137, v169\n"
      "v_mfma_f32_16x16x32_f16 v[92:95], a[52:55], v[116:119], v[92:95]\n"
      "v_pk_mul_f16 v106, v138, v170\n"
      "v_pk_mul_f16 v107, v139, v171\n"
      "v_mfma_f32_16x16x32_f16 v[92:95], a[48:51], v[112:115], v[92:95]\n"
      "v_fma_f32 v195, |v88|, v56, v195\n"
      "v_fma_f32 v195, |v89|, v57, v195\n"
      "v_mfma_f32_16x16x32_f16 v[80:83], a[64:67], v[112:115], v[32:35]\n"
      "v_fma_f32 v195, |v90|, v58, v195\n"
      "v_fma_f32 v195, |v91|, v59, v195\n"
      "v_mfma_f32_16x16x32_f16 v[80:83], a[68:71], v[116:119], v[80:83]\n"
      "v_add_u32_sdwa v220, s38, v198 dst_sel:DWORD dst_unused:UNUSED_PAD src0_sel:DWORD src1_sel:WORD_0\n"
      "v_add_u32_sdwa v221, s38, v198 dst_sel:DWORD dst_unused:UNUSED_PAD src0_sel:DWORD src1_sel:WORD_1\n"
      "v_mfma_f32_16x16x32_f16 v[80:83], a[72:75], v[120:123], v[80:83]\n"
      "v_fma_f32 v195, |v92|, v60, v195\n"
      "v_fma_f32 v195, |v93|, v61, v195\n"
      "v_mfma_f32_16x16x32_f16 v[80:83], a[76:79], v[124:127], v[80:83]\n"
      "v_fma_f32 v195, |v94|, v62, v195\n"
      "v_fma_f32 v195, |v95|, v63, v195\n"
      "v_mfma_f32_16x16x32_f16 v[84:87], a[92:95], v[124:127], v[36:39]\n"
      "s_waitcnt lgkmcnt(0)\n"
      "v_pk_mul_f16 v108, v140, v172\n"
      "v_pk_mul_f16 v109, v141, v173\n"
      "v_mfma_f32_16x16x32_f16 v[84:87], a[88:91], v[120:123], v[84:87]\n"
      "v_pk_mul_f16 v110, v142, v174\n"
      "v_pk_mul_f16 v111, v143, v175\n"
      "v_mfma_f32_16x16x32_f16 v[84:87], a[84:87], v[116:119], v[84:87]\n"
      "v_fma_f32 v195, |v80|, v64, v195\n"
      "v_fma_f32 v195, |v81|, v65, v195\n"
      "v_mfma_f32_16x16x32_f16 v[84:87], a[80:83], v[112:115], v[84:87]\n"
      "v_fma_f32 v195, |v82|, v66, v195\n"
      "v_fma_f32 v195, |v83|, v67, v195\n"
      "v_mfma_f32_16x16x32_f16 v[88:91], a[96:99], v[112:115], v[40:43]\n"
      "v_cvt_pk_f16_f32 v184, v176, v177\n"
      "v_cvt_pk_f16_f32 v185, v178, v179\n"
      "v_mfma_f32_16x16x32_f16 v[88:91], a[100:103], v[116:119], v[88:91]\n"
      "v_cvt_pk_f16_f32 v186, v180, v181\n"
      "v_cvt_pk_f16_f32 v187, v182, v183\n"
      "v_mfma_f32_16x16x32_f16 v[88:91], a[104:107], v[120:123], v[88:91]\n"
      "ds_write_b128 v1, v[184:187] offset:5760\n"
      "v_mfma_f32_16x16x32_f16 v[88:91], a[108:111], v[124:127], v[88:91]\n"
      "v_fma_f32 v195, |v84|, v68, v195\n"
      "v_fma_f32 v195, |v85|, v69, v195\n"
      "v_mfma_f32_16x16x32_f16 v[92:95], a[124:127], v[124:127], v[44:47]\n"
      "v_fma_f32 v195, |v86|, v70, v195\n"
      "v_fma_f32 v195, |v87|, v71, v195\n"
      "v_mfma_f32_16x16x32_f16 v[92:95], a[120:123], v[120:123], v[92:95]\n"
      "v_pk_mul_f16 v120, v152, v168\n"
      "v_pk_mul_f16 v121, v153, v169\n"
      "v_mfma_f32_16x16x32_f16 v[92:95], a[116:119], v[116:119], v[92:95]\n"
      "v_fma_f32 v195, |v88|, v72, v195\n"
      "v_fma_f32 v195, |v89|, v73, v195\n"
      "v_mfma_f32_16x16x32_f16 v[92:95], a[112:115], v[112:115], v[92:95]\n"
      "v_fma_f32 v195, |v90|, v74, v195\n"
      "v_fma_f32 v195, |v91|, v75, v195\n"
      "v_mfma_f32_16x16x32_f16 v[80:83], a[0:3], v[96:99], v[16:19]\n"
      "v_pk_mul_f16 v112, v144, v160\n"
      "v_pk_mul_f16 v113, v145, v161\n"
      "v_mfma_f32_16x16x32_f16 v[80:83], a[4:7], v[100:103], v[80:83]\n"
      "v_pk_mul_f16 v114, v146, v162\n"
      "v_pk_mul_f16 v115, v147, v163\n"
      "v_mfma_f32_16x16x32_f16 v[80:83], a[8:11], v[104:107], v[80:83]\n"
      "v_fma_f32 v195, |v92|, v76, v195\n"
      "v_fma_f32 v195, |v93|, v77, v195\n"
      "v_mfma_f32_16x16x32_f16 v[80:83], a[12:15], v[108:111], v[80:83]\n"
      "v_fma_f32 v195, |v94|, v78, v195\n"
      "v_fma_f32 v195, |v95|, v79, v195\n"
      "v_mfma_f32_16x16x32_f16 v[84:87], a[28:31], v[108:111], v[20:23]\n"
      "v_pk_mul_f16 v116, v148, v164\n"
      "v_pk_mul_f16 v117, v149, v165\n"
      "v_mfma_f32_16x16x32_f16 v[84:87], a[24:27], v[104:107], v[84:87]\n"
      "v_pk_mul_f16 v118, v150, v166\n"
      "v_pk_mul_f16 v119, v151, v167\n"
      "v_mfma_f32_16x16x32_f16 v[84:87], a[20:23], v[100:103], v[84:87]\n"
      "v_fma_f32 v190, |v80|, v48, 4.0\n"
      "v_fma_f32 v190, |v81|, v49, v190\n"
      "v_mfma_f32_16x16x32_f16 v[84:87], a[16:19], v[96:99], v[84:87]\n"
      "v_fma_f32 v190, |v82|, v50, v190\n"
      "v_fma_f32 v190, |v83|, v51, v190\n"
      "v_mfma_f32_16x16x32_f16 v[88:91], a[32:35], v[96:99], v[24:27]\n"
      "v_pk_mul_f16 v122, v154, v170\n"
      "v_pk_mul_f16 v123, v155, v171\n"
      "v_mfma_f32_16x16x32_f16 v[88:91], a[36:39], v[100:103], v[88:91]\n"
      "v_pk_mul_f16 v124, v156, v172\n"
      "v_pk_mul_f16 v125, v157, v173\n"
      "v_mfma_f32_16x16x32_f16 v[88:91], a[40:43], v[104:107], v[88:91]\n"
      "v_fma_f32 v190, |v84|, v52, v190\n"
      "v_fma_f32 v190, |v85|, v53, v190\n"
      "v_mfma_f32_16x16x32_f16 v[88:91], a[44:47], v[108:111], v[88:91]\n"
      "v_fma_f32 v190, |v86|, v54, v190\n"
      "v_fma_f32 v190, |v87|, v55, v190\n"
      "v_mfma_f32_16x16x32_f16 v[92:95], a[60:63], v[108:111], v[28:31]\n"
      "v_pk_mul_f16 v126, v158, v174\n"
      "v_pk_mul_f16 v127, v159, v175\n"
      "v_mfma_f32_16x16x32_f16 v[92:95], a[56:59], v[104:107], v[92:95]\n"
      "v_cvt_pk_f16_f32 v184, v228, v229\n"
      "v_cvt_pk_f16_f32 v185, v230, v231\n"
      "v_mfma_f32_16x16x32_f16 v[92:95], a[52:55], v[100:103], v[92:95]\n"
      "v_fma_f32 v190, |v88|, v56, v190\n"
      "v_fma_f32 v190, |v89|, v57, v190\n"
      "v_mfma_f32_16x16x32_f16 v[92:95], a[48:51], v[96:99], v[92:95]\n"
      "v_fma_f32 v190, |v90|, v58, v190\n"
      "v_fma_f32 v190, |v91|, v59, v190\n"
      "v_mfma_f32_16x16x32_f16 v[80:83], a[64:67], v[96:99], v[32:35]\n"
      "v_cvt_pk_f16_f32 v186, v232, v233\n"
      "v_cvt_pk_f16_f32 v187, v234, v235\n"
      "v_mfma_f32_16x16x32_f16 v[80:83], a[68:71], v[100:103], v[80:83]\n"
      "ds_write_b128 v1, v[184:187] offset:6912\n"
      "v_mfma_f32_16x16x32_f16 v[80:83], a[72:75], v[104:107], v[80:83]\n"
      "v_fma_f32 v190, |v92|, v60, v190\n"
      "v_fma_f32 v190, |v93|, v61, v190\n"
      "v_mfma_f32_16x16x32_f16 v[80:83], a[76:79], v[108:111], v[80:83]\n"
      "v_fma_f32 v190, |v94|, v62, v190\n"
      "v_fma_f32 v190, |v95|, v63, v190\n"
      "v_mfma_f32_16x16x32_f16 v[84:87], a[92:95], v[108:111], v[36:39]\n"
      "ds_read_b128 v[160:163], v203 offset:5760\n"
      "ds_read_b128 v[176:179], v214 offset:11904\n"
      "v_mfma_f32_16x16x32_f16 v[84:87], a[88:91], v[104:107], v[84:87]\n"
      "ds_read_b128 a[152:155], v219 offset:0\n"
      "ds_read_b128 a[168:171], v220 offset:0\n"
      "v_mfma_f32_16x16x32_f16 v[84:87], a[84:87], v[100:103], v[84:87]\n"
      "v_fma_f32 v190, |v80|, v64, v190\n"
      "v_fma_f32 v190, |v81|, v65, v190\n"
      "v_mfma_f32_16x16x32_f16 v[84:87], a[80:83], v[96:99], v[84:87]\n"
      "v_fma_f32 v190, |v82|, v66, v190\n"
      "v_fma_f32 v190, |v83|, v67, v190\n"
      "v_mfma_f32_16x16x32_f16 v[88:91], a[96:99], v[96:99], v[40:43]\n"
      "ds_read_b128 a[184:187], v221 offset:0\n"
      "v_add_u32_sdwa v216, s38, v196 dst_sel:DWORD dst_unused:UNUSED_PAD src0_sel:DWORD src1_sel:WORD_0\n"
      "v_mfma_f32_16x16x32_f16 v[88:91], a[100:103], v[100:103], v[88:91]\n"
      "ds_read_b128 a[156:159], v219 offset:64\n"
      "v_add_u32_sdwa v217, s38, v196 dst_sel:DWORD dst_unused:UNUSED_PAD src0_sel:DWORD src1_sel:WORD_1\n"
      "v_mfma_f32_16x16x32_f16 v[88:91], a[104:107], v[104:107], v[88:91]\n"
      "v_fma_f32 v190, |v84|, v68, v190\n"
      "v_fma_f32 v190, |v85|, v69, v190\n"
      "v_mfma_f32_16x16x32_f16 v[88:91], a[108:111], v[108:111], v[88:91]\n"
      "v_fma_f32 v190, |v86|, v70, v190\n"
      "v_fma_f32 v190, |v87|, v71, v190\n"
      "v_mfma_f32_16x16x32_f16 v[92:95], a[124:127], v[108:111], v[44:47]\n"
      "s_waitcnt lgkmcnt(4)\n"
      "ds_read_b128 v[164:167], v203 offset:5824\n"
      "v_pk_mul_f16 v228, v128, v176\n"
      "v_mfma_f32_16x16x32_f16 v[92:95], a[120:123], v[104:107], v[92:95]\n"
      "v_pk_mul_f16 v229, v129, v177\n"
      "v_pk_mul_f16 v230, v130, v178\n"
      "v_mfma_f32_16x16x32_f16 v[92:95], a[116:119], v[100:103], v[92:95]\n"
      "v_fma_f32 v190, |v88|, v72, v190\n"
      "v_fma_f32 v190, |v89|, v73, v190\n"
      "v_mfma_f32_16x16x32_f16 v[92:95], a[112:115], v[96:99], v[92:95]\n"
      "v_fma_f32 v190, |v90|, v74, v190\n"
      "v_fma_f32 v190, |v91|, v75, v190\n"
      "v_mfma_f32_16x16x32_f16 v[80:83], a[0:3], v[112:115], v[16:19]\n"
      "v_pk_mul_f16 v96, v128, v160\n"
      "v_pk_mul_f16 v97, v129, v161\n"
      "v_mfma_f32_16x16x32_f16 v[80:83], a[4:7], v[116:119], v[80:83]\n"
      "v_pk_mul_f16 v98, v130, v162\n"
      "v_pk_mul_f16 v99, v131, v163\n"
      "v_mfma_f32_16x16x32_f16 v[80:83], a[8:11], v[120:123], v[80:83]\n"
      "v_fma_f32 v190, |v92|, v76, v190\n"
      "v_fma_f32 v190, |v93|, v77, v190\n"
      "v_mfma_f32_16x16x32_f16 v[80:83], a[12:15], v[124:127], v[80:83]\n"
      "v_fma_f32 v190, |v94|, v78, v190\n"
      "v_fma_f32 v190, |v95|, v79, v190\n"
      "v_mfma_f32_16x16x32_f16 v[84:87], a[28:31], v[124:127], v[20:23]\n"
      "s_waitcnt lgkmcnt(0)\n"
      "ds_read_b128 v[168:171], v203 offset:5888\n"
      "v_pk_mul_f16 v100, v132, v164\n"
      "v_mfma_f32_16x16x32_f16 v[84:87], a[24:27], v[120:123], v[84:87]\n"
      "v_pk_mul_f16 v101, v133, v165\n"
      "v_pk_mul_f16 v102, v134, v166\n"
      "v_mfma_f32_16x16x32_f16 v[84:87], a[20:23], v[116:119], v[84:87]\n"
      "v_fma_f32 v191, |v80|, v48, 4.0\n"
      "v_fma_f32 v191, |v81|, v49, v191\n"
      "v_mfma_f32_16x16x32_f16 v[84:87], a[16:19], v[112:115], v[84:87]\n"
      "v_fma_f32 v191, |v82|, v50, v191\n"
      "v_fma_f32 v191, |v83|, v51, v191\n"
      "v_mfma_f32_16x16x32_f16 v[88:91], a[32:35], v[112:115], v[24:27]\n"
      "v_pk_mul_f16 v103, v135, v167\n"
      "v_pk_mul_f16 v231, v131, v179\n"
      "v_mfma_f32_16x16x32_f16 v[88:91], a[36:39], v[116:119], v[88:91]\n"
      "ds_read_b128 v[172:175], v203 offset:5952\n"
      "ds_read_b128 v[240:243], v201 offset:4608\n"
      "v_mfma_f32_16x16x32_f16 v[88:91], a[40:43], v[120:123], v[88:91]\n"
      "v_fma_f32 v191, |v84|, v52, v191\n"
      "v_fma_f32 v191, |v85|, v53, v191\n"
      "v_mfma_f32_16x16x32_f16 v[88:91], a[44:47], v[124:127], v[88:91]\n"
      "v_fma_f32 v191, |v86|, v54, v191\n"
      "v_fma_f32 v191, |v87|, v55, v191\n"
      "v_mfma_f32_16x16x32_f16 v[92:95], a[60:63], v[124:127], v[28:31]\n"
      "s_waitcnt lgkmcnt(2)\n"
      "v_pk_mul_f16 v104, v136, v168\n"
      "v_pk_mul_f16 v105, v137, v169\n"
      "v_mfma_f32_16x16x32_f16 v[92:95], a[56:59], v[120:123], v[92:95]\n"
      "v_pk_mul_f16 v106, v138, v170\n"
      "v_pk_mul_f16 v107, v139, v171\n"
      "v_mfma_f32_16x16x32_f16 v[92:95], a[52:55], v[116:119], v[92:95]\n"
      "v_fma_f32 v191, |v88|, v56, v191\n"
      "v_fma_f32 v191, |v89|, v57, v191\n"
      "v_mfma_f32_16x16x32_f16 v[92:95], a[48:51], v[112:115], v[92:95]\n"
      "v_fma_f32 v191, |v90|, v58, v191\n"
      "v_fma_f32 v191, |v91|, v59, v191\n"
      "v_mfma_f32_16x16x32_f16 v[80:83], a[64:67], v[112:115], v[32:35]\n"
      "ds_read_b128 v[244:247], v201 offset:4672\n"
      "ds_read_b128 v[248:251], v201 offset:4736\n"
      "v_mfma_f32_16x16x32_f16 v[80:83], a[68:71], v[116:119], v[80:83]\n"
      "s_waitcnt lgkmcnt(3)\n"
      "ds_read_b128 v[252:255], v201 offset:4800\n"
      "v_pk_mul_f16 v108, v140, v172\n"
      "v_mfma_f32_16x16x32_f16 v[80:83], a[72:75], v[120:123], v[80:83]\n"
      "v_fma_f32 v191, |v92|, v60, v191\n"
      "v_fma_f32 v191, |v93|, v61, v191\n"
      "v_mfma_f32_16x16x32_f16 v[80:83], a[76:79], v[124:127], v[80:83]\n"
      "v_fma_f32 v191, |v94|, v62, v191\n"
      "v_fma_f32 v191, |v95|, v63, v191\n"
      "v_mfma_f32_16x16x32_f16 v[84:87], a[92:95], v[124:127], v[36:39]\n"
      "v_pk_mul_f16 v109, v141, v173\n"
      "v_pk_mul_f16 v110, v142, v174\n"
      "v_mfma_f32_16x16x32_f16 v[84:87], a[88:91], v[120:123], v[84:87]\n"
      "v_pk_mul_f16 v111, v143, v175\n"
      "v_pk_mul_f16 v232, v144, v176\n"
      "v_mfma_f32_16x16x32_f16 v[84:87], a[84:87], v[116:119], v[84:87]\n"
      "v_fma_f32 v191, |v80|, v64, v191\n"
      "v_fma_f32 v191, |v81|, v65, v191\n"
      "v_mfma_f32_16x16x32_f16 v[84:87], a[80:83], v[112:115], v[84:87]\n"
      "v_fma_f32 v191, |v82|, v66, v191\n"
      "v_fma_f32 v191, |v83|, v67, v191\n"
      "v_mfma_f32_16x16x32_f16 v[88:91], a[96:99], v[112:115], v[40:43]\n"
      "v_pk_mul_f16 v233, v145, v177\n"
      "v_pk_mul_f16 v234, v146, v178\n"
      "v_mfma_f32_16x16x32_f16 v[88:91], a[100:103], v[116:119], v[88:91]\n"
      "s_waitcnt lgkmcnt(3)\n"
      "v_pk_mul_f16 v235, v147, v179\n"
      "v_pk_mul_f16 v236, v240, v176\n"
      "v_mfma_f32_16x16x32_f16 v[88:91], a[104:107], v[120:123], v[88:91]\n"
      "v_fma_f32 v191, |v84|, v68, v191\n"
      "v_fma_f32 v191, |v85|, v69, v191\n"
      "v_mfma_f32_16x16x32_f16 v[88:91], a[108:111], v[124:127], v[88:91]\n"
      "v_fma_f32 v191, |v86|, v70, v191\n"
      "v_fma_f32 v191, |v87|, v71, v191\n"
      "v_mfma_f32_16x16x32_f16 v[92:95], a[124:127], v[124:127], v[44:47]\n"
      "v_pk_mul_f16 v237, v241, v177\n"
      "v_pk_mul_f16 v238, v242, v178\n"
      "v_mfma_f32_16x16x32_f16 v[92:95], a[120:123], v[120:123], v[92:95]\n"
      "v_pk_mul_f16 v120, v152, v168\n"
      "v_pk_mul_f16 v121, v153, v169\n"
      "v_mfma_f32_16x16x32_f16 v[92:95], a[116:119], v[116:119], v[92:95]\n"
      "v_fma_f32 v191, |v88|, v72, v191\n"
      "v_fma_f32 v191, |v89|, v73, v191\n"
      "v_mfma_f32_16x16x32_f16 v[92:95], a[112:115], v[112:115], v[92:95]\n"
      "v_fma_f32 v191, |v90|, v74, v191\n"
      "v_fma_f32 v191, |v91|, v75, v191\n"
      "v_mfma_f32_16x16x32_f16 v[80:83], a[0:3], v[96:99], v[16:19]\n"
      "v_pk_mul_f16 v112, v144, v160\n"
      "v_pk_mul_f16 v113, v145, v161\n"
      "v_mfma_f32_16x16x32_f16 v[80:83], a[4:7], v[100:103], v[80:83]\n"
      "v_pk_mul_f16 v114, v146, v162\n"
      "v_pk_mul_f16 v115, v147, v163\n"
      "v_mfma_f32_16x16x32_f16 v[80:83], a[8:11], v[104:107], v[80:83]\n"
      "v_fma_f32 v191, |v92|, v76, v191\n"
      "v_fma_f32 v191, |v93|, v77, v191\n"
      "v_mfma_f32_16x16x32_f16 v[80:83], a[12:15], v[108:111], v[80:83]\n"
      "v_fma_f32 v191, |v94|, v78, v191\n"
      "v_fma_f32 v191, |v95|, v79, v191\n"
      "v_mfma_f32_16x16x32_f16 v[84:87], a[28:31], v[108:111], v[20:23]\n"
      "v_pk_mul_f16 v116, v148, v164\n"
      "v_pk_mul_f16 v117, v149, v165\n"
      "v_mfma_f32_16x16x32_f16 v[84:87], a[24:27], v[104:107], v[84:87]\n"
      "v_pk_mul_f16 v118, v150, v166\n"
      "v_pk_mul_f16 v119, v151, v167\n"
      "v_mfma_f32_16x16x32_f16 v[84:87], a[20:23], v[100:103], v[84:87]\n"
      "v_fma_f32 v192, |v80|, v48, 4.0\n"
      "v_fma_f32 v192, |v81|, v49, v192\n"
      "v_mfma_f32_16x16x32_f16 v[84:87], a[16:19], v[96:99], v[84:87]\n"
      "v_fma_f32 v192, |v82|, v50, v192\n"
      "v_fma_f32 v192, |v83|, v51, v192\n"
      "v_mfma_f32_16x16x32_f16 v[88:91], a[32:35], v[96:99], v[24:27]\n"
      "v_pk_mul_f16 v122, v154, v170\n"
      "v_pk_mul_f16 v123, v155, v171\n"
      "v_mfma_f32_16x16x32_f16 v[88:91], a[36:39], v[100:103], v[88:91]\n"
      "v_pk_mul_f16 v239, v243, v179\n"
      "v_pk_mul_f16 v124, v156, v172\n"
      "v_mfma_f32_16x16x32_f16 v[88:91], a[40:43], v[104:107], v[88:91]\n"
      "v_fma_f32 v192, |v84|, v52, v192\n"
      "v_fma_f32 v192, |v85|, v53, v192\n"
      "v_mfma_f32_16x16x32_f16 v[88:91], a[44:47], v[108:111], v[88:91]\n"
      "v_fma_f32 v192, |v86|, v54, v192\n"
      "v_fma_f32 v192, |v87|, v55, v192\n"
      "v_mfma_f32_16x16x32_f16 v[92:95], a[60:63], v[108:111], v[28:31]\n"
      "v_pk_mul_f16 v125, v157, v173\n"
      "v_pk_mul_f16 v126, v158, v174\n"
      "v_mfma_f32_16x16x32_f16 v[92:95], a[56:59], v[104:107], v[92:95]\n"
      "ds_read_b128 v[176:179], v214 offset:11968\n"
      "v_pk_mul_f16 v127, v159, v175\n"
      "v_mfma_f32_16x16x32_f16 v[92:95], a[52:55], v[100:103], v[92:95]\n"
      "v_fma_f32 v192, |v88|, v56, v192\n"
      "v_fma_f32 v192, |v89|, v57, v192\n"
      "v_mfma_f32_16x16x32_f16 v[92:95], a[48:51], v[96:99], v[92:95]\n"
      "v_fma_f32 v192, |v90|, v58, v192\n"
      "v_fma_f32 v192, |v91|, v59, v192\n"
      "v_mfma_f32_16x16x32_f16 a[128:131], v[228:231], a[152:155], 0\n"
      "v_add_u32_sdwa v218, s38, v197 dst_sel:DWORD dst_unused:UNUSED_PAD src0_sel:DWORD src1_sel:WORD_0\n"
      "v_add_u32_sdwa v215, s38, v207 dst_sel:DWORD dst_unused:UNUSED_PAD src0_sel:DWORD src1_sel:WORD_1\n"
      "v_mfma_f32_16x16x32_f16 v[80:83], a[64:67], v[96:99], v[32:35]\n"
      "v_add_u32_sdwa v211, s38, v207 dst_sel:DWORD dst_unused:UNUSED_PAD src0_sel:DWORD src1_sel:WORD_0\n"
      "v_add_u32_sdwa v209, s38, v206 dst_sel:DWORD dst_unused:UNUSED_PAD src0_sel:DWORD src1_sel:WORD_1\n"
      "v_mfma_f32_16x16x32_f16 v[80:83], a[68:71], v[100:103], v[80:83]\n"
      "v_fma_f32 v192, |v92|, v60, v192\n"
      "v_fma_f32 v192, |v93|, v61, v192\n"
      "v_mfma_f32_16x16x32_f16 v[80:83], a[72:75], v[104:107], v[80:83]\n"
      "v_fma_f32 v192, |v94|, v62, v192\n"
      "v_fma_f32 v192, |v95|, v63, v192\n"
      "v_mfma_f32_16x16x32_f16 v[80:83], a[76:79], v[108:111], v[80:83]\n"
      "v_add_u32_sdwa v208, s38, v206 dst_sel:DWORD dst_unused:UNUSED_PAD src0_sel:DWORD src1_sel:WORD_0\n"
      "v_add_u32_sdwa v207, s38, v205 dst_sel:DWORD dst_unused:UNUSED_PAD src0_sel:DWORD src1_sel:WORD_1\n"
      "v_mfma_f32_16x16x32_f16 v[84:87], a[92:95], v[108:111], v[36:39]\n"
      "s_waitcnt lgkmcnt(0)\n"
      "v_pk_mul_f16 v228, v132, v176\n"
      "v_pk_mul_f16 v229, v133, v177\n"
      "v_mfma_f32_16x16x32_f16 v[84:87], a[88:91], v[104:107], v[84:87]\n"
      "v_pk_mul_f16 v230, v134, v178\n"
      "v_pk_mul_f16 v231, v135, v179\n"
      "v_mfma_f32_16x16x32_f16 v[84:87], a[84:87], v[100:103], v[84:87]\n"
      "v_fma_f32 v192, |v80|, v64, v192\n"
      "v_fma_f32 v192, |v81|, v65, v192\n"
      "v_mfma_f32_16x16x32_f16 v[84:87], a[80:83], v[96:99], v[84:87]\n"
      "v_fma_f32 v192, |v82|, v66, v192\n"
      "v_fma_f32 v192, |v83|, v67, v192\n"
      "v_mfma_f32_16x16x32_f16 a[132:135], v[232:235], a[168:171], 0\n"
      "ds_read_b128 v[160:163], v203 offset:6336\n"
      "v_add_u32_sdwa v206, s38, v205 dst_sel:DWORD dst_unused:UNUSED_PAD src0_sel:DWORD src1_sel:WORD_0\n"
      "v_mfma_f32_16x16x32_f16 v[88:91], a[96:99], v[96:99], v[40:43]\n"
      "v_add_u32_sdwa v205, s38, v204 dst_sel:DWORD dst_unused:UNUSED_PAD src0_sel:DWORD src1_sel:WORD_1\n"
      "v_add_u32_sdwa v204, s38, v204 dst_sel:DWORD dst_unused:UNUSED_PAD src0_sel:DWORD src1_sel:WORD_0\n"
      "v_mfma_f32_16x16x32_f16 v[88:91], a[100:103], v[100:103], v[88:91]\n"
      "v_fma_f32 v192, |v84|, v68, v192\n"
      "v_fma_f32 v192, |v85|, v69, v192\n"
      "v_mfma_f32_16x16x32_f16 v[88:91], a[104:107], v[104:107], v[88:91]\n"
      "v_fma_f32 v192, |v86|, v70, v192\n"
      "v_fma_f32 v192, |v87|, v71, v192\n"
      "v_mfma_f32_16x16x32_f16 v[88:91], a[108:111], v[108:111], v[88:91]\n"
      "ds_read_b128 v[164:167], v203 offset:6400\n"
      "ds_read_b128 a[172:175], v220 offset:64\n"
      "v_mfma_f32_16x16x32_f16 v[92:95], a[124:127], v[108:111], v[44:47]\n"
      "v_pk_mul_f16 v232, v148, v176\n"
      "v_pk_mul_f16 v233, v149, v177\n"
      "v_mfma_f32_16x16x32_f16 v[92:95], a[120:123], v[104:107], v[92:95]\n"
      "v_pk_mul_f16 v234, v150, v178\n"
      "v_pk_mul_f16 v235, v151, v179\n"
      "v_mfma_f32_16x16x32_f16 v[92:95], a[116:119], v[100:103], v[92:95]\n"
      "v_fma_f32 v192, |v88|, v72, v192\n"
      "v_fma_f32 v192, |v89|, v73, v192\n"
      "v_mfma_f32_16x16x32_f16 v[92:95], a[112:115], v[96:99], v[92:95]\n"
      "v_fma_f32 v192, |v90|, v74, v192\n"
      "v_fma_f32 v192, |v91|, v75, v192\n"
      "v_mfma_f32_16x16x32_f16 a[136:139], v[236:239], a[184:187], 0\n"
      "s_waitcnt lgkmcnt(2)\n"
      "v_pk_mul_f16 v96, v128, v160\n"
      "v_pk_mul_f16 v97, v129, v161\n"
      "v_mfma_f32_16x16x32_f16 v[80:83], a[0:3], v[112:115], v[16:19]\n"
      "v_pk_mul_f16 v98, v130, v162\n"
      "v_pk_mul_f16 v99, v131, v163\n"
      "v_mfma_f32_16x16x32_f16 v[80:83], a[4:7], v[116:119], v[80:83]\n"
      "v_fma_f32 v192, |v92|, v76, v192\n"
      "v_fma_f32 v192, |v93|, v77, v192\n"
      "v_mfma_f32_16x16x32_f16 v[80:83], a[8:11], v[120:123], v[80:83]\n"
      "v_fma_f32 v192, |v94|, v78, v192\n"
      "v_fma_f32 v192, |v95|, v79, v192\n"
      "v_mfma_f32_16x16x32_f16 v[80:83], a[12:15], v[124:127], v[80:83]\n"
      "s_waitcnt lgkmcnt(1)\n"
      "ds_read_b128 v[168:171], v203 offset:6464\n"
      "v_pk_mul_f16 v100, v132, v164\n"
      "v_mfma_f32_16x16x32_f16 v[84:87], a[28:31], v[124:127], v[20:23]\n"
      "v_permlane32_swap_b32 v194, v192\n"
      "v_mfma_f32_16x16x32_f16 v[84:87], a[24:27], v[120:123], v[84:87]\n"
      "v_pk_mul_f16 v101, v133, v165\n"
      "v_pk_mul_f16 v102, v134, v166\n"
      "v_mfma_f32_16x16x32_f16 v[84:87], a[20:23], v[116:119], v[84:87]\n"
      "v_fma_f32 v193, |v80|, v48, 4.0\n"
      "v_fma_f32 v193, |v81|, v49, v193\n"
      "v_mfma_f32_16x16x32_f16 v[84:87], a[16:19], v[112:115], v[84:87]\n"
      "v_fma_f32 v193, |v82|, v50, v193\n"
      "v_fma_f32 v193, |v83|, v51, v193\n"
      "v_mfma_f32_16x16x32_f16 v[88:91], a[32:35], v[112:115], v[24:27]\n"
      "v_pk_mul_f16 v103, v135, v167\n"
      "v_pk_mul_f16 v236, v244, v176\n"
      "v_mfma_f32_16x16x32_f16 v[88:91], a[36:39], v[116:119], v[88:91]\n"
      "ds_read_b128 a[188:191], v221 offset:64\n"
      "v_pk_mul_f16 v237, v245, v177\n"
      "v_mfma_f32_16x16x32_f16 v[88:91], a[40:43], v[120:123], v[88:91]\n"
      "v_fma_f32 v193, |v84|, v52, v193\n"
      "v_fma_f32 v193, |v85|, v53, v193\n"
      "v_mfma_f32_16x16x32_f16 v[88:91], a[44:47], v[124:127], v[88:91]\n"
      "v_fma_f32 v193, |v86|, v54, v193\n"
      "v_fma_f32 v193, |v87|, v55, v193\n"
      "v_mfma_f32_16x16x32_f16 v[92:95], a[60:63], v[124:127], v[28:31]\n"
      "s_waitcnt lgkmcnt(1)\n"
      "v_pk_mul_f16 v104, v136, v168\n"
      "v_pk_mul_f16 v105, v137, v169\n"
      "v_mfma_f32_16x16x32_f16 v[92:95], a[56:59], v[120:123], v[92:95]\n"
      "v_pk_mul_f16 v106, v138, v170\n"
      "v_pk_mul_f16 v107, v139, v171\n"
      "v_mfma_f32_16x16x32_f16 v[92:95], a[52:55], v[116:119], v[92:95]\n"
      "v_fma_f32 v193, |v88|, v56, v193\n"
      "v_fma_f32 v193, |v89|, v57, v193\n"
      "v_mfma_f32_16x16x32_f16 v[92:95], a[48:51], v[112:115], v[92:95]\n"
      "v_fma_f32 v193, |v90|, v58, v193\n"
      "v_fma_f32 v193, |v91|, v59, v193\n"
      "v_mfma_f32_16x16x32_f16 a[128:131], v[228:231], a[156:159], a[128:131]\n"
      "ds_read_b128 v[172:175], v203 offset:6528\n"
      "v_pk_mul_f16 v238, v246, v178\n"
      "v_mfma_f32_16x16x32_f16 v[80:83], a[64:67], v[112:115], v[32:35]\n"
      "v_add_u32_sdwa v10, s99, v199 dst_sel:DWORD dst_unused:UNUSED_PAD src0_sel:DWORD src1_sel:WORD_0\n"
      "v_add_u32_sdwa v11, s99, v199 dst_sel:DWORD dst_unused:UNUSED_PAD src0_sel:DWORD src1_sel:WORD_1\n"
      "v_mfma_f32_16x16x32_f16 v[80:83], a[68:71], v[116:119], v[80:83]\n"
      "v_fma_f32 v193, |v92|, v60, v193\n"
      "v_fma_f32 v193, |v93|, v61, v193\n"
      "v_mfma_f32_16x16x32_f16 v[80:83], a[72:75], v[120:123], v[80:83]\n"
      "v_fma_f32 v193, |v94|, v62, v193\n"
      "v_fma_f32 v193, |v95|, v63, v193\n"
      "v_mfma_f32_16x16x32_f16 v[80:83], a[76:79], v[124:127], v[80:83]\n"
      "v_pk_mul_f16 v239, v247, v179\n"
      "v_mov_b32 v7, 0xf149f2ca\n"
      "v_mfma_f32_16x16x32_f16 v[84:87], a[92:95], v[124:127], v[36:39]\n"
      "42:\n"
      "s_cbranch_execz 44b\n"
      "v_add_u32 v12, s98, v1\n"
      "v_mfma_f32_16x16x32_f16 v[84:87], a[88:91], v[120:123], v[84:87]\n"
      "s_and_b32 s100, s100, 0x37373737\n"
      "ds_read_b128 v[176:179], v214 offset:12032\n"
      "v_mov_b32 v8, 0xf149f2ca\n"
      "v_mfma_f32_16x16x32_f16 v[84:87], a[84:87], v[116:119], v[84:87]\n"
      "s_xor_b32 s100, s100, 0x370037\n"
      "v_fma_f32 v193, |v80|, v64, v193\n"
      "v_fma_f32 v193, |v81|, v65, v193\n"
      "v_mfma_f32_16x16x32_f16 v[84:87], a[80:83], v[112:115], v[84:87]\n"
      "s_waitcnt lgkmcnt(1)\n"
      "v_pk_mul_f16 v108, v140, v172\n"
      "v_pk_mul_f16 v109, v141, v173\n"
      "v_mfma_f32_16x16x32_f16 a[132:135], v[232:235], a[172:175], a[132:135]\n"
      "s_mov_b32 s101, s100\n"
      "v_fma_f32 v193, |v82|, v66, v193\n"
      "v_fma_f32 v193, |v83|, v67, v193\n"
      "v_mfma_f32_16x16x32_f16 v[88:91], a[96:99], v[112:115], v[40:43]\n"
      "s_or_b32 s44, s55, s41\n"
      "v_pk_mul_f16 v110, v142, v174\n"
      "v_pk_mul_f16 v111, v143, v175\n"
      "v_mfma_f32_16x16x32_f16 v[88:91], a[100:103], v[116:119], v[88:91]\n"
      "s_cselect_b32 s44, 2, 1\n"
      "v_fma_f32 v193, |v84|, v68, v193\n"
      "v_fma_f32 v193, |v85|, v69, v193\n"
      "v_mfma_f32_16x16x32_f16 v[88:91], a[104:107], v[120:123], v[88:91]\n"
      "v_fma_f32 v193, |v86|, v70, v193\n"
      "v_fma_f32 v193, |v87|, v71, v193\n"
      "v_mfma_f32_16x16x32_f16 v[88:91], a[108:111], v[124:127], v[88:91]\n"
      "ds_read_b128 a[160:163], v219 offset:128\n"
      "ds_read_b128 a[176:179], v220 offset:128\n"
      "v_mfma_f32_16x16x32_f16 v[92:95], a[124:127], v[124:127], v[44:47]\n"
      "s_waitcnt lgkmcnt(2)\n"
      "v_pk_mul_f16 v228, v136, v176\n"
      "v_pk_mul_f16 v229, v137, v177\n"
      "v_mfma_f32_16x16x32_f16 v[92:95], a[120:123], v[120:123], v[92:95]\n"
      "v_pk_mul_f16 v230, v138, v178\n"
      "v_pk_mul_f16 v231, v139, v179\n"
      "v_mfma_f32_16x16x32_f16 v[92:95], a[116:119], v[116:119], v[92:95]\n"
      "v_fma_f32 v193, |v88|, v72, v193\n"
      "v_fma_f32 v193, |v89|, v73, v193\n"
      "v_mfma_f32_16x16x32_f16 v[92:95], a[112:115], v[112:115], v[92:95]\n"
      "v_fma_f32 v193, |v90|, v74, v193\n"
      "v_fma_f32 v193, |v91|, v75, v193\n"
      "v_mfma_f32_16x16x32_f16 a[136:139], v[236:239], a[188:191], a[136:139]\n"
      "v_pk_mul_f16 v112, v144, v160\n"
      "v_pk_mul_f16 v113, v145, v161\n"
      "v_mfma_f32_16x16x32_f16 v[80:83], a[0:3], v[96:99], v[16:19]\n"
      "v_pk_mul_f16 v114, v146, v162\n"
      "v_pk_mul_f16 v115, v147, v163\n"
      "v_mfma_f32_16x16x32_f16 v[80:83], a[4:7], v[100:103], v[80:83]\n"
      "v_fma_f32 v193, |v92|, v76, v193\n"
      "v_fma_f32 v193, |v93|, v77, v193\n"
      "v_mfma_f32_16x16x32_f16 v[80:83], a[8:11], v[104:107], v[80:83]\n"
      "v_fma_f32 v193, |v94|, v78, v193\n"
      "v_fma_f32 v193, |v95|, v79, v193\n"
      "v_mfma_f32_16x16x32_f16 v[80:83], a[12:15], v[108:111], v[80:83]\n"
      "v_pk_mul_f16 v116, v148, v164\n"
      "v_pk_mul_f16 v117, v149, v165\n"
      "v_mfma_f32_16x16x32_f16 v[84:87], a[28:31], v[108:111], v[20:23]\n"
      "v_permlane32_swap_b32 v195, v193\n"
      "v_mfma_f32_16x16x32_f16 v[84:87], a[24:27], v[104:107], v[84:87]\n"
      "v_pk_mul_f16 v118, v150, v166\n"
      "v_pk_mul_f16 v119, v151, v167\n"
      "v_mfma_f32_16x16x32_f16 v[84:87], a[20:23], v[100:103], v[84:87]\n"
      "v_fma_f32 v196, |v80|, v48, 4.0\n"
      "v_fma_f32 v196, |v81|, v49, v196\n"
      "v_mfma_f32_16x16x32_f16 v[84:87], a[16:19], v[96:99], v[84:87]\n"
      "v_fma_f32 v196, |v82|, v50, v196\n"
      "v_fma_f32 v196, |v83|, v51, v196\n"
      "v_mfma_f32_16x16x32_f16 v[88:91], a[32:35], v[96:99], v[24:27]\n"
      "v_pk_mul_f16 v232, v152, v176\n"
      "v_pk_mul_f16 v120, v152, v168\n"
      "v_mfma_f32_16x16x32_f16 v[88:91], a[36:39], v[100:103], v[88:91]\n"
      "v_pk_mul_f16 v233, v153, v177\n"
      "v_pk_mul_f16 v121, v153, v169\n"
      "v_mfma_f32_16x16x32_f16 v[88:91], a[40:43], v[104:107], v[88:91]\n"
      "v_fma_f32 v196, |v84|, v52, v196\n"
      "v_fma_f32 v196, |v85|, v53, v196\n"
      "v_mfma_f32_16x16x32_f16 v[88:91], a[44:47], v[108:111], v[88:91]\n"
      "v_fma_f32 v196, |v86|, v54, v196\n"
      "v_fma_f32 v196, |v87|, v55, v196\n"
      "v_mfma_f32_16x16x32_f16 v[92:95], a[60:63], v[108:111], v[28:31]\n"
      "v_pk_mul_f16 v234, v154, v178\n"
      "v_pk_mul_f16 v122, v154, v170\n"
      "v_mfma_f32_16x16x32_f16 v[92:95], a[56:59], v[104:107], v[92:95]\n"
      "v_pk_mul_f16 v235, v155, v179\n"
      "v_pk_mul_f16 v123, v155, v171\n"
      "v_mfma_f32_16x16x32_f16 v[92:95], a[52:55], v[100:103], v[92:95]\n"
      "v_fma_f32 v196, |v88|, v56, v196\n"
      "v_fma_f32 v196, |v89|, v57, v196\n"
      "v_mfma_f32_16x16x32_f16 v[92:95], a[48:51], v[96:99], v[92:95]\n"
      "s_waitcnt lgkmcnt(1)\n"
      "ds_read_b128 a[192:195], v221 offset:128\n"
      "v_fma_f32 v196, |v90|, v58, v196\n"
      "v_mfma_f32_16x16x32_f16 a[128:131], v[228:231], a[160:163], a[128:131]\n"
      "v_fma_f32 v196, |v91|, v59, v196\n"
      "v_pk_mul_f16 v236, v248, v176\n"
      "v_mfma_f32_16x16x32_f16 v[80:83], a[64:67], v[96:99], v[32:35]\n"
      "v_pk_mul_f16 v237, v249, v177\n"
      "v_pk_mul_f16 v238, v250, v178\n"
      "v_mfma_f32_16x16x32_f16 v[80:83], a[68:71], v[100:103], v[80:83]\n"
      "v_fma_f32 v196, |v92|, v60, v196\n"
      "v_fma_f32 v196, |v93|, v61, v196\n"
      "v_mfma_f32_16x16x32_f16 v[80:83], a[72:75], v[104:107], v[80:83]\n"
      "v_fma_f32 v196, |v94|, v62, v196\n"
      "v_fma_f32 v196, |v95|, v63, v196\n"
      "v_mfma_f32_16x16x32_f16 v[80:83], a[76:79], v[108:111], v[80:83]\n"
      "v_pk_mul_f16 v239, v251, v179\n"
      "v_pk_mul_f16 v124, v156, v172\n"
      "v_mfma_f32_16x16x32_f16 v[84:87], a[92:95], v[108:111], v[36:39]\n"
      "v_pk_mul_f16 v125, v157, v173\n"
      "v_pk_mul_f16 v126, v158, v174\n"
      "v_mfma_f32_16x16x32_f16 v[84:87], a[88:91], v[104:107], v[84:87]\n"
      "ds_read_b128 v[176:179], v214 offset:12096\n"
      "v_mov_b32 v223, s44\n"
      "v_mfma_f32_16x16x32_f16 v[84:87], a[84:87], v[100:103], v[84:87]\n"
      "ds_write_b32 v2, v223 offset:8\n"
      "v_mfma_f32_16x16x32_f16 v[84:87], a[80:83], v[96:99], v[84:87]\n"
      "s_waitcnt lgkmcnt(3)\n"
      "v_fma_f32 v196, |v80|, v64, v196\n"
      "v_fma_f32 v196, |v81|, v65, v196\n"
      "v_mfma_f32_16x16x32_f16 a[132:135], v[232:235], a[176:179], a[132:135]\n"
      "v_fma_f32 v196, |v82|, v66, v196\n"
      "v_fma_f32 v196, |v83|, v67, v196\n"
      "v_mfma_f32_16x16x32_f16 v[88:91], a[96:99], v[96:99], v[40:43]\n"
      "v_add_f32 v194, v194, v192\n"
      "v_add_f32 v195, v195, v193\n"
      "v_mfma_f32_16x16x32_f16 v[88:91], a[100:103], v[100:103], v[88:91]\n"
      "ds_read_b128 v[160:163], v202 offset:4608\n"
      "v_pk_mul_f16 v127, v159, v175\n"
      "v_mfma_f32_16x16x32_f16 v[88:91], a[104:107], v[104:107], v[88:91]\n"
      "v_fma_f32 v196, |v84|, v68, v196\n"
      "v_fma_f32 v196, |v85|, v69, v196\n"
      "v_mfma_f32_16x16x32_f16 v[88:91], a[108:111], v[108:111], v[88:91]\n"
      "v_fma_f32 v196, |v86|, v70, v196\n"
      "v_fma_f32 v196, |v87|, v71, v196\n"
      "v_mfma_f32_16x16x32_f16 v[92:95], a[124:127], v[108:111], v[44:47]\n"
      "ds_read_b128 a[164:167], v219 offset:192\n"
      "ds_read_b128 v[164:167], v202 offset:4672\n"
      "v_mfma_f32_16x16x32_f16 v[92:95], a[120:123], v[104:107], v[92:95]\n"
      "s_waitcnt lgkmcnt(4)\n"
      "v_pk_mul_f16 v228, v140, v176\n"
      "v_pk_mul_f16 v229, v141, v177\n"
      "v_mfma_f32_16x16x32_f16 v[92:95], a[116:119], v[100:103], v[92:95]\n"
      "v_fma_f32 v196, |v88|, v72, v196\n"
      "v_fma_f32 v196, |v89|, v73, v196\n"
      "v_mfma_f32_16x16x32_f16 v[92:95], a[112:115], v[96:99], v[92:95]\n"
      "v_fma_f32 v196, |v90|, v74, v196\n"
      "v_fma_f32 v196, |v91|, v75, v196\n"
      "v_mfma_f32_16x16x32_f16 a[136:139], v[236:239], a[192:195], a[136:139]\n"
      "v_pk_mul_f16 v230, v142, v178\n"
      "v_pk_mul_f16 v231, v143, v179\n"
      "v_mfma_f32_16x16x32_f16 v[80:83], a[0:3], v[112:115], v[16:19]\n"
      "s_waitcnt lgkmcnt(2)\n"
      "v_pk_mul_f16 v96, v240, v160\n"
      "v_pk_mul_f16 v97, v241, v161\n"
      "v_mfma_f32_16x16x32_f16 v[80:83], a[4:7], v[116:119], v[80:83]\n"
      "v_fma_f32 v196, |v92|, v76, v196\n"
      "v_fma_f32 v196, |v93|, v77, v196\n"
      "v_mfma_f32_16x16x32_f16 v[80:83], a[8:11], v[120:123], v[80:83]\n"
      "v_fma_f32 v196, |v94|, v78, v196\n"
      "v_fma_f32 v196, |v95|, v79, v196\n"
      "v_mfma_f32_16x16x32_f16 v[80:83], a[12:15], v[124:127], v[80:83]\n"
      "v_pk_mul_f16 v98, v242, v162\n"
      "v_pk_mul_f16 v99, v243, v163\n"
      "v_mfma_f32_16x16x32_f16 v[84:87], a[28:31], v[124:127], v[20:23]\n"
      "v_permlane32_swap_b32 v190, v196\n"
      "v_mfma_f32_16x16x32_f16 v[84:87], a[24:27], v[120:123], v[84:87]\n"
      "s_waitcnt lgkmcnt(0)\n"
      "ds_read_b128 v[168:171], v202 offset:4736\n"
      "v_pk_mul_f16 v100, v244, v164\n"
      "v_mfma_f32_16x16x32_f16 v[84:87], a[20:23], v[116:119], v[84:87]\n"
      "v_fma_f32 v197, |v80|, v48, 4.0\n"
      "v_fma_f32 v197, |v81|, v49, v197\n"
      "v_mfma_f32_16x16x32_f16 v[84:87], a[16:19], v[112:115], v[84:87]\n"
      "ds_read_b128 a[180:183], v220 offset:192\n"
      "v_fma_f32 v197, |v82|, v50, v197\n"
      "v_mfma_f32_16x16x32_f16 v[88:91], a[32:35], v[112:115], v[24:27]\n"
      "v_fma_f32 v197, |v83|, v51, v197\n"
      "v_pk_mul_f16 v101, v245, v165\n"
      "v_mfma_f32_16x16x32_f16 v[88:91], a[36:39], v[116:119], v[88:91]\n"
      "v_pk_mul_f16 v102, v246, v166\n"
      "v_pk_mul_f16 v103, v247, v167\n"
      "v_mfma_f32_16x16x32_f16 v[88:91], a[40:43], v[120:123], v[88:91]\n"
      "v_fma_f32 v197, |v84|, v52, v197\n"
      "v_fma_f32 v197, |v85|, v53, v197\n"
      "v_mfma_f32_16x16x32_f16 v[88:91], a[44:47], v[124:127], v[88:91]\n"
      "v_fma_f32 v197, |v86|, v54, v197\n"
      "v_fma_f32 v197, |v87|, v55, v197\n"
      "v_mfma_f32_16x16x32_f16 v[92:95], a[60:63], v[124:127], v[28:31]\n"
      "v_pk_mul_f16 v232, v156, v176\n"
      "v_pk_mul_f16 v233, v157, v177\n"
      "v_mfma_f32_16x16x32_f16 v[92:95], a[56:59], v[120:123], v[92:95]\n"
      "s_waitcnt lgkmcnt(1)\n"
      "v_pk_mul_f16 v104, v248, v168\n"
      "v_pk_mul_f16 v105, v249, v169\n"
      "v_mfma_f32_16x16x32_f16 v[92:95], a[52:55], v[116:119], v[92:95]\n"
      "v_pk_mul_f16 v234, v158, v178\n"
      "v_pk_mul_f16 v106, v250, v170\n"
      "v_mfma_f32_16x16x32_f16 v[92:95], a[48:51], v[112:115], v[92:95]\n"
      "v_pk_mul_f16 v235, v159, v179\n"
      "v_pk_mul_f16 v107, v251, v171\n"
      "v_mfma_f32_16x16x32_f16 a[128:131], v[228:231], a[164:167], a[128:131]\n"
      "v_fma_f32 v197, |v88|, v56, v197\n"
      "v_fma_f32 v197, |v89|, v57, v197\n"
      "v_mfma_f32_16x16x32_f16 v[80:83], a[64:67], v[112:115], v[32:35]\n"
      "ds_read_b128 a[196:199], v221 offset:192\n"
      "v_fma_f32 v197, |v90|, v58, v197\n"
      "v_mfma_f32_16x16x32_f16 v[80:83], a[68:71], v[116:119], v[80:83]\n"
      "v_fma_f32 v197, |v91|, v59, v197\n"
      "v_fma_f32 v197, |v92|, v60, v197\n"
      "v_mfma_f32_16x16x32_f16 v[80:83], a[72:75], v[120:123], v[80:83]\n"
      "ds_write_b64 v218, a[128:129] offset:0\n"
      "v_mfma_f32_16x16x32_f16 v[80:83], a[76:79], v[124:127], v[80:83]\n"
      "v_fma_f32 v197, |v93|, v61, v197\n"
      "v_fma_f32 v197, |v94|, v62, v197\n"
      "v_mfma_f32_16x16x32_f16 v[84:87], a[92:95], v[124:127], v[36:39]\n"
      "ds_read_b128 v[172:175], v202 offset:4800\n"
      "v_fma_f32 v197, |v95|, v63, v197\n"
      "v_mfma_f32_16x16x32_f16 v[84:87], a[88:91], v[120:123], v[84:87]\n"
      "ds_write_b64 v218, a[130:131] offset:8\n"
      "v_mfma_f32_16x16x32_f16 v[84:87], a[84:87], v[116:119], v[84:87]\n"
      "v_pk_mul_f16 v236, v252, v176\n"
      "v_pk_mul_f16 v237, v253, v177\n"
      "v_mfma_f32_16x16x32_f16 v[84:87], a[80:83], v[112:115], v[84:87]\n"
      "s_waitcnt lgkmcnt(4)\n"
      "v_pk_mul_f16 v238, v254, v178\n"
      "v_pk_mul_f16 v239, v255, v179\n"
      "v_mfma_f32_16x16x32_f16 a[132:135], v[232:235], a[180:183], a[132:135]\n"
      "v_fma_f32 v197, |v80|, v64, v197\n"
      "v_fma_f32 v197, |v81|, v65, v197\n"
      "v_mfma_f32_16x16x32_f16 v[88:91], a[96:99], v[112:115], v[40:43]\n"
      "v_fma_f32 v197, |v82|, v66, v197\n"
      "v_fma_f32 v197, |v83|, v67, v197\n"
      "v_mfma_f32_16x16x32_f16 v[88:91], a[100:103], v[116:119], v[88:91]\n"
      "ds_read_b128 v[160:163], v203 offset:6912\n"
      "v_fma_f32 v197, |v84|, v68, v197\n"
      "v_mfma_f32_16x16x32_f16 v[88:91], a[104:107], v[120:123], v[88:91]\n"
      "ds_write_b64 v218, a[132:133] offset:1280\n"
      "v_mfma_f32_16x16x32_f16 v[88:91], a[108:111], v[124:127], v[88:91]\n"
      "s_waitcnt lgkmcnt(3)\n"
      "v_pk_mul_f16 v108, v252, v172\n"
      "v_pk_mul_f16 v109, v253, v173\n"
      "v_mfma_f32_16x16x32_f16 v[92:95], a[124:127], v[124:127], v[44:47]\n"
      "ds_write_b64 v218, a[134:135] offset:1288\n"
      "v_mfma_f32_16x16x32_f16 v[92:95], a[120:123], v[120:123], v[92:95]\n"
      "v_pk_mul_f16 v110, v254, v174\n"
      "v_pk_mul_f16 v111, v255, v175\n"
      "v_mfma_f32_16x16x32_f16 v[92:95], a[116:119], v[116:119], v[92:95]\n"
      "v_fma_f32 v197, |v85|, v69, v197\n"
      "v_fma_f32 v197, |v86|, v70, v197\n"
      "v_mfma_f32_16x16x32_f16 v[92:95], a[112:115], v[112:115], v[92:95]\n"
      "v_fma_f32 v197, |v87|, v71, v197\n"
      "v_fma_f32 v197, |v88|, v72, v197\n"
      "v_mfma_f32_16x16x32_f16 a[136:139], v[236:239], a[196:199], a[136:139]\n"
      "v_fma_f32 v197, |v89|, v73, v197\n"
      "v_fma_f32 v197, |v90|, v74, v197\n"
      "v_mfma_f32_16x16x32_f16 v[80:83], a[0:3], v[96:99], v[16:19]\n"
      "ds_read_b128 v[164:167], v203 offset:6976\n"
      "v_fma_f32 v197, |v91|, v75, v197\n"
      "v_mfma_f32_16x16x32_f16 v[80:83], a[4:7], v[100:103], v[80:83]\n"
      "s_waitcnt lgkmcnt(3)\n"
      "v_pk_mul_f16 v112, v128, v160\n"
      "v_pk_mul_f16 v113, v129, v161\n"
      "v_mfma_f32_16x16x32_f16 v[80:83], a[8:11], v[104:107], v[80:83]\n"
      "ds_write_b128 v218, a[136:139] offset:2560\n"
      "v_mfma_f32_16x16x32_f16 v[80:83], a[12:15], v[108:111], v[80:83]\n"
      "v_fma_f32 v197, |v92|, v76, v197\n"
      "v_fma_f32 v197, |v93|, v77, v197\n"
      "v_mfma_f32_16x16x32_f16 v[84:87], a[28:31], v[108:111], v[20:23]\n"
      "v_fma_f32 v197, |v94|, v78, v197\n"
      "v_fma_f32 v197, |v95|, v79, v197\n"
      "v_mfma_f32_16x16x32_f16 v[84:87], a[24:27], v[104:107], v[84:87]\n"
      "v_pk_mul_f16 v114, v130, v162\n"
      "v_pk_mul_f16 v115, v131, v163\n"
      "v_mfma_f32_16x16x32_f16 v[84:87], a[20:23], v[100:103], v[84:87]\n"
      "v_permlane32_swap_b32 v191, v197\n"
      "v_mfma_f32_16x16x32_f16 v[84:87], a[16:19], v[96:99], v[84:87]\n"
      "ds_read_b128 v[168:171], v203 offset:7040\n"
      "v_add_f32 v190, v190, v196\n"
      "v_mfma_f32_16x16x32_f16 v[88:91], a[32:35], v[96:99], v[24:27]\n"
      "v_fma_f32 v198, |v80|, v48, 4.0\n"
      "v_fma_f32 v198, |v81|, v49, v198\n"
      "v_mfma_f32_16x16x32_f16 v[88:91], a[36:39], v[100:103], v[88:91]\n"
      "v_permlane16_swap_b32 v194, v190\n"
      "v_mfma_f32_16x16x32_f16 v[88:91], a[40:43], v[104:107], v[88:91]\n"
      "v_fma_f32 v198, |v82|, v50, v198\n"
      "v_fma_f32 v198, |v83|, v51, v198\n"
      "v_mfma_f32_16x16x32_f16 v[88:91], a[44:47], v[108:111], v[88:91]\n"
      "s_waitcnt lgkmcnt(2)\n"
      "v_pk_mul_f16 v116, v132, v164\n"
      "v_pk_mul_f16 v117, v133, v165\n"
      "v_mfma_f32_16x16x32_f16 v[92:95], a[60:63], v[108:111], v[28:31]\n"
      "v_add_f32 v194, v194, v190\n"
      "v_add_f32 v191, v191, v197\n"
      "v_mfma_f32_16x16x32_f16 v[92:95], a[56:59], v[104:107], v[92:95]\n"
      "v_pk_mul_f16 v118, v134, v166\n"
      "v_pk_mul_f16 v119, v135, v167\n"
      "v_mfma_f32_16x16x32_f16 v[92:95], a[52:55], v[100:103], v[92:95]\n"
      "v_fma_f32 v198, |v84|, v52, v198\n"
      "v_fma_f32 v198, |v85|, v53, v198\n"
      "v_mfma_f32_16x16x32_f16 v[92:95], a[48:51], v[96:99], v[92:95]\n"
      "v_fma_f32 v198, |v86|, v54, v198\n"
      "v_fma_f32 v198, |v87|, v55, v198\n"
      "v_mfma_f32_16x16x32_f16 v[80:83], a[64:67], v[96:99], v[32:35]\n"
      "v_permlane16_swap_b32 v195, v191\n"
      "v_mfma_f32_16x16x32_f16 v[80:83], a[68:71], v[100:103], v[80:83]\n"
      "s_waitcnt lgkmcnt(0)\n"
      "v_pk_mul_f16 v120, v136, v168\n"
      "v_pk_mul_f16 v121, v137, v169\n"
      "v_mfma_f32_16x16x32_f16 v[80:83], a[72:75], v[104:107], v[80:83]\n"
      "v_fma_f32 v198, |v88|, v56, v198\n"
      "v_fma_f32 v198, |v89|, v57, v198\n"
      "v_mfma_f32_16x16x32_f16 v[80:83], a[76:79], v[108:111], v[80:83]\n"
      "ds_read_b128 v[172:175], v203 offset:7104\n"
      "v_fma_f32 v198, |v90|, v58, v198\n"
      "v_mfma_f32_16x16x32_f16 v[84:87], a[92:95], v[108:111], v[36:39]\n"
      "v_fma_f32 v198, |v91|, v59, v198\n"
      "v_fma_f32 v198, |v92|, v60, v198\n"
      "v_mfma_f32_16x16x32_f16 v[84:87], a[88:91], v[104:107], v[84:87]\n"
      "v_pk_mul_f16 v122, v138, v170\n"
      "v_pk_mul_f16 v123, v139, v171\n"
      "v_mfma_f32_16x16x32_f16 v[84:87], a[84:87], v[100:103], v[84:87]\n"
      "v_fma_f32 v198, |v93|, v61, v198\n"
      "v_fma_f32 v198, |v94|, v62, v198\n"
      "v_mfma_f32_16x16x32_f16 v[84:87], a[80:83], v[96:99], v[84:87]\n"
      "v_fma_f32 v198, |v95|, v63, v198\n"
      "v_fma_f32 v198, |v80|, v64, v198\n"
      "v_mfma_f32_16x16x32_f16 v[88:91], a[96:99], v[96:99], v[40:43]\n"
      "v_fma_f32 v198, |v81|, v65, v198\n"
      "v_fma_f32 v198, |v82|, v66, v198\n"
      "v_mfma_f32_16x16x32_f16 v[88:91], a[100:103], v[100:103], v[88:91]\n"
      "v_fma_f32 v198, |v83|, v67, v198\n"
      "v_add_f32 v195, v195, v191\n"
      "v_mfma_f32_16x16x32_f16 v[88:91], a[104:107], v[104:107], v[88:91]\n"
      "v_fma_f32 v198, |v84|, v68, v198\n"
      "v_fma_f32 v198, |v85|, v69, v198\n"
      "v_mfma_f32_16x16x32_f16 v[88:91], a[108:111], v[108:111], v[88:91]\n"
      "s_waitcnt lgkmcnt(0)\n"
      "v_pk_mul_f16 v124, v140, v172\n"
      "v_pk_mul_f16 v125, v141, v173\n"
      "v_mfma_f32_16x16x32_f16 v[92:95], a[124:127], v[108:111], v[44:47]\n"
      "v_pk_mul_f16 v126, v142, v174\n"
      "v_pk_mul_f16 v127, v143, v175\n"
      "v_mfma_f32_16x16x32_f16 v[92:95], a[120:123], v[104:107], v[92:95]\n"
      "v_fma_f32 v198, |v86|, v70, v198\n"
      "v_fma_f32 v198, |v87|, v71, v198\n"
      "v_mfma_f32_16x16x32_f16 v[92:95], a[116:119], v[100:103], v[92:95]\n"
      "v_fma_f32 v198, |v88|, v72, v198\n"
      "v_fma_f32 v198, |v89|, v73, v198\n"
      "v_mfma_f32_16x16x32_f16 v[92:95], a[112:115], v[96:99], v[92:95]\n"
      "v_fma_f32 v198, |v90|, v74, v198\n"
      "v_fma_f32 v198, |v91|, v75, v198\n"
      "v_mfma_f32_16x16x32_f16 v[80:83], a[0:3], v[112:115], v[16:19]\n"
      "v_pk_mul_f16 v96, v144, v160\n"
      "v_pk_mul_f16 v97, v145, v161\n"
      "v_mfma_f32_16x16x32_f16 v[80:83], a[4:7], v[116:119], v[80:83]\n"
      "v_pk_mul_f16 v98, v146, v162\n"
      "v_pk_mul_f16 v99, v147, v163\n"
      "v_mfma_f32_16x16x32_f16 v[80:83], a[8:11], v[120:123], v[80:83]\n"
      "v_fma_f32 v198, |v92|, v76, v198\n"
      "v_fma_f32 v198, |v93|, v77, v198\n"
      "v_mfma_f32_16x16x32_f16 v[80:83], a[12:15], v[124:127], v[80:83]\n"
      "v_fma_f32 v198, |v94|, v78, v198\n"
      "v_fma_f32 v198, |v95|, v79, v198\n"
      "v_mfma_f32_16x16x32_f16 v[84:87], a[28:31], v[124:127], v[20:23]\n"
      "v_pk_mul_f16 v100, v148, v164\n"
      "v_pk_mul_f16 v101, v149, v165\n"
      "v_mfma_f32_16x16x32_f16 v[84:87], a[24:27], v[120:123], v[84:87]\n"
      "v_pk_mul_f16 v102, v150, v166\n"
      "v_pk_mul_f16 v103, v151, v167\n"
      "v_mfma_f32_16x16x32_f16 v[84:87], a[20:23], v[116:119], v[84:87]\n"
      "v_fma_f32 v192, |v80|, v48, 4.0\n"
      "v_fma_f32 v192, |v81|, v49, v192\n"
      "v_mfma_f32_16x16x32_f16 v[84:87], a[16:19], v[112:115], v[84:87]\n"
      "v_fma_f32 v192, |v82|, v50, v192\n"
      "v_fma_f32 v192, |v83|, v51, v192\n"
      "v_mfma_f32_16x16x32_f16 v[88:91], a[32:35], v[112:115], v[24:27]\n"
      "v_pk_mul_f16 v104, v152, v168\n"
      "v_pk_mul_f16 v105, v153, v169\n"
      "v_mfma_f32_16x16x32_f16 v[88:91], a[36:39], v[116:119], v[88:91]\n"
      "v_pk_mul_f16 v106, v154, v170\n"
      "v_pk_mul_f16 v107, v155, v171\n"
      "v_mfma_f32_16x16x32_f16 v[88:91], a[40:43], v[120:123], v[88:91]\n"
      "v_fma_f32 v192, |v84|, v52, v192\n"
      "v_fma_f32 v192, |v85|, v53, v192\n"
      "v_mfma_f32_16x16x32_f16 v[88:91], a[44:47], v[124:127], v[88:91]\n"
      "v_fma_f32 v192, |v86|, v54, v192\n"
      "v_fma_f32 v192, |v87|, v55, v192\n"
      "v_mfma_f32_16x16x32_f16 v[92:95], a[60:63], v[124:127], v[28:31]\n"
      "ds_read2_b32 v[228:229], v204 offset0:0 offset1:8\n"
      "ds_read2_b32 v[230:231], v205 offset0:0 offset1:8\n"
      "v_mfma_f32_16x16x32_f16 v[92:95], a[56:59], v[120:123], v[92:95]\n"
      "ds_read_b32 v224, v227 offset:37640\n"
      "ds_read2_b32 v[232:233], v206 offset0:0 offset1:8\n"
      "v_mfma_f32_16x16x32_f16 v[92:95], a[52:55], v[116:119], v[92:95]\n"
      "v_fma_f32 v192, |v88|, v56, v192\n"
      "v_fma_f32 v192, |v89|, v57, v192\n"
      "v_mfma_f32_16x16x32_f16 v[92:95], a[48:51], v[112:115], v[92:95]\n"
      "v_fma_f32 v192, |v90|, v58, v192\n"
      "v_fma_f32 v192, |v91|, v59, v192\n"
      "v_mfma_f32_16x16x32_f16 v[80:83], a[64:67], v[112:115], v[32:35]\n"
      "v_pk_mul_f16 v108, v156, v172\n"
      "v_pk_mul_f16 v109, v157, v173\n"
      "v_mfma_f32_16x16x32_f16 v[80:83], a[68:71], v[116:119], v[80:83]\n"
      "ds_read2_b32 v[234:235], v207 offset0:0 offset1:8\n"
      "ds_read2_b32 v[236:237], v208 offset0:0 offset1:8\n"
      "v_mfma_f32_16x16x32_f16 v[80:83], a[72:75], v[120:123], v[80:83]\n"
      "v_fma_f32 v192, |v92|, v60, v192\n"
      "v_fma_f32 v192, |v93|, v61, v192\n"
      "v_mfma_f32_16x16x32_f16 v[80:83], a[76:79], v[124:127], v[80:83]\n"
      "v_fma_f32 v192, |v94|, v62, v192\n"
      "v_fma_f32 v192, |v95|, v63, v192\n"
      "v_mfma_f32_16x16x32_f16 v[84:87], a[92:95], v[124:127], v[36:39]\n"
      "v_pk_mul_f16 v110, v158, v174\n"
      "v_pk_mul_f16 v111, v159, v175\n"
      "v_mfma_f32_16x16x32_f16 v[84:87], a[88:91], v[120:123], v[84:87]\n"
      "s_waitcnt lgkmcnt(3)\n"
      "ds_read2_b32 v[238:239], v209 offset0:0 offset1:8\n"
      "v_cmp_eq_u32 vcc, 1, v224\n"
      "v_mfma_f32_16x16x32_f16 v[84:87], a[84:87], v[116:119], v[84:87]\n"
      "v_fma_f32 v192, |v80|, v64, v192\n"
      "v_fma_f32 v192, |v81|, v65, v192\n"
      "v_mfma_f32_16x16x32_f16 v[84:87], a[80:83], v[112:115], v[84:87]\n"
      "s_and_b32 s44, vcc_lo, s54\n"
      "v_fma_f32 v192, |v82|, v66, v192\n"
      "v_fma_f32 v192, |v83|, v67, v192\n"
      "v_mfma_f32_16x16x32_f16 v[88:91], a[96:99], v[112:115], v[40:43]\n"
      "s_cmp_lg_u32 s44, s54\n"
      "ds_read_b128 v[128:131], v210 offset:0\n"
      "ds_read_b128 v[144:147], v211 offset:0\n"
      "v_mfma_f32_16x16x32_f16 v[88:91], a[100:103], v[116:119], v[88:91]\n"
      "s_cbranch_scc1 80f\n"
      "81:\n"
      "v_mfma_f32_16x16x32_f16 v[88:91], a[104:107], v[120:123], v[88:91]\n"
      "v_fma_f32 v192, |v84|, v68, v192\n"
      "v_fma_f32 v192, |v85|, v69, v192\n"
      "v_mfma_f32_16x16x32_f16 v[88:91], a[108:111], v[124:127], v[88:91]\n"
      "v_fma_f32 v192, |v86|, v70, v192\n"
      "v_fma_f32 v192, |v87|, v71, v192\n"
      "v_mfma_f32_16x16x32_f16 v[92:95], a[124:127], v[124:127], v[44:47]\n"
      "v_pk_mul_f16 v124, v252, v172\n"
      "v_pk_mul_f16 v125, v253, v173\n"
      "v_mfma_f32_16x16x32_f16 v[92:95], a[120:123], v[120:123], v[92:95]\n"
      "v_pk_mul_f16 v120, v248, v168\n"
      "v_pk_mul_f16 v121, v249, v169\n"
      "v_mfma_f32_16x16x32_f16 v[92:95], a[116:119], v[116:119], v[92:95]\n"
      "v_fma_f32 v192, |v88|, v72, v192\n"
      "v_fma_f32 v192, |v89|, v73, v192\n"
      "v_mfma_f32_16x16x32_f16 v[92:95], a[112:115], v[112:115], v[92:95]\n"
      "v_fma_f32 v192, |v90|, v74, v192\n"
      "v_fma_f32 v192, |v91|, v75, v192\n"
      "v_mfma_f32_16x16x32_f16 v[80:83], a[0:3], v[96:99], v[16:19]\n"
      "v_pk_mul_f16 v112, v240, v160\n"
      "v_pk_mul_f16 v113, v241, v161\n"
      "v_mfma_f32_16x16x32_f16 v[80:83], a[4:7], v[100:103], v[80:83]\n"
      "v_pk_mul_f16 v114, v242, v162\n"
      "v_pk_mul_f16 v115, v243, v163\n"
      "v_mfma_f32_16x16x32_f16 v[80:83], a[8:11], v[104:107], v[80:83]\n"
      "v_fma_f32 v192, |v92|, v76, v192\n"
      "v_fma_f32 v192, |v93|, v77, v192\n"
      "v_mfma_f32_16x16x32_f16 v[80:83], a[12:15], v[108:111], v[80:83]\n"
      "v_fma_f32 v192, |v94|, v78, v192\n"
      "v_fma_f32 v192, |v95|, v79, v192\n"
      "v_mfma_f32_16x16x32_f16 v[84:87], a[28:31], v[108:111], v[20:23]\n"
      "v_pk_mul_f16 v116, v244, v164\n"
      "v_pk_mul_f16 v117, v245, v165\n"
      "v_mfma_f32_16x16x32_f16 v[84:87], a[24:27], v[104:107], v[84:87]\n"
      "v_pk_mul_f16 v118, v246, v166\n"
      "v_pk_mul_f16 v119, v247, v167\n"
      "v_mfma_f32_16x16x32_f16 v[84:87], a[20:23], v[100:103], v[84:87]\n"
      "v_fma_f32 v193, |v80|, v48, 4.0\n"
      "v_fma_f32 v193, |v81|, v49, v193\n"
      "v_mfma_f32_16x16x32_f16 v[84:87], a[16:19], v[96:99], v[84:87]\n"
      "v_fma_f32 v193, |v82|, v50, v193\n"
      "v_fma_f32 v193, |v83|, v51, v193\n"
      "v_mfma_f32_16x16x32_f16 v[88:91], a[32:35], v[96:99], v[24:27]\n"
      "v_pk_mul_f16 v122, v250, v170\n"
      "v_pk_mul_f16 v123, v251, v171\n"
      "v_mfma_f32_16x16x32_f16 v[88:91], a[36:39], v[100:103], v[88:91]\n"
      "v_pk_mul_f16 v126, v254, v174\n"
      "v_pk_mul_f16 v127, v255, v175\n"
      "v_mfma_f32_16x16x32_f16 v[88:91], a[40:43], v[104:107], v[88:91]\n"
      "v_fma_f32 v193, |v84|, v52, v193\n"
      "v_fma_f32 v193, |v85|, v53, v193\n"
      "v_mfma_f32_16x16x32_f16 v[88:91], a[44:47], v[108:111], v[88:91]\n"
      "v_fma_f32 v193, |v86|, v54, v193\n"
      "v_fma_f32 v193, |v87|, v55, v193\n"
      "v_mfma_f32_16x16x32_f16 v[92:95], a[60:63], v[108:111], v[28:31]\n"
      "ds_read_b128 v[160:163], v10 offset:0\n"
      "ds_read_b128 v[240:243], v11 offset:0\n"
      "v_mfma_f32_16x16x32_f16 v[92:95], a[56:59], v[104:107], v[92:95]\n"
      "ds_read_b128 v[164:167], v10 offset:64\n"
      "ds_read_b128 v[244:247], v11 offset:64\n"
      "v_mfma_f32_16x16x32_f16 v[92:95], a[52:55], v[100:103], v[92:95]\n"
      "v_fma_f32 v193, |v88|, v56, v193\n"
      "v_fma_f32 v193, |v89|, v57, v193\n"
      "v_mfma_f32_16x16x32_f16 v[92:95], a[48:51], v[96:99], v[92:95]\n"
      "v_fma_f32 v193, |v90|, v58, v193\n"
      "v_fma_f32 v193, |v91|, v59, v193\n"
      "v_mfma_f32_16x16x32_f16 v[80:83], a[64:67], v[96:99], v[32:35]\n"
      "ds_read_b128 v[168:171], v10 offset:128\n"
      "ds_read_b128 v[248:251], v11 offset:128\n"
      "v_mfma_f32_16x16x32_f16 v[80:83], a[68:71], v[100:103], v[80:83]\n"
      "ds_read_b128 v[172:175], v10 offset:192\n"
      "ds_read_b128 v[252:255], v11 offset:192\n"
      "v_mfma_f32_16x16x32_f16 v[80:83], a[72:75], v[104:107], v[80:83]\n"
      "v_fma_f32 v193, |v92|, v60, v193\n"
      "v_fma_f32 v193, |v93|, v61, v193\n"
      "v_mfma_f32_16x16x32_f16 v[80:83], a[76:79], v[108:111], v[80:83]\n"
      "v_fma_f32 v193, |v94|, v62, v193\n"
      "v_fma_f32 v193, |v95|, v63, v193\n"
      "v_mfma_f32_16x16x32_f16 v[84:87], a[92:95], v[108:111], v[36:39]\n"
      "s_waitcnt lgkmcnt(6)\n"
      "v_pk_mul_f16 v160, v160, v240\n"
      "v_pk_mul_f16 v161, v161, v241\n"
      "v_mfma_f32_16x16x32_f16 v[84:87], a[88:91], v[104:107], v[84:87]\n"
      "v_pk_mul_f16 v162, v162, v242\n"
      "v_pk_mul_f16 v163, v163, v243\n"
      "v_mfma_f32_16x16x32_f16 v[84:87], a[84:87], v[100:103], v[84:87]\n"
      "v_fma_f32 v193, |v80|, v64, v193\n"
      "v_fma_f32 v193, |v81|, v65, v193\n"
      "v_mfma_f32_16x16x32_f16 v[84:87], a[80:83], v[96:99], v[84:87]\n"
      "v_fma_f32 v193, |v82|, v66, v193\n"
      "v_fma_f32 v193, |v83|, v67, v193\n"
      "v_mfma_f32_16x16x32_f16 v[88:91], a[96:99], v[96:99], v[40:43]\n"
      "s_waitcnt lgkmcnt(4)\n"
      "v_pk_mul_f16 v164, v164, v244\n"
      "v_pk_mul_f16 v165, v165, v245\n"
      "v_mfma_f32_16x16x32_f16 v[88:91], a[100:103], v[100:103], v[88:91]\n"
      "43:\n"
      "s_cbranch_execz 44b\n"
      "v_pk_mul_f16 v166, v166, v246\n"
      "v_mfma_f32_16x16x32_f16 v[88:91], a[104:107], v[104:107], v[88:91]\n"
      "v_fma_f32 v193, |v84|, v68, v193\n"
      "v_fma_f32 v193, |v85|, v69, v193\n"
      "v_mfma_f32_16x16x32_f16 v[88:91], a[108:111], v[108:111], v[88:91]\n"
      "v_fma_f32 v193, |v86|, v70, v193\n"
      "v_fma_f32 v193, |v87|, v71, v193\n"
      "v_mfma_f32_16x16x32_f16 v[92:95], a[124:127], v[108:111], v[44:47]\n"
      "s_waitcnt lgkmcnt(2)\n"
      "v_pk_mul_f16 v167, v167, v247\n"
      "v_pk_mul_f16 v168, v168, v248\n"
      "v_mfma_f32_16x16x32_f16 v[92:95], a[120:123], v[104:107], v[92:95]\n"
      "v_pk_mul_f16 v169, v169, v249\n"
      "v_pk_mul_f16 v170, v170, v250\n"
      "v_mfma_f32_16x16x32_f16 v[92:95], a[116:119], v[100:103], v[92:95]\n"
      "v_fma_f32 v193, |v88|, v72, v193\n"
      "v_fma_f32 v193, |v89|, v73, v193\n"
      "v_mfma_f32_16x16x32_f16 v[92:95], a[112:115], v[96:99], v[92:95]\n"
      "v_fma_f32 v193, |v90|, v74, v193\n"
      "v_fma_f32 v193, |v91|, v75, v193\n"
      "v_mfma_f32_16x16x32_f16 v[80:83], a[0:3], v[112:115], v[16:19]\n"
      "ds_read_b128 v[132:135], v210 offset:64\n"
      "ds_read_b128 v[148:151], v211 offset:64\n"
      "v_mfma_f32_16x16x32_f16 v[80:83], a[4:7], v[116:119], v[80:83]\n"
      "v_pk_mul_f16 v128, v128, v144\n"
      "v_pk_mul_f16 v171, v171, v251\n"
      "v_mfma_f32_16x16x32_f16 v[80:83], a[8:11], v[120:123], v[80:83]\n"
      "v_fma_f32 v193, |v92|, v76, v193\n"
      "v_fma_f32 v193, |v93|, v77, v193\n"
      "v_mfma_f32_16x16x32_f16 v[80:83], a[12:15], v[124:127], v[80:83]\n"
      "v_fma_f32 v193, |v94|, v78, v193\n"
      "v_fma_f32 v193, |v95|, v79, v193\n"
      "v_mfma_f32_16x16x32_f16 v[84:87], a[28:31], v[124:127], v[20:23]\n"
      "v_pk_mul_f16 v129, v129, v145\n"
      "v_pk_mul_f16 v130, v130, v146\n"
      "v_mfma_f32_16x16x32_f16 v[84:87], a[24:27], v[120:123], v[84:87]\n"
      "s_waitcnt lgkmcnt(2)\n"
      "v_pk_mul_f16 v131, v131, v147\n"
      "v_pk_mul_f16 v172, v172, v252\n"
      "v_mfma_f32_16x16x32_f16 v[84:87], a[20:23], v[116:119], v[84:87]\n"
      "v_fma_f32 v190, |v80|, v48, 4.0\n"
      "v_fma_f32 v190, |v81|, v49, v190\n"
      "v_mfma_f32_16x16x32_f16 v[84:87], a[16:19], v[112:115], v[84:87]\n"
      "v_fma_f32 v190, |v82|, v50, v190\n"
      "v_fma_f32 v190, |v83|, v51, v190\n"
      "v_mfma_f32_16x16x32_f16 v[88:91], a[32:35], v[112:115], v[24:27]\n"
      "v_pk_mul_f16 v173, v173, v253\n"
      "v_pk_mul_f16 v174, v174, v254\n"
      "v_mfma_f32_16x16x32_f16 v[88:91], a[36:39], v[116:119], v[88:91]\n"
      "ds_read_b128 v[136:139], v210 offset:128\n"
      "ds_read_b128 v[152:155], v211 offset:128\n"
      "v_mfma_f32_16x16x32_f16 v[88:91], a[40:43], v[120:123], v[88:91]\n"
      "v_fma_f32 v190, |v84|, v52, v190\n"
      "v_fma_f32 v190, |v85|, v53, v190\n"
      "v_mfma_f32_16x16x32_f16 v[88:91], a[44:47], v[124:127], v[88:91]\n"
      "v_fma_f32 v190, |v86|, v54, v190\n"
      "v_fma_f32 v190, |v87|, v55, v190\n"
      "v_mfma_f32_16x16x32_f16 v[92:95], a[60:63], v[124:127], v[28:31]\n"
      "s_waitcnt lgkmcnt(2)\n"
      "v_pk_mul_f16 v132, v132, v148\n"
      "v_pk_mul_f16 v175, v175, v255\n"
      "v_mfma_f32_16x16x32_f16 v[92:95], a[56:59], v[120:123], v[92:95]\n"
      "v_pk_mul_f16 v133, v133, v149\n"
      "v_pk_mul_f16 v134, v134, v150\n"
      "v_mfma_f32_16x16x32_f16 v[92:95], a[52:55], v[116:119], v[92:95]\n"
      "v_fma_f32 v190, |v88|, v56, v190\n"
      "v_fma_f32 v190, |v89|, v57, v190\n"
      "v_mfma_f32_16x16x32_f16 v[92:95], a[48:51], v[112:115], v[92:95]\n"
      "v_fma_f32 v190, |v90|, v58, v190\n"
      "v_fma_f32 v190, |v91|, v59, v190\n"
      "v_mfma_f32_16x16x32_f16 v[80:83], a[64:67], v[112:115], v[32:35]\n"
      "v_fmac_f32 v189, v200, v228\n"
      "v_fmac_f32 v194, v200, v230\n"
      "v_mfma_f32_16x16x32_f16 v[80:83], a[68:71], v[116:119], v[80:83]\n"
      "v_permlane32_swap_b32 v198, v192\n"
      "v_mfma_f32_16x16x32_f16 v[80:83], a[72:75], v[120:123], v[80:83]\n"
      "v_fma_f32 v190, |v92|, v60, v190\n"
      "v_fma_f32 v190, |v93|, v61, v190\n"
      "v_mfma_f32_16x16x32_f16 v[80:83], a[76:79], v[124:127], v[80:83]\n"
      "v_fma_f32 v190, |v94|, v62, v190\n"
      "v_fma_f32 v190, |v95|, v63, v190\n"
      "v_mfma_f32_16x16x32_f16 v[84:87], a[92:95], v[124:127], v[36:39]\n"
      "s_waitcnt lgkmcnt(0)\n"
      "v_pk_mul_f16 v135, v135, v151\n"
      "v_pk_mul_f16 v136, v136, v152\n"
      "v_mfma_f32_16x16x32_f16 v[84:87], a[88:91], v[120:123], v[84:87]\n"
      "v_fmac_f32 v195, v200, v232\n"
      "v_max3_f32 v219, v189, v194, v195\n"
      "v_mfma_f32_16x16x32_f16 v[84:87], a[84:87], v[116:119], v[84:87]\n"
      "v_fma_f32 v190, |v80|, v64, v190\n"
      "v_fma_f32 v190, |v81|, v65, v190\n"
      "v_mfma_f32_16x16x32_f16 v[84:87], a[80:83], v[112:115], v[84:87]\n"
      "v_fma_f32 v190, |v82|, v66, v190\n"
      "v_fma_f32 v190, |v83|, v67, v190\n"
      "v_mfma_f32_16x16x32_f16 v[88:91], a[96:99], v[112:115], v[40:43]\n"
      "ds_read_b128 a[200:203], v215 offset:11904\n"
      "v_min3_f32 v226, v189, v194, v195\n"
      "v_mfma_f32_16x16x32_f16 v[88:91], a[100:103], v[116:119], v[88:91]\n"
      "ds_read_b128 v[140:143], v210 offset:192\n"
      "ds_read_b128 v[156:159], v211 offset:192\n"
      "v_mfma_f32_16x16x32_f16 v[88:91], a[104:107], v[120:123], v[88:91]\n"
      "v_fma_f32 v190, |v84|, v68, v190\n"
      "v_fma_f32 v190, |v85|, v69, v190\n"
      "v_mfma_f32_16x16x32_f16 v[88:91], a[108:111], v[124:127], v[88:91]\n"
      "v_fma_f32 v190, |v86|, v70, v190\n"
      "v_fma_f32 v190, |v87|, v71, v190\n"
      "v_mfma_f32_16x16x32_f16 v[92:95], a[124:127], v[124:127], v[44:47]\n"
      "ds_read_b128 a[204:207], v215 offset:11968\n"
      "v_sub_f32 v189, v189, v219\n"
      "v_mfma_f32_16x16x32_f16 v[92:95], a[120:123], v[120:123], v[92:95]\n"
      "ds_read_b128 a[208:211], v215 offset:12032\n"
      "v_sub_f32 v194, v194, v219\n"
      "v_mfma_f32_16x16x32_f16 v[92:95], a[116:119], v[116:119], v[92:95]\n"
      "v_fma_f32 v190, |v88|, v72, v190\n"
      "v_fma_f32 v190, |v89|, v73, v190\n"
      "v_mfma_f32_16x16x32_f16 v[92:95], a[112:115], v[112:115], v[92:95]\n"
      "v_fma_f32 v190, |v90|, v74, v190\n"
      "v_fma_f32 v190, |v91|, v75, v190\n"
      "v_mfma_f32_16x16x32_f16 v[80:83], a[0:3], v[160:163], v[16:19]\n"
      "v_pk_mul_f16 v137, v137, v153\n"
      "v_pk_mul_f16 v138, v138, v154\n"
      "v_mfma_f32_16x16x32_f16 v[80:83], a[4:7], v[164:167], v[80:83]\n"
      "ds_read_b128 a[212:215], v215 offset:12096\n"
      "v_sub_f32 v195, v195, v219\n"
      "v_mfma_f32_16x16x32_f16 v[80:83], a[8:11], v[168:171], v[80:83]\n"
      "v_exp_f32 v189, v189\n"
      "v_mfma_f32_16x16x32_f16 v[80:83], a[12:15], v[172:175], v[80:83]\n"
      "v_exp_f32 v194, v194\n"
      "v_mfma_f32_16x16x32_f16 v[84:87], a[28:31], v[172:175], v[20:23]\n"
      "v_exp_f32 v195, v195\n"
      "v_mfma_f32_16x16x32_f16 v[84:87], a[24:27], v[168:171], v[84:87]\n"
      "v_fma_f32 v190, |v92|, v76, v190\n"
      "v_fma_f32 v190, |v93|, v77, v190\n"
      "v_mfma_f32_16x16x32_f16 v[84:87], a[20:23], v[164:167], v[84:87]\n"
      "s_waitcnt lgkmcnt(3)\n"
      "v_pk_mul_f16 v139, v139, v155\n"
      "v_pk_mul_f16 v140, v140, v156\n"
      "v_mfma_f32_16x16x32_f16 v[84:87], a[16:19], v[160:163], v[84:87]\n"
      "v_fma_f32 v190, |v94|, v78, v190\n"
      "v_fma_f32 v190, |v95|, v79, v190\n"
      "v_mfma_f32_16x16x32_f16 v[88:91], a[32:35], v[160:163], v[24:27]\n"
      "v_mul_f32 v221, v189, v229\n"
      "v_add_f32 v198, v198, v192\n"
      "v_mfma_f32_16x16x32_f16 v[88:91], a[36:39], v[164:167], v[88:91]\n"
      "v_add_f32 v220, v189, v194\n"
      "v_fma_f32 v199, |v80|, v48, 2.0\n"
      "v_mfma_f32_16x16x32_f16 v[88:91], a[40:43], v[168:171], v[88:91]\n"
      "v_permlane16_swap_b32 v188, v198\n"
      "v_mfma_f32_16x16x32_f16 v[88:91], a[44:47], v[172:175], v[88:91]\n"
      "v_fma_f32 v199, |v81|, v49, v199\n"
      "v_fma_f32 v199, |v82|, v50, v199\n"
      "v_mfma_f32_16x16x32_f16 v[92:95], a[60:63], v[172:175], v[28:31]\n"
      "v_fmac_f32 v221, v194, v231\n"
      "v_fma_f32 v199, |v83|, v51, v199\n"
      "v_mfma_f32_16x16x32_f16 v[92:95], a[56:59], v[168:171], v[92:95]\n"
      "v_pk_mul_f16 v141, v141, v157\n"
      "v_add_f32 v220, v220, v195\n"
      "v_mfma_f32_16x16x32_f16 v[92:95], a[52:55], v[164:167], v[92:95]\n"
      "v_add_f32 v188, v188, v198\n"
      "v_pk_mul_f16 v142, v142, v158\n"
      "v_mfma_f32_16x16x32_f16 v[92:95], a[48:51], v[160:163], v[92:95]\n"
      "v_fmac_f32 v221, v195, v233\n"
      "v_fmac_f32 v188, v200, v234\n"
      "v_mfma_f32_16x16x32_f16 v[80:83], a[0:3], v[128:131], v[16:19]\n"
      "v_pk_mul_f16 v143, v143, v159\n"
      "v_fma_f32 v199, |v84|, v52, v199\n"
      "v_mfma_f32_16x16x32_f16 v[80:83], a[4:7], v[132:135], v[80:83]\n"
      "v_fma_f32 v199, |v85|, v53, v199\n"
      "v_fma_f32 v199, |v86|, v54, v199\n"
      "v_mfma_f32_16x16x32_f16 v[80:83], a[8:11], v[136:139], v[80:83]\n"
      "v_fma_f32 v199, |v87|, v55, v199\n"
      "v_fma_f32 v199, |v88|, v56, v199\n"
      "v_mfma_f32_16x16x32_f16 v[80:83], a[12:15], v[140:143], v[80:83]\n"
      "v_fma_f32 v199, |v89|, v57, v199\n"
      "v_fma_f32 v199, |v90|, v58, v199\n"
      "v_mfma_f32_16x16x32_f16 v[84:87], a[28:31], v[140:143], v[20:23]\n"
      "v_fma_f32 v199, |v91|, v59, v199\n"
      "v_fma_f32 v199, |v92|, v60, v199\n"
      "v_mfma_f32_16x16x32_f16 v[84:87], a[24:27], v[136:139], v[84:87]\n"
      "ds_read_b128 v[112:115], v216 offset:0\n"
      "ds_read_b128 v[144:147], v217 offset:0\n"
      "v_mfma_f32_16x16x32_f16 v[84:87], a[20:23], v[132:135], v[84:87]\n"
      "v_permlane32_swap_b32 v193, v190\n"
      "v_mfma_f32_16x16x32_f16 v[84:87], a[16:19], v[128:131], v[84:87]\n"
      "v_fma_f32 v199, |v93|, v61, v199\n"
      "v_fma_f32 v199, |v94|, v62, v199\n"
      "v_mfma_f32_16x16x32_f16 v[180:183], a[200:203], v[160:163], 0\n"
      "v_fma_f32 v199, |v95|, v63, v199\n"
      "v_fma_f32 v191, |v80|, v48, 4.0\n"
      "v_mfma_f32_16x16x32_f16 v[88:91], a[32:35], v[128:131], v[24:27]\n"
      "ds_write_b32 v1, v199 offset:12992\n"
      "v_mfma_f32_16x16x32_f16 v[88:91], a[36:39], v[132:135], v[88:91]\n"
      "ds_read_b128 v[116:119], v216 offset:64\n"
      "ds_read_b128 v[148:151], v217 offset:64\n"
      "v_mfma_f32_16x16x32_f16 v[88:91], a[40:43], v[136:139], v[88:91]\n"
      "v_fma_f32 v191, |v81|, v49, v191\n"
      "v_fma_f32 v191, |v82|, v50, v191\n"
      "v_mfma_f32_16x16x32_f16 v[88:91], a[44:47], v[140:143], v[88:91]\n"
      "v_fma_f32 v191, |v83|, v51, v191\n"
      "v_fma_f32 v191, |v84|, v52, v191\n"
      "v_mfma_f32_16x16x32_f16 v[92:95], a[60:63], v[140:143], v[28:31]\n"
      "s_waitcnt lgkmcnt(3)\n"
      "v_pk_mul_f16 v112, v112, v144\n"
      "v_pk_mul_f16 v113, v113, v145\n"
      "v_mfma_f32_16x16x32_f16 v[92:95], a[56:59], v[136:139], v[92:95]\n"
      "v_pk_mul_f16 v114, v114, v146\n"
      "v_pk_mul_f16 v115, v115, v147\n"
      "v_mfma_f32_16x16x32_f16 v[92:95], a[52:55], v[132:135], v[92:95]\n"
      "ds_read_b128 v[120:123], v216 offset:128\n"
      "ds_read_b128 v[152:155], v217 offset:128\n"
      "v_mfma_f32_16x16x32_f16 v[92:95], a[48:51], v[128:131], v[92:95]\n"
      "v_fma_f32 v191, |v85|, v53, v191\n"
      "v_fma_f32 v191, |v86|, v54, v191\n"
      "v_mfma_f32_16x16x32_f16 v[180:183], a[204:207], v[164:167], v[180:183]\n"
      "v_fma_f32 v191, |v87|, v55, v191\n"
      "v_fma_f32 v191, |v88|, v56, v191\n"
      "v_mfma_f32_16x16x32_f16 v[80:83], a[64:67], v[128:131], v[32:35]\n"
      "s_waitcnt lgkmcnt(2)\n"
      "v_pk_mul_f16 v116, v116, v148\n"
      "v_pk_mul_f16 v117, v117, v149\n"
      "v_mfma_f32_16x16x32_f16 v[80:83], a[68:71], v[132:135], v[80:83]\n"
      "v_fma_f32 v191, |v89|, v57, v191\n"
      "v_fma_f32 v191, |v90|, v58, v191\n"
      "v_mfma_f32_16x16x32_f16 v[80:83], a[72:75], v[136:139], v[80:83]\n"
      "v_pk_mul_f16 v118, v118, v150\n"
      "v_pk_mul_f16 v119, v119, v151\n"
      "v_mfma_f32_16x16x32_f16 v[80:83], a[76:79], v[140:143], v[80:83]\n"
      "ds_read_b128 v[124:127], v216 offset:192\n"
      "ds_read_b128 v[156:159], v217 offset:192\n"
      "v_mfma_f32_16x16x32_f16 v[84:87], a[92:95], v[140:143], v[36:39]\n"
      "v_fma_f32 v191, |v91|, v59, v191\n"
      "v_fma_f32 v191, |v92|, v60, v191\n"
      "v_mfma_f32_16x16x32_f16 v[84:87], a[88:91], v[136:139], v[84:87]\n"
      "s_waitcnt lgkmcnt(2)\n"
      "v_pk_mul_f16 v120, v120, v152\n"
      "v_pk_mul_f16 v121, v121, v153\n"
      "v_mfma_f32_16x16x32_f16 v[84:87], a[84:87], v[132:135], v[84:87]\n"
      "v_fma_f32 v191, |v93|, v61, v191\n"
      "v_fma_f32 v191, |v94|, v62, v191\n"
      "v_mfma_f32_16x16x32_f16 v[84:87], a[80:83], v[128:131], v[84:87]\n"
      "v_fma_f32 v191, |v95|, v63, v191\n"
      "v_fma_f32 v191, |v80|, v64, v191\n"
      "v_mfma_f32_16x16x32_f16 v[88:91], a[96:99], v[128:131], v[40:43]\n"
      "v_pk_mul_f16 v122, v122, v154\n"
      "v_pk_mul_f16 v123, v123, v155\n"
      "v_mfma_f32_16x16x32_f16 v[88:91], a[100:103], v[132:135], v[88:91]\n"
      "v_fma_f32 v191, |v81|, v65, v191\n"
      "v_fma_f32 v191, |v82|, v66, v191\n"
      "v_mfma_f32_16x16x32_f16 v[88:91], a[104:107], v[136:139], v[88:91]\n"
      "v_fma_f32 v191, |v83|, v67, v191\n"
      "v_fma_f32 v191, |v84|, v68, v191\n"
      "v_mfma_f32_16x16x32_f16 v[88:91], a[108:111], v[140:143], v[88:91]\n"
      "s_waitcnt lgkmcnt(0)\n"
      "v_pk_mul_f16 v124, v124, v156\n"
      "v_pk_mul_f16 v125, v125, v157\n"
      "v_mfma_f32_16x16x32_f16 v[92:95], a[124:127], v[140:143], v[44:47]\n"
      "v_pk_mul_f16 v126, v126, v158\n"
      "v_pk_mul_f16 v127, v127, v159\n"
      "v_mfma_f32_16x16x32_f16 v[92:95], a[120:123], v[136:139], v[92:95]\n"
      "v_fma_f32 v191, |v85|, v69, v191\n"
      "v_fma_f32 v191, |v86|, v70, v191\n"
      "v_mfma_f32_16x16x32_f16 v[92:95], a[116:119], v[132:135], v[92:95]\n"
      "v_fma_f32 v191, |v87|, v71, v191\n"
      "v_fma_f32 v191, |v88|, v72, v191\n"
      "v_mfma_f32_16x16x32_f16 v[92:95], a[112:115], v[128:131], v[92:95]\n"
      "v_fma_f32 v191, |v89|, v73, v191\n"
      "v_fma_f32 v191, |v90|, v74, v191\n"
      "v_mfma_f32_16x16x32_f16 v[80:83], a[0:3], v[112:115], v[16:19]\n"
      "v_fma_f32 v191, |v91|, v75, v191\n"
      "v_add_f32 v193, v193, v190\n"
      "v_mfma_f32_16x16x32_f16 v[80:83], a[4:7], v[116:119], v[80:83]\n"
      "ds_read_b32 v224, v12 offset:12992\n"
      "v_mfma_f32_16x16x32_f16 v[80:83], a[8:11], v[120:123], v[80:83]\n"
      "v_permlane16_swap_b32 v193, v7\n"
      "v_mfma_f32_16x16x32_f16 v[80:83], a[12:15], v[124:127], v[80:83]\n"
      "v_fma_f32 v191, |v92|, v76, v191\n"
      "v_fma_f32 v191, |v93|, v77, v191\n"
      "v_mfma_f32_16x16x32_f16 v[84:87], a[28:31], v[124:127], v[20:23]\n"
      "v_fma_f32 v191, |v94|, v78, v191\n"
      "v_fma_f32 v191, |v95|, v79, v191\n"
      "v_mfma_f32_16x16x32_f16 v[84:87], a[24:27], v[120:123], v[84:87]\n"
      "v_add_f32 v193, v193, v7\n"
      "v_fmac_f32 v193, v200, v236\n"
      "v_mfma_f32_16x16x32_f16 v[84:87], a[20:23], v[116:119], v[84:87]\n"
      "v_fma_f32 v196, |v80|, v48, 4.0\n"
      "v_fma_f32 v196, |v81|, v49, v196\n"
      "v_mfma_f32_16x16x32_f16 v[84:87], a[16:19], v[112:115], v[84:87]\n"
      "v_fma_f32 v196, |v82|, v50, v196\n"
      "v_fma_f32 v196, |v83|, v51, v196\n"
      "v_mfma_f32_16x16x32_f16 v[88:91], a[32:35], v[112:115], v[24:27]\n"
      "v_max3_f32 v222, v219, v188, v193\n"
      "v_min3_f32 v226, v226, v188, v193\n"
      "v_mfma_f32_16x16x32_f16 v[88:91], a[36:39], v[116:119], v[88:91]\n"
      "v_sub_f32 v219, v219, v222\n"
      "v_sub_f32 v188, v188, v222\n"
      "v_mfma_f32_16x16x32_f16 v[88:91], a[40:43], v[120:123], v[88:91]\n"
      "v_fma_f32 v196, |v84|, v52, v196\n"
      "v_fma_f32 v196, |v85|, v53, v196\n"
      "v_mfma_f32_16x16x32_f16 v[88:91], a[44:47], v[124:127], v[88:91]\n"
      "v_fma_f32 v196, |v86|, v54, v196\n"
      "v_fma_f32 v196, |v87|, v55, v196\n"
      "v_mfma_f32_16x16x32_f16 v[92:95], a[60:63], v[124:127], v[28:31]\n"
      "s_waitcnt lgkmcnt(0)\n"
      "v_sub_f32 v193, v193, v222\n"
      "v_cmp_eq_u32 vcc, s37, v224\n"
      "v_mfma_f32_16x16x32_f16 v[92:95], a[56:59], v[120:123], v[92:95]\n"
      "v_exp_f32 v219, v219\n"
      "v_mfma_f32_16x16x32_f16 v[92:95], a[52:55], v[116:119], v[92:95]\n"
      "s_or_b32 vcc_lo, vcc_lo, s36\n"
      "v_exp_f32 v188, v188\n"
      "v_mfma_f32_16x16x32_f16 v[92:95], a[48:51], v[112:115], v[92:95]\n"
      "s_cmp_lg_u64 vcc, 0\n"
      "v_exp_f32 v193, v193\n"
      "v_mfma_f32_16x16x32_f16 v[80:83], a[64:67], v[112:115], v[32:35]\n"
      "s_cbranch_scc1 82f\n"
      "83:\n"
      "v_mfma_f32_16x16x32_f16 v[80:83], a[68:71], v[116:119], v[80:83]\n"
      "v_fma_f32 v196, |v88|, v56, v196\n"
      "v_fma_f32 v196, |v89|, v57, v196\n"
      "v_mfma_f32_16x16x32_f16 v[80:83], a[72:75], v[120:123], v[80:83]\n"
      "v_fma_f32 v196, |v90|, v58, v196\n"
      "v_fma_f32 v196, |v91|, v59, v196\n"
      "v_mfma_f32_16x16x32_f16 v[80:83], a[76:79], v[124:127], v[80:83]\n"
      "v_mul_f32 v221, v221, v219\n"
      "v_fma_f32 v196, |v92|, v60, v196\n"
      "v_mfma_f32_16x16x32_f16 v[84:87], a[92:95], v[124:127], v[36:39]\n"
      "v_fma_f32 v196, |v93|, v61, v196\n"
      "v_fma_f32 v220, v220, v219, v188\n"
      "v_mfma_f32_16x16x32_f16 v[84:87], a[88:91], v[120:123], v[84:87]\n"
      "v_fma_f32 v196, |v94|, v62, v196\n"
      "v_fma_f32 v196, |v95|, v63, v196\n"
      "v_mfma_f32_16x16x32_f16 v[84:87], a[84:87], v[116:119], v[84:87]\n"
      "v_fma_f32 v196, |v80|, v64, v196\n"
      "v_fma_f32 v196, |v81|, v65, v196\n"
      "v_mfma_f32_16x16x32_f16 v[84:87], a[80:83], v[112:115], v[84:87]\n"
      "v_fma_f32 v196, |v82|, v66, v196\n"
      "v_fma_f32 v196, |v83|, v67, v196\n"
      "v_mfma_f32_16x16x32_f16 v[88:91], a[96:99], v[112:115], v[40:43]\n"
      "v_fmac_f32 v221, v188, v235\n"
      "v_add_f32 v199, v199, v224\n"
      "v_mfma_f32_16x16x32_f16 v[88:91], a[100:103], v[116:119], v[88:91]\n"
      "v_add_f32 v220, v220, v193\n"
      "v_cndmask_b32 v199, v8, v199, s[100:101]\n"
      "v_mfma_f32_16x16x32_f16 v[88:91], a[104:107], v[120:123], v[88:91]\n"
      "v_fma_f32 v196, |v84|, v68, v196\n"
      "v_fma_f32 v196, |v85|, v69, v196\n"
      "v_mfma_f32_16x16x32_f16 v[88:91], a[108:111], v[124:127], v[88:91]\n"
      "v_fma_f32 v196, |v86|, v70, v196\n"
      "v_fma_f32 v196, |v87|, v71, v196\n"
      "v_mfma_f32_16x16x32_f16 v[92:95], a[124:127], v[124:127], v[44:47]\n"
      "v_fmac_f32 v221, v193, v237\n"
      "v_fma_f32 v220, v226, 0, v220\n"
      "v_mfma_f32_16x16x32_f16 v[92:95], a[120:123], v[120:123], v[92:95]\n"
      "v_permlane32_swap_b32 v199, v191\n"
      "v_mfma_f32_16x16x32_f16 v[92:95], a[116:119], v[116:119], v[92:95]\n"
      "v_fma_f32 v196, |v88|, v72, v196\n"
      "v_fma_f32 v196, |v89|, v73, v196\n"
      "v_mfma_f32_16x16x32_f16 v[92:95], a[112:115], v[112:115], v[92:95]\n"
      "v_fma_f32 v196, |v90|, v74, v196\n"
      "v_fma_f32 v196, |v91|, v75, v196\n"
      "v_add_f32 v199, v199, v191\n"
      "v_mfma_f32_16x16x32_f16 v[180:183], a[208:211], v[168:171], v[180:183]\n"
      "s_movk_i32 s45, 0x207\n"
      "v_max_f32_dpp v223, v222, v222 row_ror:4 row_mask:0xf bank_mask:0xf\n"
      "v_max_f32_dpp v223, v222, v223 row_ror:8 row_mask:0xf bank_mask:0xf\n"
      "v_mfma_f32_16x16x32_f16 v[180:183], a[212:215], v[172:175], v[180:183]\n"
      "v_fma_f32 v196, |v92|, v76, v196\n"
      "v_fma_f32 v196, |v93|, v77, v196\n"
      "v_fma_f32 v196, |v94|, v78, v196\n"
      "v_fma_f32 v196, |v95|, v79, v196\n"
      "v_max_f32_dpp v223, v222, v223 row_ror:12 row_mask:0xf bank_mask:0xf\n"
      "v_ldexp_f32 v221, v221, s39\n"
      "v_permlane32_swap_b32 v196, v8\n"
      "v_add_f32 v196, v196, v8\n"
      "v_mov_b32_dpp v238, v180 quad_perm:[0,1,2,3] row_mask:0x1 bank_mask:0xf\n"
      "v_mov_b32_dpp v239, v181 quad_perm:[0,1,2,3] row_mask:0x1 bank_mask:0xf\n"
      "v_permlane16_swap_b32 v199, v196\n"
      "v_add_f32 v199, v199, v196\n"
      "v_fmac_f32 v199, v200, v238\n"
      "v_max_f32 v223, v223, v199\n"
      "v_fma_f32 v220, v199, 0, v220\n"
      "v_max_f32_dpp v223, v199, v223 row_ror:4 row_mask:0xf bank_mask:0xf\n"
      "v_max_f32_dpp v223, v199, v223 row_ror:8 row_mask:0xf bank_mask:0xf\n"
      "v_max_f32_dpp v223, v199, v223 row_ror:12 row_mask:0xf bank_mask:0xf\n"
      "v_ldexp_f32 v239, v239, s39\n"
      "s_nop 0\n"
      "v_max_f32_dpp v225, v223, v223 row_ror:1 row_mask:0xf bank_mask:0xf\n"
      "v_max_f32_dpp v225, v223, v225 row_ror:2 row_mask:0xf bank_mask:0xf\n"
      "v_max_f32_dpp v225, v223, v225 row_ror:3 row_mask:0xf bank_mask:0xf\n"
      "s_nop 1\n"
      "v_max_f32_dpp v225, v225, v225 row_bcast:15 row_mask:0xa bank_mask:0xf\n"
      "s_nop 1\n"
      "v_max_f32_dpp v225, v225, v225 row_bcast:31 row_mask:0xc bank_mask:0xf\n"
      "s_nop 0\n"
      "v_readlane_b32 s44, v225, 63\n"
      "s_nop 1\n"
      "v_subrev_f32 v223, s44, v222\n"
      "v_subrev_f32 v199, s44, v199\n"
      "v_exp_f32 v223, v223\n"
      "v_exp_f32 v199, v199\n"
      "v_mul_f32 v221, v221, v223\n"
      "v_fma_f32 v220, v220, v223, v199\n"
      "v_fmac_f32 v221, v199, v239\n"
      "s_nop 0\n"
      "v_add_f32_dpp v220, v220, v220 row_ror:8 row_mask:0xf bank_mask:0xf\n"
      "v_add_f32_dpp v221, v221, v221 row_ror:8 row_mask:0xf bank_mask:0xf\n"
      "s_nop 0\n"
      "v_add_f32_dpp v220, v220, v220 row_ror:4 row_mask:0xf bank_mask:0xf\n"
      "v_add_f32_dpp v221, v221, v221 row_ror:4 row_mask:0xf bank_mask:0xf\n"
      "s_nop 0\n"
      "v_add_f32_dpp v220, v220, v220 row_ror:2 row_mask:0xf bank_mask:0xf\n"
      "v_add_f32_dpp v221, v221, v221 row_ror:2 row_mask:0xf bank_mask:0xf\n"
      "s_nop 0\n"
      "v_add_f32_dpp v220, v220, v220 row_ror:1 row_mask:0xf bank_mask:0xf\n"
      "v_add_f32_dpp v221, v221, v221 row_ror:1 row_mask:0xf bank_mask:0xf\n"
      "s_nop 0\n"
      "v_add_f32_dpp v220, v220, v220 row_bcast:15 row_mask:0xa bank_mask:0xf\n"
      "v_add_f32_dpp v221, v221, v221 row_bcast:15 row_mask:0xa bank_mask:0xf\n"
      "s_nop 0\n"
      "v_add_f32_dpp v220, v220, v220 row_bcast:31 row_mask:0xc bank_mask:0xf\n"
      "v_add_f32_dpp v221, v221, v221 row_bcast:31 row_mask:0xc bank_mask:0xf\n"
      "v_rcp_f32 v220, v220\n"
      "s_brev_b64 exec, 1\n"
      "v_mul_f32 v221, v221, v220\n"
      "v_cvt_pk_bf16_f32 v220, v221, v221\n"
      "global_store_short v3, v220, s[24:25]\n"
      "v_cmp_class_f32 vcc, v221, s45\n"
      "s_nop 0\n"
      "s_cmp_lt_i32 vcc_hi, 0\n"
      "s_cbranch_scc1 7f\n"
      "s_endpgm\n"
      "s_mov_b64 exec, -1\n"
      "s_branch 9f\n"
      "80:\n"
      "s_waitcnt vmcnt(0) lgkmcnt(0)\n"
      "ds_read_b32 v224, v227 offset:37640\n"
      "s_waitcnt lgkmcnt(0)\n"
      "v_cmp_eq_u32 vcc, 0, v224\n"
      "s_nop 4\n"
      "s_and_b32 s44, vcc_lo, s54\n"
      "s_cbranch_scc1 80b\n"
      "v_cmp_eq_u32 vcc, 1, v224\n"
      "s_nop 4\n"
      "s_and_b32 s44, vcc_lo, s54\n"
      "s_cmp_eq_u32 s44, s54\n"
      "s_cbranch_scc1 81b\n"
      "s_mov_b32 s36, 1\n"
      "s_add_u32 s44, s38, 1152\n"
      "v_cndmask_b32 v10, v201, v10, s[100:101]\n"
      "v_cndmask_b32 v11, v201, v11, s[100:101]\n"
      "v_mov_b32 v1, s44\n"
      "s_nop 1\n"
      "s_branch 81b\n"
      "82:\n"
      "s_cmp_lg_u32 s36, 0\n"
      "s_cbranch_scc1 84f\n"
      "85:\n"
      "ds_read_b32 v224, v12 offset:12992\n"
      "s_waitcnt lgkmcnt(0)\n"
      "v_cmp_eq_u32 vcc, s37, v224\n"
      "s_nop 4\n"
      "s_cmp_lg_u64 vcc, 0\n"
      "s_cbranch_scc1 85b\n"
      "s_branch 83b\n"
      "84:\n"
      "s_waitcnt lgkmcnt(0)\n"
      "v_mfma_f32_16x16x32_f16 v[240:243], a[64:67], v[160:163], v[32:35]\n"
      "v_mfma_f32_16x16x32_f16 v[240:243], a[68:71], v[164:167], v[240:243]\n"
      "v_mfma_f32_16x16x32_f16 v[240:243], a[72:75], v[168:171], v[240:243]\n"
      "v_mfma_f32_16x16x32_f16 v[240:243], a[76:79], v[172:175], v[240:243]\n"
      "v_mfma_f32_16x16x32_f16 v[244:247], a[80:83], v[160:163], v[36:39]\n"
      "v_mfma_f32_16x16x32_f16 v[244:247], a[84:87], v[164:167], v[244:247]\n"
      "v_mfma_f32_16x16x32_f16 v[244:247], a[88:91], v[168:171], v[244:247]\n"
      "v_mfma_f32_16x16x32_f16 v[244:247], a[92:95], v[172:175], v[244:247]\n"
      "v_mfma_f32_16x16x32_f16 v[248:251], a[96:99], v[160:163], v[40:43]\n"
      "v_mfma_f32_16x16x32_f16 v[248:251], a[100:103], v[164:167], v[248:251]\n"
      "v_mfma_f32_16x16x32_f16 v[248:251], a[104:107], v[168:171], v[248:251]\n"
      "v_mfma_f32_16x16x32_f16 v[248:251], a[108:111], v[172:175], v[248:251]\n"
      "v_mfma_f32_16x16x32_f16 v[252:255], a[112:115], v[160:163], v[44:47]\n"
      "v_mfma_f32_16x16x32_f16 v[252:255], a[116:119], v[164:167], v[252:255]\n"
      "v_mfma_f32_16x16x32_f16 v[252:255], a[120:123], v[168:171], v[252:255]\n"
      "v_mfma_f32_16x16x32_f16 v[252:255], a[124:127], v[172:175], v[252:255]\n"
      "s_nop 7\n"
      "v_fma_f32 v224, |v240|, v64, 2.0\n"
      "v_fma_f32 v224, |v241|, v65, v224\n"
      "v_fma_f32 v224, |v242|, v66, v224\n"
      "v_fma_f32 v224, |v243|, v67, v224\n"
      "v_fma_f32 v224, |v244|, v68, v224\n"
      "v_fma_f32 v224, |v245|, v69, v224\n"
      "v_fma_f32 v224, |v246|, v70, v224\n"
      "v_fma_f32 v224, |v247|, v71, v224\n"
      "v_fma_f32 v224, |v248|, v72, v224\n"
      "v_fma_f32 v224, |v249|, v73, v224\n"
      "v_fma_f32 v224, |v250|, v74, v224\n"
      "v_fma_f32 v224, |v251|, v75, v224\n"
      "v_fma_f32 v224, |v252|, v76, v224\n"
      "v_fma_f32 v224, |v253|, v77, v224\n"
      "v_fma_f32 v224, |v254|, v78, v224\n"
      "v_fma_f32 v224, |v255|, v79, v224\n"
      "s_nop 1\n"
      "s_branch 83b\n"
      "5:\n"
      "s_waitcnt lgkmcnt(0)\n"
      "ds_read_b32 v224, v227 offset:37632\n"
      "s_waitcnt lgkmcnt(0)\n"
      "v_cmp_eq_u32 vcc, 0, v224\n"
      "s_nop 4\n"
      "s_cmp_lg_u32 vcc_lo, 0\n"
      "s_cbranch_scc1 5b\n"
      "v_cmp_gt_u32 vcc, 4, v224\n"
      "s_nop 4\n"
      "s_cmp_lg_u32 vcc_lo, 0\n"
      "s_cbranch_scc1 8f\n"
      "s_cmp_lg_u32 s58, 0\n"
      "s_cbranch_scc1 52f\n"
      "ds_read_b32 v226, v213 offset:12480\n"
      "v_cmp_eq_u32 s[44:45], 5, v224\n"
      "s_waitcnt lgkmcnt(0)\n"
      "v_max_f16_sdwa v226, |v226|, |v226| dst_sel:DWORD dst_unused:UNUSED_PAD src0_sel:WORD_0 src1_sel:WORD_1\n"
      "s_nop 1\n"
      "v_cmp_le_f16 vcc, 0x6400, v226\n"
      "s_nop 4\n"
      "s_or_b64 vcc, vcc, s[44:45]\n"
      "s_cbranch_scc0 52f\n"
      "v_and_b32 v224, 63, %3\n"
      "v_lshlrev_b32 v224, 9, v224\n"
      "global_load_dwordx4 v[176:179], v224, s[16:17]\n"
      "global_load_dwordx4 v[180:183], v224, s[16:17] offset:336\n"
      "s_waitcnt vmcnt(0)\n"
      "v_max3_f32 v224, |v176|, |v177|, |v178|\n"
      "v_max3_f32 v224, |v179|, |v180|, v224\n"
      "v_max3_f32 v224, |v181|, |v182|, v224\n"
      "v_max_f32 v224, |v183|, v224\n"
      "s_nop 1\n"
      "v_max_f32_dpp v224, v224, v224 row_ror:8 row_mask:0xf bank_mask:0xf\n"
      "s_nop 1\n"
      "v_max_f32_dpp v224, v224, v224 row_ror:4 row_mask:0xf bank_mask:0xf\n"
      "s_nop 1\n"
      "v_max_f32_dpp v224, v224, v224 row_ror:2 row_mask:0xf bank_mask:0xf\n"
      "s_nop 1\n"
      "v_max_f32_dpp v224, v224, v224 row_ror:1 row_mask:0xf bank_mask:0xf\n"
      "s_nop 0\n"
      "v_readlane_b32 s44, v224, 0\n"
      "v_readlane_b32 s45, v224, 16\n"
      "v_readlane_b32 s46, v224, 32\n"
      "v_readlane_b32 s47, v224, 48\n"
      "s_max_u32 s44, s44, s45\n"
      "s_max_u32 s46, s46, s47\n"
      "s_max_u32 s44, s44, s46\n"
      "s_lshr_b32 s44, s44, 23\n"
      "s_sub_i32 s58, 135, s44\n"
      "s_cmp_eq_u32 s44, 0\n"
      "s_cselect_b32 s58, 0, s58\n"
      "s_max_i32 s58, s58, 11\n"
      "s_cmp_gt_u32 s58, 51\n"
      "s_cbranch_scc1 8f\n"
      "s_mov_b32 s41, 1\n"
      "s_mov_b32 s35, 0\n"
      "s_waitcnt vmcnt(0) lgkmcnt(0)\n"
      "s_barrier\n"
      "s_branch 0b\n"
      "52:\n"
      "ds_read_b64 v[176:177], v5 offset:37120\n"
      "v_cmp_eq_u32 vcc, 9, v224\n"
      "v_cmp_eq_u32 s[50:51], 5, v224\n"
      "s_waitcnt lgkmcnt(0)\n"
      "v_max_f32 v225, |v176|, |v177|\n"
      "s_nop 1\n"
      "v_max_f32_dpp v225, v225, v225 row_ror:8 row_mask:0xf bank_mask:0xf\n"
      "s_nop 1\n"
      "v_max_f32_dpp v225, v225, v225 row_ror:4 row_mask:0xf bank_mask:0xf\n"
      "s_nop 1\n"
      "v_max_f32_dpp v225, v225, v225 row_ror:2 row_mask:0xf bank_mask:0xf\n"
      "s_nop 1\n"
      "v_max_f32_dpp v225, v225, v225 row_ror:1 row_mask:0xf bank_mask:0xf\n"
      "s_nop 0\n"
      "v_readlane_b32 s44, v225, 0\n"
      "v_readlane_b32 s45, v225, 16\n"
      "v_readlane_b32 s46, v225, 32\n"
      "v_readlane_b32 s47, v225, 48\n"
      "s_max_u32 s44, s44, s45\n"
      "s_max_u32 s46, s46, s47\n"
      "s_max_u32 s44, s44, s46\n"
      "v_frexp_exp_i32_f32 v224, s44\n"
      "v_sub_u32 v224, 0, v224\n"
      "s_or_b32 s30, s30, vcc_lo\n"
      "s_cmp_lg_u32 s55, 0\n"
      "s_cselect_b32 s30, 0, s30\n"
      "v_readfirstlane_b32 s51, v224\n"
      "s_cmp_lg_u32 s30, 0\n"
      "s_cbranch_scc1 6f\n"
      "v_ldexp_f32 v176, v176, v224\n"
      "v_ldexp_f32 v177, v177, v224\n"
      "v_readfirstlane_b32 s56, v224\n"
      "s_nop 1\n"
      "s_branch 4b\n"
      "22:\n"
      "s_nop 7\n"
      "v_lshlrev_b32 v176, 16, v192\n"
      "v_and_b32 v177, s43, v192\n"
      "v_lshlrev_b32 v178, 16, v193\n"
      "v_and_b32 v179, s43, v193\n"
      "v_lshlrev_b32 v180, 16, v194\n"
      "v_and_b32 v181, s43, v194\n"
      "v_lshlrev_b32 v182, 16, v195\n"
      "v_and_b32 v183, s43, v195\n"
      "v_max3_f32 v222, |v176|, |v177|, |v178|\n"
      "v_max3_f32 v222, |v179|, |v180|, v222\n"
      "v_max3_f32 v222, |v181|, |v182|, v222\n"
      "v_max_f32 v222, |v183|, v222\n"
      "s_nop 1\n"
      "v_max_f32_dpp v222, v222, v222 row_ror:8 row_mask:0xf bank_mask:0xf\n"
      "s_nop 1\n"
      "v_max_f32_dpp v222, v222, v222 row_ror:4 row_mask:0xf bank_mask:0xf\n"
      "s_nop 1\n"
      "v_max_f32_dpp v222, v222, v222 row_ror:2 row_mask:0xf bank_mask:0xf\n"
      "s_nop 1\n"
      "v_max_f32_dpp v222, v222, v222 row_ror:1 row_mask:0xf bank_mask:0xf\n"
      "v_frexp_exp_i32_f32 v90, v222\n"
      "v_sub_u32 v90, 0, v90\n"
      "v_ldexp_f32 v176, v176, v90\n"
      "v_ldexp_f32 v177, v177, v90\n"
      "v_ldexp_f32 v178, v178, v90\n"
      "v_ldexp_f32 v179, v179, v90\n"
      "v_ldexp_f32 v180, v180, v90\n"
      "v_ldexp_f32 v181, v181, v90\n"
      "v_ldexp_f32 v182, v182, v90\n"
      "v_ldexp_f32 v183, v183, v90\n"
      "v_ldexp_f32 v176, v176, 9\n"
      "v_ldexp_f32 v177, v177, 9\n"
      "v_ldexp_f32 v178, v178, 9\n"
      "v_ldexp_f32 v179, v179, 9\n"
      "v_ldexp_f32 v180, v180, 9\n"
      "v_ldexp_f32 v181, v181, 9\n"
      "v_ldexp_f32 v182, v182, 9\n"
      "v_ldexp_f32 v183, v183, 9\n"
      "v_readlane_b32 s59, v90, 0\n"
      "v_readlane_b32 s57, v90, 32\n"
      "s_lshl_b64 exec, -1, 48\n"
      "v_xor_b32 v176, 0x80000000, v176\n"
      "v_xor_b32 v177, 0x80000000, v177\n"
      "v_xor_b32 v178, 0x80000000, v178\n"
      "v_xor_b32 v179, 0x80000000, v179\n"
      "v_xor_b32 v180, 0x80000000, v180\n"
      "v_xor_b32 v181, 0x80000000, v181\n"
      "v_xor_b32 v182, 0x80000000, v182\n"
      "v_xor_b32 v183, 0x80000000, v183\n"
      "s_mov_b64 exec, -1\n"
      "s_cmp_lt_i32 s57, 0\n"
      "s_cselect_b32 s41, 1, s41\n"
      "s_branch 23b\n"
      "70:\n"
      "s_mov_b32 s49, 0\n"
      "s_add_u32 s44, s58, 127\n"
      "s_lshl_b32 s44, s44, 23\n"
      "s_waitcnt vmcnt(0) lgkmcnt(0)\n"
      "s_nop 7\n"
      "v_lshlrev_b32 v128, 16, v96\n"
      "v_and_b32 v129, s43, v96\n"
      "v_mul_f32 v128, s44, v128\n"
      "v_mul_f32 v129, s44, v129\n"
      "v_cvt_pk_bf16_f32 v96, v128, v129\n"
      "v_lshlrev_b32 v128, 16, v97\n"
      "v_and_b32 v129, s43, v97\n"
      "v_mul_f32 v128, s44, v128\n"
      "v_mul_f32 v129, s44, v129\n"
      "v_cvt_pk_bf16_f32 v97, v128, v129\n"
      "v_lshlrev_b32 v128, 16, v98\n"
      "v_and_b32 v129, s43, v98\n"
      "v_mul_f32 v128, s44, v128\n"
      "v_mul_f32 v129, s44, v129\n"
      "v_cvt_pk_bf16_f32 v98, v128, v129\n"
      "v_lshlrev_b32 v128, 16, v99\n"
      "v_and_b32 v129, s43, v99\n"
      "v_mul_f32 v128, s44, v128\n"
      "v_mul_f32 v129, s44, v129\n"
      "v_cvt_pk_bf16_f32 v99, v128, v129\n"
      "v_lshlrev_b32 v128, 16, v100\n"
      "v_and_b32 v129, s43, v100\n"
      "v_mul_f32 v128, s44, v128\n"
      "v_mul_f32 v129, s44, v129\n"
      "v_cvt_pk_bf16_f32 v100, v128, v129\n"
      "v_lshlrev_b32 v128, 16, v101\n"
      "v_and_b32 v129, s43, v101\n"
      "v_mul_f32 v128, s44, v128\n"
      "v_mul_f32 v129, s44, v129\n"
      "v_cvt_pk_bf16_f32 v101, v128, v129\n"
      "v_lshlrev_b32 v128, 16, v102\n"
      "v_and_b32 v129, s43, v102\n"
      "v_mul_f32 v128, s44, v128\n"
      "v_mul_f32 v129, s44, v129\n"
      "v_cvt_pk_bf16_f32 v102, v128, v129\n"
      "v_lshlrev_b32 v128, 16, v103\n"
      "v_and_b32 v129, s43, v103\n"
      "v_mul_f32 v128, s44, v128\n"
      "v_mul_f32 v129, s44, v129\n"
      "v_cvt_pk_bf16_f32 v103, v128, v129\n"
      "v_lshlrev_b32 v128, 16, v104\n"
      "v_and_b32 v129, s43, v104\n"
      "v_mul_f32 v128, s44, v128\n"
      "v_mul_f32 v129, s44, v129\n"
      "v_cvt_pk_bf16_f32 v104, v128, v129\n"
      "v_lshlrev_b32 v128, 16, v105\n"
      "v_and_b32 v129, s43, v105\n"
      "v_mul_f32 v128, s44, v128\n"
      "v_mul_f32 v129, s44, v129\n"
      "v_cvt_pk_bf16_f32 v105, v128, v129\n"
      "v_lshlrev_b32 v128, 16, v106\n"
      "v_and_b32 v129, s43, v106\n"
      "v_mul_f32 v128, s44, v128\n"
      "v_mul_f32 v129, s44, v129\n"
      "v_cvt_pk_bf16_f32 v106, v128, v129\n"
      "v_lshlrev_b32 v128, 16, v107\n"
      "v_and_b32 v129, s43, v107\n"
      "v_mul_f32 v128, s44, v128\n"
      "v_mul_f32 v129, s44, v129\n"
      "v_cvt_pk_bf16_f32 v107, v128, v129\n"
      "v_lshlrev_b32 v128, 16, v108\n"
      "v_and_b32 v129, s43, v108\n"
      "v_mul_f32 v128, s44, v128\n"
      "v_mul_f32 v129, s44, v129\n"
      "v_cvt_pk_bf16_f32 v108, v128, v129\n"
      "v_lshlrev_b32 v128, 16, v109\n"
      "v_and_b32 v129, s43, v109\n"
      "v_mul_f32 v128, s44, v128\n"
      "v_mul_f32 v129, s44, v129\n"
      "v_cvt_pk_bf16_f32 v109, v128, v129\n"
      "v_lshlrev_b32 v128, 16, v110\n"
      "v_and_b32 v129, s43, v110\n"
      "v_mul_f32 v128, s44, v128\n"
      "v_mul_f32 v129, s44, v129\n"
      "v_cvt_pk_bf16_f32 v110, v128, v129\n"
      "v_lshlrev_b32 v128, 16, v111\n"
      "v_and_b32 v129, s43, v111\n"
      "v_mul_f32 v128, s44, v128\n"
      "v_mul_f32 v129, s44, v129\n"
      "v_cvt_pk_bf16_f32 v111, v128, v129\n"
      "v_lshlrev_b32 v128, 16, v112\n"
      "v_and_b32 v129, s43, v112\n"
      "v_mul_f32 v128, s44, v128\n"
      "v_mul_f32 v129, s44, v129\n"
      "v_cvt_pk_bf16_f32 v112, v128, v129\n"
      "v_lshlrev_b32 v128, 16, v113\n"
      "v_and_b32 v129, s43, v113\n"
      "v_mul_f32 v128, s44, v128\n"
      "v_mul_f32 v129, s44, v129\n"
      "v_cvt_pk_bf16_f32 v113, v128, v129\n"
      "v_lshlrev_b32 v128, 16, v114\n"
      "v_and_b32 v129, s43, v114\n"
      "v_mul_f32 v128, s44, v128\n"
      "v_mul_f32 v129, s44, v129\n"
      "v_cvt_pk_bf16_f32 v114, v128, v129\n"
      "v_lshlrev_b32 v128, 16, v115\n"
      "v_and_b32 v129, s43, v115\n"
      "v_mul_f32 v128, s44, v128\n"
      "v_mul_f32 v129, s44, v129\n"
      "v_cvt_pk_bf16_f32 v115, v128, v129\n"
      "v_lshlrev_b32 v128, 16, v116\n"
      "v_and_b32 v129, s43, v116\n"
      "v_mul_f32 v128, s44, v128\n"
      "v_mul_f32 v129, s44, v129\n"
      "v_cvt_pk_bf16_f32 v116, v128, v129\n"
      "v_lshlrev_b32 v128, 16, v117\n"
      "v_and_b32 v129, s43, v117\n"
      "v_mul_f32 v128, s44, v128\n"
      "v_mul_f32 v129, s44, v129\n"
      "v_cvt_pk_bf16_f32 v117, v128, v129\n"
      "v_lshlrev_b32 v128, 16, v118\n"
      "v_and_b32 v129, s43, v118\n"
      "v_mul_f32 v128, s44, v128\n"
      "v_mul_f32 v129, s44, v129\n"
      "v_cvt_pk_bf16_f32 v118, v128, v129\n"
      "v_lshlrev_b32 v128, 16, v119\n"
      "v_and_b32 v129, s43, v119\n"
      "v_mul_f32 v128, s44, v128\n"
      "v_mul_f32 v129, s44, v129\n"
      "v_cvt_pk_bf16_f32 v119, v128, v129\n"
      "v_lshlrev_b32 v128, 16, v120\n"
      "v_and_b32 v129, s43, v120\n"
      "v_mul_f32 v128, s44, v128\n"
      "v_mul_f32 v129, s44, v129\n"
      "v_cvt_pk_bf16_f32 v120, v128, v129\n"
      "v_lshlrev_b32 v128, 16, v121\n"
      "v_and_b32 v129, s43, v121\n"
      "v_mul_f32 v128, s44, v128\n"
      "v_mul_f32 v129, s44, v129\n"
      "v_cvt_pk_bf16_f32 v121, v128, v129\n"
      "v_lshlrev_b32 v128, 16, v122\n"
      "v_and_b32 v129, s43, v122\n"
      "v_mul_f32 v128, s44, v128\n"
      "v_mul_f32 v129, s44, v129\n"
      "v_cvt_pk_bf16_f32 v122, v128, v129\n"
      "v_lshlrev_b32 v128, 16, v123\n"
      "v_and_b32 v129, s43, v123\n"
      "v_mul_f32 v128, s44, v128\n"
      "v_mul_f32 v129, s44, v129\n"
      "v_cvt_pk_bf16_f32 v123, v128, v129\n"
      "v_lshlrev_b32 v128, 16, v124\n"
      "v_and_b32 v129, s43, v124\n"
      "v_mul_f32 v128, s44, v128\n"
      "v_mul_f32 v129, s44, v129\n"
      "v_cvt_pk_bf16_f32 v124, v128, v129\n"
      "v_lshlrev_b32 v128, 16, v125\n"
      "v_and_b32 v129, s43, v125\n"
      "v_mul_f32 v128, s44, v128\n"
      "v_mul_f32 v129, s44, v129\n"
      "v_cvt_pk_bf16_f32 v125, v128, v129\n"
      "v_lshlrev_b32 v128, 16, v126\n"
      "v_and_b32 v129, s43, v126\n"
      "v_mul_f32 v128, s44, v128\n"
      "v_mul_f32 v129, s44, v129\n"
      "v_cvt_pk_bf16_f32 v126, v128, v129\n"
      "v_lshlrev_b32 v128, 16, v127\n"
      "v_and_b32 v129, s43, v127\n"
      "v_mul_f32 v128, s44, v128\n"
      "v_mul_f32 v129, s44, v129\n"
      "v_cvt_pk_bf16_f32 v127, v128, v129\n"
      "s_nop 1\n"
      "s_branch 71b\n"
      "72:\n"
      "s_nop 7\n"
      "s_sub_u32 s44, 0, s58\n"
      "v_ldexp_f32 v16, v16, s58\n"
      "v_ldexp_f32 v48, v48, s44\n"
      "v_ldexp_f32 v17, v17, s58\n"
      "v_ldexp_f32 v49, v49, s44\n"
      "v_ldexp_f32 v18, v18, s58\n"
      "v_ldexp_f32 v50, v50, s44\n"
      "v_ldexp_f32 v19, v19, s58\n"
      "v_ldexp_f32 v51, v51, s44\n"
      "v_ldexp_f32 v20, v20, s58\n"
      "v_ldexp_f32 v52, v52, s44\n"
      "v_ldexp_f32 v21, v21, s58\n"
      "v_ldexp_f32 v53, v53, s44\n"
      "v_ldexp_f32 v22, v22, s58\n"
      "v_ldexp_f32 v54, v54, s44\n"
      "v_ldexp_f32 v23, v23, s58\n"
      "v_ldexp_f32 v55, v55, s44\n"
      "v_ldexp_f32 v24, v24, s58\n"
      "v_ldexp_f32 v56, v56, s44\n"
      "v_ldexp_f32 v25, v25, s58\n"
      "v_ldexp_f32 v57, v57, s44\n"
      "v_ldexp_f32 v26, v26, s58\n"
      "v_ldexp_f32 v58, v58, s44\n"
      "v_ldexp_f32 v27, v27, s58\n"
      "v_ldexp_f32 v59, v59, s44\n"
      "v_ldexp_f32 v28, v28, s58\n"
      "v_ldexp_f32 v60, v60, s44\n"
      "v_ldexp_f32 v29, v29, s58\n"
      "v_ldexp_f32 v61, v61, s44\n"
      "v_ldexp_f32 v30, v30, s58\n"
      "v_ldexp_f32 v62, v62, s44\n"
      "v_ldexp_f32 v31, v31, s58\n"
      "v_ldexp_f32 v63, v63, s44\n"
      "v_ldexp_f32 v32, v32, s58\n"
      "v_ldexp_f32 v64, v64, s44\n"
      "v_ldexp_f32 v33, v33, s58\n"
      "v_ldexp_f32 v65, v65, s44\n"
      "v_ldexp_f32 v34, v34, s58\n"
      "v_ldexp_f32 v66, v66, s44\n"
      "v_ldexp_f32 v35, v35, s58\n"
      "v_ldexp_f32 v67, v67, s44\n"
      "v_ldexp_f32 v36, v36, s58\n"
      "v_ldexp_f32 v68, v68, s44\n"
      "v_ldexp_f32 v37, v37, s58\n"
      "v_ldexp_f32 v69, v69, s44\n"
      "v_ldexp_f32 v38, v38, s58\n"
      "v_ldexp_f32 v70, v70, s44\n"
      "v_ldexp_f32 v39, v39, s58\n"
      "v_ldexp_f32 v71, v71, s44\n"
      "v_ldexp_f32 v40, v40, s58\n"
      "v_ldexp_f32 v72, v72, s44\n"
      "v_ldexp_f32 v41, v41, s58\n"
      "v_ldexp_f32 v73, v73, s44\n"
      "v_ldexp_f32 v42, v42, s58\n"
      "v_ldexp_f32 v74, v74, s44\n"
      "v_ldexp_f32 v43, v43, s58\n"
      "v_ldexp_f32 v75, v75, s44\n"
      "v_ldexp_f32 v44, v44, s58\n"
      "v_ldexp_f32 v76, v76, s44\n"
      "v_ldexp_f32 v45, v45, s58\n"
      "v_ldexp_f32 v77, v77, s44\n"
      "v_ldexp_f32 v46, v46, s58\n"
      "v_ldexp_f32 v78, v78, s44\n"
      "v_ldexp_f32 v47, v47, s58\n"
      "v_ldexp_f32 v79, v79, s44\n"
      "s_branch 73b\n"
      "6:\n"
      "s_waitcnt vmcnt(0) lgkmcnt(0)\n"
      "s_nop 7\n"
      "s_mov_b32 s55, 1\n"
      "s_mov_b32 s41, 1\n"
      "s_mov_b32 s45, 0x7fff7fff\n"
      "v_mov_b32 v226, 0\n"
      "v_and_b32 v176, s45, v228\n"
      "v_pk_max_u16 v226, v226, v176\n"
      "v_and_b32 v176, s45, v229\n"
      "v_pk_max_u16 v226, v226, v176\n"
      "v_and_b32 v176, s45, v230\n"
      "v_pk_max_u16 v226, v226, v176\n"
      "v_and_b32 v176, s45, v231\n"
      "v_pk_max_u16 v226, v226, v176\n"
      "v_and_b32 v176, s45, v232\n"
      "v_pk_max_u16 v226, v226, v176\n"
      "v_and_b32 v176, s45, v233\n"
      "v_pk_max_u16 v226, v226, v176\n"
      "v_and_b32 v176, s45, v234\n"
      "v_pk_max_u16 v226, v226, v176\n"
      "v_and_b32 v176, s45, v235\n"
      "v_pk_max_u16 v226, v226, v176\n"
      "v_and_b32 v176, s45, v236\n"
      "v_pk_max_u16 v226, v226, v176\n"
      "v_and_b32 v176, s45, v237\n"
      "v_pk_max_u16 v226, v226, v176\n"
      "v_and_b32 v176, s45, v238\n"
      "v_pk_max_u16 v226, v226, v176\n"
      "v_and_b32 v176, s45, v239\n"
      "v_pk_max_u16 v226, v226, v176\n"
      "v_and_b32 v176, s45, v240\n"
      "v_pk_max_u16 v226, v226, v176\n"
      "v_and_b32 v176, s45, v241\n"
      "v_pk_max_u16 v226, v226, v176\n"
      "v_and_b32 v176, s45, v242\n"
      "v_pk_max_u16 v226, v226, v176\n"
      "v_and_b32 v176, s45, v243\n"
      "v_pk_max_u16 v226, v226, v176\n"
      "v_and_b32 v176, s45, v244\n"
      "v_pk_max_u16 v226, v226, v176\n"
      "v_and_b32 v176, s45, v245\n"
      "v_pk_max_u16 v226, v226, v176\n"
      "v_and_b32 v176, s45, v246\n"
      "v_pk_max_u16 v226, v226, v176\n"
      "v_and_b32 v176, s45, v247\n"
      "v_pk_max_u16 v226, v226, v176\n"
      "v_and_b32 v176, s45, v248\n"
      "v_pk_max_u16 v226, v226, v176\n"
      "v_and_b32 v176, s45, v249\n"
      "v_pk_max_u16 v226, v226, v176\n"
      "v_and_b32 v176, s45, v250\n"
      "v_pk_max_u16 v226, v226, v176\n"
      "v_and_b32 v176, s45, v251\n"
      "v_pk_max_u16 v226, v226, v176\n"
      "v_and_b32 v176, s45, v252\n"
      "v_pk_max_u16 v226, v226, v176\n"
      "v_and_b32 v176, s45, v253\n"
      "v_pk_max_u16 v226, v226, v176\n"
      "v_and_b32 v176, s45, v254\n"
      "v_pk_max_u16 v226, v226, v176\n"
      "v_and_b32 v176, s45, v255\n"
      "v_pk_max_u16 v226, v226, v176\n"
      "v_lshlrev_b32 v176, 16, v226\n"
      "v_max_u32 v226, v226, v176\n"
      "s_nop 1\n"
      "v_max_u32_dpp v226, v226, v226 row_ror:8 row_mask:0xf bank_mask:0xf\n"
      "s_nop 1\n"
      "v_max_u32_dpp v226, v226, v226 row_ror:4 row_mask:0xf bank_mask:0xf\n"
      "s_nop 1\n"
      "v_max_u32_dpp v226, v226, v226 row_ror:2 row_mask:0xf bank_mask:0xf\n"
      "s_nop 1\n"
      "v_max_u32_dpp v226, v226, v226 row_ror:1 row_mask:0xf bank_mask:0xf\n"
      "s_nop 0\n"
      "v_readlane_b32 s44, v226, 0\n"
      "v_readlane_b32 s45, v226, 16\n"
      "v_readlane_b32 s46, v226, 32\n"
      "v_readlane_b32 s47, v226, 48\n"
      "s_max_u32 s44, s44, s45\n"
      "s_max_u32 s46, s46, s47\n"
      "s_max_u32 s44, s44, s46\n"
      "s_lshr_b32 s44, s44, 23\n"
      "s_sub_i32 s40, 138, s44\n"
      "s_cmp_eq_u32 s44, 0\n"
      "s_cselect_b32 s40, 0, s40\n"
      "s_add_i32 s45, s40, 32\n"
      "s_cmp_gt_u32 s45, 80\n"
      "s_cbranch_scc1 8f\n"
      "s_lshl1_add_u32 s44, s40, s58\n"
      "s_add_i32 s44, s44, s57\n"
      "s_add_i32 s45, s44, s51\n"
      "s_cmp_lt_i32 s45, 2\n"
      "s_cbranch_scc1 8f\n"
      "s_cmp_lg_u32 s50, 0\n"
      "s_cselect_b32 s44, s44, 64\n"
      "s_cmp_lt_i32 s44, 26\n"
      "s_cbranch_scc1 8f\n"
      "s_add_u32 s39, s40, 119\n"
      "s_lshl_b32 s39, s39, 23\n"
      "s_lshl_b32 s44, s40, 1\n"
      "s_sub_u32 s45, 127, s44\n"
      "s_add_u32 s44, s44, 127\n"
      "s_lshl_b32 s44, s44, 23\n"
      "s_lshl_b32 s45, s45, 23\n"
      "v_mul_f32 v16, s44, v16\n"
      "v_mul_f32 v48, s45, v48\n"
      "v_mul_f32 v17, s44, v17\n"
      "v_mul_f32 v49, s45, v49\n"
      "v_mul_f32 v18, s44, v18\n"
      "v_mul_f32 v50, s45, v50\n"
      "v_mul_f32 v19, s44, v19\n"
      "v_mul_f32 v51, s45, v51\n"
      "v_mul_f32 v20, s44, v20\n"
      "v_mul_f32 v52, s45, v52\n"
      "v_mul_f32 v21, s44, v21\n"
      "v_mul_f32 v53, s45, v53\n"
      "v_mul_f32 v22, s44, v22\n"
      "v_mul_f32 v54, s45, v54\n"
      "v_mul_f32 v23, s44, v23\n"
      "v_mul_f32 v55, s45, v55\n"
      "v_mul_f32 v24, s44, v24\n"
      "v_mul_f32 v56, s45, v56\n"
      "v_mul_f32 v25, s44, v25\n"
      "v_mul_f32 v57, s45, v57\n"
      "v_mul_f32 v26, s44, v26\n"
      "v_mul_f32 v58, s45, v58\n"
      "v_mul_f32 v27, s44, v27\n"
      "v_mul_f32 v59, s45, v59\n"
      "v_mul_f32 v28, s44, v28\n"
      "v_mul_f32 v60, s45, v60\n"
      "v_mul_f32 v29, s44, v29\n"
      "v_mul_f32 v61, s45, v61\n"
      "v_mul_f32 v30, s44, v30\n"
      "v_mul_f32 v62, s45, v62\n"
      "v_mul_f32 v31, s44, v31\n"
      "v_mul_f32 v63, s45, v63\n"
      "v_mul_f32 v32, s44, v32\n"
      "v_mul_f32 v64, s45, v64\n"
      "v_mul_f32 v33, s44, v33\n"
      "v_mul_f32 v65, s45, v65\n"
      "v_mul_f32 v34, s44, v34\n"
      "v_mul_f32 v66, s45, v66\n"
      "v_mul_f32 v35, s44, v35\n"
      "v_mul_f32 v67, s45, v67\n"
      "v_mul_f32 v36, s44, v36\n"
      "v_mul_f32 v68, s45, v68\n"
      "v_mul_f32 v37, s44, v37\n"
      "v_mul_f32 v69, s45, v69\n"
      "v_mul_f32 v38, s44, v38\n"
      "v_mul_f32 v70, s45, v70\n"
      "v_mul_f32 v39, s44, v39\n"
      "v_mul_f32 v71, s45, v71\n"
      "v_mul_f32 v40, s44, v40\n"
      "v_mul_f32 v72, s45, v72\n"
      "v_mul_f32 v41, s44, v41\n"
      "v_mul_f32 v73, s45, v73\n"
      "v_mul_f32 v42, s44, v42\n"
      "v_mul_f32 v74, s45, v74\n"
      "v_mul_f32 v43, s44, v43\n"
      "v_mul_f32 v75, s45, v75\n"
      "v_mul_f32 v44, s44, v44\n"
      "v_mul_f32 v76, s45, v76\n"
      "v_mul_f32 v45, s44, v45\n"
      "v_mul_f32 v77, s45, v77\n"
      "v_mul_f32 v46, s44, v46\n"
      "v_mul_f32 v78, s45, v78\n"
      "v_mul_f32 v47, s44, v47\n"
      "v_mul_f32 v79, s45, v79\n"
      "v_lshlrev_b32 v176, 16, v228\n"
      "v_and_b32 v177, s43, v228\n"
      "v_lshlrev_b32 v178, 16, v229\n"
      "v_and_b32 v179, s43, v229\n"
      "v_lshlrev_b32 v180, 16, v230\n"
      "v_and_b32 v181, s43, v230\n"
      "v_lshlrev_b32 v182, 16, v231\n"
      "v_and_b32 v183, s43, v231\n"
      "v_mul_f32 v176, s39, v176\n"
      "v_mul_f32 v177, s39, v177\n"
      "v_mul_f32 v178, s39, v178\n"
      "v_mul_f32 v179, s39, v179\n"
      "v_mul_f32 v180, s39, v180\n"
      "v_mul_f32 v181, s39, v181\n"
      "v_mul_f32 v182, s39, v182\n"
      "v_mul_f32 v183, s39, v183\n"
      "v_cvt_pk_f16_f32 v184, v176, v177\n"
      "v_cvt_pk_f16_f32 v185, v178, v179\n"
      "v_cvt_pk_f16_f32 v186, v180, v181\n"
      "v_cvt_pk_f16_f32 v187, v182, v183\n"
      "ds_write_b128 v1, v[184:187] offset:0\n"
      "v_lshlrev_b32 v176, 16, v232\n"
      "v_and_b32 v177, s43, v232\n"
      "v_lshlrev_b32 v178, 16, v233\n"
      "v_and_b32 v179, s43, v233\n"
      "v_lshlrev_b32 v180, 16, v234\n"
      "v_and_b32 v181, s43, v234\n"
      "v_lshlrev_b32 v182, 16, v235\n"
      "v_and_b32 v183, s43, v235\n"
      "v_mul_f32 v176, s39, v176\n"
      "v_mul_f32 v177, s39, v177\n"
      "v_mul_f32 v178, s39, v178\n"
      "v_mul_f32 v179, s39, v179\n"
      "v_mul_f32 v180, s39, v180\n"
      "v_mul_f32 v181, s39, v181\n"
      "v_mul_f32 v182, s39, v182\n"
      "v_mul_f32 v183, s39, v183\n"
      "v_cvt_pk_f16_f32 v184, v176, v177\n"
      "v_cvt_pk_f16_f32 v185, v178, v179\n"
      "v_cvt_pk_f16_f32 v186, v180, v181\n"
      "v_cvt_pk_f16_f32 v187, v182, v183\n"
      "ds_write_b128 v1, v[184:187] offset:1152\n"
      "v_lshlrev_b32 v176, 16, v236\n"
      "v_and_b32 v177, s43, v236\n"
      "v_lshlrev_b32 v178, 16, v237\n"
      "v_and_b32 v179, s43, v237\n"
      "v_lshlrev_b32 v180, 16, v238\n"
      "v_and_b32 v181, s43, v238\n"
      "v_lshlrev_b32 v182, 16, v239\n"
      "v_and_b32 v183, s43, v239\n"
      "v_mul_f32 v176, s39, v176\n"
      "v_mul_f32 v177, s39, v177\n"
      "v_mul_f32 v178, s39, v178\n"
      "v_mul_f32 v179, s39, v179\n"
      "v_mul_f32 v180, s39, v180\n"
      "v_mul_f32 v181, s39, v181\n"
      "v_mul_f32 v182, s39, v182\n"
      "v_mul_f32 v183, s39, v183\n"
      "v_cvt_pk_f16_f32 v184, v176, v177\n"
      "v_cvt_pk_f16_f32 v185, v178, v179\n"
      "v_cvt_pk_f16_f32 v186, v180, v181\n"
      "v_cvt_pk_f16_f32 v187, v182, v183\n"
      "ds_write_b128 v1, v[184:187] offset:2304\n"
      "v_lshlrev_b32 v176, 16, v240\n"
      "v_and_b32 v177, s43, v240\n"
      "v_lshlrev_b32 v178, 16, v241\n"
      "v_and_b32 v179, s43, v241\n"
      "v_lshlrev_b32 v180, 16, v242\n"
      "v_and_b32 v181, s43, v242\n"
      "v_lshlrev_b32 v182, 16, v243\n"
      "v_and_b32 v183, s43, v243\n"
      "v_mul_f32 v176, s39, v176\n"
      "v_mul_f32 v177, s39, v177\n"
      "v_mul_f32 v178, s39, v178\n"
      "v_mul_f32 v179, s39, v179\n"
      "v_mul_f32 v180, s39, v180\n"
      "v_mul_f32 v181, s39, v181\n"
      "v_mul_f32 v182, s39, v182\n"
      "v_mul_f32 v183, s39, v183\n"
      "v_cvt_pk_f16_f32 v184, v176, v177\n"
      "v_cvt_pk_f16_f32 v185, v178, v179\n"
      "v_cvt_pk_f16_f32 v186, v180, v181\n"
      "v_cvt_pk_f16_f32 v187, v182, v183\n"
      "ds_write_b128 v1, v[184:187] offset:3456\n"
      "v_lshlrev_b32 v176, 16, v244\n"
      "v_and_b32 v177, s43, v244\n"
      "v_lshlrev_b32 v178, 16, v245\n"
      "v_and_b32 v179, s43, v245\n"
      "v_lshlrev_b32 v180, 16, v246\n"
      "v_and_b32 v181, s43, v246\n"
      "v_lshlrev_b32 v182, 16, v247\n"
      "v_and_b32 v183, s43, v247\n"
      "v_mul_f32 v176, s39, v176\n"
      "v_mul_f32 v177, s39, v177\n"
      "v_mul_f32 v178, s39, v178\n"
      "v_mul_f32 v179, s39, v179\n"
      "v_mul_f32 v180, s39, v180\n"
      "v_mul_f32 v181, s39, v181\n"
      "v_mul_f32 v182, s39, v182\n"
      "v_mul_f32 v183, s39, v183\n"
      "v_cvt_pk_f16_f32 v184, v176, v177\n"
      "v_cvt_pk_f16_f32 v185, v178, v179\n"
      "v_cvt_pk_f16_f32 v186, v180, v181\n"
      "v_cvt_pk_f16_f32 v187, v182, v183\n"
      "ds_write_b128 v1, v[184:187] offset:4608\n"
      "v_lshlrev_b32 v176, 16, v248\n"
      "v_and_b32 v177, s43, v248\n"
      "v_lshlrev_b32 v178, 16, v249\n"
      "v_and_b32 v179, s43, v249\n"
      "v_lshlrev_b32 v180, 16, v250\n"
      "v_and_b32 v181, s43, v250\n"
      "v_lshlrev_b32 v182, 16, v251\n"
      "v_and_b32 v183, s43, v251\n"
      "v_mul_f32 v176, s39, v176\n"
      "v_mul_f32 v177, s39, v177\n"
      "v_mul_f32 v178, s39, v178\n"
      "v_mul_f32 v179, s39, v179\n"
      "v_mul_f32 v180, s39, v180\n"
      "v_mul_f32 v181, s39, v181\n"
      "v_mul_f32 v182, s39, v182\n"
      "v_mul_f32 v183, s39, v183\n"
      "v_cvt_pk_f16_f32 v184, v176, v177\n"
      "v_cvt_pk_f16_f32 v185, v178, v179\n"
      "v_cvt_pk_f16_f32 v186, v180, v181\n"
      "v_cvt_pk_f16_f32 v187, v182, v183\n"
      "ds_write_b128 v1, v[184:187] offset:5760\n"
      "v_lshlrev_b32 v176, 16, v252\n"
      "v_and_b32 v177, s43, v252\n"
      "v_lshlrev_b32 v178, 16, v253\n"
      "v_and_b32 v179, s43, v253\n"
      "v_lshlrev_b32 v180, 16, v254\n"
      "v_and_b32 v181, s43, v254\n"
      "v_lshlrev_b32 v182, 16, v255\n"
      "v_and_b32 v183, s43, v255\n"
      "v_mul_f32 v176, s39, v176\n"
      "v_mul_f32 v177, s39, v177\n"
      "v_mul_f32 v178, s39, v178\n"
      "v_mul_f32 v179, s39, v179\n"
      "v_mul_f32 v180, s39, v180\n"
      "v_mul_f32 v181, s39, v181\n"
      "v_mul_f32 v182, s39, v182\n"
      "v_mul_f32 v183, s39, v183\n"
      "v_cvt_pk_f16_f32 v184, v176, v177\n"
      "v_cvt_pk_f16_f32 v185, v178, v179\n"
      "v_cvt_pk_f16_f32 v186, v180, v181\n"
      "v_cvt_pk_f16_f32 v187, v182, v183\n"
      "ds_write_b128 v1, v[184:187] offset:6912\n"
      "v_mfma_f32_16x16x32_f16 v[192:195], a[184:187], a[152:155], 0\n"
      "v_mfma_f32_16x16x32_f16 v[192:195], a[188:191], a[156:159], v[192:195]\n"
      "v_mfma_f32_16x16x32_f16 v[192:195], a[192:195], a[160:163], v[192:195]\n"
      "v_mfma_f32_16x16x32_f16 v[192:195], a[196:199], a[164:167], v[192:195]\n"
      "v_mfma_f32_16x16x32_f16 v[252:255], a[184:187], a[168:171], 0\n"
      "v_mfma_f32_16x16x32_f16 v[252:255], a[188:191], a[172:175], v[252:255]\n"
      "v_mfma_f32_16x16x32_f16 v[252:255], a[192:195], a[176:179], v[252:255]\n"
      "v_mfma_f32_16x16x32_f16 v[252:255], a[196:199], a[180:183], v[252:255]\n"
      "s_sub_u32 s44, 96512, s38\n"
      "s_mov_b32 s98, 0\n"
      "v_add_u32 v1, s44, v1\n"
      "s_waitcnt lgkmcnt(0)\n"
      "ds_read_b128 v[128:131], v201 offset:0\n"
      "ds_read_b128 v[160:163], v202 offset:0\n"
      "ds_read_b128 v[132:135], v201 offset:64\n"
      "ds_read_b128 v[164:167], v202 offset:64\n"
      "ds_read_b128 v[136:139], v201 offset:128\n"
      "ds_read_b128 v[168:171], v202 offset:128\n"
      "ds_read_b128 v[140:143], v201 offset:192\n"
      "ds_read_b128 v[172:175], v202 offset:192\n"
      "s_waitcnt lgkmcnt(0)\n"
      "v_pk_mul_f16 v96, v128, v160\n"
      "v_pk_mul_f16 v97, v129, v161\n"
      "v_pk_mul_f16 v98, v130, v162\n"
      "v_pk_mul_f16 v99, v131, v163\n"
      "v_pk_mul_f16 v100, v132, v164\n"
      "v_pk_mul_f16 v101, v133, v165\n"
      "v_pk_mul_f16 v102, v134, v166\n"
      "v_pk_mul_f16 v103, v135, v167\n"
      "v_pk_mul_f16 v104, v136, v168\n"
      "v_pk_mul_f16 v105, v137, v169\n"
      "v_pk_mul_f16 v106, v138, v170\n"
      "v_pk_mul_f16 v107, v139, v171\n"
      "s_nop 1\n"
      "s_branch 3b\n"
      "7:\n"
      "s_mov_b64 exec, -1\n"
      "s_cmp_lg_u32 s41, 0\n"
      "s_cbranch_scc1 8f\n"
      "s_mov_b32 s41, 1\n"
      "s_waitcnt vmcnt(0)\n"
      "s_branch 0b\n"
      "8:\n"
      "v_mov_b32 v222, 2\n"
      "s_mov_b32 s42, 1\n"
      "ds_write_b32 v2, v222 offset:8\n"
      "9:\n"
      "s_nop 7\n"
      "s_waitcnt vmcnt(0) lgkmcnt(0)\n"
      "s_mov_b32 %0, s42\n"
      : "=s"(flag)
      : "s"(__builtin_amdgcn_kernarg_segment_ptr()), "s"(blockIdx.x), "v"(threadIdx.x), "s"(lbase)
      :
      "v1", "v2", "v3", "v4", "v5", "v6", "v7", "v8", "v9", "v10", "v11", "v12", "v13", "v14", "v15", "v16", "v17", "v18", "v19", "v20", "v21", "v22", "v23", "v24",
      "v25", "v26", "v27", "v28", "v29", "v30", "v31", "v32", "v33", "v34", "v35", "v36", "v37", "v38", "v39", "v40", "v41", "v42", "v43", "v44", "v45", "v46", "v47", "v48",
      "v49", "v50", "v51", "v52", "v53", "v54", "v55", "v56", "v57", "v58", "v59", "v60", "v61", "v62", "v63", "v64", "v65", "v66", "v67", "v68", "v69", "v70", "v71", "v72",
      "v73", "v74", "v75", "v76", "v77", "v78", "v79", "v80", "v81", "v82", "v83", "v84", "v85", "v86", "v87", "v88", "v89", "v90", "v91", "v92", "v93", "v94", "v95", "v96",
      "v97", "v98", "v99", "v100", "v101", "v102", "v103", "v104", "v105", "v106", "v107", "v108", "v109", "v110", "v111", "v112", "v113", "v114", "v115", "v116", "v117", "v118", "v119", "v120",
      "v121", "v122", "v123", "v124", "v125", "v126", "v127", "v128", "v129", "v130", "v131", "v132", "v133", "v134", "v135", "v136", "v137", "v138", "v139", "v140", "v141", "v142", "v143", "v144",
      "v145", "v146", "v147", "v148", "v149", "v150", "v151", "v152", "v153", "v154", "v155", "v156", "v157", "v158", "v159", "v160", "v161", "v162", "v163", "v164", "v165", "v166", "v167", "v168",
      "v169", "v170", "v171", "v172", "v173", "v174", "v175", "v176", "v177", "v178", "v179", "v180", "v181", "v182", "v183", "v184", "v185", "v186", "v187", "v188", "v189", "v190", "v191", "v192",
      "v193", "v194", "v195", "v196", "v197", "v198", "v199", "v200", "v201", "v202", "v203", "v204", "v205", "v206", "v207", "v208", "v209", "v210", "v211", "v212", "v213", "v214", "v215", "v216",
      "v217", "v218", "v219", "v220", "v221", "v222", "v223", "v224", "v225", "v226", "v227", "v228", "v229", "v230", "v231", "v232", "v233", "v234", "v235", "v236", "v237", "v238", "v239", "v240",
      "v241", "v242", "v243", "v244", "v245", "v246", "v247", "v248", "v249", "v250", "v251", "v252", "v253", "v254", "v255", "a0", "a1", "a2", "a3", "a4", "a5", "a6", "a7", "a8",
      "a9", "a10", "a11", "a12", "a13", "a14", "a15", "a16", "a17", "a18", "a19", "a20", "a21", "a22", "a23", "a24", "a25", "a26", "a27", "a28", "a29", "a30", "a31", "a32",
      "a33", "a34", "a35", "a36", "a37", "a38", "a39", "a40", "a41", "a42", "a43", "a44", "a45", "a46", "a47", "a48", "a49", "a50", "a51", "a52", "a53", "a54", "a55", "a56",
      "a57", "a58", "a59", "a60", "a61", "a62", "a63", "a64", "a65", "a66", "a67", "a68", "a69", "a70", "a71", "a72", "a73", "a74", "a75", "a76", "a77", "a78", "a79", "a80",
      "a81", "a82", "a83", "a84", "a85", "a86", "a87", "a88", "a89", "a90", "a91", "a92", "a93", "a94", "a95", "a96", "a97", "a98", "a99", "a100", "a101", "a102", "a103", "a104",
      "a105", "a106", "a107", "a108", "a109", "a110", "a111", "a112", "a113", "a114", "a115", "a116", "a117", "a118", "a119", "a120", "a121", "a122", "a123", "a124", "a125", "a126", "a127", "a128",
      "a129", "a130", "a131", "a132", "a133", "a134", "a135", "a136", "a137", "a138", "a139", "a140", "a141", "a142", "a143", "a144", "a145", "a146", "a147", "a148", "a149", "a150", "a151", "a152",
      "a153", "a154", "a155", "a156", "a157", "a158", "a159", "a160", "a161", "a162", "a163", "a164", "a165", "a166", "a167", "a168", "a169", "a170", "a171", "a172", "a173", "a174", "a175", "a176",
      "a177", "a178", "a179", "a180", "a181", "a182", "a183", "a184", "a185", "a186", "a187", "a188", "a189", "a190", "a191", "a192", "a193", "a194", "a195", "a196", "a197", "a198", "a199", "a200",
      "a201", "a202", "a203", "a204", "a205", "a206", "a207", "a208", "a209", "a210", "a211", "a212", "a213", "a214", "a215", "a216", "a217", "s8", "s9", "s10", "s11", "s12", "s13", "s14",
      "s15", "s16", "s17", "s18", "s19", "s20", "s21", "s22", "s23", "s24", "s25", "s26", "s27", "s28", "s29", "s30", "s31", "s33", "s34", "s35", "s36", "s37", "s38", "s39",
      "s40", "s41", "s42", "s43", "s44", "s45", "s46", "s47", "s48", "s49", "s50", "s51", "s52", "s53", "s54", "s55", "s56", "s57", "s58", "s59", "s60", "s61", "s62", "s63",
      "s64", "s65", "s66", "s67", "s68", "s69", "s70", "s71", "s72", "s73", "s74", "s75", "s76", "s77", "s78", "s79", "s80", "s81", "s82", "s83", "s84", "s85", "s86", "s87",
      "s88", "s89", "s90", "s91", "s92", "s93", "s94", "s95", "s96", "s97", "s98", "s99", "s100", "s101", "vcc", "scc", "memory");
  if (flag | lbase) {
    const int F = 26, P = 325;
    const int lane = threadIdx.x & 63;
    const int b = (int)(min((blockIdx.x & 7) * kx + ((blockIdx.x >> 3) * 4 + (threadIdx.x >> 6)) * 104u, lim) / 104u);
    {
      float m = -INFINITY, z = 0.f, n = 0.f;
      for (int p = lane; p < P; p += 64) {
        const unsigned short* ei = table + (size_t)idx[b * F + ii[p]] * 128;
        const unsigned short* ej = table + (size_t)idx[b * F + jj[p]] * 128;
        float e = 0.f, s = 0.f;
        for (int a = 0; a < 128; ++a) {
          float acc = 0.f;
          for (int d = 0; d < 128; ++d) acc = fmaf(rbf(bf2f(ei[d]) * bf2f(ej[d])), bf2f(W[d * 128 + a]), acc);
          float x = acc + bf2f(bvec[a]);
          x = 0.5f * (x + fabsf(x));
          e = fmaf(x, bf2f(hvec[a]), e);
        }
        for (int d = 0; d < 128; ++d) s = fmaf(rbf(bf2f(ei[d]) * bf2f(ej[d])), bf2f(pvec[d]), s);
        float mn = fmaxf(m, e);
        float fa = __expf(m - mn), fb = __expf(e - mn);
        z = z * fa + fb; n = n * fa + fb * s; m = mn;
      }
      float mx = m;
      for (int o = 1; o < 64; o <<= 1) mx = fmaxf(mx, __shfl_xor(mx, o));
      float f = (m == -INFINITY) ? 0.f : __expf(m - mx);
      z *= f; n *= f;
      for (int o = 1; o < 64; o <<= 1) { z += __shfl_xor(z, o); n += __shfl_xor(n, o); }
      if (lane == 0) out[b] = f2bf(n / z);
    }
  }
}

extern "C" __global__ __launch_bounds__(256) void afm_any(const int* __restrict__ idx, const unsigned short* __restrict__ table,
                                   const int* __restrict__ ii, const int* __restrict__ jj,
                                   const unsigned short* __restrict__ W, const unsigned short* __restrict__ bvec,
                                   const unsigned short* __restrict__ hvec, const unsigned short* __restrict__ pvec,
                                   unsigned short* __restrict__ out, float* __restrict__ ws,
                                   int B, int F, int P, int D, int A) {
  const int b = blockIdx.x;
  float* ebuf = ws + (size_t)b * 2 * P;
  float* sbuf = ebuf + P;
  for (int p = threadIdx.x; p < P; p += 256) {
    const unsigned short* ei = table + (size_t)idx[b * F + ii[p]] * D;
    const unsigned short* ej = table + (size_t)idx[b * F + jj[p]] * D;
    float e = 0.f, sacc = 0.f;
    for (int a = 0; a < A; ++a) {
      float acc = 0.f;
      for (int d = 0; d < D; ++d) acc = fmaf(rbf(bf2f(ei[d]) * bf2f(ej[d])), bf2f(W[(size_t)d * A + a]), acc);
      float x = acc + bf2f(bvec[a]);
      x = 0.5f * (x + fabsf(x));
      e = fmaf(x, bf2f(hvec[a]), e);
    }
    for (int d = 0; d < D; ++d) sacc = fmaf(rbf(bf2f(ei[d]) * bf2f(ej[d])), bf2f(pvec[d]), sacc);
    ebuf[p] = e; sbuf[p] = sacc;
  }
  __syncthreads();
  if (threadIdx.x == 0) {
    float mx = -INFINITY;
    for (int p = 0; p < P; ++p) mx = fmaxf(mx, ebuf[p]);
    float z = 0.f, n = 0.f;
    for (int p = 0; p < P; ++p) { float w = __expf(ebuf[p] - mx); z += w; n += w * sbuf[p]; }
    out[b] = f2bf(n / z);
  }
}

extern "C" void kernel_launch(void* const* d_in, const int* in_sizes, int n_in,
                              void* d_out, int out_size, void* d_ws, size_t ws_size,
                              hipStream_t stream) {
  const int* idx = (const int*)d_in[0];
  const unsigned short* table = (const unsigned short*)d_in[1];
  const int* ii = (const int*)d_in[2];
  const int* jj = (const int*)d_in[3];
  const unsigned short* W = (const unsigned short*)d_in[4];
  const unsigned short* bvec = (const unsigned short*)d_in[5];
  const unsigned short* hvec = (const unsigned short*)d_in[6];
  const unsigned short* pvec = (const unsigned short*)d_in[7];
  unsigned short* out = (unsigned short*)d_out;
  const int B = out_size;
  const int P = in_sizes[2];
  const int D = in_sizes[7];
  const int A = in_sizes[5];
  if (B <= 0) return;
  const int F = in_sizes[0] / B;
  if (D == 128 && A == 128 && F == 26 && P == 325 && B < (1 << 24)) {
    const int g8 = (B + 31) / 32;
    afm_fast<<<8 * g8, 256, 0, stream>>>(idx, table, ii, jj, W, bvec, hvec, pvec, out, 104u * (unsigned)(B - 1), 416u * (unsigned)g8
        );
  } else {
    afm_any<<<B, 256, 0, stream>>>(idx, table, ii, jj, W, bvec, hvec, pvec, out, (float*)d_ws, B, F, P, D, A);
  }
}
```
